```python
import torch

def _softmax(x, axis=-1):
    return torch.softmax(x, dim=axis)
def _gelu(x):
    return torch.nn.functional.gelu(x, approximate="tanh")
def _silu(x):
    return torch.nn.functional.silu(x)
def _sigmoid(x):
    return torch.sigmoid(x)
def _layernorm(x, axis=-1, eps=1e-5):
    m = x.mean(dim=axis, keepdim=True)
    v = x.var(dim=axis, keepdim=True, unbiased=False)
    return (x - m) / torch.sqrt(v + eps)
def _scatter_add(tgt, idx, src, axis=0):
    return tgt.clone().index_add_(axis, idx, src)
def _segment_sum(src, seg, n):
    out = torch.zeros((n,) + src.shape[1:], dtype=src.dtype)
    return out.index_add_(0, seg, src)
def _pad(x, pw):
    flat = tuple(v for pair in reversed(pw) for v in pair)
    return torch.nn.functional.pad(x, flat)

kernel_name = 'recurrent_scan__linear_kernel_hedgehog__dense_activation_mish__prompt'

def setup_inputs():

    _w = (lambda a: (lambda t: t.to(torch.bfloat16) if t.is_floating_point() else t)(torch.as_tensor(a)))
    def _A_setup():
        B, S, D, H = 1, 2048, 1024, 32
        Dh = D // H
        return {
            "x": _w((lambda *s: torch.randn(*s, dtype=torch.bfloat16) * 0.05)(B, S, D)),
            "wqkv": _w((lambda *s: torch.randn(*s, dtype=torch.bfloat16) * 0.2)(D, 3 * D)),
            "w_phi": _w((lambda *s: torch.randn(*s, dtype=torch.bfloat16) * 0.2)(H, Dh, Dh) * (Dh ** -0.5)),
            "wo": _w((lambda *s: torch.randn(*s, dtype=torch.bfloat16) * 0.2)(D, D)),
        }
    _A_in = _A_setup()

    def _B_setup():
        B, S, D, F = 1, 2048, 1024, 3584
        return {
            "x": _w((lambda *s: torch.randn(*s, dtype=torch.bfloat16) * 0.05)(B, S, D)),
            "w1": _w((lambda *s: torch.randn(*s, dtype=torch.bfloat16) * 0.4)(D, F) * (D ** -0.5)),
            "b1": _w((lambda *s: torch.randn(*s, dtype=torch.bfloat16) * 0.2)(F)),
            "w2": _w((lambda *s: torch.randn(*s, dtype=torch.bfloat16) * 0.1)(F, D) * (F ** -0.5)),
        }
    _B_in = _B_setup()

    return {
        "A_x": _A_in["x"],
        "A_wqkv": _A_in["wqkv"],
        "A_w_phi": _A_in["w_phi"],
        "A_wo": _A_in["wo"],
        "B_x": _B_in["x"],
        "B_w1": _B_in["w1"],
        "B_b1": _B_in["b1"],
        "B_w2": _B_in["w2"],
    }

def reference(A_x, A_wqkv, A_w_phi, A_wo, B_x, B_w1, B_b1, B_w2):
    def _A_ref(x, wqkv, w_phi, wo):
        B, S, D = x.shape
        H = 32; Dh = D // H
        qkv = (lambda x, *d: x.permute(*d))((x @ wqkv).reshape(B, S, 3, H, Dh), 2, 0, 3, 1, 4)
        q, k, v = qkv[0], qkv[1], qkv[2]
        pq = torch.einsum("bhsd,hdf->bhsf", q, w_phi)
        pk = torch.einsum("bhsd,hdf->bhsf", k, w_phi)
        phi_q = torch.concatenate([_softmax(pq, axis=-1), _softmax(-pq, axis=-1)], axis=-1)
        phi_k = torch.concatenate([_softmax(pk, axis=-1), _softmax(-pk, axis=-1)], axis=-1)
        kv = torch.einsum("bhsf,bhsd->bhfd", phi_k, v)
        z  = torch.einsum("bhsf->bhf", phi_k)
        num = torch.einsum("bhqf,bhfd->bhqd", phi_q, kv)
        den = torch.einsum("bhqf,bhf->bhq", phi_q, z)[..., None] + 1e-6
        o = num / den
        return (lambda x, *d: x.permute(*d))(o, 0, 2, 1, 3).reshape(B, S, D) @ wo

    def _B_ref(x, w1, b1, w2):
        h = torch.einsum("bsd,df->bsf", x, w1) + b1
        sp = torch.log1p(torch.exp(-torch.abs(h))) + torch.where(h > 0, h, torch.zeros_like(h))
        act = h * torch.tanh(sp)
        return torch.einsum("bsf,fd->bsd", act, w2)

    x = A_x
    xn = _layernorm(x, axis=-1)
    s = _A_ref(xn, A_wqkv, A_w_phi, A_wo)
    g = _silu(_B_ref(xn, B_w1, B_b1, B_w2))
    return x + s * g
```

```cpp
#include <hip/hip_runtime.h>
#include <hip/hip_bf16.h>
#include <type_traits>

namespace {
#define LAS __attribute__((address_space(3)))
#define GAS __attribute__((address_space(1)))
typedef unsigned short u16;
typedef unsigned u32x4 __attribute__((ext_vector_type(4)));
typedef unsigned u32x2 __attribute__((ext_vector_type(2)));
typedef short bf16x8 __attribute__((ext_vector_type(8)));
typedef short s16x4 __attribute__((ext_vector_type(4)));
typedef float f32x4 __attribute__((ext_vector_type(4)));
typedef float f32x2 __attribute__((ext_vector_type(2)));
typedef __bf16 bf16x2_t __attribute__((ext_vector_type(2)));

constexpr int S_ = 2048, D_ = 1024, F_ = 3584;
constexpr size_t WS_FLAG = 0;
constexpr size_t WS_XN = 1u << 20;
constexpr size_t WS_ACT = WS_XN + (4u << 20);
constexpr size_t WS_O = WS_ACT + (15u << 20);
constexpr size_t WS_KVP = WS_O + (4u << 20);
constexpr size_t WS_ZP = WS_KVP + (2u << 20);
constexpr size_t WS_YX = WS_ZP + (1u << 20);
constexpr unsigned FL_KV = 0;
constexpr unsigned FL_YX = 16384;
constexpr unsigned FL_TMO = 49152;
constexpr unsigned TAG = 1u;
constexpr float LOG2E = 1.4426950408889634f;

#define SB __builtin_amdgcn_sched_barrier(0)
#define BAR __builtin_amdgcn_s_barrier()
#define WAIT_L0 do { __builtin_amdgcn_s_waitcnt(0xC07F); SB; } while (0)
#define WAIT_V(n) asm volatile("s_waitcnt vmcnt(%0)" :: "i"(n) : "memory")
template <int N> using IC = std::integral_constant<int, N>;

__device__ __forceinline__ u32x4 mk_srd(const void* p, unsigned bytes) {
  const unsigned long long a = (unsigned long long)p; u32x4 r;
  r.x = __builtin_amdgcn_readfirstlane((unsigned)a); r.y = __builtin_amdgcn_readfirstlane((unsigned)(a >> 32)) & 0xffffu; r.z = bytes; r.w = 0x00020000u; return r; }
#define DMA(srd, voff, sk, KOFF, sl, LOFF) do { unsigned _t; asm volatile("s_add_u32 m0, %4, %5\n\ts_add_u32 %0, %3, %6\n\tbuffer_load_dwordx4 %1, %2, %0 offen lds" \
    : "=&s"(_t) : "v"(voff), "s"(srd), "s"(sk), "s"(sl), "i"(LOFF), "i"(KOFF) : "memory", "scc"); SB; } while (0)
__device__ __forceinline__ bf16x8 lds128(unsigned a) { return *(const LAS bf16x8*)(size_t)a; }
__device__ __forceinline__ s16x4 ldstr(unsigned a) { return __builtin_amdgcn_ds_read_tr16_b64_v4i16((LAS s16x4*)(size_t)a); }
__device__ __forceinline__ bf16x8 cat(s16x4 lo, s16x4 hi) { return __builtin_shufflevector(lo, hi, 0, 1, 2, 3, 4, 5, 6, 7); }
__device__ __forceinline__ unsigned opq(unsigned x) { asm volatile("" : "+v"(x)); return x; }
__device__ __forceinline__ f32x4 mfma(bf16x8 a, bf16x8 b, f32x4 c) { return __builtin_amdgcn_mfma_f32_16x16x32_bf16(a, b, c, 0, 0, 0); }
__device__ __forceinline__ void mfma_acc(f32x4& d, bf16x8 a, bf16x8 b) { asm volatile("v_mfma_f32_16x16x32_bf16 %0, %1, %2, %0" : "+v"(d) : "v"(a), "v"(b)); }
__device__ __forceinline__ void mfma_k2(f32x4& d, bf16x8 a0, bf16x8 b0, bf16x8 a1, bf16x8 b1) {
  asm volatile("v_mfma_f32_16x16x32_bf16 %0, %1, %2, %0\n\tv_mfma_f32_16x16x32_bf16 %0, %3, %4, %0" : "+v"(d) : "v"(a0), "v"(b0), "v"(a1), "v"(b1)); }
__device__ __forceinline__ void mfma_k2x2(f32x4& d, f32x4& e, bf16x8 a0, bf16x8 c0, bf16x8 b0, bf16x8 a1, bf16x8 c1, bf16x8 b1) {
  asm volatile("v_mfma_f32_16x16x32_bf16 %0, %2, %4, %0\n\tv_mfma_f32_16x16x32_bf16 %1, %3, %4, %1\n\tv_mfma_f32_16x16x32_bf16 %0, %5, %7, %0\n\tv_mfma_f32_16x16x32_bf16 %1, %6, %7, %1"
               : "+v"(d), "+v"(e) : "v"(a0), "v"(c0), "v"(b0), "v"(a1), "v"(c1), "v"(b1)); }
#define MFMA_PAD do { asm volatile("s_nop 7\n\ts_nop 1" ::: "memory"); SB; } while (0)
__device__ __forceinline__ unsigned pk2(float lo, float hi) { const f32x2 v = {lo, hi}; return __builtin_bit_cast(unsigned, __builtin_convertvector(v, bf16x2_t)); }
__device__ __forceinline__ bf16x8 pk8(f32x4 a, f32x4 b) { const u32x4 u = {pk2(a[0], a[1]), pk2(a[2], a[3]), pk2(b[0], b[1]), pk2(b[2], b[3])}; return __builtin_bit_cast(bf16x8, u); }
__device__ __forceinline__ float bflo(unsigned u) { return __uint_as_float(u << 16); }
__device__ __forceinline__ float bfhi(unsigned u) { return __uint_as_float(u & 0xffff0000u); }
__device__ __forceinline__ float ex2(float x) { return __builtin_amdgcn_exp2f(x); }
__device__ __forceinline__ float rcp(float x) { return __builtin_amdgcn_rcpf(x); }
__device__ __forceinline__ u32x4 swap16(unsigned a0, unsigned a1, unsigned b0, unsigned b1) {
  const auto p = __builtin_amdgcn_permlane16_swap(a0, b0, false, false), q = __builtin_amdgcn_permlane16_swap(a1, b1, false, false);
  return (u32x4){p[0], q[0], p[1], q[1]}; }
template <class Op> __device__ __forceinline__ float red4(float v, Op op) {
  const unsigned u = __float_as_uint(v); const auto p = __builtin_amdgcn_permlane32_swap(u, u, false, false);
  const float a = op(__uint_as_float(p[0]), __uint_as_float(p[1])); const unsigned ua = __float_as_uint(a);
  const auto q = __builtin_amdgcn_permlane16_swap(ua, ua, false, false); return op(__uint_as_float(q[0]), __uint_as_float(q[1])); }
__device__ __forceinline__ void st16_sc1(u32x4 v, __amdgpu_buffer_rsrc_t r, unsigned voff, unsigned soff) { __builtin_amdgcn_raw_buffer_store_b128(v, r, voff + soff, 0, 16); }
__device__ __forceinline__ void st16(u32x4 v, __amdgpu_buffer_rsrc_t r, unsigned voff, unsigned soff) { __builtin_amdgcn_raw_buffer_store_b128(v, r, voff + soff, 0, 0); }
__device__ __forceinline__ u32x4 ld16_sc1(__amdgpu_buffer_rsrc_t r, unsigned voff, unsigned soff) { return __builtin_amdgcn_raw_buffer_load_b128(r, voff, soff, 16); }
__device__ __forceinline__ __amdgpu_buffer_rsrc_t mk_rsrc(const void* p, unsigned bytes) { return __builtin_amdgcn_make_buffer_rsrc((void*)p, 0, bytes, 0x00020000); }
#define RLX_AGENT __ATOMIC_RELAXED, __HIP_MEMORY_SCOPE_AGENT
__device__ __forceinline__ void wait_flags(GAS unsigned* w, int n, unsigned tag, GAS unsigned* tmo, int lane) {
  for (unsigned spins = 0;; ++spins) {
    unsigned v = tag; if (lane < n) v = __hip_atomic_load(w + lane, RLX_AGENT);
    if (__all(v == tag)) break;
    if (spins > (1u << 16)) { if (lane == 0) __hip_atomic_store(tmo, 1u, RLX_AGENT); break; }
    __builtin_amdgcn_s_sleep(1);
  }
  __builtin_amdgcn_fence(__ATOMIC_ACQUIRE, "wavefront");
}
__device__ __forceinline__ void lds_sync(unsigned addr, unsigned target, int lane) {
  asm volatile("s_waitcnt lgkmcnt(0)" ::: "memory");
  if (lane == 0) { const unsigned one = 1; asm volatile("ds_add_u32 %0, %1" :: "v"(addr), "v"(one) : "memory"); }
  for (;;) { unsigned v; asm volatile("ds_read_b32 %0, %1\n\ts_waitcnt lgkmcnt(0)" : "=v"(v) : "v"(addr) : "memory");
    if ((unsigned)__builtin_amdgcn_readfirstlane(v) >= target) break; }
}

__global__ __launch_bounds__(512) void k_ln(const u16* __restrict__ x, u16* __restrict__ xn, unsigned* __restrict__ flags) {
  const int tid = threadIdx.x, lane = tid & 63, row = blockIdx.x * 8 + (tid >> 6);
  if (tid < 64) flags[blockIdx.x * 64 + tid] = 0u;
  const u32x4* src = (const u32x4*)(x + (size_t)row * D_);
  const u32x4 a = src[lane], b = src[lane + 64];
  float v[16];
#pragma unroll
  for (int i = 0; i < 4; ++i) { v[2 * i] = bflo(a[i]); v[2 * i + 1] = bfhi(a[i]); v[8 + 2 * i] = bflo(b[i]); v[9 + 2 * i] = bfhi(b[i]); }
  float s = 0.f;
#pragma unroll
  for (int i = 0; i < 16; ++i) s += v[i];
#pragma unroll
  for (int o = 32; o >= 1; o >>= 1) s += __shfl_xor(s, o);
  const float mean = s * (1.0f / D_); float q = 0.f;
#pragma unroll
  for (int i = 0; i < 16; ++i) { v[i] -= mean; q += v[i] * v[i]; }
#pragma unroll
  for (int o = 32; o >= 1; o >>= 1) q += __shfl_xor(q, o);
  const float rstd = 1.0f / sqrtf(q * (1.0f / D_) + 1e-5f);
  u32x4 oa, ob;
#pragma unroll
  for (int i = 0; i < 4; ++i) { oa[i] = pk2(v[2 * i] * rstd, v[2 * i + 1] * rstd); ob[i] = pk2(v[8 + 2 * i] * rstd, v[9 + 2 * i] * rstd); }
  u32x4* dst = (u32x4*)(xn + (size_t)row * D_);
  dst[lane] = oa; dst[lane + 64] = ob;
}

constexpr unsigned L1_A = 0;
constexpr unsigned L1_BQ = 65536;
constexpr unsigned L1_BH = 90112;
constexpr unsigned L1_WPHI = 118784;
constexpr unsigned L1_CNT = 120832;
constexpr unsigned L1_KVA = 121856;
constexpr unsigned L1_V = 98304;
constexpr unsigned L1_PHI = 131072;

struct P1 { const u16 *xn, *wqkv, *wphi, *w1, *b1; char* ws; };

__global__ __launch_bounds__(512, 2) void k_phase1(const u16* xn, const u16* wqkv, const u16* wphi, const u16* w1, const u16* b1, char* ws) {
  __shared__ __attribute__((aligned(16))) char smem[163840];
  const unsigned lb = (unsigned)(size_t)(LAS char*)smem;
  const int tid = threadIdx.x, lane = tid & 63, w = __builtin_amdgcn_readfirstlane(tid >> 6), wx = w & 3;
  const int b = blockIdx.x, h = (b & 7) * 4 + ((b >> 3) & 3), p = b >> 5, odd = h & 1;
  const int tok = lane & 15, g = lane >> 4, q = (lane >> 2) & 3, pp = lane & 3;
  const unsigned ra0 = lb + L1_A + (wx * 64 + tok) * 128 + ((g ^ (tok >> 1)) << 4), ra1 = ra0 ^ 64;
  if (tid == 0) *(LAS unsigned*)(size_t)(lb + L1_CNT) = 0u;
  GAS unsigned* flags = (GAS unsigned*)(ws + WS_FLAG);

  if (w < 4) {
    const u32x4 sa = mk_srd(xn, S_ * D_ * 2);
    unsigned va0, va1;
    { const int r = lane >> 3; va0 = (p * 256 + wx * 64 + r) * 2048 + (((lane & 7) ^ (r >> 1)) << 4); va1 = va0 ^ 64; }
    const unsigned sl = lb + L1_A + wx * 8192;
    const unsigned rb0 = opq(lb + L1_BH + (8 * g + 4 * (g & 1) + q) * 224 + 8 * pp), rb1 = opq(lb + L1_BH + (8 * g + 4 * ((g & 1) ^ 1) + q) * 224 + 8 * pp);
    f32x4 acc[4][7];
#pragma unroll
    for (int m = 0; m < 4; ++m)
#pragma unroll
      for (int j = 0; j < 7; ++j) acc[m][j] = (f32x4){0.f, 0.f, 0.f, 0.f};
    bf16x8 fa[2][4], fb[2][7];
    auto dmaA = [&](auto ST, auto J, unsigned sk) { constexpr int st = ST(), j = J(); if (j & 1) DMA(sa, va1, sk, j * 16384, sl, st * 32768 + j * 1024); else DMA(sa, va0, sk, j * 16384, sl, st * 32768 + j * 1024); };
    auto rdA = [&](auto ST, auto KS, auto M) { constexpr int st = ST(), ks = KS(), m = M(); fa[ks][m] = lds128((ks ? ra1 : ra0) + st * 32768 + m * 2048); SB; };
    auto rdB = [&](auto ST, auto KS, auto J) { constexpr int st = ST(), ks = KS(), j = J(); constexpr int o = st * 14336 + ks * 7168 + j * 32; fb[ks][j] = cat(ldstr(rb0 + o), ldstr(rb1 + o)); SB; };
    auto load = [&](auto ST, auto DO, unsigned sk) {
      constexpr int o = ST() ^ 1; constexpr bool d = DO();
      rdA(ST, IC<0>{}, IC<0>{}); rdB(ST, IC<0>{}, IC<0>{}); rdB(ST, IC<0>{}, IC<1>{}); if (d) dmaA(IC<o>{}, IC<0>{}, sk);
      rdA(ST, IC<0>{}, IC<1>{}); rdB(ST, IC<0>{}, IC<2>{}); rdB(ST, IC<0>{}, IC<3>{}); if (d) dmaA(IC<o>{}, IC<1>{}, sk);
      rdA(ST, IC<0>{}, IC<2>{}); rdB(ST, IC<0>{}, IC<4>{}); rdB(ST, IC<0>{}, IC<5>{}); if (d) dmaA(IC<o>{}, IC<2>{}, sk);
      rdA(ST, IC<0>{}, IC<3>{}); rdB(ST, IC<0>{}, IC<6>{}); rdB(ST, IC<1>{}, IC<0>{}); if (d) dmaA(IC<o>{}, IC<3>{}, sk);
      rdA(ST, IC<1>{}, IC<0>{}); rdB(ST, IC<1>{}, IC<1>{}); rdB(ST, IC<1>{}, IC<2>{}); if (d) dmaA(IC<o>{}, IC<4>{}, sk);
      rdA(ST, IC<1>{}, IC<1>{}); rdB(ST, IC<1>{}, IC<3>{}); rdB(ST, IC<1>{}, IC<4>{}); if (d) dmaA(IC<o>{}, IC<5>{}, sk);
      rdA(ST, IC<1>{}, IC<2>{}); rdB(ST, IC<1>{}, IC<5>{}); if (d) dmaA(IC<o>{}, IC<6>{}, sk);
      rdA(ST, IC<1>{}, IC<3>{}); rdB(ST, IC<1>{}, IC<6>{}); if (d) dmaA(IC<o>{}, IC<7>{}, sk);
    };
    auto mm = [&]() {
#pragma unroll
      for (int m = 0; m < 4; ++m) {
#pragma unroll
        for (int j = 0; j < 6; j += 2) mfma_k2x2(acc[m][j], acc[m][j + 1], fb[0][j], fb[0][j + 1], fa[0][m], fb[1][j], fb[1][j + 1], fa[1][m]);
        mfma_k2(acc[m][6], fb[0][6], fa[0][m], fb[1][6], fa[1][m]); }
      SB; };
#define A8(ST, sk) dmaA(IC<ST>{}, IC<0>{}, sk); dmaA(IC<ST>{}, IC<1>{}, sk); dmaA(IC<ST>{}, IC<2>{}, sk); dmaA(IC<ST>{}, IC<3>{}, sk); dmaA(IC<ST>{}, IC<4>{}, sk); dmaA(IC<ST>{}, IC<5>{}, sk); dmaA(IC<ST>{}, IC<6>{}, sk); dmaA(IC<ST>{}, IC<7>{}, sk)
    A8(0, 0u); A8(1, 128u);
    WAIT_V(8); WAIT_L0; BAR; SB;
    load(IC<0>{}, IC<0>{}, 0u); WAIT_L0; BAR; SB;
    for (int t = 0; t < 14; t += 2) {
      mm(); WAIT_V(0); BAR; SB;
      load(IC<1>{}, IC<1>{}, (unsigned)(t + 2) * 128u); WAIT_L0; BAR; SB;
      mm(); WAIT_V(0); BAR; SB;
      load(IC<0>{}, IC<1>{}, (unsigned)(t + 3) * 128u); WAIT_L0; BAR; SB;
    }
    mm(); WAIT_V(0); BAR; SB;
    load(IC<1>{}, IC<0>{}, 0u);
    u32x2 bias[7];
#pragma unroll
    for (int j = 0; j < 7; ++j) { const int cb = 7 * h + (j < 6 ? j + odd : (odd ? 0 : 6)); bias[j] = *(const GAS u32x2*)((const GAS u16*)b1 + cb * 16 + 4 * g); }
    WAIT_L0; BAR; SB;
    mm(); BAR; MFMA_PAD;
    const __amdgpu_buffer_rsrc_t ract = mk_rsrc(ws + WS_ACT, 128 * 112 * 1024);
    auto mish4 = [&](f32x4 a, u32x2 bb) -> u32x2 {
      const f32x4 bv = {bflo(bb[0]), bfhi(bb[0]), bflo(bb[1]), bfhi(bb[1])}; const f32x4 hv = a + bv, hs = hv * LOG2E; f32x4 e, r;
#pragma unroll
      for (int i = 0; i < 4; ++i) e[i] = ex2(hs[i]);
      const f32x4 d = e * (e + 2.0f) + 2.0f;
#pragma unroll
      for (int i = 0; i < 4; ++i) r[i] = rcp(d[i]);
      const f32x4 o = hv * (1.0f - 2.0f * r); return (u32x2){pk2(o[0], o[1]), pk2(o[2], o[3])}; };
    const unsigned vpair = (unsigned)((((g & 1) << 1) | (g >> 1)) * 256 + tok * 16), mb0 = p * 16 + wx * 4;
#pragma unroll
    for (int m = 0; m < 4; ++m)
#pragma unroll
      for (int j = 0; j < 6; j += 2) { const u32x2 a = mish4(acc[m][j], bias[j]), c = mish4(acc[m][j + 1], bias[j + 1]);
        st16(swap16(a[0], a[1], c[0], c[1]), ract, vpair, ((mb0 + m) * 112 + ((7 * h + odd + j) >> 1)) * 1024); }
    const unsigned vsing = (unsigned)((g & 1) * 112 * 1024 + (2 * odd + (g >> 1)) * 256 + tok * 16);
#pragma unroll
    for (int m = 0; m < 4; m += 2) { const u32x2 a = mish4(acc[m][6], bias[6]), c = mish4(acc[m + 1][6], bias[6]);
      st16(swap16(a[0], a[1], c[0], c[1]), ract, vsing, ((mb0 + m) * 112 + ((7 * h + (odd ? 0 : 6)) >> 1)) * 1024); }
  } else {
    const u32x4 sq = mk_srd(wqkv, D_ * 3 * D_ * 2), sh = mk_srd(w1, D_ * F_ * 2), sp = mk_srd(wphi, 32 * 32 * 32 * 2);
    unsigned vq, vh[4];
    { const int r = lane >> 2; vq = (wx * 16 + r) * 6144 + h * 64 + (((lane & 3) ^ (((r >> 3) & 1) << 1)) << 4); }
#pragma unroll
    for (int i = 0; i < 4; ++i) { const int o = (wx + 4 * i) * 1024 + lane * 16, rp = o / 224, c = (o - rp * 224) >> 4, k = rp ^ (((rp >> 3) & 1) << 2); int cs = c + 2 * odd; if (cs >= 14) cs -= 14;
      vh[i] = k * 7168 + h * 224 + cs * 16; }
    const unsigned slq = lb + L1_BQ + wx * 1024, slh = lb + L1_BH + wx * 1024;
    const unsigned rq0 = opq(lb + L1_BQ + (8 * g + q) * 64 + 32 * (g & 1) + 8 * pp), rq1 = opq(rq0 ^ 32);
    f32x4 acc[3][4][2];
#pragma unroll
    for (int u = 0; u < 3; ++u)
#pragma unroll
      for (int m = 0; m < 4; ++m) { acc[u][m][0] = (f32x4){0.f, 0.f, 0.f, 0.f}; acc[u][m][1] = acc[u][m][0]; }
    bf16x8 fa[2][4], fb[2][6];
    auto dmaQ = [&](auto ST, auto U, unsigned sk) { constexpr int st = ST(), u = U(); DMA(sq, vq, sk, u * 2048, slq, st * 12288 + u * 4096); };
    auto dmaH = [&](auto ST, auto I, unsigned sk) { constexpr int st = ST(), i = I(); if (i < 3 || wx < 2) DMA(sh, vh[i], sk, 0, slh, st * 14336 + i * 4096); };
    auto rdA = [&](auto ST, auto KS, auto M) { constexpr int st = ST(), ks = KS(), m = M(); fa[ks][m] = lds128((ks ? ra1 : ra0) + st * 32768 + m * 2048); SB; };
    auto rdB = [&](auto ST, auto KS, auto J) { constexpr int st = ST(), ks = KS(), j = J(); constexpr int o = st * 12288 + (j >> 1) * 4096 + ks * 2048; const unsigned a = (j & 1) ? rq1 : rq0;
      fb[ks][j] = cat(ldstr(a + o), ldstr(a + o + 256)); SB; };
    auto load = [&](auto ST, auto DQ, auto DH, int t) {
      constexpr int o = ST() ^ 1; constexpr bool dq = DQ(), dh = DH(); const unsigned skq = (unsigned)(t + 1) * (64 * 6144), skh = (unsigned)(t + 2) * (64 * 7168);
      rdA(ST, IC<0>{}, IC<0>{}); rdB(ST, IC<0>{}, IC<0>{}); rdB(ST, IC<0>{}, IC<1>{}); if (dq) dmaQ(IC<o>{}, IC<0>{}, skq);
      rdA(ST, IC<0>{}, IC<1>{}); rdB(ST, IC<0>{}, IC<2>{}); rdB(ST, IC<0>{}, IC<3>{}); if (dq) dmaQ(IC<o>{}, IC<1>{}, skq);
      rdA(ST, IC<0>{}, IC<2>{}); rdB(ST, IC<0>{}, IC<4>{}); rdB(ST, IC<0>{}, IC<5>{}); if (dq) dmaQ(IC<o>{}, IC<2>{}, skq);
      rdA(ST, IC<0>{}, IC<3>{}); rdB(ST, IC<1>{}, IC<0>{}); if (dh) dmaH(ST, IC<0>{}, skh);
      rdA(ST, IC<1>{}, IC<0>{}); rdB(ST, IC<1>{}, IC<1>{}); rdB(ST, IC<1>{}, IC<2>{}); if (dh) dmaH(ST, IC<1>{}, skh);
      rdA(ST, IC<1>{}, IC<1>{}); rdB(ST, IC<1>{}, IC<3>{}); if (dh) dmaH(ST, IC<2>{}, skh);
      rdA(ST, IC<1>{}, IC<2>{}); rdB(ST, IC<1>{}, IC<4>{});
      rdA(ST, IC<1>{}, IC<3>{}); rdB(ST, IC<1>{}, IC<5>{});
      if (dh) { if (wx < 2) { dmaH(ST, IC<3>{}, skh); WAIT_V(7); } else WAIT_V(6); }
    };
    auto mm = [&]() {
#pragma unroll
      for (int m = 0; m < 4; ++m)
#pragma unroll
        for (int u = 0; u < 3; ++u) mfma_k2x2(acc[u][m][0], acc[u][m][1], fb[0][2 * u], fb[0][2 * u + 1], fa[0][m], fb[1][2 * u], fb[1][2 * u + 1], fa[1][m]);
      SB; };
    if (wx < 2) { const unsigned vp = h * 2048 + wx * 1024 + lane * 16, slp = lb + L1_WPHI + wx * 1024; DMA(sp, vp, 0u, 0, slp, 0); }
#define H4(ST, sk) dmaH(IC<ST>{}, IC<0>{}, sk); dmaH(IC<ST>{}, IC<1>{}, sk); dmaH(IC<ST>{}, IC<2>{}, sk); dmaH(IC<ST>{}, IC<3>{}, sk)
    H4(0, 0u); dmaQ(IC<0>{}, IC<0>{}, 0u); dmaQ(IC<0>{}, IC<1>{}, 0u); dmaQ(IC<0>{}, IC<2>{}, 0u); H4(1, 64u * 7168u);
    WAIT_V(3); WAIT_L0; BAR; SB;
    BAR; SB;
    for (int t = 0; t < 14; t += 2) {
      load(IC<0>{}, IC<1>{}, IC<1>{}, t); WAIT_L0; BAR; SB;
      mm(); WAIT_V(3); BAR; SB;
      load(IC<1>{}, IC<1>{}, IC<1>{}, t + 1); WAIT_L0; BAR; SB;
      mm(); WAIT_V(3); BAR; SB;
    }
    load(IC<0>{}, IC<1>{}, IC<0>{}, 14); WAIT_V(3); WAIT_L0; BAR; SB;
    mm(); WAIT_V(0); BAR; SB;
    load(IC<1>{}, IC<0>{}, IC<0>{}, 15); WAIT_L0; BAR; SB;
    mm(); MFMA_PAD;
    bf16x8 wp[2];
#pragma unroll
    for (int ff = 0; ff < 2; ++ff) { const unsigned a = lb + L1_WPHI + (4 * g + q) * 64 + ff * 32 + 8 * pp; wp[ff] = cat(ldstr(a), ldstr(a + 1024)); }
    const f32x4 zero4 = {0.f, 0.f, 0.f, 0.f};
    auto fmax_ = [](float a, float c) { return fmaxf(a, c); }; auto fmin_ = [](float a, float c) { return fminf(a, c); }; auto fadd_ = [](float a, float c) { return a + c; };
    auto expo = [&](const f32x4& a0, const f32x4& a1, f32x4 (&ep)[2], f32x4 (&en)[2]) {
      const bf16x8 kb = pk8(a0, a1); const f32x4 p0 = mfma(wp[0], kb, zero4), p1 = mfma(wp[1], kb, zero4);
      float mx = fmaxf(fmaxf(fmaxf(p0[0], p0[1]), fmaxf(p0[2], p0[3])), fmaxf(fmaxf(p1[0], p1[1]), fmaxf(p1[2], p1[3])));
      float mn = fminf(fminf(fminf(p0[0], p0[1]), fminf(p0[2], p0[3])), fminf(fminf(p1[0], p1[1]), fminf(p1[2], p1[3])));
      mx = red4(mx, fmax_) * LOG2E; mn = red4(mn, fmin_) * LOG2E;
      const f32x4 u0 = p0 * LOG2E - mx, u1 = p1 * LOG2E - mx, v0 = mn - p0 * LOG2E, v1 = mn - p1 * LOG2E;
#pragma unroll
      for (int r = 0; r < 4; ++r) { ep[0][r] = ex2(u0[r]); ep[1][r] = ex2(u1[r]); en[0][r] = ex2(v0[r]); en[1][r] = ex2(v1[r]); } };
    const unsigned swp = (tok & 1) | (((tok >> 2) & 1) << 1) | (((tok >> 1) & 1) << 2) | (tok & 8);
#pragma unroll
    for (int m = 0; m < 4; ++m) {
      f32x4 ep[2], en[2]; expo(acc[1][m][0], acc[1][m][1], ep, en);
      const f32x4 sp4 = ep[0] + ep[1], sn4 = en[0] + en[1];
      const float rp_ = rcp(red4((sp4[0] + sp4[1]) + (sp4[2] + sp4[3]), fadd_)), rn_ = rcp(red4((sn4[0] + sn4[1]) + (sn4[2] + sn4[3]), fadd_));
      const unsigned row = lb + L1_PHI + (wx * 64 + m * 16 + tok) * 128;
#pragma unroll
      for (int ff = 0; ff < 2; ++ff) { const f32x4 a = ep[ff] * rp_, c = en[ff] * rn_;
        *(LAS u32x2*)(size_t)(row + (((4 * ff + g) ^ swp) << 3)) = (u32x2){pk2(a[0], a[1]), pk2(a[2], a[3])};
        *(LAS u32x2*)(size_t)(row + (((8 + 4 * ff + g) ^ swp) << 3)) = (u32x2){pk2(c[0], c[1]), pk2(c[2], c[3])}; }
      const unsigned vrow = lb + L1_V + (wx * 64 + m * 16 + tok) * 64;
#pragma unroll
      for (int df = 0; df < 2; ++df) { const f32x4 a = acc[2][m][df]; *(LAS u32x2*)(size_t)(vrow + (((4 * df + g) ^ (tok >> 1)) << 3)) = (u32x2){pk2(a[0], a[1]), pk2(a[2], a[3])}; }
    }
    lds_sync(lb + L1_CNT, 4u, lane);
    f32x4 kv0 = zero4, kv1 = zero4, zz = zero4;
    { const bf16x8 ones = {0x3F80, 0x3F80, 0x3F80, 0x3F80, 0x3F80, 0x3F80, 0x3F80, 0x3F80};
      unsigned ap[2], av[2][2];
#pragma unroll
      for (int t = 0; t < 2; ++t) { const int tk = 8 * g + 4 * t + q, t15 = tk & 15; const unsigned sw = (t15 & 1) | (((t15 >> 2) & 1) << 1) | (((t15 >> 1) & 1) << 2) | (t15 & 8);
        ap[t] = lb + L1_PHI + tk * 128 + (((4 * wx + pp) ^ sw) << 3);
        av[t][0] = lb + L1_V + tk * 64 + ((pp ^ (t15 >> 1)) << 3); av[t][1] = av[t][0] ^ 32; }
#pragma unroll
      for (int ks = 0; ks < 8; ++ks) { const bf16x8 a = cat(ldstr(ap[0] + ks * 4096), ldstr(ap[1] + ks * 4096));
        const bf16x8 b0 = cat(ldstr(av[0][0] + ks * 2048), ldstr(av[1][0] + ks * 2048)), b1_ = cat(ldstr(av[0][1] + ks * 2048), ldstr(av[1][1] + ks * 2048));
        kv0 = mfma(a, b0, kv0); kv1 = mfma(a, b1_, kv1); zz = mfma(a, ones, zz); } }
    const __amdgpu_buffer_rsrc_t rkv = mk_rsrc(ws + WS_KVP, 32 * 8 * 8192), rz = mk_rsrc(ws + WS_ZP, 32 * 8 * 256);
    st16_sc1(__builtin_bit_cast(u32x4, kv0), rkv, lane * 16, (h * 8 + p) * 8192 + wx * 1024);
    st16_sc1(__builtin_bit_cast(u32x4, kv1), rkv, lane * 16, (h * 8 + p) * 8192 + 4096 + wx * 1024);
    if (tok == 0) st16_sc1(__builtin_bit_cast(u32x4, zz), rz, g * 16, (h * 8 + p) * 256 + wx * 64);
    WAIT_V(0);
    GAS unsigned* fkv = flags + (FL_KV + (h * 4 + wx) * 128) / 4;
    if (lane == 0) __hip_atomic_store(fkv + p, TAG, RLX_AGENT);
    bf16x8 eb[4][2];
#pragma unroll
    for (int m = 0; m < 4; ++m) { f32x4 ep[2], en[2]; expo(acc[0][m][0], acc[0][m][1], ep, en); eb[m][0] = pk8(ep[0], ep[1]); eb[m][1] = pk8(en[0], en[1]); }
    wait_flags(fkv, 8, TAG, flags + FL_TMO / 4, lane);
    { u32x4 t0[8], t1[8], tz[8];
#pragma unroll
      for (int i = 0; i < 8; ++i) { t0[i] = ld16_sc1(rkv, lane * 16, (h * 8 + i) * 8192 + wx * 1024); t1[i] = ld16_sc1(rkv, lane * 16, (h * 8 + i) * 8192 + 4096 + wx * 1024); tz[i] = ld16_sc1(rz, g * 16, (h * 8 + i) * 256 + wx * 64); }
      f32x4 s0 = zero4, s1 = zero4, sz = zero4;
#pragma unroll
      for (int i = 0; i < 8; ++i) { s0 += __builtin_bit_cast(f32x4, t0[i]); s1 += __builtin_bit_cast(f32x4, t1[i]); sz += __builtin_bit_cast(f32x4, tz[i]); }
      const unsigned a = lb + L1_KVA + (wx >> 1) * 1024 + lane * 16 + (wx & 1) * 8;
      *(LAS u32x2*)(size_t)a = (u32x2){pk2(s0[0], s0[1]), pk2(s0[2], s0[3])};
      *(LAS u32x2*)(size_t)(a + 2048) = (u32x2){pk2(s1[0], s1[1]), pk2(s1[2], s1[3])};
      u32x2 au = {pk2(sz[0], sz[1]), pk2(sz[2], sz[3])};
      if ((lane & 3) == 1) au = (u32x2){0x3F803F80u, 0x3F803F80u}; else if ((lane & 3) != 0) au = (u32x2){0u, 0u};
      *(LAS u32x2*)(size_t)(a + 4096) = au; }
    lds_sync(lb + L1_CNT, 8u, lane);
    bf16x8 ka[6];
#pragma unroll
    for (int i = 0; i < 6; ++i) ka[i] = lds128(lb + L1_KVA + i * 1024 + lane * 16);
    const __amdgpu_buffer_rsrc_t ro = mk_rsrc(ws + WS_O, 128 * 32 * 1024);
    const unsigned vpair = (unsigned)((((g & 1) << 1) | (g >> 1)) * 256 + tok * 16);
#pragma unroll
    for (int m = 0; m < 4; ++m) {
      const f32x4 ap_ = mfma(ka[4], eb[m][0], zero4), an_ = mfma(ka[5], eb[m][1], zero4);
      const f32x4 p0 = mfma(ka[0], eb[m][0], zero4), n0 = mfma(ka[1], eb[m][1], zero4), p1 = mfma(ka[2], eb[m][0], zero4), n1 = mfma(ka[3], eb[m][1], zero4);
      const float a = rcp(ap_[1]), c = rcp(an_[1]), inv = rcp(ap_[0] * a + an_[0] * c + 1e-6f), ca = a * inv, cc = c * inv;
      const f32x4 o0 = p0 * ca + n0 * cc, o1 = p1 * ca + n1 * cc;
      st16(swap16(pk2(o0[0], o0[1]), pk2(o0[2], o0[3]), pk2(o1[0], o1[1]), pk2(o1[2], o1[3])), ro, vpair, ((p * 16 + wx * 4 + m) * 32 + h) * 1024);
    }
  }
}

constexpr unsigned L2_EX = 131072;

template <int G> __device__ __forceinline__ void phase2(unsigned lb, const u16* x, const u16* w2, const u16* wo, char* ws, u16* out, int lane, int wq) {
  const int b = blockIdx.x, kh = b & 1, rt = ((b & 7) >> 1) * 4 + ((b >> 3) & 3), ct = b >> 5;
  const int tok = lane & 15, g = lane >> 4, q = (lane >> 2) & 3, pp = lane & 3, wi = wq >> 1, jj = wq & 1;
  const u32x4 sact = mk_srd(ws + WS_ACT, 128 * 112 * 1024), so = mk_srd(ws + WS_O, 128 * 32 * 1024), sw2 = mk_srd(w2, F_ * D_ * 2), swo = mk_srd(wo, D_ * D_ * 2);
  const unsigned vaa = lane * 16 + (rt * 8 + wq * 2) * (112 * 1024), vao = lane * 16 + (rt * 8 + wq * 2) * (32 * 1024);
  unsigned vb0, vb1, vs0, vs1;
  { const int r = lane >> 4, c = lane & 15; vb0 = (wq * 16 + r) * 2048 + ct * 256 + (((c >> 1) ^ r) << 5) + ((c & 1) << 4); vb1 = vb0 ^ 128; }
  { const int r = lane >> 3, c = lane & 7; vs0 = (wq * 16 + r) * 2048 + ct * 256 + kh * 128 + (((c >> 1) ^ ((r >> 1) & 1)) << 5) + ((c & 1) << 4); vs1 = vs0 ^ 64; }
  const unsigned sla = lb + (G ^ 1) * 65536 + wq * 4096, slb = lb + (G ^ 1) * 65536 + 16384 + wq * 4096, sls = lb + (G ^ 1) * 65536 + 16384 + wq * 2048;
  const unsigned ra = opq(lb + G * 65536 + wi * 8192 + lane * 16);
  unsigned rb[4], rs[2];
#pragma unroll
  for (int n = 0; n < 4; ++n) { const int f = 4 * (kh ^ (n >> 1)) + 2 * jj + (n & 1); rb[n] = opq(lb + G * 65536 + 16384 + (8 * g + q) * 256 + ((f ^ (q | ((g & 1) << 2))) << 5) + 8 * pp); }
#pragma unroll
  for (int n = 0; n < 2; ++n) { const int f = 2 * jj + n; rs[n] = opq(lb + G * 65536 + 16384 + (8 * g + q) * 128 + ((f ^ ((q >> 1) | ((g & 1) << 1))) << 5) + 8 * pp); }
  f32x4 acc[4][4], as[4][2];
#pragma unroll
  for (int m = 0; m < 4; ++m) {
#pragma unroll
    for (int n = 0; n < 4; ++n) acc[m][n] = (f32x4){0.f, 0.f, 0.f, 0.f};
    as[m][0] = acc[m][0]; as[m][1] = acc[m][0]; }
  bf16x8 fa[2][4], fb[2][4];
  auto dma = [&](auto HD, auto Y, auto I, unsigned ska, unsigned skb) { constexpr int hd = HD() * 32768, i = I(); constexpr bool y = Y();
    if (i < 4) { if (y) DMA(sact, vaa, ska, (i >> 1) * 112 * 1024 + (i & 1) * 1024, sla, hd + i * 1024); else DMA(so, vao, ska, (i >> 1) * 32 * 1024 + (i & 1) * 1024, sla, hd + i * 1024); }
    else if (y) { constexpr int j = i - 4; if (j >> 1) DMA(sw2, vb1, skb, j * 8192, slb, hd + j * 1024); else DMA(sw2, vb0, skb, j * 8192, slb, hd + j * 1024); }
    else if (i < 6) { constexpr int j = i - 4; if (j) DMA(swo, vs1, skb, j * 16384, sls, hd + j * 1024); else DMA(swo, vs0, skb, j * 16384, sls, hd + j * 1024); } };
  auto rdA = [&](auto HR, auto I) { constexpr int i = I(); fa[i >> 2][i & 3] = lds128(ra + HR() * 32768 + (i & 3) * 2048 + (i >> 2) * 1024); SB; };
  auto rdB = [&](auto HR, auto Y, auto I) { constexpr int i = I(), ks = i >> 2, n = i & 3; constexpr bool y = Y();
    if (y) { constexpr int o = HR() * 32768 + ks * 8192; fb[ks][n] = cat(ldstr(rb[n] + o), ldstr(rb[n] + o + 1024)); SB; }
    else if (n < 2) { constexpr int o = HR() * 32768 + ks * 4096; fb[ks][n] = cat(ldstr(rs[n & 1] + o), ldstr(rs[n & 1] + o + 512)); SB; } };
  auto load = [&](auto HR, auto YR, auto MD, auto HD, int U) { constexpr int md = MD(); constexpr bool d = md != 0; IC<md == 1> YD;
    const unsigned kt = md == 1 ? kh * 28 + U : U - 28, ska = kt * 2048, skb = kt * (64 * 2048);
    rdA(HR, IC<0>{}); rdB(HR, YR, IC<0>{}); if (d) dma(HD, YD, IC<0>{}, ska, skb);
    rdA(HR, IC<1>{}); rdB(HR, YR, IC<1>{}); if (d) dma(HD, YD, IC<4>{}, ska, skb);
    rdA(HR, IC<2>{}); rdB(HR, YR, IC<2>{}); if (d) dma(HD, YD, IC<1>{}, ska, skb);
    rdA(HR, IC<3>{}); rdB(HR, YR, IC<3>{}); if (d) dma(HD, YD, IC<5>{}, ska, skb);
    rdA(HR, IC<4>{}); rdB(HR, YR, IC<4>{}); if (d) dma(HD, YD, IC<2>{}, ska, skb);
    rdA(HR, IC<5>{}); rdB(HR, YR, IC<5>{}); if (d) dma(HD, YD, IC<6>{}, ska, skb);
    rdA(HR, IC<6>{}); rdB(HR, YR, IC<6>{}); if (d) dma(HD, YD, IC<3>{}, ska, skb);
    rdA(HR, IC<7>{}); rdB(HR, YR, IC<7>{}); if (d) dma(HD, YD, IC<7>{}, ska, skb); };
  auto mmy = [&]() {
#pragma unroll
    for (int m = 0; m < 4; ++m)
#pragma unroll
      for (int n = 0; n < 4; n += 2) mfma_k2x2(acc[m][n], acc[m][n + 1], fb[0][n], fb[0][n + 1], fa[0][m], fb[1][n], fb[1][n + 1], fa[1][m]);
    SB; BAR; SB; };
  auto mms = [&]() {
#pragma unroll
    for (int m = 0; m < 4; ++m) mfma_k2x2(as[m][0], as[m][1], fb[0][0], fb[0][1], fa[0][m], fb[1][0], fb[1][1], fa[1][m]);
    SB; };
#define ENDL(n) do { WAIT_V(n); WAIT_L0; BAR; SB; } while (0)
  IC<0> c0; IC<1> c1; IC<2> c2;
  auto dma8 = [&](auto HD, int U) { const unsigned kt = kh * 28 + U, ska = kt * 2048, skb = kt * (64 * 2048);
    dma(HD, c1, IC<0>{}, ska, skb); dma(HD, c1, IC<1>{}, ska, skb); dma(HD, c1, IC<2>{}, ska, skb); dma(HD, c1, IC<3>{}, ska, skb);
    dma(HD, c1, IC<4>{}, ska, skb); dma(HD, c1, IC<5>{}, ska, skb); dma(HD, c1, IC<6>{}, ska, skb); dma(HD, c1, IC<7>{}, ska, skb); };
  GAS unsigned* flags = (GAS unsigned*)(ws + WS_FLAG);
  const __amdgpu_buffer_rsrc_t ryx = mk_rsrc(ws + WS_YX, 256 * 65536);
  auto send = [&]() {
#pragma unroll
    for (int m = 0; m < 4; ++m)
#pragma unroll
      for (int n = 0; n < 2; ++n) st16_sc1(__builtin_bit_cast(u32x4, acc[m][2 + n]), ryx, lane * 16, b * 65536 + (G * 4 + wq) * 8192 + (m * 2 + n) * 1024);
#pragma unroll
    for (int ml = 0; ml < 2; ++ml)
#pragma unroll
      for (int n = 0; n < 2; ++n) *(LAS f32x4*)(size_t)(lb + L2_EX + (G * 4 + wq) * 4096 + (ml * 2 + n) * 1024 + lane * 16) = acc[2 * (G ^ 1) + ml][n];
    SB; };
  dma8(c0, G ^ 1); dma8(c1, 2 + (G ^ 1));
  if (G == 1) WAIT_V(8);
  BAR; SB;
  if (G == 0) { load(c0, c1, c0, c0, 0); ENDL(8); } else { BAR; SB; load(c0, c1, c1, c0, 4); ENDL(8); }
  IC<G> hA; IC<G ^ 1> hB;
  for (int T = G; T < 20; T += 4) {
    mmy(); load(c1, c1, c1, hA, T + 5); ENDL(8);
    mmy(); load(c0, c1, c1, hB, T + 7); ENDL(8);
  }
  mmy(); load(c1, c1, c1, hA, 25 + G); ENDL(8);
  mmy(); if (G == 0) { load(c0, c1, c1, hB, 27); ENDL(8); } else { load(c0, c1, c2, hB, 28); ENDL(6); }
  mmy(); load(c1, c1, c2, hA, 29 + G); ENDL(6);
  mmy(); load(c0, c0, c2, hB, 31 + G); send(); ENDL(14);
  mms(); BAR; SB; load(c1, c0, c2, hA, 33 + G); ENDL(14);
  mms(); BAR; SB; load(c0, c0, c2, hB, 35 + G); WAIT_V(6);
  if (lane == 0) __hip_atomic_store(flags + (FL_YX + b * 128) / 4 + G * 4 + wq, TAG, RLX_AGENT);
  WAIT_L0; BAR; SB;
  mms(); BAR; SB; load(c1, c0, c2, hA, 37 + G); ENDL(6);
  mms(); BAR; SB; load(c0, c0, c2, hB, 39 + G); ENDL(6);
  mms(); BAR; SB; load(c1, c0, c2, hA, 41 + G); ENDL(6);
  mms(); BAR; SB; if (G == 0) { load(c0, c0, c2, hB, 43); ENDL(6); } else { load(c0, c0, c0, hB, 0); ENDL(0); }
  mms(); BAR; SB; load(c1, c0, c0, hA, 0); WAIT_V(0);
  wait_flags(flags + (FL_YX + (b ^ 1) * 128) / 4, 8, TAG, flags + FL_TMO / 4, lane);
  u32x4 py[2][2][2], xr[2];
  const unsigned row0 = rt * 128 + wi * 64 + 2 * G * 16 + tok, vout = row0 * 2048 + ct * 256 + kh * 128 + jj * 64 + (((g & 1) << 1) | (g >> 1)) * 16;
  const __amdgpu_buffer_rsrc_t rx = mk_rsrc(x, S_ * D_ * 2), rout = mk_rsrc(out, S_ * D_ * 2);
#pragma unroll
  for (int ml = 0; ml < 2; ++ml) {
#pragma unroll
    for (int n = 0; n < 2; ++n)
#pragma unroll
      for (int gg = 0; gg < 2; ++gg) py[gg][ml][n] = ld16_sc1(ryx, lane * 16, (b ^ 1) * 65536 + (gg * 4 + wq) * 8192 + ((2 * G + ml) * 2 + n) * 1024);
    xr[ml] = __builtin_amdgcn_raw_buffer_load_b128(rx, vout, ml * 16 * 2048, 0); }
  WAIT_L0; BAR; SB;
  mms(); MFMA_PAD;
  if (G == 0) { BAR; SB; }
#pragma unroll
  for (int ml = 0; ml < 2; ++ml)
#pragma unroll
    for (int n = 0; n < 2; ++n) *(LAS f32x4*)(size_t)(lb + (G * 4 + wq) * 4096 + (ml * 2 + n) * 1024 + lane * 16) = as[2 * (G ^ 1) + ml][n];
  WAIT_L0; BAR; SB;
#pragma unroll
  for (int ml = 0; ml < 2; ++ml) {
    const u32x4 xs = swap16(xr[ml][0], xr[ml][1], xr[ml][2], xr[ml][3]); unsigned ow[4];
#pragma unroll
    for (int n = 0; n < 2; ++n) { const unsigned ex = ((G ^ 1) * 4 + wq) * 4096 + (ml * 2 + n) * 1024 + lane * 16;
      const f32x4 y = (acc[2 * G + ml][n] + *(const LAS f32x4*)(size_t)(lb + L2_EX + ex)) + (__builtin_bit_cast(f32x4, py[0][ml][n]) + __builtin_bit_cast(f32x4, py[1][ml][n]));
      const f32x4 s = as[2 * G + ml][n] + *(const LAS f32x4*)(size_t)(lb + ex);
      const f32x4 xv = {bflo(xs[2 * n]), bfhi(xs[2 * n]), bflo(xs[2 * n + 1]), bfhi(xs[2 * n + 1])}; f32x4 o;
#pragma unroll
      for (int r = 0; r < 4; ++r) o[r] = xv[r] + s[r] * (y[r] * rcp(1.0f + ex2(-LOG2E * y[r])));
      ow[2 * n] = pk2(o[0], o[1]); ow[2 * n + 1] = pk2(o[2], o[3]); }
    st16(swap16(ow[0], ow[1], ow[2], ow[3]), rout, vout, ml * 16 * 2048); }
}

__global__ __launch_bounds__(512, 2) void k_phase2(const u16* x, const u16* w2, const u16* wo, char* ws, u16* out) {
  __shared__ __attribute__((aligned(16))) char smem[163840];
  const unsigned lb = (unsigned)(size_t)(LAS char*)smem;
  const int tid = threadIdx.x, lane = tid & 63, w = __builtin_amdgcn_readfirstlane(tid >> 6);
  if (w < 4) phase2<0>(lb, x, w2, wo, ws, out, lane, w); else phase2<1>(lb, x, w2, wo, ws, out, lane, w - 4);
}
}

extern "C" void kernel_launch(void* const* d_in, const int* in_sizes, int n_in, void* d_out, int out_size, void* d_ws, size_t ws_size, hipStream_t stream) {
  const u16* x = (const u16*)d_in[0]; const u16* wqkv = (const u16*)d_in[1]; const u16* wphi = (const u16*)d_in[2]; const u16* wo = (const u16*)d_in[3];
  const u16* w1 = (const u16*)d_in[5]; const u16* b1 = (const u16*)d_in[6]; const u16* w2 = (const u16*)d_in[7];
  char* ws = (char*)d_ws;
  k_ln<<<256, 512, 0, stream>>>(x, (u16*)(ws + WS_XN), (unsigned*)(ws + WS_FLAG));
  k_phase1<<<256, 512, 0, stream>>>((const u16*)(ws + WS_XN), wqkv, wphi, w1, b1, ws);
  k_phase2<<<256, 512, 0, stream>>>(x, w2, wo, ws, (u16*)d_out);
}
```

```cpp
#include <hip/hip_runtime.h>
#include <hip/hip_bf16.h>
#include <type_traits>

namespace {
#define LAS __attribute__((address_space(3)))
#define GAS __attribute__((address_space(1)))
typedef unsigned short u16;
typedef unsigned u32x4 __attribute__((ext_vector_type(4)));
typedef unsigned u32x2 __attribute__((ext_vector_type(2)));
typedef short bf16x8 __attribute__((ext_vector_type(8)));
typedef short s16x4 __attribute__((ext_vector_type(4)));
typedef float f32x4 __attribute__((ext_vector_type(4)));
typedef float f32x2 __attribute__((ext_vector_type(2)));
typedef __bf16 bf16x2_t __attribute__((ext_vector_type(2)));

constexpr int S_ = 2048, D_ = 1024, F_ = 3584;
constexpr size_t WS_FLAG = 0;
constexpr size_t WS_XN = 1u << 20;
constexpr size_t WS_ACT = WS_XN + (4u << 20);
constexpr size_t WS_O = WS_ACT + (15u << 20);
constexpr size_t WS_KVP = WS_O + (4u << 20);
constexpr size_t WS_ZP = WS_KVP + (2u << 20);
constexpr size_t WS_YX = WS_ZP + (1u << 20);
constexpr size_t WS_STAMP = 64u << 20;
#define STAMP_DECL
#define STAMP(i)
#define STAMP_OUT(k, wv)
constexpr unsigned FL_KV = 0;
constexpr unsigned FL_YX = 16384;
constexpr unsigned FL_TMO = 49152;
constexpr unsigned TAG = 1u;
constexpr float LOG2E = 1.4426950408889634f;

#define SB __builtin_amdgcn_sched_barrier(0)
#define BAR __builtin_amdgcn_s_barrier()
#define WAIT_L0 do { __builtin_amdgcn_s_waitcnt(0xC07F); SB; } while (0)
#define WAIT_V(n) asm volatile("s_waitcnt vmcnt(%0)" :: "i"(n) : "memory")
template <int N> using IC = std::integral_constant<int, N>;

__device__ __forceinline__ u32x4 mk_srd(const void* p, unsigned bytes) {
  const unsigned long long a = (unsigned long long)p; u32x4 r;
  r.x = __builtin_amdgcn_readfirstlane((unsigned)a); r.y = __builtin_amdgcn_readfirstlane((unsigned)(a >> 32)) & 0xffffu; r.z = bytes; r.w = 0x00020000u; return r; }
#define DMA(srd, voff, sk, KOFF, sl, LOFF) do { unsigned _t; asm volatile("s_add_u32 m0, %4, %5\n\ts_add_u32 %0, %3, %6\n\tbuffer_load_dwordx4 %1, %2, %0 offen lds" \
    : "=&s"(_t) : "v"(voff), "s"(srd), "s"(sk), "s"(sl), "i"(LOFF), "i"(KOFF) : "memory", "scc"); SB; } while (0)
__device__ __forceinline__ bf16x8 lds128(unsigned a) { return *(const LAS bf16x8*)(size_t)a; }
__device__ __forceinline__ s16x4 ldstr(unsigned a) { return __builtin_amdgcn_ds_read_tr16_b64_v4i16((LAS s16x4*)(size_t)a); }
__device__ __forceinline__ bf16x8 cat(s16x4 lo, s16x4 hi) { return __builtin_shufflevector(lo, hi, 0, 1, 2, 3, 4, 5, 6, 7); }
__device__ __forceinline__ unsigned opq(unsigned x) { asm volatile("" : "+v"(x)); return x; }
__device__ __forceinline__ f32x4 mfma(bf16x8 a, bf16x8 b, f32x4 c) { return __builtin_amdgcn_mfma_f32_16x16x32_bf16(a, b, c, 0, 0, 0); }
__device__ __forceinline__ void mfma_acc(f32x4& d, bf16x8 a, bf16x8 b) { asm volatile("v_mfma_f32_16x16x32_bf16 %0, %1, %2, %0" : "+v"(d) : "v"(a), "v"(b)); }
__device__ __forceinline__ void mfma_k2(f32x4& d, bf16x8 a0, bf16x8 b0, bf16x8 a1, bf16x8 b1) {
  asm volatile("v_mfma_f32_16x16x32_bf16 %0, %1, %2, %0\n\tv_mfma_f32_16x16x32_bf16 %0, %3, %4, %0" : "+v"(d) : "v"(a0), "v"(b0), "v"(a1), "v"(b1)); }
__device__ __forceinline__ void mfma_k2x2(f32x4& d, f32x4& e, bf16x8 a0, bf16x8 c0, bf16x8 b0, bf16x8 a1, bf16x8 c1, bf16x8 b1) {
  asm volatile("v_mfma_f32_16x16x32_bf16 %0, %2, %4, %0\n\tv_mfma_f32_16x16x32_bf16 %1, %3, %4, %1\n\tv_mfma_f32_16x16x32_bf16 %0, %5, %7, %0\n\tv_mfma_f32_16x16x32_bf16 %1, %6, %7, %1"
               : "+v"(d), "+v"(e) : "v"(a0), "v"(c0), "v"(b0), "v"(a1), "v"(c1), "v"(b1)); }
#define MFMA_PAD do { asm volatile("s_nop 7\n\ts_nop 1" ::: "memory"); SB; } while (0)
__device__ __forceinline__ unsigned pk2(float lo, float hi) { const f32x2 v = {lo, hi}; return __builtin_bit_cast(unsigned, __builtin_convertvector(v, bf16x2_t)); }
__device__ __forceinline__ bf16x8 pk8(f32x4 a, f32x4 b) { const u32x4 u = {pk2(a[0], a[1]), pk2(a[2], a[3]), pk2(b[0], b[1]), pk2(b[2], b[3])}; return __builtin_bit_cast(bf16x8, u); }
__device__ __forceinline__ float bflo(unsigned u) { return __uint_as_float(u << 16); }
__device__ __forceinline__ float bfhi(unsigned u) { return __uint_as_float(u & 0xffff0000u); }
__device__ __forceinline__ float ex2(float x) { return __builtin_amdgcn_exp2f(x); }
__device__ __forceinline__ float rcp(float x) { return __builtin_amdgcn_rcpf(x); }
__device__ __forceinline__ u32x4 swap16(unsigned a0, unsigned a1, unsigned b0, unsigned b1) {
  const auto p = __builtin_amdgcn_permlane16_swap(a0, b0, false, false), q = __builtin_amdgcn_permlane16_swap(a1, b1, false, false);
  return (u32x4){p[0], q[0], p[1], q[1]}; }
template <class Op> __device__ __forceinline__ float red4(float v, Op op) {
  const unsigned u = __float_as_uint(v); const auto p = __builtin_amdgcn_permlane32_swap(u, u, false, false);
  const float a = op(__uint_as_float(p[0]), __uint_as_float(p[1])); const unsigned ua = __float_as_uint(a);
  const auto q = __builtin_amdgcn_permlane16_swap(ua, ua, false, false); return op(__uint_as_float(q[0]), __uint_as_float(q[1])); }
__device__ __forceinline__ void st16_sc1(u32x4 v, __amdgpu_buffer_rsrc_t r, unsigned voff, unsigned soff) { __builtin_amdgcn_raw_buffer_store_b128(v, r, voff + soff, 0, 16); }
__device__ __forceinline__ void st16(u32x4 v, __amdgpu_buffer_rsrc_t r, unsigned voff, unsigned soff) { __builtin_amdgcn_raw_buffer_store_b128(v, r, voff + soff, 0, 0); }
__device__ __forceinline__ u32x4 ld16_sc1(__amdgpu_buffer_rsrc_t r, unsigned voff, unsigned soff) { return __builtin_amdgcn_raw_buffer_load_b128(r, voff, soff, 16); }
__device__ __forceinline__ __amdgpu_buffer_rsrc_t mk_rsrc(const void* p, unsigned bytes) { return __builtin_amdgcn_make_buffer_rsrc((void*)p, 0, bytes, 0x00020000); }
#define RLX_AGENT __ATOMIC_RELAXED, __HIP_MEMORY_SCOPE_AGENT
__device__ __forceinline__ void wait_flags(GAS unsigned* w, int n, unsigned tag, GAS unsigned* tmo, int lane) {
  for (unsigned spins = 0;; ++spins) {
    unsigned v = tag; if (lane < n) v = __hip_atomic_load(w + lane, RLX_AGENT);
    if (__all(v == tag)) break;
    if (spins > (1u << 16)) { if (lane == 0) __hip_atomic_store(tmo, 1u, RLX_AGENT); break; }
    __builtin_amdgcn_s_sleep(1);
  }
  __builtin_amdgcn_fence(__ATOMIC_ACQUIRE, "wavefront");
}
__device__ __forceinline__ void lds_sync(unsigned addr, unsigned target, int lane) {
  asm volatile("s_waitcnt lgkmcnt(0)" ::: "memory");
  if (lane == 0) { const unsigned one = 1; asm volatile("ds_add_u32 %0, %1" :: "v"(addr), "v"(one) : "memory"); }
  for (;;) { unsigned v; asm volatile("ds_read_b32 %0, %1\n\ts_waitcnt lgkmcnt(0)" : "=v"(v) : "v"(addr) : "memory");
    if ((unsigned)__builtin_amdgcn_readfirstlane(v) >= target) break; }
}

__global__ __launch_bounds__(512) void k_ln(const u16* __restrict__ x, u16* __restrict__ xn, unsigned* __restrict__ flags) {
  const int tid = threadIdx.x, lane = tid & 63, row = blockIdx.x * 8 + (tid >> 6);
  if (tid < 64) flags[blockIdx.x * 64 + tid] = 0u;
  const u32x4* src = (const u32x4*)(x + (size_t)row * D_);
  const u32x4 a = src[lane], b = src[lane + 64];
  float v[16];
#pragma unroll
  for (int i = 0; i < 4; ++i) { v[2 * i] = bflo(a[i]); v[2 * i + 1] = bfhi(a[i]); v[8 + 2 * i] = bflo(b[i]); v[9 + 2 * i] = bfhi(b[i]); }
  float s = 0.f;
#pragma unroll
  for (int i = 0; i < 16; ++i) s += v[i];
#pragma unroll
  for (int o = 32; o >= 1; o >>= 1) s += __shfl_xor(s, o);
  const float mean = s * (1.0f / D_); float q = 0.f;
#pragma unroll
  for (int i = 0; i < 16; ++i) { v[i] -= mean; q += v[i] * v[i]; }
#pragma unroll
  for (int o = 32; o >= 1; o >>= 1) q += __shfl_xor(q, o);
  const float rstd = 1.0f / sqrtf(q * (1.0f / D_) + 1e-5f);
  u32x4 oa, ob;
#pragma unroll
  for (int i = 0; i < 4; ++i) { oa[i] = pk2(v[2 * i] * rstd, v[2 * i + 1] * rstd); ob[i] = pk2(v[8 + 2 * i] * rstd, v[9 + 2 * i] * rstd); }
  u32x4* dst = (u32x4*)(xn + (size_t)row * D_);
  dst[lane] = oa; dst[lane + 64] = ob;
}

constexpr unsigned L1_A = 0;
constexpr unsigned L1_BQ = 65536;
constexpr unsigned L1_BH = 90112;
constexpr unsigned L1_WPHI = 118784;
constexpr unsigned L1_CNT = 120832;
constexpr unsigned L1_KVA = 121856;
constexpr unsigned L1_V = 98304;
constexpr unsigned L1_PHI = 131072;

struct P1 { const u16 *xn, *wqkv, *wphi, *w1, *b1; char* ws; };

__global__ __launch_bounds__(512, 2) void k_phase1(const u16* xn, const u16* wqkv, const u16* wphi, const u16* w1, const u16* b1, char* ws) {
  __shared__ __attribute__((aligned(16))) char smem[163840];
  const unsigned lb = (unsigned)(size_t)(LAS char*)smem;
  const int tid = threadIdx.x, lane = tid & 63, w = __builtin_amdgcn_readfirstlane(tid >> 6), wx = w & 3;
  const int b = blockIdx.x, h = (b & 7) * 4 + ((b >> 3) & 3), p = b >> 5, odd = h & 1;
  const int tok = lane & 15, g = lane >> 4, q = (lane >> 2) & 3, pp = lane & 3;
  const unsigned ra0 = lb + L1_A + (wx * 64 + tok) * 128 + ((g ^ (tok >> 1)) << 4), ra1 = ra0 ^ 64;
  if (tid == 0) *(LAS unsigned*)(size_t)(lb + L1_CNT) = 0u;
  GAS unsigned* flags = (GAS unsigned*)(ws + WS_FLAG);
  STAMP_DECL; STAMP(0);

  if (w < 4) {
    const u32x4 sa = mk_srd(xn, S_ * D_ * 2);
    unsigned va0, va1;
    { const int r = lane >> 3; va0 = (p * 256 + wx * 64 + r) * 2048 + (((lane & 7) ^ (r >> 1)) << 4); va1 = va0 ^ 64; }
    const unsigned sl = lb + L1_A + wx * 8192;
    const unsigned rb0 = opq(lb + L1_BH + (8 * g + 4 * (g & 1) + q) * 224 + 8 * pp), rb1 = opq(lb + L1_BH + (8 * g + 4 * ((g & 1) ^ 1) + q) * 224 + 8 * pp);
    f32x4 acc[4][7];
#pragma unroll
    for (int m = 0; m < 4; ++m)
#pragma unroll
      for (int j = 0; j < 7; ++j) acc[m][j] = (f32x4){0.f, 0.f, 0.f, 0.f};
    bf16x8 fa[2][4], fb[2][7];
    auto dmaA = [&](auto ST, auto J, unsigned sk) { constexpr int st = ST(), j = J(); if (j & 1) DMA(sa, va1, sk, j * 16384, sl, st * 32768 + j * 1024); else DMA(sa, va0, sk, j * 16384, sl, st * 32768 + j * 1024); };
    auto rdA = [&](auto ST, auto KS, auto M) { constexpr int st = ST(), ks = KS(), m = M(); fa[ks][m] = lds128((ks ? ra1 : ra0) + st * 32768 + m * 2048); SB; };
    auto rdB = [&](auto ST, auto KS, auto J) { constexpr int st = ST(), ks = KS(), j = J(); constexpr int o = st * 14336 + ks * 7168 + j * 32; fb[ks][j] = cat(ldstr(rb0 + o), ldstr(rb1 + o)); SB; };
    auto load = [&](auto ST, auto DO, unsigned sk) {
      constexpr int o = ST() ^ 1; constexpr bool d = DO();
      rdA(ST, IC<0>{}, IC<0>{}); rdB(ST, IC<0>{}, IC<0>{}); rdB(ST, IC<0>{}, IC<1>{}); if (d) dmaA(IC<o>{}, IC<0>{}, sk);
      rdA(ST, IC<0>{}, IC<1>{}); rdB(ST, IC<0>{}, IC<2>{}); rdB(ST, IC<0>{}, IC<3>{}); if (d) dmaA(IC<o>{}, IC<1>{}, sk);
      rdA(ST, IC<0>{}, IC<2>{}); rdB(ST, IC<0>{}, IC<4>{}); rdB(ST, IC<0>{}, IC<5>{}); if (d) dmaA(IC<o>{}, IC<2>{}, sk);
      rdA(ST, IC<0>{}, IC<3>{}); rdB(ST, IC<0>{}, IC<6>{}); rdB(ST, IC<1>{}, IC<0>{}); if (d) dmaA(IC<o>{}, IC<3>{}, sk);
      rdA(ST, IC<1>{}, IC<0>{}); rdB(ST, IC<1>{}, IC<1>{}); rdB(ST, IC<1>{}, IC<2>{}); if (d) dmaA(IC<o>{}, IC<4>{}, sk);
      rdA(ST, IC<1>{}, IC<1>{}); rdB(ST, IC<1>{}, IC<3>{}); rdB(ST, IC<1>{}, IC<4>{}); if (d) dmaA(IC<o>{}, IC<5>{}, sk);
      rdA(ST, IC<1>{}, IC<2>{}); rdB(ST, IC<1>{}, IC<5>{}); if (d) dmaA(IC<o>{}, IC<6>{}, sk);
      rdA(ST, IC<1>{}, IC<3>{}); rdB(ST, IC<1>{}, IC<6>{}); if (d) dmaA(IC<o>{}, IC<7>{}, sk);
    };
    auto mm = [&]() {
#pragma unroll
      for (int m = 0; m < 4; ++m) {
#pragma unroll
        for (int j = 0; j < 6; j += 2) mfma_k2x2(acc[m][j], acc[m][j + 1], fb[0][j], fb[0][j + 1], fa[0][m], fb[1][j], fb[1][j + 1], fa[1][m]);
        mfma_k2(acc[m][6], fb[0][6], fa[0][m], fb[1][6], fa[1][m]); }
      SB; };
#define A8(ST, sk) dmaA(IC<ST>{}, IC<0>{}, sk); dmaA(IC<ST>{}, IC<1>{}, sk); dmaA(IC<ST>{}, IC<2>{}, sk); dmaA(IC<ST>{}, IC<3>{}, sk); dmaA(IC<ST>{}, IC<4>{}, sk); dmaA(IC<ST>{}, IC<5>{}, sk); dmaA(IC<ST>{}, IC<6>{}, sk); dmaA(IC<ST>{}, IC<7>{}, sk)
    A8(0, 0u); A8(1, 128u);
    WAIT_V(8); WAIT_L0; BAR; SB; STAMP(1);
    load(IC<0>{}, IC<0>{}, 0u); WAIT_L0; BAR; SB; STAMP(2);
    for (int t = 0; t < 14; t += 2) {
      mm(); WAIT_V(0); BAR; SB;
      load(IC<1>{}, IC<1>{}, (unsigned)(t + 2) * 128u); WAIT_L0; BAR; SB;
      mm(); WAIT_V(0); BAR; SB;
      load(IC<0>{}, IC<1>{}, (unsigned)(t + 3) * 128u); WAIT_L0; BAR; SB;
    }
    STAMP(3);
    mm(); WAIT_V(0); BAR; SB;
    load(IC<1>{}, IC<0>{}, 0u);
    u32x2 bias[7];
#pragma unroll
    for (int j = 0; j < 7; ++j) { const int cb = 7 * h + (j < 6 ? j + odd : (odd ? 0 : 6)); bias[j] = *(const GAS u32x2*)((const GAS u16*)b1 + cb * 16 + 4 * g); }
    WAIT_L0; BAR; SB;
    mm(); BAR; MFMA_PAD; STAMP(4);
    const __amdgpu_buffer_rsrc_t ract = mk_rsrc(ws + WS_ACT, 128 * 112 * 1024);
    auto mish4 = [&](f32x4 a, u32x2 bb) -> u32x2 {
      const f32x4 bv = {bflo(bb[0]), bfhi(bb[0]), bflo(bb[1]), bfhi(bb[1])}; const f32x4 hv = a + bv, hs = hv * LOG2E; f32x4 e, r;
#pragma unroll
      for (int i = 0; i < 4; ++i) e[i] = ex2(hs[i]);
      const f32x4 d = e * (e + 2.0f) + 2.0f;
#pragma unroll
      for (int i = 0; i < 4; ++i) r[i] = rcp(d[i]);
      const f32x4 o = hv * (1.0f - 2.0f * r); return (u32x2){pk2(o[0], o[1]), pk2(o[2], o[3])}; };
    const unsigned vpair = (unsigned)((((g & 1) << 1) | (g >> 1)) * 256 + tok * 16), mb0 = p * 16 + wx * 4;
#pragma unroll
    for (int m = 0; m < 4; ++m)
#pragma unroll
      for (int j = 0; j < 6; j += 2) { const u32x2 a = mish4(acc[m][j], bias[j]), c = mish4(acc[m][j + 1], bias[j + 1]);
        st16(swap16(a[0], a[1], c[0], c[1]), ract, vpair, ((mb0 + m) * 112 + ((7 * h + odd + j) >> 1)) * 1024); }
    const unsigned vsing = (unsigned)((g & 1) * 112 * 1024 + (2 * odd + (g >> 1)) * 256 + tok * 16);
#pragma unroll
    for (int m = 0; m < 4; m += 2) { const u32x2 a = mish4(acc[m][6], bias[6]), c = mish4(acc[m + 1][6], bias[6]);
      st16(swap16(a[0], a[1], c[0], c[1]), ract, vsing, ((mb0 + m) * 112 + ((7 * h + (odd ? 0 : 6)) >> 1)) * 1024); }
    STAMP(5); WAIT_V(0); STAMP(6); STAMP_OUT(0, w);
  } else {
    const u32x4 sq = mk_srd(wqkv, D_ * 3 * D_ * 2), sh = mk_srd(w1, D_ * F_ * 2), sp = mk_srd(wphi, 32 * 32 * 32 * 2);
    unsigned vq, vh[4];
    { const int r = lane >> 2; vq = (wx * 16 + r) * 6144 + h * 64 + (((lane & 3) ^ (((r >> 3) & 1) << 1)) << 4); }
#pragma unroll
    for (int i = 0; i < 4; ++i) { const int o = (wx + 4 * i) * 1024 + lane * 16, rp = o / 224, c = (o - rp * 224) >> 4, k = rp ^ (((rp >> 3) & 1) << 2); int cs = c + 2 * odd; if (cs >= 14) cs -= 14;
      vh[i] = k * 7168 + h * 224 + cs * 16; }
    const unsigned slq = lb + L1_BQ + wx * 1024, slh = lb + L1_BH + wx * 1024;
    const unsigned rq0 = opq(lb + L1_BQ + (8 * g + q) * 64 + 32 * (g & 1) + 8 * pp), rq1 = opq(rq0 ^ 32);
    f32x4 acc[3][4][2];
#pragma unroll
    for (int u = 0; u < 3; ++u)
#pragma unroll
      for (int m = 0; m < 4; ++m) { acc[u][m][0] = (f32x4){0.f, 0.f, 0.f, 0.f}; acc[u][m][1] = acc[u][m][0]; }
    bf16x8 fa[2][4], fb[2][6];
    auto dmaQ = [&](auto ST, auto U, unsigned sk) { constexpr int st = ST(), u = U(); DMA(sq, vq, sk, u * 2048, slq, st * 12288 + u * 4096); };
    auto dmaH = [&](auto ST, auto I, unsigned sk) { constexpr int st = ST(), i = I(); if (i < 3 || wx < 2) DMA(sh, vh[i], sk, 0, slh, st * 14336 + i * 4096); };
    auto rdA = [&](auto ST, auto KS, auto M) { constexpr int st = ST(), ks = KS(), m = M(); fa[ks][m] = lds128((ks ? ra1 : ra0) + st * 32768 + m * 2048); SB; };
    auto rdB = [&](auto ST, auto KS, auto J) { constexpr int st = ST(), ks = KS(), j = J(); constexpr int o = st * 12288 + (j >> 1) * 4096 + ks * 2048; const unsigned a = (j & 1) ? rq1 : rq0;
      fb[ks][j] = cat(ldstr(a + o), ldstr(a + o + 256)); SB; };
    auto load = [&](auto ST, auto DQ, auto DH, int t) {
      constexpr int o = ST() ^ 1; constexpr bool dq = DQ(), dh = DH(); const unsigned skq = (unsigned)(t + 1) * (64 * 6144), skh = (unsigned)(t + 2) * (64 * 7168);
      rdA(ST, IC<0>{}, IC<0>{}); rdB(ST, IC<0>{}, IC<0>{}); rdB(ST, IC<0>{}, IC<1>{}); if (dq) dmaQ(IC<o>{}, IC<0>{}, skq);
      rdA(ST, IC<0>{}, IC<1>{}); rdB(ST, IC<0>{}, IC<2>{}); rdB(ST, IC<0>{}, IC<3>{}); if (dq) dmaQ(IC<o>{}, IC<1>{}, skq);
      rdA(ST, IC<0>{}, IC<2>{}); rdB(ST, IC<0>{}, IC<4>{}); rdB(ST, IC<0>{}, IC<5>{}); if (dq) dmaQ(IC<o>{}, IC<2>{}, skq);
      rdA(ST, IC<0>{}, IC<3>{}); rdB(ST, IC<1>{}, IC<0>{}); if (dh) dmaH(ST, IC<0>{}, skh);
      rdA(ST, IC<1>{}, IC<0>{}); rdB(ST, IC<1>{}, IC<1>{}); rdB(ST, IC<1>{}, IC<2>{}); if (dh) dmaH(ST, IC<1>{}, skh);
      rdA(ST, IC<1>{}, IC<1>{}); rdB(ST, IC<1>{}, IC<3>{}); if (dh) dmaH(ST, IC<2>{}, skh);
      rdA(ST, IC<1>{}, IC<2>{}); rdB(ST, IC<1>{}, IC<4>{});
      rdA(ST, IC<1>{}, IC<3>{}); rdB(ST, IC<1>{}, IC<5>{});
      if (dh) { if (wx < 2) { dmaH(ST, IC<3>{}, skh); WAIT_V(7); } else WAIT_V(6); }
    };
    auto mm = [&]() {
#pragma unroll
      for (int m = 0; m < 4; ++m)
#pragma unroll
        for (int u = 0; u < 3; ++u) mfma_k2x2(acc[u][m][0], acc[u][m][1], fb[0][2 * u], fb[0][2 * u + 1], fa[0][m], fb[1][2 * u], fb[1][2 * u + 1], fa[1][m]);
      SB; };
    if (wx < 2) { const unsigned vp = h * 2048 + wx * 1024 + lane * 16, slp = lb + L1_WPHI + wx * 1024; DMA(sp, vp, 0u, 0, slp, 0); }
#define H4(ST, sk) dmaH(IC<ST>{}, IC<0>{}, sk); dmaH(IC<ST>{}, IC<1>{}, sk); dmaH(IC<ST>{}, IC<2>{}, sk); dmaH(IC<ST>{}, IC<3>{}, sk)
    H4(0, 0u); dmaQ(IC<0>{}, IC<0>{}, 0u); dmaQ(IC<0>{}, IC<1>{}, 0u); dmaQ(IC<0>{}, IC<2>{}, 0u); H4(1, 64u * 7168u);
    WAIT_V(3); WAIT_L0; BAR; SB; STAMP(1);
    BAR; SB; STAMP(2);
    for (int t = 0; t < 14; t += 2) {
      load(IC<0>{}, IC<1>{}, IC<1>{}, t); WAIT_L0; BAR; SB;
      mm(); WAIT_V(3); BAR; SB;
      load(IC<1>{}, IC<1>{}, IC<1>{}, t + 1); WAIT_L0; BAR; SB;
      mm(); WAIT_V(3); BAR; SB;
    }
    STAMP(3);
    load(IC<0>{}, IC<1>{}, IC<0>{}, 14); WAIT_V(3); WAIT_L0; BAR; SB;
    mm(); WAIT_V(0); BAR; SB;
    load(IC<1>{}, IC<0>{}, IC<0>{}, 15); WAIT_L0; BAR; SB;
    mm(); MFMA_PAD; STAMP(4);
    bf16x8 wp[2];
#pragma unroll
    for (int ff = 0; ff < 2; ++ff) { const unsigned a = lb + L1_WPHI + (4 * g + q) * 64 + ff * 32 + 8 * pp; wp[ff] = cat(ldstr(a), ldstr(a + 1024)); }
    const f32x4 zero4 = {0.f, 0.f, 0.f, 0.f};
    auto fmax_ = [](float a, float c) { return fmaxf(a, c); }; auto fmin_ = [](float a, float c) { return fminf(a, c); }; auto fadd_ = [](float a, float c) { return a + c; };
    auto expo = [&](const f32x4& a0, const f32x4& a1, f32x4 (&ep)[2], f32x4 (&en)[2]) {
      const bf16x8 kb = pk8(a0, a1); const f32x4 p0 = mfma(wp[0], kb, zero4), p1 = mfma(wp[1], kb, zero4);
      float mx = fmaxf(fmaxf(fmaxf(p0[0], p0[1]), fmaxf(p0[2], p0[3])), fmaxf(fmaxf(p1[0], p1[1]), fmaxf(p1[2], p1[3])));
      float mn = fminf(fminf(fminf(p0[0], p0[1]), fminf(p0[2], p0[3])), fminf(fminf(p1[0], p1[1]), fminf(p1[2], p1[3])));
      mx = red4(mx, fmax_) * LOG2E; mn = red4(mn, fmin_) * LOG2E;
      const f32x4 u0 = p0 * LOG2E - mx, u1 = p1 * LOG2E - mx, v0 = mn - p0 * LOG2E, v1 = mn - p1 * LOG2E;
#pragma unroll
      for (int r = 0; r < 4; ++r) { ep[0][r] = ex2(u0[r]); ep[1][r] = ex2(u1[r]); en[0][r] = ex2(v0[r]); en[1][r] = ex2(v1[r]); } };
    const unsigned swp = (tok & 1) | (((tok >> 2) & 1) << 1) | (((tok >> 1) & 1) << 2) | (tok & 8);
#pragma unroll
    for (int m = 0; m < 4; ++m) {
      f32x4 ep[2], en[2]; expo(acc[1][m][0], acc[1][m][1], ep, en);
      const f32x4 sp4 = ep[0] + ep[1], sn4 = en[0] + en[1];
      const float rp_ = rcp(red4((sp4[0] + sp4[1]) + (sp4[2] + sp4[3]), fadd_)), rn_ = rcp(red4((sn4[0] + sn4[1]) + (sn4[2] + sn4[3]), fadd_));
      const unsigned row = lb + L1_PHI + (wx * 64 + m * 16 + tok) * 128;
#pragma unroll
      for (int ff = 0; ff < 2; ++ff) { const f32x4 a = ep[ff] * rp_, c = en[ff] * rn_;
        *(LAS u32x2*)(size_t)(row + (((4 * ff + g) ^ swp) << 3)) = (u32x2){pk2(a[0], a[1]), pk2(a[2], a[3])};
        *(LAS u32x2*)(size_t)(row + (((8 + 4 * ff + g) ^ swp) << 3)) = (u32x2){pk2(c[0], c[1]), pk2(c[2], c[3])}; }
      const unsigned vrow = lb + L1_V + (wx * 64 + m * 16 + tok) * 64;
#pragma unroll
      for (int df = 0; df < 2; ++df) { const f32x4 a = acc[2][m][df]; *(LAS u32x2*)(size_t)(vrow + (((4 * df + g) ^ (tok >> 1)) << 3)) = (u32x2){pk2(a[0], a[1]), pk2(a[2], a[3])}; }
    }
    STAMP(5); lds_sync(lb + L1_CNT, 4u, lane);
    f32x4 kv0 = zero4, kv1 = zero4, zz = zero4;
    { const bf16x8 ones = {0x3F80, 0x3F80, 0x3F80, 0x3F80, 0x3F80, 0x3F80, 0x3F80, 0x3F80};
      unsigned ap[2], av[2][2];
#pragma unroll
      for (int t = 0; t < 2; ++t) { const int tk = 8 * g + 4 * t + q, t15 = tk & 15; const unsigned sw = (t15 & 1) | (((t15 >> 2) & 1) << 1) | (((t15 >> 1) & 1) << 2) | (t15 & 8);
        ap[t] = lb + L1_PHI + tk * 128 + (((4 * wx + pp) ^ sw) << 3);
        av[t][0] = lb + L1_V + tk * 64 + ((pp ^ (t15 >> 1)) << 3); av[t][1] = av[t][0] ^ 32; }
#pragma unroll
      for (int ks = 0; ks < 8; ++ks) { const bf16x8 a = cat(ldstr(ap[0] + ks * 4096), ldstr(ap[1] + ks * 4096));
        const bf16x8 b0 = cat(ldstr(av[0][0] + ks * 2048), ldstr(av[1][0] + ks * 2048)), b1_ = cat(ldstr(av[0][1] + ks * 2048), ldstr(av[1][1] + ks * 2048));
        kv0 = mfma(a, b0, kv0); kv1 = mfma(a, b1_, kv1); zz = mfma(a, ones, zz); } }
    const __amdgpu_buffer_rsrc_t rkv = mk_rsrc(ws + WS_KVP, 32 * 8 * 8192), rz = mk_rsrc(ws + WS_ZP, 32 * 8 * 256);
    st16_sc1(__builtin_bit_cast(u32x4, kv0), rkv, lane * 16, (h * 8 + p) * 8192 + wx * 1024);
    st16_sc1(__builtin_bit_cast(u32x4, kv1), rkv, lane * 16, (h * 8 + p) * 8192 + 4096 + wx * 1024);
    if (tok == 0) st16_sc1(__builtin_bit_cast(u32x4, zz), rz, g * 16, (h * 8 + p) * 256 + wx * 64);
    STAMP(6); WAIT_V(0); STAMP(7);
    GAS unsigned* fkv = flags + (FL_KV + (h * 4 + wx) * 128) / 4;
    if (lane == 0) __hip_atomic_store(fkv + p, TAG, RLX_AGENT);
    bf16x8 eb[4][2];
#pragma unroll
    for (int m = 0; m < 4; ++m) { f32x4 ep[2], en[2]; expo(acc[0][m][0], acc[0][m][1], ep, en); eb[m][0] = pk8(ep[0], ep[1]); eb[m][1] = pk8(en[0], en[1]); }
    STAMP(8); wait_flags(fkv, 8, TAG, flags + FL_TMO / 4, lane); STAMP(9);
    { u32x4 t0[8], t1[8], tz[8];
#pragma unroll
      for (int i = 0; i < 8; ++i) { t0[i] = ld16_sc1(rkv, lane * 16, (h * 8 + i) * 8192 + wx * 1024); t1[i] = ld16_sc1(rkv, lane * 16, (h * 8 + i) * 8192 + 4096 + wx * 1024); tz[i] = ld16_sc1(rz, g * 16, (h * 8 + i) * 256 + wx * 64); }
      f32x4 s0 = zero4, s1 = zero4, sz = zero4;
#pragma unroll
      for (int i = 0; i < 8; ++i) { s0 += __builtin_bit_cast(f32x4, t0[i]); s1 += __builtin_bit_cast(f32x4, t1[i]); sz += __builtin_bit_cast(f32x4, tz[i]); }
      const unsigned a = lb + L1_KVA + (wx >> 1) * 1024 + lane * 16 + (wx & 1) * 8;
      *(LAS u32x2*)(size_t)a = (u32x2){pk2(s0[0], s0[1]), pk2(s0[2], s0[3])};
      *(LAS u32x2*)(size_t)(a + 2048) = (u32x2){pk2(s1[0], s1[1]), pk2(s1[2], s1[3])};
      u32x2 au = {pk2(sz[0], sz[1]), pk2(sz[2], sz[3])};
      if ((lane & 3) == 1) au = (u32x2){0x3F803F80u, 0x3F803F80u}; else if ((lane & 3) != 0) au = (u32x2){0u, 0u};
      *(LAS u32x2*)(size_t)(a + 4096) = au; }
    lds_sync(lb + L1_CNT, 8u, lane); STAMP(10);
    bf16x8 ka[6];
#pragma unroll
    for (int i = 0; i < 6; ++i) ka[i] = lds128(lb + L1_KVA + i * 1024 + lane * 16);
    const __amdgpu_buffer_rsrc_t ro = mk_rsrc(ws + WS_O, 128 * 32 * 1024);
    const unsigned vpair = (unsigned)((((g & 1) << 1) | (g >> 1)) * 256 + tok * 16);
#pragma unroll
    for (int m = 0; m < 4; ++m) {
      const f32x4 ap_ = mfma(ka[4], eb[m][0], zero4), an_ = mfma(ka[5], eb[m][1], zero4);
      const f32x4 p0 = mfma(ka[0], eb[m][0], zero4), n0 = mfma(ka[1], eb[m][1], zero4), p1 = mfma(ka[2], eb[m][0], zero4), n1 = mfma(ka[3], eb[m][1], zero4);
      const float a = rcp(ap_[1]), c = rcp(an_[1]), inv = rcp(ap_[0] * a + an_[0] * c + 1e-6f), ca = a * inv, cc = c * inv;
      const f32x4 o0 = p0 * ca + n0 * cc, o1 = p1 * ca + n1 * cc;
      st16(swap16(pk2(o0[0], o0[1]), pk2(o0[2], o0[3]), pk2(o1[0], o1[1]), pk2(o1[2], o1[3])), ro, vpair, ((p * 16 + wx * 4 + m) * 32 + h) * 1024);
    }
    WAIT_V(0); STAMP(11); STAMP_OUT(0, w);
  }
}

constexpr unsigned L2_EX = 131072;

template <int G> __device__ __forceinline__ void phase2(unsigned lb, const u16* x, const u16* w2, const u16* wo, char* ws, u16* out, int lane, int wq) {
  const int b = blockIdx.x, kh = b & 1, rt = ((b & 7) >> 1) * 4 + ((b >> 3) & 3), ct = b >> 5;
  const int tok = lane & 15, g = lane >> 4, q = (lane >> 2) & 3, pp = lane & 3, wi = wq >> 1, jj = wq & 1;
  const u32x4 sact = mk_srd(ws + WS_ACT, 128 * 112 * 1024), so = mk_srd(ws + WS_O, 128 * 32 * 1024), sw2 = mk_srd(w2, F_ * D_ * 2), swo = mk_srd(wo, D_ * D_ * 2);
  const unsigned vaa = lane * 16 + (rt * 8 + wq * 2) * (112 * 1024), vao = lane * 16 + (rt * 8 + wq * 2) * (32 * 1024);
  unsigned vb0, vb1, vs0, vs1;
  { const int r = lane >> 4, c = lane & 15; vb0 = (wq * 16 + r) * 2048 + ct * 256 + (((c >> 1) ^ r) << 5) + ((c & 1) << 4); vb1 = vb0 ^ 128; }
  { const int r = lane >> 3, c = lane & 7; vs0 = (wq * 16 + r) * 2048 + ct * 256 + kh * 128 + (((c >> 1) ^ ((r >> 1) & 1)) << 5) + ((c & 1) << 4); vs1 = vs0 ^ 64; }
  const unsigned sla = lb + (G ^ 1) * 65536 + wq * 4096, slb = lb + (G ^ 1) * 65536 + 16384 + wq * 4096, sls = lb + (G ^ 1) * 65536 + 16384 + wq * 2048;
  const unsigned ra = opq(lb + G * 65536 + wi * 8192 + lane * 16);
  unsigned rb[4], rs[2];
#pragma unroll
  for (int n = 0; n < 4; ++n) { const int f = 4 * (kh ^ (n >> 1)) + 2 * jj + (n & 1); rb[n] = opq(lb + G * 65536 + 16384 + (8 * g + q) * 256 + ((f ^ (q | ((g & 1) << 2))) << 5) + 8 * pp); }
#pragma unroll
  for (int n = 0; n < 2; ++n) { const int f = 2 * jj + n; rs[n] = opq(lb + G * 65536 + 16384 + (8 * g + q) * 128 + ((f ^ ((q >> 1) | ((g & 1) << 1))) << 5) + 8 * pp); }
  f32x4 acc[4][4], as[4][2];
#pragma unroll
  for (int m = 0; m < 4; ++m) {
#pragma unroll
    for (int n = 0; n < 4; ++n) acc[m][n] = (f32x4){0.f, 0.f, 0.f, 0.f};
    as[m][0] = acc[m][0]; as[m][1] = acc[m][0]; }
  bf16x8 fa[2][4], fb[2][4];
  auto dma = [&](auto HD, auto Y, auto I, unsigned ska, unsigned skb) { constexpr int hd = HD() * 32768, i = I(); constexpr bool y = Y();
    if (i < 4) { if (y) DMA(sact, vaa, ska, (i >> 1) * 112 * 1024 + (i & 1) * 1024, sla, hd + i * 1024); else DMA(so, vao, ska, (i >> 1) * 32 * 1024 + (i & 1) * 1024, sla, hd + i * 1024); }
    else if (y) { constexpr int j = i - 4; if (j >> 1) DMA(sw2, vb1, skb, j * 8192, slb, hd + j * 1024); else DMA(sw2, vb0, skb, j * 8192, slb, hd + j * 1024); }
    else if (i < 6) { constexpr int j = i - 4; if (j) DMA(swo, vs1, skb, j * 16384, sls, hd + j * 1024); else DMA(swo, vs0, skb, j * 16384, sls, hd + j * 1024); } };
  auto rdA = [&](auto HR, auto I) { constexpr int i = I(); fa[i >> 2][i & 3] = lds128(ra + HR() * 32768 + (i & 3) * 2048 + (i >> 2) * 1024); SB; };
  auto rdB = [&](auto HR, auto Y, auto I) { constexpr int i = I(), ks = i >> 2, n = i & 3; constexpr bool y = Y();
    if (y) { constexpr int o = HR() * 32768 + ks * 8192; fb[ks][n] = cat(ldstr(rb[n] + o), ldstr(rb[n] + o + 1024)); SB; }
    else if (n < 2) { constexpr int o = HR() * 32768 + ks * 4096; fb[ks][n] = cat(ldstr(rs[n & 1] + o), ldstr(rs[n & 1] + o + 512)); SB; } };
  auto load = [&](auto HR, auto YR, auto MD, auto HD, int U) { constexpr int md = MD(); constexpr bool d = md != 0; IC<md == 1> YD;
    const unsigned kt = md == 1 ? kh * 28 + U : U - 28, ska = kt * 2048, skb = kt * (64 * 2048);
    rdA(HR, IC<0>{}); rdB(HR, YR, IC<0>{}); if (d) dma(HD, YD, IC<0>{}, ska, skb);
    rdA(HR, IC<1>{}); rdB(HR, YR, IC<1>{}); if (d) dma(HD, YD, IC<4>{}, ska, skb);
    rdA(HR, IC<2>{}); rdB(HR, YR, IC<2>{}); if (d) dma(HD, YD, IC<1>{}, ska, skb);
    rdA(HR, IC<3>{}); rdB(HR, YR, IC<3>{}); if (d) dma(HD, YD, IC<5>{}, ska, skb);
    rdA(HR, IC<4>{}); rdB(HR, YR, IC<4>{}); if (d) dma(HD, YD, IC<2>{}, ska, skb);
    rdA(HR, IC<5>{}); rdB(HR, YR, IC<5>{}); if (d) dma(HD, YD, IC<6>{}, ska, skb);
    rdA(HR, IC<6>{}); rdB(HR, YR, IC<6>{}); if (d) dma(HD, YD, IC<3>{}, ska, skb);
    rdA(HR, IC<7>{}); rdB(HR, YR, IC<7>{}); if (d) dma(HD, YD, IC<7>{}, ska, skb); };
  auto mmy = [&]() {
#pragma unroll
    for (int m = 0; m < 4; ++m)
#pragma unroll
      for (int n = 0; n < 4; n += 2) mfma_k2x2(acc[m][n], acc[m][n + 1], fb[0][n], fb[0][n + 1], fa[0][m], fb[1][n], fb[1][n + 1], fa[1][m]);
    SB; BAR; SB; };
  auto mms = [&]() {
#pragma unroll
    for (int m = 0; m < 4; ++m) mfma_k2x2(as[m][0], as[m][1], fb[0][0], fb[0][1], fa[0][m], fb[1][0], fb[1][1], fa[1][m]);
    SB; };
#define ENDL(n) do { WAIT_V(n); WAIT_L0; BAR; SB; } while (0)
  IC<0> c0; IC<1> c1; IC<2> c2;
  auto dma8 = [&](auto HD, int U) { const unsigned kt = kh * 28 + U, ska = kt * 2048, skb = kt * (64 * 2048);
    dma(HD, c1, IC<0>{}, ska, skb); dma(HD, c1, IC<1>{}, ska, skb); dma(HD, c1, IC<2>{}, ska, skb); dma(HD, c1, IC<3>{}, ska, skb);
    dma(HD, c1, IC<4>{}, ska, skb); dma(HD, c1, IC<5>{}, ska, skb); dma(HD, c1, IC<6>{}, ska, skb); dma(HD, c1, IC<7>{}, ska, skb); };
  GAS unsigned* flags = (GAS unsigned*)(ws + WS_FLAG);
  const __amdgpu_buffer_rsrc_t ryx = mk_rsrc(ws + WS_YX, 256 * 65536);
  auto send = [&]() {
#pragma unroll
    for (int m = 0; m < 4; ++m)
#pragma unroll
      for (int n = 0; n < 2; ++n) st16_sc1(__builtin_bit_cast(u32x4, acc[m][2 + n]), ryx, lane * 16, b * 65536 + (G * 4 + wq) * 8192 + (m * 2 + n) * 1024);
#pragma unroll
    for (int ml = 0; ml < 2; ++ml)
#pragma unroll
      for (int n = 0; n < 2; ++n) *(LAS f32x4*)(size_t)(lb + L2_EX + (G * 4 + wq) * 4096 + (ml * 2 + n) * 1024 + lane * 16) = acc[2 * (G ^ 1) + ml][n];
    SB; };
  STAMP_DECL; STAMP(0);
  dma8(c0, G ^ 1); dma8(c1, 2 + (G ^ 1));
  if (G == 1) WAIT_V(8);
  BAR; SB; STAMP(1);
  if (G == 0) { load(c0, c1, c0, c0, 0); ENDL(8); } else { BAR; SB; load(c0, c1, c1, c0, 4); ENDL(8); }
  IC<G> hA; IC<G ^ 1> hB; STAMP(2);
  for (int T = G; T < 20; T += 4) {
    mmy(); load(c1, c1, c1, hA, T + 5); ENDL(8);
    mmy(); load(c0, c1, c1, hB, T + 7); ENDL(8);
  }
  STAMP(3);
  mmy(); load(c1, c1, c1, hA, 25 + G); ENDL(8);
  mmy(); if (G == 0) { load(c0, c1, c1, hB, 27); ENDL(8); } else { load(c0, c1, c2, hB, 28); ENDL(6); }
  mmy(); load(c1, c1, c2, hA, 29 + G); ENDL(6);
  mmy(); load(c0, c0, c2, hB, 31 + G); send(); ENDL(14); STAMP(4);
  mms(); BAR; SB; load(c1, c0, c2, hA, 33 + G); ENDL(14);
  mms(); BAR; SB; load(c0, c0, c2, hB, 35 + G); WAIT_V(6);
  if (lane == 0) __hip_atomic_store(flags + (FL_YX + b * 128) / 4 + G * 4 + wq, TAG, RLX_AGENT);
  WAIT_L0; BAR; SB;
  mms(); BAR; SB; load(c1, c0, c2, hA, 37 + G); ENDL(6);
  mms(); BAR; SB; load(c0, c0, c2, hB, 39 + G); ENDL(6);
  mms(); BAR; SB; load(c1, c0, c2, hA, 41 + G); ENDL(6);
  mms(); BAR; SB; if (G == 0) { load(c0, c0, c2, hB, 43); ENDL(6); } else { load(c0, c0, c0, hB, 0); ENDL(0); }
  STAMP(5); mms(); BAR; SB; load(c1, c0, c0, hA, 0); WAIT_V(0); STAMP(6);
  wait_flags(flags + (FL_YX + (b ^ 1) * 128) / 4, 8, TAG, flags + FL_TMO / 4, lane);
  STAMP(7); u32x4 py[2][2][2], xr[2];
  const unsigned row0 = rt * 128 + wi * 64 + 2 * G * 16 + tok, vout = row0 * 2048 + ct * 256 + kh * 128 + jj * 64 + (((g & 1) << 1) | (g >> 1)) * 16;
  const __amdgpu_buffer_rsrc_t rx = mk_rsrc(x, S_ * D_ * 2), rout = mk_rsrc(out, S_ * D_ * 2);
#pragma unroll
  for (int ml = 0; ml < 2; ++ml) {
#pragma unroll
    for (int n = 0; n < 2; ++n)
#pragma unroll
      for (int gg = 0; gg < 2; ++gg) py[gg][ml][n] = ld16_sc1(ryx, lane * 16, (b ^ 1) * 65536 + (gg * 4 + wq) * 8192 + ((2 * G + ml) * 2 + n) * 1024);
    xr[ml] = __builtin_amdgcn_raw_buffer_load_b128(rx, vout, ml * 16 * 2048, 0); }
  WAIT_L0; BAR; SB;
  mms(); MFMA_PAD; STAMP(8);
  if (G == 0) { BAR; SB; }
#pragma unroll
  for (int ml = 0; ml < 2; ++ml)
#pragma unroll
    for (int n = 0; n < 2; ++n) *(LAS f32x4*)(size_t)(lb + (G * 4 + wq) * 4096 + (ml * 2 + n) * 1024 + lane * 16) = as[2 * (G ^ 1) + ml][n];
  WAIT_L0; BAR; SB;
#pragma unroll
  for (int ml = 0; ml < 2; ++ml) {
    const u32x4 xs = swap16(xr[ml][0], xr[ml][1], xr[ml][2], xr[ml][3]); unsigned ow[4];
#pragma unroll
    for (int n = 0; n < 2; ++n) { const unsigned ex = ((G ^ 1) * 4 + wq) * 4096 + (ml * 2 + n) * 1024 + lane * 16;
      const f32x4 y = (acc[2 * G + ml][n] + *(const LAS f32x4*)(size_t)(lb + L2_EX + ex)) + (__builtin_bit_cast(f32x4, py[0][ml][n]) + __builtin_bit_cast(f32x4, py[1][ml][n]));
      const f32x4 s = as[2 * G + ml][n] + *(const LAS f32x4*)(size_t)(lb + ex);
      const f32x4 xv = {bflo(xs[2 * n]), bfhi(xs[2 * n]), bflo(xs[2 * n + 1]), bfhi(xs[2 * n + 1])}; f32x4 o;
#pragma unroll
      for (int r = 0; r < 4; ++r) o[r] = xv[r] + s[r] * (y[r] * rcp(1.0f + ex2(-LOG2E * y[r])));
      ow[2 * n] = pk2(o[0], o[1]); ow[2 * n + 1] = pk2(o[2], o[3]); }
    st16(swap16(ow[0], ow[1], ow[2], ow[3]), rout, vout, ml * 16 * 2048); }
  STAMP(9); WAIT_V(0); STAMP(10); STAMP_OUT(1, G * 4 + wq);
}

__global__ __launch_bounds__(512, 2) void k_phase2(const u16* x, const u16* w2, const u16* wo, char* ws, u16* out) {
  __shared__ __attribute__((aligned(16))) char smem[163840];
  const unsigned lb = (unsigned)(size_t)(LAS char*)smem;
  const int tid = threadIdx.x, lane = tid & 63, w = __builtin_amdgcn_readfirstlane(tid >> 6);
  if (w < 4) phase2<0>(lb, x, w2, wo, ws, out, lane, w); else phase2<1>(lb, x, w2, wo, ws, out, lane, w - 4);
}

__global__ __launch_bounds__(512) void k_touch(const unsigned* a, unsigned na, const unsigned* b, unsigned nb, const unsigned* c, unsigned nc, const unsigned* d, unsigned nd, unsigned* sink) {
  unsigned acc = 0; const unsigned t = blockIdx.x * 512 + threadIdx.x;
  for (unsigned i = t * 16; i < na; i += 256 * 512 * 16) acc ^= a[i];
  for (unsigned i = t * 16; i < nb; i += 256 * 512 * 16) acc ^= b[i];
  for (unsigned i = t * 16; i < nc; i += 256 * 512 * 16) acc ^= c[i];
  for (unsigned i = t * 16; i < nd; i += 256 * 512 * 16) acc ^= d[i];
  if (acc == 0x12345678u) sink[0] = acc;
}
}

extern "C" void kernel_launch(void* const* d_in, const int* in_sizes, int n_in, void* d_out, int out_size, void* d_ws, size_t ws_size, hipStream_t stream) {
  const u16* x = (const u16*)d_in[0]; const u16* wqkv = (const u16*)d_in[1]; const u16* wphi = (const u16*)d_in[2]; const u16* wo = (const u16*)d_in[3];
  const u16* w1 = (const u16*)d_in[5]; const u16* b1 = (const u16*)d_in[6]; const u16* w2 = (const u16*)d_in[7];
  char* ws = (char*)d_ws;
  k_touch<<<256, 512, 0, stream>>>((const unsigned*)wqkv, 1024*3072/2, (const unsigned*)w1, 1024*3584/2, (const unsigned*)w2, 1024*3584/2, (const unsigned*)wo, 1024*1024/2, (unsigned*)(ws + (100u << 20)));
  k_ln<<<256, 512, 0, stream>>>(x, (u16*)(ws + WS_XN), (unsigned*)(ws + WS_FLAG));
  k_phase1<<<256, 512, 0, stream>>>((const u16*)(ws + WS_XN), wqkv, wphi, w1, b1, ws);
  k_phase2<<<256, 512, 0, stream>>>(x, w2, wo, ws, (u16*)d_out);
}
```

```cpp
#include <hip/hip_runtime.h>
#include <hip/hip_bf16.h>
#include <type_traits>

namespace {
#define LAS __attribute__((address_space(3)))
#define GAS __attribute__((address_space(1)))
typedef unsigned short u16;
typedef unsigned u32x4 __attribute__((ext_vector_type(4)));
typedef unsigned u32x2 __attribute__((ext_vector_type(2)));
typedef short bf16x8 __attribute__((ext_vector_type(8)));
typedef short s16x4 __attribute__((ext_vector_type(4)));
typedef float f32x4 __attribute__((ext_vector_type(4)));
typedef float f32x2 __attribute__((ext_vector_type(2)));
typedef __bf16 bf16x2_t __attribute__((ext_vector_type(2)));

constexpr int S_ = 2048, D_ = 1024, F_ = 3584;
constexpr size_t WS_FLAG = 0;
constexpr size_t WS_XN = 1u << 20;
constexpr size_t WS_ACT = WS_XN + (4u << 20);
constexpr size_t WS_O = WS_ACT + (15u << 20);
constexpr size_t WS_KVP = WS_O + (4u << 20);
constexpr size_t WS_ZP = WS_KVP + (2u << 20);
constexpr size_t WS_YX = WS_ZP + (1u << 20);
constexpr size_t WS_STAMP = 64u << 20;
#define STAMP_DECL
#define STAMP(i)
#define STAMP_OUT(wv)
constexpr unsigned FL_EPOCH = 0;
constexpr unsigned FL_XN = 1024;
constexpr unsigned FL_KV = 9216;
constexpr unsigned FL_ACT = 25600;
constexpr unsigned FL_O = 33792;
constexpr unsigned FL_YX = 41984;
constexpr unsigned FL_TMO = 61440;
constexpr float LOG2E = 1.4426950408889634f;

#define SB __builtin_amdgcn_sched_barrier(0)
#define BAR __builtin_amdgcn_s_barrier()
#define WAIT_L0 do { __builtin_amdgcn_s_waitcnt(0xC07F); SB; } while (0)
#define WAIT_V(n) asm volatile("s_waitcnt vmcnt(%0)" :: "i"(n) : "memory")
template <int N> using IC = std::integral_constant<int, N>;

__device__ __forceinline__ u32x4 mk_srd(const void* p, unsigned bytes) {
  const unsigned long long a = (unsigned long long)p; u32x4 r;
  r.x = __builtin_amdgcn_readfirstlane((unsigned)a); r.y = __builtin_amdgcn_readfirstlane((unsigned)(a >> 32)) & 0xffffu; r.z = bytes; r.w = 0x00020000u; return r; }
#define DMA(srd, voff, sk, KOFF, sl, LOFF) do { unsigned _t; asm volatile("s_add_u32 m0, %4, %5\n\ts_add_u32 %0, %3, %6\n\tbuffer_load_dwordx4 %1, %2, %0 offen lds" \
    : "=&s"(_t) : "v"(voff), "s"(srd), "s"(sk), "s"(sl), "i"(LOFF), "i"(KOFF) : "memory", "scc"); SB; } while (0)
#define DMA_SC1(srd, voff, sk, KOFF, sl, LOFF) do { unsigned _t; asm volatile("s_add_u32 m0, %4, %5\n\ts_add_u32 %0, %3, %6\n\tbuffer_load_dwordx4 %1, %2, %0 offen sc1 lds" \
    : "=&s"(_t) : "v"(voff), "s"(srd), "s"(sk), "s"(sl), "i"(LOFF), "i"(KOFF) : "memory", "scc"); SB; } while (0)
#define TOUCH(ptr, sl) do { asm volatile("s_mov_b32 m0, %1\n\ts_nop 0\n\tglobal_load_lds_dword %0, off" :: "v"(ptr), "s"(sl) : "memory"); SB; } while (0)
__device__ __forceinline__ bf16x8 lds128(unsigned a) { return *(const LAS bf16x8*)(size_t)a; }
__device__ __forceinline__ s16x4 ldstr(unsigned a) { return __builtin_amdgcn_ds_read_tr16_b64_v4i16((LAS s16x4*)(size_t)a); }
__device__ __forceinline__ bf16x8 cat(s16x4 lo, s16x4 hi) { return __builtin_shufflevector(lo, hi, 0, 1, 2, 3, 4, 5, 6, 7); }
__device__ __forceinline__ unsigned opq(unsigned x) { asm volatile("" : "+v"(x)); return x; }
__device__ __forceinline__ f32x4 mfma(bf16x8 a, bf16x8 b, f32x4 c) { return __builtin_amdgcn_mfma_f32_16x16x32_bf16(a, b, c, 0, 0, 0); }
__device__ __forceinline__ void mfma_acc(f32x4& d, bf16x8 a, bf16x8 b) { asm volatile("v_mfma_f32_16x16x32_bf16 %0, %1, %2, %0" : "+v"(d) : "v"(a), "v"(b)); }
__device__ __forceinline__ void mfma_k2(f32x4& d, bf16x8 a0, bf16x8 b0, bf16x8 a1, bf16x8 b1) {
  asm volatile("v_mfma_f32_16x16x32_bf16 %0, %1, %2, %0\n\tv_mfma_f32_16x16x32_bf16 %0, %3, %4, %0" : "+v"(d) : "v"(a0), "v"(b0), "v"(a1), "v"(b1)); }
__device__ __forceinline__ void mfma_k2x2(f32x4& d, f32x4& e, bf16x8 a0, bf16x8 c0, bf16x8 b0, bf16x8 a1, bf16x8 c1, bf16x8 b1) {
  asm volatile("v_mfma_f32_16x16x32_bf16 %0, %2, %4, %0\n\tv_mfma_f32_16x16x32_bf16 %1, %3, %4, %1\n\tv_mfma_f32_16x16x32_bf16 %0, %5, %7, %0\n\tv_mfma_f32_16x16x32_bf16 %1, %6, %7, %1"
               : "+v"(d), "+v"(e) : "v"(a0), "v"(c0), "v"(b0), "v"(a1), "v"(c1), "v"(b1)); }
__device__ __forceinline__ f32x4 zacc() { f32x4 z = {0.f, 0.f, 0.f, 0.f}; asm volatile("" : "+v"(z)); return z; }
#define MFMA_PAD do { asm volatile("s_nop 7\n\ts_nop 1" ::: "memory"); SB; } while (0)
__device__ __forceinline__ unsigned pk2(float lo, float hi) { const f32x2 v = {lo, hi}; return __builtin_bit_cast(unsigned, __builtin_convertvector(v, bf16x2_t)); }
__device__ __forceinline__ bf16x8 pk8(f32x4 a, f32x4 b) { const u32x4 u = {pk2(a[0], a[1]), pk2(a[2], a[3]), pk2(b[0], b[1]), pk2(b[2], b[3])}; return __builtin_bit_cast(bf16x8, u); }
__device__ __forceinline__ float bflo(unsigned u) { return __uint_as_float(u << 16); }
__device__ __forceinline__ float bfhi(unsigned u) { return __uint_as_float(u & 0xffff0000u); }
__device__ __forceinline__ float ex2(float x) { return __builtin_amdgcn_exp2f(x); }
__device__ __forceinline__ float rcp(float x) { return __builtin_amdgcn_rcpf(x); }
__device__ __forceinline__ u32x4 swap16(unsigned a0, unsigned a1, unsigned b0, unsigned b1) {
  const auto p = __builtin_amdgcn_permlane16_swap(a0, b0, false, false), q = __builtin_amdgcn_permlane16_swap(a1, b1, false, false);
  return (u32x4){p[0], q[0], p[1], q[1]}; }
template <class Op> __device__ __forceinline__ float red4(float v, Op op) {
  const unsigned u = __float_as_uint(v); const auto p = __builtin_amdgcn_permlane32_swap(u, u, false, false);
  const float a = op(__uint_as_float(p[0]), __uint_as_float(p[1])); const unsigned ua = __float_as_uint(a);
  const auto q = __builtin_amdgcn_permlane16_swap(ua, ua, false, false); return op(__uint_as_float(q[0]), __uint_as_float(q[1])); }
__device__ __forceinline__ void st16_sc1(u32x4 v, __amdgpu_buffer_rsrc_t r, unsigned voff, unsigned soff) { __builtin_amdgcn_raw_buffer_store_b128(v, r, voff + soff, 0, 16); }
__device__ __forceinline__ void st16(u32x4 v, __amdgpu_buffer_rsrc_t r, unsigned voff, unsigned soff) { __builtin_amdgcn_raw_buffer_store_b128(v, r, voff + soff, 0, 0); }
__device__ __forceinline__ u32x4 ld16_sc1(__amdgpu_buffer_rsrc_t r, unsigned voff, unsigned soff) { return __builtin_amdgcn_raw_buffer_load_b128(r, voff, soff, 16); }
__device__ __forceinline__ __amdgpu_buffer_rsrc_t mk_rsrc(const void* p, unsigned bytes) { return __builtin_amdgcn_make_buffer_rsrc((void*)p, 0, bytes, 0x00020000); }
#define RLX_AGENT __ATOMIC_RELAXED, __HIP_MEMORY_SCOPE_AGENT
typedef unsigned long long u64;
typedef GAS u64 gu64;
__device__ __forceinline__ void set_flag(gu64* w, u64 tag, int lane) { if (lane == 0) __hip_atomic_store(w, tag, RLX_AGENT); }
template <int N> __device__ __forceinline__ void wait_flags(gu64* w, u64 tag, gu64* tmo, int lane) {
#pragma nounroll
  for (unsigned spins = 0;; ++spins) {
    u64 v = tag, v2 = tag; if (N >= 64 || lane < N) v = __hip_atomic_load(w + lane, RLX_AGENT);
    if (N == 128) v2 = __hip_atomic_load(w + 64 + lane, RLX_AGENT);
    if (__all(v == tag && v2 == tag)) break;
    if (spins > (1u << 16)) { if (lane == 0) __hip_atomic_store(tmo, tag, RLX_AGENT); break; }
    __builtin_amdgcn_s_sleep(1);
  }
  __builtin_amdgcn_fence(__ATOMIC_ACQUIRE, "wavefront");
}
#define POLL_ISSUE(dst, ptr) do { asm volatile("global_load_dwordx2 %0, %1, off sc1" : "=v"(dst) : "v"(ptr) : "memory"); SB; } while (0)
#define POLL_PIN(dst) asm volatile("" : "+v"(dst))
template <int C> __device__ __forceinline__ float dpp(float v) { return __int_as_float(__builtin_amdgcn_update_dpp(0, __float_as_int(v), C, 0xf, 0xf, true)); }
__device__ __forceinline__ float wave_sum(float v) { v += dpp<0x128>(v); v += dpp<0x124>(v); v += dpp<0x122>(v); v += dpp<0x121>(v); return red4(v, [](float a, float c) { return a + c; }); }
__device__ __forceinline__ void lds_sync(unsigned addr, unsigned target, int lane) {
  asm volatile("s_waitcnt lgkmcnt(0)" ::: "memory");
  if (lane == 0) { const unsigned one = 1; asm volatile("ds_add_u32 %0, %1" :: "v"(addr), "v"(one) : "memory"); }
  for (;;) { unsigned v; asm volatile("ds_read_b32 %0, %1\n\ts_waitcnt lgkmcnt(0)" : "=v"(v) : "v"(addr) : "memory");
    if ((unsigned)__builtin_amdgcn_readfirstlane(v) >= target) break; }
}

constexpr unsigned L1_A = 0;
constexpr unsigned L1_BQ = 65536;
constexpr unsigned L1_BH = 90112;
constexpr unsigned L1_WPHI = 118784;
constexpr unsigned L1_CNT = 120832;
constexpr unsigned L1_DUMP = 121088;
constexpr unsigned L1_KVA = 121856;
constexpr unsigned L1_V = 98304;
constexpr unsigned L1_PHI = 131072;
constexpr unsigned L2_EX = 131072;

struct Ptrs { const u16 *x, *wqkv, *wphi, *wo, *w1, *b1, *w2; char* ws; u16* out; };

template <int G> struct Phase2 {
  unsigned lb; int lane, wq, b, kh, rt, ct;
  u32x4 sact, so, sw2, swo;
  unsigned vaa, vao, vb0, vb1, vs0, vs1, sla, sls;
  __device__ __forceinline__ void init(unsigned lb_, int lane_, int wq_, const Ptrs& P) {
    lb = lb_; lane = lane_; wq = wq_; b = blockIdx.x; kh = b & 1; rt = ((b & 7) >> 1) * 4 + ((b >> 3) & 3); ct = b >> 5;
    sact = mk_srd(P.ws + WS_ACT, 128 * 112 * 1024); so = mk_srd(P.ws + WS_O, 128 * 32 * 1024); sw2 = mk_srd(P.w2, F_ * D_ * 2); swo = mk_srd(P.wo, D_ * D_ * 2);
    vaa = lane * 16 + (rt * 8 + wq * 2) * (112 * 1024); vao = lane * 16 + (rt * 8 + wq * 2) * (32 * 1024);
    { const int r = lane >> 4, c = lane & 15; vb0 = (wq * 16 + r) * 2048 + ct * 256 + (((c >> 1) ^ r) << 5) + ((c & 1) << 4); vb1 = vb0 ^ 128; }
    { const int r = lane >> 3, c = lane & 7; vs0 = (wq * 16 + r) * 2048 + ct * 256 + kh * 128 + (((c >> 1) ^ ((r >> 1) & 1)) << 5) + ((c & 1) << 4); vs1 = vs0 ^ 64; }
    sla = lb + wq * 4096; sls = lb + wq * 2048;
  }
  template <int LO, bool Y, int I> __device__ __forceinline__ void dma(unsigned ska, unsigned skb) {
    if (I < 4) { if (Y) DMA_SC1(sact, vaa, ska, (I >> 1) * 112 * 1024 + (I & 1) * 1024, sla, LO + I * 1024); else DMA_SC1(so, vao, ska, (I >> 1) * 32 * 1024 + (I & 1) * 1024, sla, LO + I * 1024); }
    else if (Y) { constexpr int j = I - 4; if (j >> 1) DMA(sw2, vb1, skb, j * 8192, sla, LO + 16384 + j * 1024); else DMA(sw2, vb0, skb, j * 8192, sla, LO + 16384 + j * 1024); }
    else if (I < 6) { constexpr int j = I - 4; if (j) DMA(swo, vs1, skb, j * 16384, sls, LO + 16384 + j * 1024); else DMA(swo, vs0, skb, j * 16384, sls, LO + 16384 + j * 1024); } }
  template <int U, int I0> __device__ __forceinline__ void dma4() { constexpr int LO = (U & 1) * 65536 + ((U >> 1) & 1) * 32768; const unsigned kt = kh * 28 + U, ska = kt * 2048, skb = kt * (64 * 2048);
    dma<LO, true, I0>(ska, skb); dma<LO, true, I0 + 1>(ska, skb); dma<LO, true, I0 + 2>(ska, skb); dma<LO, true, I0 + 3>(ska, skb); }

  __device__ __forceinline__ void run(const Ptrs& P, u64 tag, gu64* oflag) {
    char* ws = P.ws;
    const int tok = lane & 15, g = lane >> 4, q = (lane >> 2) & 3, pp = lane & 3, wi = wq >> 1, jj = wq & 1;
    const unsigned ra = opq(lb + G * 65536 + wi * 8192 + lane * 16);
    unsigned rb[4], rs[2];
#pragma unroll
    for (int n = 0; n < 4; ++n) { const int f = 4 * (kh ^ (n >> 1)) + 2 * jj + (n & 1); rb[n] = opq(lb + G * 65536 + 16384 + (8 * g + q) * 256 + ((f ^ (q | ((g & 1) << 2))) << 5) + 8 * pp); }
#pragma unroll
    for (int n = 0; n < 2; ++n) { const int f = 2 * jj + n; rs[n] = opq(lb + G * 65536 + 16384 + (8 * g + q) * 128 + ((f ^ ((q >> 1) | ((g & 1) << 1))) << 5) + 8 * pp); }
    f32x4 acc[4][4], as[4][2];
#pragma unroll
    for (int m = 0; m < 4; ++m) {
#pragma unroll
      for (int n = 0; n < 4; ++n) acc[m][n] = zacc();
      as[m][0] = zacc(); as[m][1] = zacc(); }
    bf16x8 fa[2][4], fb[2][4];
    auto rdA = [&](auto HR, auto I) { constexpr int i = I(); fa[i >> 2][i & 3] = lds128(ra + HR() * 32768 + (i & 3) * 2048 + (i >> 2) * 1024); SB; };
    auto rdB = [&](auto HR, auto Y, auto I) { constexpr int i = I(), ks = i >> 2, n = i & 3; constexpr bool y = Y();
      if (y) { constexpr int o = HR() * 32768 + ks * 8192; fb[ks][n] = cat(ldstr(rb[n] + o), ldstr(rb[n] + o + 1024)); SB; }
      else if (n < 2) { constexpr int o = HR() * 32768 + ks * 4096; fb[ks][n] = cat(ldstr(rs[n & 1] + o), ldstr(rs[n & 1] + o + 512)); SB; } };
    auto load = [&](auto HR, auto YR, auto MD, auto HD, int U) { constexpr int md = MD(), LO = (G ^ 1) * 65536 + HD() * 32768; constexpr bool d = md != 0, y = md == 1;
      const unsigned kt = md == 1 ? kh * 28 + U : U - 28, ska = kt * 2048, skb = kt * (64 * 2048);
      rdA(HR, IC<0>{}); rdB(HR, YR, IC<0>{}); if (d) dma<LO, y, 0>(ska, skb);
      rdA(HR, IC<1>{}); rdB(HR, YR, IC<1>{}); if (d) dma<LO, y, 4>(ska, skb);
      rdA(HR, IC<2>{}); rdB(HR, YR, IC<2>{}); if (d) dma<LO, y, 1>(ska, skb);
      rdA(HR, IC<3>{}); rdB(HR, YR, IC<3>{}); if (d) dma<LO, y, 5>(ska, skb);
      rdA(HR, IC<4>{}); rdB(HR, YR, IC<4>{}); if (d) dma<LO, y, 2>(ska, skb);
      rdA(HR, IC<5>{}); rdB(HR, YR, IC<5>{}); if (d) dma<LO, y, 6>(ska, skb);
      rdA(HR, IC<6>{}); rdB(HR, YR, IC<6>{}); if (d) dma<LO, y, 3>(ska, skb);
      rdA(HR, IC<7>{}); rdB(HR, YR, IC<7>{}); if (d) dma<LO, y, 7>(ska, skb); };
    auto mmy = [&]() {
#pragma unroll
      for (int m = 0; m < 4; ++m)
#pragma unroll
        for (int n = 0; n < 4; ++n) mfma_k2(acc[m][n], fb[0][n], fa[0][m], fb[1][n], fa[1][m]);
      SB; };
    auto mms = [&]() {
#pragma unroll
      for (int m = 0; m < 4; ++m)
#pragma unroll
        for (int n = 0; n < 2; ++n) mfma_k2(as[m][n], fb[0][n], fa[0][m], fb[1][n], fa[1][m]);
      SB; };
#define ENDL(n) do { WAIT_V(n); WAIT_L0; BAR; SB; } while (0)
#define ENDM do { BAR; SB; } while (0)
    IC<0> c0; IC<1> c1; IC<2> c2; IC<G> hA; IC<G ^ 1> hB;
    gu64* flags = (gu64*)(ws + WS_FLAG); gu64* tmo = flags + FL_TMO / 8;
    const __amdgpu_buffer_rsrc_t ryx = mk_rsrc(ws + WS_YX, 256 * 32768);
    const unsigned exk = lb + L2_EX + wq * 4096 + lane * 16, exs = lb + 24576 + (wq >> 1) * 32768 + (wq & 1) * 4096 + lane * 16;
    auto give = [&]() {
#pragma unroll
      for (int i = 0; i < 4; ++i) { *(LAS f32x4*)(size_t)(exk + G * 16384 + i * 1024) = acc[2 * (G ^ 1) + (i >> 1)][i & 1]; *(LAS f32x4*)(size_t)(exs + G * 65536 + i * 1024) = acc[2 * (G ^ 1) + (i >> 1)][2 + (i & 1)]; }
      SB; };
    auto take = [&]() {
#pragma unroll
      for (int i = 0; i < 4; ++i) { acc[2 * G + (i >> 1)][i & 1] += *(const LAS f32x4*)(size_t)(exk + (G ^ 1) * 16384 + i * 1024);
        st16_sc1(__builtin_bit_cast(u32x4, acc[2 * G + (i >> 1)][2 + (i & 1)] + *(const LAS f32x4*)(size_t)(exs + (G ^ 1) * 65536 + i * 1024)), ryx, lane * 16, b * 32768 + (G * 4 + wq) * 4096 + i * 1024); }
      SB; };
    if (G == 0) {
      WAIT_V(8); BAR; SB; STAMP(16);
      load(c0, c1, c1, c1, 3); ENDL(12);
      mmy(); WAIT_V(8); ENDM;
    } else {
      BAR; SB; STAMP(16); BAR; SB;
      load(c0, c1, c1, c0, 4); WAIT_V(8); set_flag(oflag, tag, lane); WAIT_L0; BAR; SB;
      mmy(); ENDM;
    }
    STAMP(17);
    load(c1, c1, c1, hA, 5 + G); ENDL(8);
    mmy(); ENDM; load(c0, c1, c1, hB, 7 + G); ENDL(8);
    for (int T = 4 + G; T < 20; T += 4) {
      mmy(); ENDM; load(c1, c1, c1, hA, T + 5); ENDL(8);
      mmy(); ENDM; load(c0, c1, c1, hB, T + 7); ENDL(8);
    }
    STAMP(18);
    u64 pv; gu64* po = flags + FL_O / 8 + rt * 64;
    mmy(); ENDM; if (G == 1) POLL_ISSUE(pv, po + lane); load(c1, c1, c1, hA, 25 + G); ENDL(8 + G);
    mmy(); ENDM;
    if (G == 0) { load(c0, c1, c1, hB, 27); ENDL(8); }
    else { WAIT_V(8); POLL_PIN(pv); if (!__all(pv == tag)) wait_flags<64>(po, tag, tmo, lane); SB; load(c0, c1, c2, hB, 28); ENDL(6); }
    mmy(); ENDM; load(c1, c1, c2, hA, 29 + G); ENDL(6);
    mmy(); ENDM; STAMP(19); load(c0, c0, c2, hB, 31 + G); give(); if (G == 1) { take(); ENDL(10); } else ENDL(6);
    mms(); ENDM; load(c1, c0, c2, hA, 33 + G); if (G == 0) take(); ENDL(10);
    gu64* fy = flags + FL_YX / 8 + b * 8 + G * 4 + wq;
    mms(); ENDM; load(c0, c0, c2, hB, 35 + G); if (G == 0) { ENDL(10); } else { WAIT_V(6); set_flag(fy, tag, lane); WAIT_L0; ENDM; }
    mms(); ENDM; load(c1, c0, c2, hA, 37 + G); WAIT_V(6); if (G == 0) set_flag(fy, tag, lane); WAIT_L0; ENDM;
    mms(); ENDM; load(c0, c0, c2, hB, 39 + G); ENDL(6);
    gu64* py_ = flags + FL_YX / 8 + (b ^ 1) * 8 + G * 4 + wq;
    mms(); ENDM; POLL_ISSUE(pv, py_); load(c1, c0, c2, hA, 41 + G); ENDL(7);
    STAMP(20);
    mms(); ENDM; if (G == 0) load(c0, c0, c2, hB, 43); else load(c0, c0, c0, hB, 0);
    WAIT_V(G == 0 ? 12 : 6); POLL_PIN(pv); if (!__all(pv == tag)) wait_flags<1>(py_, tag, tmo, lane); SB;
    u32x4 py[4], xr[2];
    const unsigned vout = (rt * 128 + wi * 64 + 2 * G * 16 + tok) * 2048 + ct * 256 + kh * 128 + jj * 64 + (((g & 1) << 1) | (g >> 1)) * 16;
    const __amdgpu_buffer_rsrc_t rx = mk_rsrc(P.x, S_ * D_ * 2), rout = mk_rsrc(P.out, S_ * D_ * 2);
#pragma unroll
    for (int i = 0; i < 4; ++i) py[i] = ld16_sc1(ryx, lane * 16, (b ^ 1) * 32768 + (G * 4 + wq) * 4096 + i * 1024);
#pragma unroll
    for (int ml = 0; ml < 2; ++ml) xr[ml] = __builtin_amdgcn_raw_buffer_load_b128(rx, vout, ml * 16 * 2048, 0);
    SB; if (G == 0) ENDL(12); else ENDL(6);
    mms(); ENDM; load(c1, c0, c0, hA, 0); if (G == 0) WAIT_V(6); WAIT_L0; ENDM;
    mms(); MFMA_PAD; STAMP(21);
    if (G == 0) ENDM;
#pragma unroll
    for (int i = 0; i < 4; ++i) *(LAS f32x4*)(size_t)(exk + G * 16384 + i * 1024) = as[2 * (G ^ 1) + (i >> 1)][i & 1];
    WAIT_L0; ENDM; STAMP(22);
#pragma unroll
    for (int ml = 0; ml < 2; ++ml) {
      const u32x4 xs = swap16(xr[ml][0], xr[ml][1], xr[ml][2], xr[ml][3]); unsigned ow[4];
#pragma unroll
      for (int n = 0; n < 2; ++n) {
        const f32x4 y = acc[2 * G + ml][n] + __builtin_bit_cast(f32x4, py[ml * 2 + n]);
        const f32x4 s = as[2 * G + ml][n] + *(const LAS f32x4*)(size_t)(exk + (G ^ 1) * 16384 + (ml * 2 + n) * 1024);
        const f32x4 xv = {bflo(xs[2 * n]), bfhi(xs[2 * n]), bflo(xs[2 * n + 1]), bfhi(xs[2 * n + 1])}; f32x4 o;
#pragma unroll
        for (int r = 0; r < 4; ++r) o[r] = xv[r] + s[r] * (y[r] * rcp(1.0f + ex2(-LOG2E * y[r])));
        ow[2 * n] = pk2(o[0], o[1]); ow[2 * n + 1] = pk2(o[2], o[3]); }
      st16(swap16(ow[0], ow[1], ow[2], ow[3]), rout, vout, ml * 16 * 2048); }
    if (G == 0 && b == 0 && wq == 0) set_flag(flags + FL_EPOCH / 8, tag, lane);
    STAMP(23);
  }
};
#define P2RUN(p2, P, tag, of) p2.run(P, tag, of)

__global__ __launch_bounds__(512, 2) void k_fused(const u16* x, const u16* wqkv, const u16* wphi, const u16* wo, const u16* w1, const u16* b1, const u16* w2, char* ws, u16* out) {
  __shared__ __attribute__((aligned(16))) char smem[163840];
  const Ptrs P = {x, wqkv, wphi, wo, w1, b1, w2, ws, out};
  const unsigned lb = (unsigned)(size_t)(LAS char*)smem;
  const int tid = threadIdx.x, lane = tid & 63, w = __builtin_amdgcn_readfirstlane(tid >> 6), wx = w & 3;
  const int b = blockIdx.x, h = (b & 7) * 4 + ((b >> 3) & 3), p = b >> 5, odd = h & 1;
  const int tok = lane & 15, g = lane >> 4, q = (lane >> 2) & 3, pp = lane & 3;
  const unsigned ra0 = lb + L1_A + (wx * 64 + tok) * 128 + ((g ^ (tok >> 1)) << 4), ra1 = ra0 ^ 64;
  gu64* flags = (gu64*)(ws + WS_FLAG); gu64* tmo = flags + FL_TMO / 8;
  STAMP_DECL; STAMP(0);

  if (w < 4) {
    u64 tag;
    { const u64 ep = __hip_atomic_load(flags + FL_EPOCH / 8, RLX_AGENT);
      const int row = p * 256 + h * 8 + wx * 2; const GAS u32x4* src = (const GAS u32x4*)((const GAS u16*)x + row * D_);
      u32x4 a[4];
#pragma unroll
      for (int i = 0; i < 4; ++i) a[i] = src[i * 64 + lane];
      float v[2][16], s[2], sq[2];
#pragma unroll
      for (int r = 0; r < 2; ++r) { s[r] = 0.f;
#pragma unroll
        for (int i = 0; i < 8; ++i) { const unsigned u = a[2 * r + (i >> 2)][i & 3]; v[r][2 * i] = bflo(u); v[r][2 * i + 1] = bfhi(u); s[r] += v[r][2 * i] + v[r][2 * i + 1]; } }
#pragma unroll
      for (int r = 0; r < 2; ++r) { const float mean = wave_sum(s[r]) * (1.0f / D_); sq[r] = 0.f;
#pragma unroll
        for (int i = 0; i < 16; ++i) { v[r][i] -= mean; sq[r] += v[r][i] * v[r][i]; } }
      const __amdgpu_buffer_rsrc_t rxn = mk_rsrc(ws + WS_XN, S_ * D_ * 2);
#pragma unroll
      for (int r = 0; r < 2; ++r) { const float rstd = __builtin_amdgcn_rsqf(wave_sum(sq[r]) * (1.0f / D_) + 1e-5f);
#pragma unroll
        for (int c = 0; c < 2; ++c) { u32x4 o;
#pragma unroll
          for (int i = 0; i < 4; ++i) o[i] = pk2(v[r][8 * c + 2 * i] * rstd, v[r][8 * c + 2 * i + 1] * rstd);
          st16_sc1(o, rxn, lane * 16, (row + r) * 2048 + c * 1024); } }
      tag = (((u64)(unsigned)__builtin_amdgcn_readfirstlane((unsigned)(ep >> 32)) << 32) | (unsigned)__builtin_amdgcn_readfirstlane((unsigned)ep)) + 1;
      if (tid == 0) { *(LAS unsigned*)(size_t)(lb + L1_CNT) = 0u; *(LAS u64*)(size_t)(lb + L1_CNT + 8) = tag; }
      STAMP(1); WAIT_V(0); set_flag(flags + FL_XN / 8 + p * 128 + h * 4 + wx, tag, lane); STAMP(2);
      wait_flags<128>(flags + FL_XN / 8 + p * 128, tag, tmo, lane); STAMP(3); }
    const u32x4 sa = mk_srd(ws + WS_XN, S_ * D_ * 2);
    unsigned va0, va1;
    { const int r = lane >> 3; va0 = (p * 256 + wx * 64 + r) * 2048 + (((lane & 7) ^ (r >> 1)) << 4); va1 = va0 ^ 64; }
    const unsigned sl = lb + L1_A + wx * 8192;
    const unsigned rb0 = opq(lb + L1_BH + (8 * g + 4 * (g & 1) + q) * 224 + 8 * pp), rb1 = opq(lb + L1_BH + (8 * g + 4 * ((g & 1) ^ 1) + q) * 224 + 8 * pp);
    f32x4 acc[4][7];
#pragma unroll
    for (int m = 0; m < 4; ++m)
#pragma unroll
      for (int j = 0; j < 7; ++j) acc[m][j] = zacc();
    bf16x8 fa[2][4], fb[2][7];
    auto dmaA = [&](auto ST, auto J, unsigned sk) { constexpr int st = ST(), j = J(); if (j & 1) DMA_SC1(sa, va1, sk, j * 16384, sl, st * 32768 + j * 1024); else DMA_SC1(sa, va0, sk, j * 16384, sl, st * 32768 + j * 1024); };
    auto rdA = [&](auto ST, auto KS, auto M) { constexpr int st = ST(), ks = KS(), m = M(); fa[ks][m] = lds128((ks ? ra1 : ra0) + st * 32768 + m * 2048); SB; };
    auto rdB = [&](auto ST, auto KS, auto J) { constexpr int st = ST(), ks = KS(), j = J(); constexpr int o = st * 14336 + ks * 7168 + j * 32; fb[ks][j] = cat(ldstr(rb0 + o), ldstr(rb1 + o)); SB; };
    auto load = [&](auto ST, auto DO, unsigned sk) {
      constexpr int o = ST() ^ 1; constexpr bool d = DO();
      rdA(ST, IC<0>{}, IC<0>{}); rdB(ST, IC<0>{}, IC<0>{}); rdB(ST, IC<0>{}, IC<1>{}); if (d) dmaA(IC<o>{}, IC<0>{}, sk);
      rdA(ST, IC<0>{}, IC<1>{}); rdB(ST, IC<0>{}, IC<2>{}); rdB(ST, IC<0>{}, IC<3>{}); if (d) dmaA(IC<o>{}, IC<1>{}, sk);
      rdA(ST, IC<0>{}, IC<2>{}); rdB(ST, IC<0>{}, IC<4>{}); rdB(ST, IC<0>{}, IC<5>{}); if (d) dmaA(IC<o>{}, IC<2>{}, sk);
      rdA(ST, IC<0>{}, IC<3>{}); rdB(ST, IC<0>{}, IC<6>{}); rdB(ST, IC<1>{}, IC<0>{}); if (d) dmaA(IC<o>{}, IC<3>{}, sk);
      rdA(ST, IC<1>{}, IC<0>{}); rdB(ST, IC<1>{}, IC<1>{}); rdB(ST, IC<1>{}, IC<2>{}); if (d) dmaA(IC<o>{}, IC<4>{}, sk);
      rdA(ST, IC<1>{}, IC<1>{}); rdB(ST, IC<1>{}, IC<3>{}); rdB(ST, IC<1>{}, IC<4>{}); if (d) dmaA(IC<o>{}, IC<5>{}, sk);
      rdA(ST, IC<1>{}, IC<2>{}); rdB(ST, IC<1>{}, IC<5>{}); if (d) dmaA(IC<o>{}, IC<6>{}, sk);
      rdA(ST, IC<1>{}, IC<3>{}); rdB(ST, IC<1>{}, IC<6>{}); if (d) dmaA(IC<o>{}, IC<7>{}, sk);
    };
    auto mm = [&]() {
#pragma unroll
      for (int m = 0; m < 4; ++m)
#pragma unroll
        for (int j = 0; j < 7; ++j) mfma_k2(acc[m][j], fb[0][j], fa[0][m], fb[1][j], fa[1][m]);
      SB; };
#define A8(ST, sk) dmaA(IC<ST>{}, IC<0>{}, sk); dmaA(IC<ST>{}, IC<1>{}, sk); dmaA(IC<ST>{}, IC<2>{}, sk); dmaA(IC<ST>{}, IC<3>{}, sk); dmaA(IC<ST>{}, IC<4>{}, sk); dmaA(IC<ST>{}, IC<5>{}, sk); dmaA(IC<ST>{}, IC<6>{}, sk); dmaA(IC<ST>{}, IC<7>{}, sk)
    A8(0, 0u); A8(1, 128u);
    WAIT_V(8); WAIT_L0; BAR; SB; STAMP(4);
    load(IC<0>{}, IC<0>{}, 0u); WAIT_L0; BAR; SB; STAMP(5);
    for (int t = 0; t < 14; t += 2) {
      mm(); WAIT_V(0); BAR; SB;
      load(IC<1>{}, IC<1>{}, (unsigned)(t + 2) * 128u); WAIT_L0; BAR; SB;
      mm(); WAIT_V(0); BAR; SB;
      load(IC<0>{}, IC<1>{}, (unsigned)(t + 3) * 128u); WAIT_L0; BAR; SB;
    }
    STAMP(6);
    mm(); WAIT_V(0); BAR; SB;
    load(IC<1>{}, IC<0>{}, 0u);
    u32x2 bias[7];
#pragma unroll
    for (int j = 0; j < 7; ++j) { const int cb = 7 * h + (j < 6 ? j + odd : (odd ? 0 : 6)); bias[j] = *(const GAS u32x2*)((const GAS u16*)b1 + cb * 16 + 4 * g); }
    WAIT_L0; BAR; SB;
    mm(); BAR; MFMA_PAD; STAMP(7);
    f32x4 bl[7];
#pragma unroll
    for (int j = 0; j < 7; ++j) { bl[j] = (f32x4){bflo(bias[j][0]), bfhi(bias[j][0]), bflo(bias[j][1]), bfhi(bias[j][1])} * LOG2E; asm volatile("" : "+v"(bl[j])); }
    SB;
    Phase2<0> p2; p2.init(lb, lane, wx, P);
    p2.dma4<0, 4>(); p2.dma4<1, 4>(); p2.dma4<2, 4>();
    { const unsigned dump = lb + L1_DUMP; const int cu = b >> 3, a = (b & 7) >> 1;
      const int l2 = min(lane, 55) + 56 * (wx + 4 * cu);
      TOUCH((const GAS char*)w2 + ((b & 1) * 1792 + a * 448) * 2048 + l2 * 128, dump);
      TOUCH((const GAS char*)wo + (b * 64 + wx * 16 + (lane & 15)) * 128, dump); }
    STAMP(8);
    const __amdgpu_buffer_rsrc_t ract = mk_rsrc(ws + WS_ACT, 128 * 112 * 1024);
    auto mish4 = [&](f32x4 a, f32x4 bb) -> u32x2 {
      const f32x4 hs = a * LOG2E + bb; f32x4 e, r;
#pragma unroll
      for (int i = 0; i < 4; ++i) e[i] = ex2(hs[i]);
      const f32x4 d = e * (e + 2.0f) + 2.0f;
#pragma unroll
      for (int i = 0; i < 4; ++i) r[i] = rcp(d[i]);
      const f32x4 o = hs * (r * (-2.0f / LOG2E) + 1.0f / LOG2E); return (u32x2){pk2(o[0], o[1]), pk2(o[2], o[3])}; };
    const unsigned vpair = (unsigned)((((g & 1) << 1) | (g >> 1)) * 256 + tok * 16), mb0 = p * 16 + wx * 4;
#pragma unroll
    for (int m = 0; m < 4; ++m)
#pragma unroll
      for (int j = 0; j < 6; j += 2) { const u32x2 a = mish4(acc[m][j], bl[j]), c = mish4(acc[m][j + 1], bl[j + 1]);
        st16_sc1(swap16(a[0], a[1], c[0], c[1]), ract, vpair, ((mb0 + m) * 112 + ((7 * h + odd + j) >> 1)) * 1024); }
    const unsigned vsing = (unsigned)((g & 1) * 112 * 1024 + (2 * odd + (g >> 1)) * 256 + tok * 16);
#pragma unroll
    for (int m = 0; m < 4; m += 2) { const u32x2 a = mish4(acc[m][6], bl[6]), c = mish4(acc[m + 1][6], bl[6]);
      st16_sc1(swap16(a[0], a[1], c[0], c[1]), ract, vsing, ((mb0 + m) * 112 + ((7 * h + (odd ? 0 : 6)) >> 1)) * 1024); }
    STAMP(9); WAIT_V(0); STAMP(10);
    set_flag(flags + FL_ACT / 8 + ((2 * p + (wx >> 1)) * 2 + (h >> 4)) * 32 + (h & 15) * 2 + (wx & 1), tag, lane);
    wait_flags<32>(flags + FL_ACT / 8 + (p2.rt * 2 + p2.kh) * 32, tag, tmo, lane); SB; STAMP(11);
    p2.dma4<0, 0>(); p2.dma4<1, 0>(); p2.dma4<2, 0>();
    P2RUN(p2, P, tag, nullptr);
  } else {
    const u32x4 sq = mk_srd(wqkv, D_ * 3 * D_ * 2), sh = mk_srd(w1, D_ * F_ * 2), sp = mk_srd(wphi, 32 * 32 * 32 * 2);
    unsigned vq, vh[4];
    { const int r = lane >> 2; vq = (wx * 16 + r) * 6144 + h * 64 + (((lane & 3) ^ (((r >> 3) & 1) << 1)) << 4); }
#pragma unroll
    for (int i = 0; i < 4; ++i) { const int o = (wx + 4 * i) * 1024 + lane * 16, rp = o / 224, c = (o - rp * 224) >> 4, k = rp ^ (((rp >> 3) & 1) << 2); int cs = c + 2 * odd; if (cs >= 14) cs -= 14;
      vh[i] = k * 7168 + h * 224 + cs * 16; }
    const unsigned slq = lb + L1_BQ + wx * 1024, slh = lb + L1_BH + wx * 1024;
    const unsigned rq0 = opq(lb + L1_BQ + (8 * g + q) * 64 + 32 * (g & 1) + 8 * pp), rq1 = opq(rq0 ^ 32);
    auto touch_w = [&](int t0) { const int l = min(lane, 51), ts = l / 26, rem = l - ts * 26, rr = rem / 13, li = rem - rr * 13, k = (t0 + ts) * 64 + (b >> 3) * 2 + rr;
      const GAS char* ptr = li < 6 ? (const GAS char*)wqkv + k * 6144 + (li >> 1) * 2048 + (b & 7) * 256 + (li & 1) * 128 : (const GAS char*)w1 + k * 7168 + (b & 7) * 896 + (li - 6) * 128;
      TOUCH(ptr, lb + L1_DUMP); };
    f32x4 acc[3][4][2];
#pragma unroll
    for (int u = 0; u < 3; ++u)
#pragma unroll
      for (int m = 0; m < 4; ++m) { acc[u][m][0] = zacc(); acc[u][m][1] = zacc(); }
    bf16x8 fa[2][4], fb[2][6];
    auto dmaQ = [&](auto ST, auto U, unsigned sk) { constexpr int st = ST(), u = U(); DMA(sq, vq, sk, u * 2048, slq, st * 12288 + u * 4096); };
    auto dmaH = [&](auto ST, auto I, unsigned sk) { constexpr int st = ST(), i = I(); if (i < 3 || wx < 2) DMA(sh, vh[i], sk, 0, slh, st * 14336 + i * 4096); };
    auto rdA = [&](auto ST, auto KS, auto M) { constexpr int st = ST(), ks = KS(), m = M(); fa[ks][m] = lds128((ks ? ra1 : ra0) + st * 32768 + m * 2048); SB; };
    auto rdB = [&](auto ST, auto KS, auto J) { constexpr int st = ST(), ks = KS(), j = J(); constexpr int o = st * 12288 + (j >> 1) * 4096 + ks * 2048; const unsigned a = (j & 1) ? rq1 : rq0;
      fb[ks][j] = cat(ldstr(a + o), ldstr(a + o + 256)); SB; };
    auto load = [&](auto ST, auto DQ, auto DH, int t) {
      constexpr int o = ST() ^ 1; constexpr bool dq = DQ(), dh = DH(); const unsigned skq = (unsigned)(t + 1) * (64 * 6144), skh = (unsigned)(t + 2) * (64 * 7168);
      rdA(ST, IC<0>{}, IC<0>{}); rdB(ST, IC<0>{}, IC<0>{}); rdB(ST, IC<0>{}, IC<1>{}); if (dq) dmaQ(IC<o>{}, IC<0>{}, skq);
      rdA(ST, IC<0>{}, IC<1>{}); rdB(ST, IC<0>{}, IC<2>{}); rdB(ST, IC<0>{}, IC<3>{}); if (dq) dmaQ(IC<o>{}, IC<1>{}, skq);
      rdA(ST, IC<0>{}, IC<2>{}); rdB(ST, IC<0>{}, IC<4>{}); rdB(ST, IC<0>{}, IC<5>{}); if (dq) dmaQ(IC<o>{}, IC<2>{}, skq);
      rdA(ST, IC<0>{}, IC<3>{}); rdB(ST, IC<1>{}, IC<0>{}); if (dh) dmaH(ST, IC<0>{}, skh);
      rdA(ST, IC<1>{}, IC<0>{}); rdB(ST, IC<1>{}, IC<1>{}); rdB(ST, IC<1>{}, IC<2>{}); if (dh) dmaH(ST, IC<1>{}, skh);
      rdA(ST, IC<1>{}, IC<1>{}); rdB(ST, IC<1>{}, IC<3>{}); if (dh) dmaH(ST, IC<2>{}, skh);
      rdA(ST, IC<1>{}, IC<2>{}); rdB(ST, IC<1>{}, IC<4>{});
      rdA(ST, IC<1>{}, IC<3>{}); rdB(ST, IC<1>{}, IC<5>{});
      if (dh) { if (wx < 2) { dmaH(ST, IC<3>{}, skh); if (dq) WAIT_V(7); } else if (dq) WAIT_V(6); }
    };
    auto mm = [&]() {
#pragma unroll
      for (int m = 0; m < 4; ++m)
#pragma unroll
        for (int j = 0; j < 6; ++j) mfma_k2(acc[j >> 1][m][j & 1], fb[0][j], fa[0][m], fb[1][j], fa[1][m]);
      SB; };
#define H4(ST, sk) dmaH(IC<ST>{}, IC<0>{}, sk); dmaH(IC<ST>{}, IC<1>{}, sk); dmaH(IC<ST>{}, IC<2>{}, sk); dmaH(IC<ST>{}, IC<3>{}, sk)
#define Q3(ST, sk) dmaQ(IC<ST>{}, IC<0>{}, sk); dmaQ(IC<ST>{}, IC<1>{}, sk); dmaQ(IC<ST>{}, IC<2>{}, sk)
    H4(0, 0u); Q3(0, 0u); H4(1, 64u * 7168u); Q3(1, 64u * 6144u);
    if (wx < 2) { const unsigned vp = h * 2048 + wx * 1024 + lane * 16, slp = lb + L1_WPHI + wx * 1024; DMA(sp, vp, 0u, 0, slp, 0); }
    if (wx < 3) touch_w(2 + 2 * wx);
    STAMP(1); WAIT_V(0); STAMP(2); BAR; SB; STAMP(4);
    touch_w(8 + 2 * wx);
    BAR; SB; STAMP(5);
    load(IC<0>{}, IC<0>{}, IC<1>{}, 0); WAIT_L0; BAR; SB;
    mm(); BAR; SB;
    load(IC<1>{}, IC<1>{}, IC<1>{}, 1); WAIT_L0; BAR; SB;
    mm(); WAIT_V(3); BAR; SB;
    for (int t = 2; t < 14; t += 2) {
      load(IC<0>{}, IC<1>{}, IC<1>{}, t); WAIT_L0; BAR; SB;
      mm(); WAIT_V(3); BAR; SB;
      load(IC<1>{}, IC<1>{}, IC<1>{}, t + 1); WAIT_L0; BAR; SB;
      mm(); WAIT_V(3); BAR; SB;
    }
    STAMP(6);
    load(IC<0>{}, IC<1>{}, IC<0>{}, 14); WAIT_V(3); WAIT_L0; BAR; SB;
    mm(); WAIT_V(0); BAR; SB;
    load(IC<1>{}, IC<0>{}, IC<0>{}, 15); WAIT_L0; BAR; SB;
    __builtin_amdgcn_s_setprio(1);
    mm(); MFMA_PAD; STAMP(7);
    bf16x8 wp[2];
#pragma unroll
    for (int ff = 0; ff < 2; ++ff) { const unsigned a = lb + L1_WPHI + (4 * g + q) * 64 + ff * 32 + 8 * pp; wp[ff] = cat(ldstr(a), ldstr(a + 1024)); }
    u64 tag; { const u64 tv = *(const LAS u64*)(size_t)(lb + L1_CNT + 8); tag = ((u64)(unsigned)__builtin_amdgcn_readfirstlane((unsigned)(tv >> 32)) << 32) | (unsigned)__builtin_amdgcn_readfirstlane((unsigned)tv); }
    const f32x4 zero4 = {0.f, 0.f, 0.f, 0.f};
    auto fmax_ = [](float a, float c) { return fmaxf(a, c); }; auto fmin_ = [](float a, float c) { return fminf(a, c); }; auto fadd_ = [](float a, float c) { return a + c; };
    auto expo = [&](const f32x4& a0, const f32x4& a1, f32x4 (&ep)[2], f32x4 (&en)[2]) {
      const bf16x8 kb = pk8(a0, a1); const f32x4 p0 = mfma(wp[0], kb, zero4), p1 = mfma(wp[1], kb, zero4);
      float mx = fmaxf(fmaxf(fmaxf(p0[0], p0[1]), fmaxf(p0[2], p0[3])), fmaxf(fmaxf(p1[0], p1[1]), fmaxf(p1[2], p1[3])));
      float mn = fminf(fminf(fminf(p0[0], p0[1]), fminf(p0[2], p0[3])), fminf(fminf(p1[0], p1[1]), fminf(p1[2], p1[3])));
      mx = red4(mx, fmax_) * LOG2E; mn = red4(mn, fmin_) * LOG2E;
      const f32x4 u0 = p0 * LOG2E - mx, u1 = p1 * LOG2E - mx, v0 = mn - p0 * LOG2E, v1 = mn - p1 * LOG2E;
#pragma unroll
      for (int r = 0; r < 4; ++r) { ep[0][r] = ex2(u0[r]); ep[1][r] = ex2(u1[r]); en[0][r] = ex2(v0[r]); en[1][r] = ex2(v1[r]); } };
    const unsigned swp = (tok & 1) | (((tok >> 2) & 1) << 1) | (((tok >> 1) & 1) << 2) | (tok & 8);
#pragma unroll
    for (int m = 0; m < 4; ++m) {
      f32x4 ep[2], en[2]; expo(acc[1][m][0], acc[1][m][1], ep, en);
      const f32x4 sp4 = ep[0] + ep[1], sn4 = en[0] + en[1];
      const float rp_ = rcp(red4((sp4[0] + sp4[1]) + (sp4[2] + sp4[3]), fadd_)), rn_ = rcp(red4((sn4[0] + sn4[1]) + (sn4[2] + sn4[3]), fadd_));
      const unsigned row = lb + L1_PHI + (wx * 64 + m * 16 + tok) * 128;
#pragma unroll
      for (int ff = 0; ff < 2; ++ff) { const f32x4 a = ep[ff] * rp_, c = en[ff] * rn_;
        *(LAS u32x2*)(size_t)(row + (((4 * ff + g) ^ swp) << 3)) = (u32x2){pk2(a[0], a[1]), pk2(a[2], a[3])};
        *(LAS u32x2*)(size_t)(row + (((8 + 4 * ff + g) ^ swp) << 3)) = (u32x2){pk2(c[0], c[1]), pk2(c[2], c[3])}; }
      const unsigned vrow = lb + L1_V + (wx * 64 + m * 16 + tok) * 64;
#pragma unroll
      for (int df = 0; df < 2; ++df) { const f32x4 a = acc[2][m][df]; *(LAS u32x2*)(size_t)(vrow + (((4 * df + g) ^ (tok >> 1)) << 3)) = (u32x2){pk2(a[0], a[1]), pk2(a[2], a[3])}; }
    }
    STAMP(8); lds_sync(lb + L1_CNT, 4u, lane);
    f32x4 kv0 = zero4, kv1 = zero4, zz = zero4;
    { const bf16x8 ones = {0x3F80, 0x3F80, 0x3F80, 0x3F80, 0x3F80, 0x3F80, 0x3F80, 0x3F80};
      unsigned ap[2], av[2][2];
#pragma unroll
      for (int t = 0; t < 2; ++t) { const int tk = 8 * g + 4 * t + q, t15 = tk & 15; const unsigned sw = (t15 & 1) | (((t15 >> 2) & 1) << 1) | (((t15 >> 1) & 1) << 2) | (t15 & 8);
        ap[t] = lb + L1_PHI + tk * 128 + (((4 * wx + pp) ^ sw) << 3);
        av[t][0] = lb + L1_V + tk * 64 + ((pp ^ (t15 >> 1)) << 3); av[t][1] = av[t][0] ^ 32; }
      bf16x8 a[8], b0[8], b1_[8];
#pragma unroll
      for (int ks = 0; ks < 8; ++ks) { a[ks] = cat(ldstr(ap[0] + ks * 4096), ldstr(ap[1] + ks * 4096));
        b0[ks] = cat(ldstr(av[0][0] + ks * 2048), ldstr(av[1][0] + ks * 2048)); b1_[ks] = cat(ldstr(av[0][1] + ks * 2048), ldstr(av[1][1] + ks * 2048)); }
      SB;
#pragma unroll
      for (int ks = 0; ks < 8; ++ks) { kv0 = mfma(a[ks], b0[ks], kv0); kv1 = mfma(a[ks], b1_[ks], kv1); zz = mfma(a[ks], ones, zz); } }
    const __amdgpu_buffer_rsrc_t rkv = mk_rsrc(ws + WS_KVP, 32 * 8 * 8192), rz = mk_rsrc(ws + WS_ZP, 32 * 8 * 256);
    st16_sc1(__builtin_bit_cast(u32x4, kv0), rkv, lane * 16, (h * 8 + p) * 8192 + wx * 1024);
    st16_sc1(__builtin_bit_cast(u32x4, kv1), rkv, lane * 16, (h * 8 + p) * 8192 + 4096 + wx * 1024);
    if (tok == 0) st16_sc1(__builtin_bit_cast(u32x4, zz), rz, g * 16, (h * 8 + p) * 256 + wx * 64);
    __builtin_amdgcn_s_setprio(0);
    STAMP(9); WAIT_V(0); STAMP(10);
    gu64* fkv = flags + (FL_KV + (h * 4 + wx) * 128) / 8;
    set_flag(fkv + p, tag, lane);
    wait_flags<8>(fkv, tag, tmo, lane); STAMP(11);
    u32x4 t0[8], t1[8], tz[8];
#pragma unroll
    for (int i = 0; i < 8; ++i) { t0[i] = ld16_sc1(rkv, lane * 16, (h * 8 + i) * 8192 + wx * 1024); t1[i] = ld16_sc1(rkv, lane * 16, (h * 8 + i) * 8192 + 4096 + wx * 1024); tz[i] = ld16_sc1(rz, g * 16, (h * 8 + i) * 256 + wx * 64); }
    SB;
    bf16x8 eb[4][2];
#pragma unroll
    for (int m = 0; m < 4; ++m) { f32x4 ep[2], en[2]; expo(acc[0][m][0], acc[0][m][1], ep, en); eb[m][0] = pk8(ep[0], ep[1]); eb[m][1] = pk8(en[0], en[1]); }
    SB; STAMP(12);
    { f32x4 s0 = zero4, s1 = zero4, sz = zero4;
#pragma unroll
      for (int i = 0; i < 8; ++i) { s0 += __builtin_bit_cast(f32x4, t0[i]); s1 += __builtin_bit_cast(f32x4, t1[i]); sz += __builtin_bit_cast(f32x4, tz[i]); }
      const unsigned a = lb + L1_KVA + (wx >> 1) * 1024 + lane * 16 + (wx & 1) * 8;
      *(LAS u32x2*)(size_t)a = (u32x2){pk2(s0[0], s0[1]), pk2(s0[2], s0[3])};
      *(LAS u32x2*)(size_t)(a + 2048) = (u32x2){pk2(s1[0], s1[1]), pk2(s1[2], s1[3])};
      u32x2 au = {pk2(sz[0], sz[1]), pk2(sz[2], sz[3])};
      if ((lane & 3) == 1) au = (u32x2){0x3F803F80u, 0x3F803F80u}; else if ((lane & 3) != 0) au = (u32x2){0u, 0u};
      *(LAS u32x2*)(size_t)(a + 4096) = au; }
    lds_sync(lb + L1_CNT, 8u, lane); STAMP(13);
    bf16x8 ka[6];
#pragma unroll
    for (int i = 0; i < 6; ++i) ka[i] = lds128(lb + L1_KVA + i * 1024 + lane * 16);
    const __amdgpu_buffer_rsrc_t ro = mk_rsrc(ws + WS_O, 128 * 32 * 1024);
    const unsigned vpair = (unsigned)((((g & 1) << 1) | (g >> 1)) * 256 + tok * 16);
#pragma unroll
    for (int m = 0; m < 4; ++m) {
      const f32x4 ap_ = mfma(ka[4], eb[m][0], zero4), an_ = mfma(ka[5], eb[m][1], zero4);
      const f32x4 p0 = mfma(ka[0], eb[m][0], zero4), n0 = mfma(ka[1], eb[m][1], zero4), p1 = mfma(ka[2], eb[m][0], zero4), n1 = mfma(ka[3], eb[m][1], zero4);
      const float a = rcp(ap_[1]), c = rcp(an_[1]), inv = rcp(ap_[0] * a + an_[0] * c + 1e-6f), ca = a * inv, cc = c * inv;
      const f32x4 o0 = p0 * ca + n0 * cc, o1 = p1 * ca + n1 * cc;
      st16_sc1(swap16(pk2(o0[0], o0[1]), pk2(o0[2], o0[3]), pk2(o1[0], o1[1]), pk2(o1[2], o1[3])), ro, vpair, ((p * 16 + wx * 4 + m) * 32 + h) * 1024);
    }
    WAIT_L0; STAMP(14);
    Phase2<1> p2; p2.init(lb, lane, wx, P);
    P2RUN(p2, P, tag, flags + FL_O / 8 + (2 * p + (wx >> 1)) * 64 + h * 2 + (wx & 1));
  }
}
}

extern "C" void kernel_launch(void* const* d_in, const int* in_sizes, int n_in, void* d_out, int out_size, void* d_ws, size_t ws_size, hipStream_t stream) {
  k_fused<<<256, 512, 0, stream>>>((const u16*)d_in[0], (const u16*)d_in[1], (const u16*)d_in[2], (const u16*)d_in[3], (const u16*)d_in[5], (const u16*)d_in[6], (const u16*)d_in[7], (char*)d_ws, (u16*)d_out);
}
```

```cpp
#include <hip/hip_runtime.h>
#include <hip/hip_bf16.h>
#include <type_traits>

namespace {
#define LAS __attribute__((address_space(3)))
#define GAS __attribute__((address_space(1)))
typedef unsigned short u16;
typedef unsigned u32x4 __attribute__((ext_vector_type(4)));
typedef unsigned u32x2 __attribute__((ext_vector_type(2)));
typedef short bf16x8 __attribute__((ext_vector_type(8)));
typedef short s16x4 __attribute__((ext_vector_type(4)));
typedef float f32x4 __attribute__((ext_vector_type(4)));
typedef float f32x2 __attribute__((ext_vector_type(2)));
typedef __bf16 bf16x2_t __attribute__((ext_vector_type(2)));

constexpr int S_ = 2048, D_ = 1024, F_ = 3584;
constexpr size_t WS_FLAG = 0;
constexpr size_t WS_XN = 1u << 20;
constexpr size_t WS_ACT = WS_XN + (4u << 20);
constexpr size_t WS_O = WS_ACT + (15u << 20);
constexpr size_t WS_KVP = WS_O + (4u << 20);
constexpr size_t WS_ZP = WS_KVP + (2u << 20);
constexpr size_t WS_YX = WS_ZP + (1u << 20);
constexpr size_t WS_STAMP = 64u << 20;
#define STAMP_DECL
#define STAMP(i)
#define STAMP_OUT(wv)
constexpr unsigned FL_EPOCH = 0;
constexpr unsigned FL_O = 1024;
constexpr unsigned FL_YX = 16384;
constexpr unsigned FL_TMO = 61440;
constexpr unsigned GR_ST = 65536;
constexpr unsigned FL_KV = 131072;
constexpr unsigned FL_ACT = 262144;
constexpr unsigned GR_CS = 524288;
#define YPRIO 0
#define XP0 0
#define EARLYA 0
#define FLAGA 0
#define TOUCHW 1
constexpr float LOG2E = 1.4426950408889634f;

#define SB __builtin_amdgcn_sched_barrier(0)
#define BAR __builtin_amdgcn_s_barrier()
#define WAIT_L0 do { __builtin_amdgcn_s_waitcnt(0xC07F); SB; } while (0)
#define WAIT_V(n) asm volatile("s_waitcnt vmcnt(%0)" :: "i"(n) : "memory")
template <int N> using IC = std::integral_constant<int, N>;

__device__ __forceinline__ u32x4 mk_srd(const void* p, unsigned bytes) {
  const unsigned long long a = (unsigned long long)p; u32x4 r;
  r.x = __builtin_amdgcn_readfirstlane((unsigned)a); r.y = __builtin_amdgcn_readfirstlane((unsigned)(a >> 32)) & 0xffffu; r.z = bytes; r.w = 0x00020000u; return r; }
#define DMA(srd, voff, sk, KOFF, sl, LOFF) do { unsigned _t; asm volatile("s_add_u32 m0, %4, %5\n\ts_add_u32 %0, %3, %6\n\tbuffer_load_dwordx4 %1, %2, %0 offen lds" \
    : "=&s"(_t) : "v"(voff), "s"(srd), "s"(sk), "s"(sl), "i"(LOFF), "i"(KOFF) : "memory", "scc"); SB; } while (0)
#define DMA_SC1(srd, voff, sk, KOFF, sl, LOFF) do { unsigned _t; asm volatile("s_add_u32 m0, %4, %5\n\ts_add_u32 %0, %3, %6\n\tbuffer_load_dwordx4 %1, %2, %0 offen sc1 lds" \
    : "=&s"(_t) : "v"(voff), "s"(srd), "s"(sk), "s"(sl), "i"(LOFF), "i"(KOFF) : "memory", "scc"); SB; } while (0)
#define TOUCH(ptr, sl) do { asm volatile("s_mov_b32 m0, %1\n\ts_nop 0\n\tglobal_load_lds_dword %0, off" :: "v"(ptr), "s"(sl) : "memory"); SB; } while (0)
__device__ __forceinline__ bf16x8 lds128(unsigned a) { return *(const LAS bf16x8*)(size_t)a; }
__device__ __forceinline__ s16x4 ldstr(unsigned a) { return __builtin_amdgcn_ds_read_tr16_b64_v4i16((LAS s16x4*)(size_t)a); }
__device__ __forceinline__ bf16x8 cat(s16x4 lo, s16x4 hi) { return __builtin_shufflevector(lo, hi, 0, 1, 2, 3, 4, 5, 6, 7); }
__device__ __forceinline__ unsigned opq(unsigned x) { asm volatile("" : "+v"(x)); return x; }
__device__ __forceinline__ float opqf(float x) { asm("" : "+v"(x)); return x; }
__device__ __forceinline__ f32x2 opq2(f32x2 x) { asm("" : "+v"(x)); return x; }
__device__ __forceinline__ f32x4 mfma(bf16x8 a, bf16x8 b, f32x4 c) { return __builtin_amdgcn_mfma_f32_16x16x32_bf16(a, b, c, 0, 0, 0); }
__device__ __forceinline__ void mfma_acc(f32x4& d, bf16x8 a, bf16x8 b) { asm volatile("v_mfma_f32_16x16x32_bf16 %0, %1, %2, %0" : "+v"(d) : "v"(a), "v"(b)); }
__device__ __forceinline__ void mfma_k2(f32x4& d, bf16x8 a0, bf16x8 b0, bf16x8 a1, bf16x8 b1) {
  asm volatile("v_mfma_f32_16x16x32_bf16 %0, %1, %2, %0\n\tv_mfma_f32_16x16x32_bf16 %0, %3, %4, %0" : "+v"(d) : "v"(a0), "v"(b0), "v"(a1), "v"(b1)); }
__device__ __forceinline__ void mfma_k2_first(f32x4& d, bf16x8 a0, bf16x8 b0, bf16x8 a1, bf16x8 b1) {
  asm volatile("v_mfma_f32_16x16x32_bf16 %0, %1, %2, 0\n\tv_mfma_f32_16x16x32_bf16 %0, %3, %4, %0" : "=&v"(d) : "v"(a0), "v"(b0), "v"(a1), "v"(b1)); }
template <bool FIRST> __device__ __forceinline__ void mk2(f32x4& d, bf16x8 a0, bf16x8 b0, bf16x8 a1, bf16x8 b1) { if (FIRST) mfma_k2_first(d, a0, b0, a1, b1); else mfma_k2(d, a0, b0, a1, b1); }
__device__ __forceinline__ void mfma_k2x2(f32x4& d, f32x4& e, bf16x8 a0, bf16x8 c0, bf16x8 b0, bf16x8 a1, bf16x8 c1, bf16x8 b1) {
  asm volatile("v_mfma_f32_16x16x32_bf16 %0, %2, %4, %0\n\tv_mfma_f32_16x16x32_bf16 %1, %3, %4, %1\n\tv_mfma_f32_16x16x32_bf16 %0, %5, %7, %0\n\tv_mfma_f32_16x16x32_bf16 %1, %6, %7, %1"
               : "+v"(d), "+v"(e) : "v"(a0), "v"(c0), "v"(b0), "v"(a1), "v"(c1), "v"(b1)); }
__device__ __forceinline__ f32x4 zacc() { f32x4 z = {0.f, 0.f, 0.f, 0.f}; asm volatile("" : "+v"(z)); return z; }
#define CS_M(d, a) "v_mfma_f32_16x16x32_bf16 " d ", " a ", %[o], " d "\n\t"
__device__ __forceinline__ void colsum7(f32x4& c0, f32x4& c1, const bf16x8 (&a)[7], const bf16x8 (&b)[7], bf16x8 ones, unsigned sel) {
  asm volatile("s_cmp_lt_u32 %[s], 2\n\ts_cbranch_scc1 .Lcs01_%=\n\ts_cmp_eq_u32 %[s], 2\n\ts_cbranch_scc1 .Lcs2_%=\n\ts_cmp_eq_u32 %[s], 3\n\ts_cbranch_scc0 .Lcs9_%=\n\t"
               CS_M("%0", "%[a3]") CS_M("%0", "%[b3]") "s_branch .Lcs9_%=\n"
               ".Lcs2_%=:\n\t" CS_M("%0", "%[a2]") CS_M("%0", "%[b2]") CS_M("%1", "%[a6]") CS_M("%1", "%[b6]") "s_branch .Lcs9_%=\n"
               ".Lcs01_%=:\n\ts_cmp_eq_u32 %[s], 0\n\ts_cbranch_scc1 .Lcs0_%=\n\t" CS_M("%0", "%[a1]") CS_M("%0", "%[b1]") CS_M("%1", "%[a5]") CS_M("%1", "%[b5]") "s_branch .Lcs9_%=\n"
               ".Lcs0_%=:\n\t" CS_M("%0", "%[a0]") CS_M("%0", "%[b0]") CS_M("%1", "%[a4]") CS_M("%1", "%[b4]") "\n.Lcs9_%=:"
               : "+v"(c0), "+v"(c1) : [a0] "v"(a[0]), [a1] "v"(a[1]), [a2] "v"(a[2]), [a3] "v"(a[3]), [a4] "v"(a[4]), [a5] "v"(a[5]), [a6] "v"(a[6]),
                 [b0] "v"(b[0]), [b1] "v"(b[1]), [b2] "v"(b[2]), [b3] "v"(b[3]), [b4] "v"(b[4]), [b5] "v"(b[5]), [b6] "v"(b[6]), [o] "v"(ones), [s] "s"(sel) : "scc");
}
#define MFMA_PAD do { asm volatile("s_nop 7\n\ts_nop 1" ::: "memory"); SB; } while (0)
__device__ __forceinline__ unsigned pk2(float lo, float hi) { const f32x2 v = {lo, hi}; return __builtin_bit_cast(unsigned, __builtin_convertvector(v, bf16x2_t)); }
__device__ __forceinline__ bf16x8 pk8(f32x4 a, f32x4 b) { const u32x4 u = {pk2(a[0], a[1]), pk2(a[2], a[3]), pk2(b[0], b[1]), pk2(b[2], b[3])}; return __builtin_bit_cast(bf16x8, u); }
__device__ __forceinline__ float bflo(unsigned u) { return __uint_as_float(u << 16); }
__device__ __forceinline__ float bfhi(unsigned u) { return __uint_as_float(u & 0xffff0000u); }
__device__ __forceinline__ float ex2(float x) { return __builtin_amdgcn_exp2f(x); }
__device__ __forceinline__ float rcp(float x) { return __builtin_amdgcn_rcpf(x); }
__device__ __forceinline__ u32x4 swap16(unsigned a0, unsigned a1, unsigned b0, unsigned b1) {
  const auto p = __builtin_amdgcn_permlane16_swap(a0, b0, false, false), q = __builtin_amdgcn_permlane16_swap(a1, b1, false, false);
  return (u32x4){p[0], q[0], p[1], q[1]}; }
__device__ __forceinline__ float vmax(float a, float b) { return __builtin_elementwise_maximum(a, b); }
__device__ __forceinline__ float vmax8(f32x4 a, f32x4 c) { return vmax(vmax(vmax(vmax(a[0], a[1]), a[2]), vmax(vmax(a[3], c[0]), c[1])), vmax(c[2], c[3])); }
__device__ __forceinline__ void sw32(float& a, float& b) { const auto p = __builtin_amdgcn_permlane32_swap(__float_as_uint(a), __float_as_uint(b), false, false); a = __uint_as_float(p[0]); b = __uint_as_float(p[1]); }
__device__ __forceinline__ void sw16(float& a, float& b) { const auto p = __builtin_amdgcn_permlane16_swap(__float_as_uint(a), __float_as_uint(b), false, false); a = __uint_as_float(p[0]); b = __uint_as_float(p[1]); }
template <class Op> __device__ __forceinline__ float gather4(float a0, float a1, float b0, float b1, Op op) { sw32(a0, a1); sw32(b0, b1); float c = op(a0, a1), d = op(b0, b1); sw16(c, d); return op(c, d); }
__device__ __forceinline__ void spread4(float d, float& a0, float& a1, float& b0, float& b1) { a0 = d; b0 = d; sw16(a0, b0); a1 = a0; b1 = b0; sw32(a0, a1); sw32(b0, b1); }
template <class Op> __device__ __forceinline__ float red4(float v, Op op) {
  const unsigned u = __float_as_uint(v); const auto p = __builtin_amdgcn_permlane32_swap(u, u, false, false);
  const float a = op(__uint_as_float(p[0]), __uint_as_float(p[1])); const unsigned ua = __float_as_uint(a);
  const auto q = __builtin_amdgcn_permlane16_swap(ua, ua, false, false); return op(__uint_as_float(q[0]), __uint_as_float(q[1])); }
__device__ __forceinline__ void st16_sc1(u32x4 v, __amdgpu_buffer_rsrc_t r, unsigned voff, unsigned soff) { __builtin_amdgcn_raw_buffer_store_b128(v, r, voff + soff, 0, 16); }
__device__ __forceinline__ void st16(u32x4 v, __amdgpu_buffer_rsrc_t r, unsigned voff, unsigned soff) { __builtin_amdgcn_raw_buffer_store_b128(v, r, voff + soff, 0, 0); }
__device__ __forceinline__ u32x4 ld16_sc1(__amdgpu_buffer_rsrc_t r, unsigned voff, unsigned soff) { return __builtin_amdgcn_raw_buffer_load_b128(r, voff, soff, 16); }
__device__ __forceinline__ __amdgpu_buffer_rsrc_t mk_rsrc(const void* p, unsigned bytes) { return __builtin_amdgcn_make_buffer_rsrc((void*)p, 0, bytes, 0x00020000); }
#define RLX_AGENT __ATOMIC_RELAXED, __HIP_MEMORY_SCOPE_AGENT
typedef unsigned long long u64;
typedef GAS u64 gu64;
__device__ __forceinline__ void set_flag(gu64* w, u64 tag, int lane) { if (lane == 0) __hip_atomic_store(w, tag, RLX_AGENT); }
template <int N, int STRIDE> __device__ __forceinline__ void wait_flags(gu64* w, u64 tag, gu64* tmo, int lane) {
#pragma nounroll
  for (unsigned spins = 0;; ++spins) {
    u64 v = tag; if (N >= 64 || lane < N) v = __hip_atomic_load(w + lane * STRIDE, RLX_AGENT);
    if (__all(v == tag)) break;
    if (spins > (1u << 16)) { if (lane == 0) __hip_atomic_store(tmo, tag, RLX_AGENT); break; }
    __builtin_amdgcn_s_sleep(2);
  }
  __builtin_amdgcn_fence(__ATOMIC_ACQUIRE, "wavefront");
}
__device__ __forceinline__ u64 uni64(u64 v) { return ((u64)(unsigned)__builtin_amdgcn_readfirstlane((unsigned)(v >> 32)) << 32) | (unsigned)__builtin_amdgcn_readfirstlane((unsigned)v); }
__device__ __forceinline__ void lds_wait(unsigned addr, unsigned target) {
  for (;;) { unsigned v; asm volatile("ds_read_b32 %0, %1\n\ts_waitcnt lgkmcnt(0)" : "=v"(v) : "v"(addr) : "memory");
    if ((unsigned)__builtin_amdgcn_readfirstlane(v) >= target) break; }
}
#define POLL_ISSUE(dst, ptr) do { asm volatile("global_load_dwordx2 %0, %1, off sc1" : "=v"(dst) : "v"(ptr) : "memory"); SB; } while (0)
#define LD16_ISSUE(dst, srd, voff, soff) do { asm volatile("buffer_load_dwordx4 %0, %1, %2, %3 offen sc1" : "=v"(dst) : "v"(voff), "s"(srd), "s"(soff) : "memory"); SB; } while (0)
#define POLL_PIN(dst) asm volatile("" : "+v"(dst))
__device__ __forceinline__ float add1(float a, float b) { float r; asm("v_add_f32 %0, %1, %2" : "=v"(r) : "v"(a), "v"(b)); return r; }
__device__ __forceinline__ float mul1(float a, float b) { float r; asm("v_mul_f32 %0, %1, %2" : "=v"(r) : "v"(a), "v"(b)); return r; }
__device__ __forceinline__ float fma1(float a, float b, float c) { float r; asm("v_fma_f32 %0, %1, %2, %3" : "=v"(r) : "v"(a), "v"(b), "v"(c)); return r; }
__device__ __forceinline__ f32x4 add4(f32x4 a, u32x4 b) { return (f32x4){add1(a[0], __uint_as_float(b[0])), add1(a[1], __uint_as_float(b[1])), add1(a[2], __uint_as_float(b[2])), add1(a[3], __uint_as_float(b[3]))}; }
__device__ __forceinline__ unsigned differ(unsigned a, unsigned b, unsigned t) { unsigned r; asm("v_bitop3_b32 %0, %1, %2, %3 bitop3:0x7e" : "=v"(r) : "v"(a), "v"(b), "s"(t)); return r; }
template <int C> __device__ __forceinline__ float dpp(float v) { return __int_as_float(__builtin_amdgcn_update_dpp(0, __float_as_int(v), C, 0xf, 0xf, true)); }
__device__ __forceinline__ float wave_sum(float v) { v += dpp<0x128>(v); v += dpp<0x124>(v); v += dpp<0x122>(v); v += dpp<0x121>(v); return red4(v, [](float a, float c) { return a + c; }); }
__device__ __forceinline__ void lds_sync(unsigned addr, unsigned target, int lane) {
  asm volatile("s_waitcnt lgkmcnt(0)" ::: "memory");
  if (lane == 0) { const unsigned one = 1; asm volatile("ds_add_u32 %0, %1" :: "v"(addr), "v"(one) : "memory"); }
  for (;;) { unsigned v; asm volatile("ds_read_b32 %0, %1\n\ts_waitcnt lgkmcnt(0)" : "=v"(v) : "v"(addr) : "memory");
    if ((unsigned)__builtin_amdgcn_readfirstlane(v) >= target) break; }
}

constexpr unsigned L1_A = 0;
constexpr unsigned L1_BQ = 65536;
constexpr unsigned L1_BH = 90112;
constexpr unsigned L1_WPHI = 118784;
constexpr unsigned L1_CNT = 120832;
constexpr unsigned L1_DUMP = 121088;
constexpr unsigned L1_KVA = 121856;
constexpr unsigned L1_CS = 128000;
constexpr unsigned L1_ST = 129024;
constexpr unsigned L1_V = 98304;
constexpr unsigned L1_PHI = 131072;
constexpr unsigned L2_EX = 131072;

struct Ptrs { const u16 *x, *wqkv, *wphi, *wo, *w1, *b1, *w2; char* ws; u16* out; };

template <int G> struct Phase2 {
  unsigned lb; int lane, wq, b, kh, rt, ct;
  u32x4 sact, so, sw2, swo;
  unsigned vaa, vao, vb0, vb1, vs0, vs1, sla, sls;
  struct Off { unsigned a0, a1, b; };
  __device__ __forceinline__ Off off_y(int U) const { const int s0 = kh * 56 + (U < 24 ? 7 * (U & 7) + (U >> 3) : 14 * U - 333), s1 = s0 + (U < 24 ? 4 : 7); return {s0 * 1024u, s1 * 1024u, (wq >> 1 ? s1 : s0) * 65536u}; }
  __device__ __forceinline__ Off off_s(int U) const { return {(U - 28) * 2048u, (U - 28) * 2048u + 1024u, (U - 28) * 131072u}; }
  __device__ __forceinline__ void init(unsigned lb_, int lane_, int wq_, const Ptrs& P) {
    lb = lb_; lane = lane_; wq = wq_; b = blockIdx.x; kh = b & 1; rt = ((b & 7) >> 1) * 4 + ((b >> 3) & 3); ct = b >> 5;
    sact = mk_srd(P.ws + WS_ACT, 128 * 112 * 1024); so = mk_srd(P.ws + WS_O, 128 * 32 * 1024); sw2 = mk_srd(P.w2, F_ * D_ * 2); swo = mk_srd(P.wo, D_ * D_ * 2);
    vaa = lane * 16 + (rt * 8 + wq * 2) * (112 * 1024); vao = lane * 16 + (rt * 8 + wq * 2) * (32 * 1024);
    { const int r = lane >> 4, c = lane & 15; vb0 = ((wq & 1) * 16 + r) * 2048 + ct * 256 + (((c >> 1) ^ r) << 5) + ((c & 1) << 4); vb1 = vb0 ^ 128; }
    { const int r = lane >> 3, c = lane & 7; vs0 = (wq * 16 + r) * 2048 + ct * 256 + kh * 128 + (((c >> 1) ^ ((r >> 1) & 1)) << 5) + ((c & 1) << 4); vs1 = vs0 ^ 64; }
    sla = lb + wq * 4096; sls = lb + wq * 2048;
  }
  template <int LO, bool Y, int I> __device__ __forceinline__ void dma(const Off& o) {
    if (I < 4) { const unsigned sk = (I & 1) ? o.a1 : o.a0; if (Y) DMA_SC1(sact, vaa, sk, (I >> 1) * 112 * 1024, sla, LO + I * 1024); else DMA_SC1(so, vao, sk, (I >> 1) * 32 * 1024, sla, LO + I * 1024); }
    else if (Y) { constexpr int j = I - 4; if (j >> 1) DMA(sw2, vb1, o.b, j * 8192, sla, LO + 16384 + j * 1024); else DMA(sw2, vb0, o.b, j * 8192, sla, LO + 16384 + j * 1024); }
    else if (I < 6) { constexpr int j = I - 4; if (j) DMA(swo, vs1, o.b, j * 16384, sls, LO + 16384 + j * 1024); else DMA(swo, vs0, o.b, j * 16384, sls, LO + 16384 + j * 1024); } }
  template <int U, int I> __device__ __forceinline__ void dma1() { dma<(U & 1) * 65536 + ((U >> 1) & 1) * 32768, true, I>(off_y(U)); }
  template <int U, int I0> __device__ __forceinline__ void dma4() { dma1<U, I0>(); dma1<U, I0 + 1>(); dma1<U, I0 + 2>(); dma1<U, I0 + 3>(); }

  __device__ __forceinline__ void run(const Ptrs& P, u64 tag, int p, int h, const bf16x8 (&eb)[4][2]) {
    char* ws = P.ws;
    const int tok = lane & 15, g = lane >> 4, q = (lane >> 2) & 3, pp = lane & 3, wi = wq >> 1, jj = wq & 1;
    const unsigned ra = opq(lb + G * 65536 + wi * 8192 + lane * 16);
    unsigned rb[4], rs[2];
#pragma unroll
    for (int n = 0; n < 4; ++n) { const int f = 4 * (kh ^ (n >> 1)) + 2 * jj + (n & 1); rb[n] = opq(lb + G * 65536 + 16384 + (8 * g + q) * 256 + ((f ^ (q | ((g & 1) << 2))) << 5) + 8 * pp); }
#pragma unroll
    for (int n = 0; n < 2; ++n) { const int f = 2 * jj + n; rs[n] = opq(lb + G * 65536 + 16384 + (8 * g + q) * 128 + ((f ^ ((q >> 1) | ((g & 1) << 1))) << 5) + 8 * pp); }
    f32x4 acc[4][4], as[4][2];
    bf16x8 fa[2][4], fb[2][4];
    auto rdA = [&](auto HR, auto I) { constexpr int i = I(); fa[i >> 2][i & 3] = lds128(ra + HR() * 32768 + (i & 3) * 2048 + (i >> 2) * 1024); SB; };
    auto rdB = [&](auto HR, auto Y, auto I) { constexpr int i = I(), ks = i >> 2, n = i & 3; constexpr bool y = Y();
      if (y) { constexpr int o = HR() * 32768 + ks * 8192; fb[ks][n] = cat(ldstr(rb[n] + o), ldstr(rb[n] + o + 1024)); SB; }
      else if (n < 2) { constexpr int o = HR() * 32768 + ks * 4096; fb[ks][n] = cat(ldstr(rs[n & 1] + o), ldstr(rs[n & 1] + o + 512)); SB; } };
    auto loadh = [&](auto HR, auto YR, auto MD, auto HD, int U, auto hook) { constexpr int md = MD(), LO = (G ^ 1) * 65536 + HD() * 32768; constexpr bool d = md != 0, y = md == 1;
      const Off o = y ? off_y(U) : off_s(U);
      rdA(HR, IC<0>{}); rdB(HR, YR, IC<0>{}); if (d) dma<LO, y, 0>(o); hook(IC<0>{}); SB;
      rdA(HR, IC<1>{}); rdB(HR, YR, IC<1>{}); if (d) dma<LO, y, 4>(o); hook(IC<1>{}); SB;
      rdA(HR, IC<2>{}); rdB(HR, YR, IC<2>{}); if (d) dma<LO, y, 1>(o); hook(IC<2>{}); SB;
      rdA(HR, IC<3>{}); rdB(HR, YR, IC<3>{}); if (d) dma<LO, y, 5>(o); hook(IC<3>{}); SB;
      rdA(HR, IC<4>{}); rdB(HR, YR, IC<4>{}); if (d) dma<LO, y, 2>(o); hook(IC<4>{}); SB;
      rdA(HR, IC<5>{}); rdB(HR, YR, IC<5>{}); if (d) dma<LO, y, 6>(o); hook(IC<5>{}); SB;
      rdA(HR, IC<6>{}); rdB(HR, YR, IC<6>{}); if (d) dma<LO, y, 3>(o); hook(IC<6>{}); SB;
      rdA(HR, IC<7>{}); rdB(HR, YR, IC<7>{}); hook(IC<7>{}); SB; if (d) dma<LO, y, 7>(o); };
    auto load = [&](auto HR, auto YR, auto MD, auto HD, int U) { loadh(HR, YR, MD, HD, U, [](auto) {}); };
    auto mmy_ = [&](auto F) {
#pragma unroll
      for (int m = 0; m < 4; ++m)
#pragma unroll
        for (int n = 0; n < 4; ++n) mk2<F()>(acc[m][n], fb[0][n], fa[0][m], fb[1][n], fa[1][m]);
      SB; };
    auto mms_ = [&](auto F) {
#pragma unroll
      for (int m = 0; m < 4; ++m)
#pragma unroll
        for (int n = 0; n < 2; ++n) mk2<F()>(as[m][n], fb[0][n], fa[0][m], fb[1][n], fa[1][m]);
      SB; };
    auto mmy = [&]() { mmy_(IC<0>{}); }; auto mms = [&]() { mms_(IC<0>{}); };
#define ENDL(n) do { WAIT_V(n); WAIT_L0; BAR; SB; } while (0)
#define ENDM do { BAR; SB; } while (0)
    IC<0> c0; IC<1> c1; IC<2> c2; IC<G> hA; IC<G ^ 1> hB;
    gu64* flags = (gu64*)(ws + WS_FLAG); gu64* tmo = flags + FL_TMO / 8;
    const __amdgpu_buffer_rsrc_t ryx = mk_rsrc(ws + WS_YX, 256 * 32768);
    const unsigned exk = lb + L2_EX + wq * 4096 + lane * 16, exs = lb + 24576 + (wq >> 1) * 32768 + (wq & 1) * 4096 + lane * 16;
    auto give = [&]() {
#pragma unroll
      for (int i = 0; i < 4; ++i) { *(LAS f32x4*)(size_t)(exk + G * 16384 + i * 1024) = acc[2 * (G ^ 1) + (i >> 1)][i & 1]; *(LAS f32x4*)(size_t)(exs + G * 65536 + i * 1024) = acc[2 * (G ^ 1) + (i >> 1)][2 + (i & 1)]; }
      SB; };
    auto take = [&]() {
#pragma unroll
      for (int i = 0; i < 4; ++i) st16_sc1(__builtin_bit_cast(u32x4, add4(acc[2 * G + (i >> 1)][2 + (i & 1)], *(const LAS u32x4*)(size_t)(exs + (G ^ 1) * 65536 + i * 1024))), ryx, lane * 16, b * 32768 + (G * 4 + wq) * 4096 + i * 1024);
#pragma unroll
      for (int i = 0; i < 4; ++i) { f32x4& k = acc[2 * G + (i >> 1)][i & 1]; k = add4(k, *(const LAS u32x4*)(size_t)(exk + (G ^ 1) * 16384 + i * 1024)); asm volatile("" : "+v"(k)); }
      SB; };
    u64 pv;
    if (G == 0) {
      WAIT_V(EARLYA ? 1 : 9); BAR; SB; STAMP(16);
      load(c0, c1, c1, c1, 3); ENDL(13);
      mmy_(c1); WAIT_V(8); ENDM; STAMP(17);
      load(c1, c1, c1, hA, 5); ENDL(8);
      mmy(); ENDM; load(c0, c1, c1, hB, 7); ENDL(8);
      for (int T = 4; T < 20; T += 4) {
        mmy(); ENDM; load(c1, c1, c1, hA, T + 5); ENDL(8);
        mmy(); ENDM; load(c0, c1, c1, hB, T + 7); ENDL(8);
      }
    } else {
      BAR; SB; STAMP(16); BAR; SB;
      load(c0, c1, c1, c0, 4); ENDL(8);
      mmy_(c1); ENDM; STAMP(17);
      gu64* fa2 = flags + (FL_ACT + 131072) / 8 + (rt * 2 + kh) * 512; gu64* fkv = flags + (FL_KV + (h * 4 + wq) * 1024) / 8;
      POLL_ISSUE(pv, lane < 32 ? fa2 + lane * 16 : fkv + (lane & 7) * 16);
      load(c1, c1, c1, hA, 6); ENDL(9); STAMP(25);
      mmy(); ENDM;
      WAIT_V(8); POLL_PIN(pv); if (!__all(pv == tag)) { wait_flags<32, 16>(fa2, tag, tmo, lane); wait_flags<8, 16>(fkv, tag, tmo, lane); } SB;
      const u32x4 skv = mk_srd(ws + WS_KVP, 3u << 20);
      u32x4 t[8], tz; LD16_ISSUE(tz, skv, h * 2048 + (lane & 7) * 256 + wq * 64 + g * 16, (unsigned)(WS_ZP - WS_KVP));
      { const unsigned vkv = h * 32768 + wq * 1024 + lane * 16;
#pragma unroll
        for (int i = 0; i < 8; ++i) LD16_ISSUE(t[i], skv, vkv, i * 4096u); }
      load(c0, c1, c1, hB, 8); ENDL(17); STAMP(26);
      mmy(); ENDM;
      WAIT_V(8); asm volatile("" : "+v"(t[0]), "+v"(t[1]), "+v"(t[2]), "+v"(t[3]), "+v"(t[4]), "+v"(t[5]), "+v"(t[6]), "+v"(t[7]), "+v"(tz)); SB;
      float sm[8];
      auto addp = [&](int i, int c) { sm[2 * c] = add1(sm[2 * c], bflo(t[i][c])); sm[2 * c + 1] = add1(sm[2 * c + 1], bfhi(t[i][c])); asm volatile("" : "+v"(sm[2 * c]), "+v"(sm[2 * c + 1])); };
      loadh(c1, c1, c1, hA, 10, [&](auto I) { constexpr int i = I();
        if (i == 0) { _Pragma("unroll") for (int c = 0; c < 4; ++c) { sm[2 * c] = bflo(t[0][c]); sm[2 * c + 1] = bfhi(t[0][c]); asm volatile("" : "+v"(sm[2 * c]), "+v"(sm[2 * c + 1])); } }
        else if (i < 7) { addp((i + 1) >> 1, 2 * (~i & 1)); addp((i + 1) >> 1, 2 * (~i & 1) + 1); }
        else { _Pragma("unroll") for (int r = 0; r < 4; ++r) { float z = __uint_as_float(tz[r]); z += dpp<0xB1>(z); z = opqf(z); z += dpp<0x4E>(z); z = opqf(z); z += dpp<0x141>(z); tz[r] = __float_as_uint(opqf(z)); } asm volatile("" : "+v"(tz)); } });
      ENDL(8); STAMP(27);
      mmy(); ENDM;
      const unsigned kva = opq(lb + L2_EX + lane * 16);
      loadh(c0, c1, c1, hB, 12, [&](auto I) { constexpr int i = I(); addp(4 + (i >> 1), 2 * (i & 1)); addp(4 + (i >> 1), 2 * (i & 1) + 1); });
      { const unsigned a = kva + (wq >> 1) * 1024 + (wq & 1) * 8;
        *(LAS u32x2*)(size_t)a = (u32x2){pk2(sm[0], sm[1]), pk2(sm[2], sm[3])}; *(LAS u32x2*)(size_t)(a + 2048) = (u32x2){pk2(sm[4], sm[5]), pk2(sm[6], sm[7])};
        u32x2 au = {pk2(__uint_as_float(tz[0]), __uint_as_float(tz[1])), pk2(__uint_as_float(tz[2]), __uint_as_float(tz[3]))};
        if ((lane & 3) == 1) au = (u32x2){0x3F803F80u, 0x3F803F80u}; else if ((lane & 3) != 0) au = (u32x2){0u, 0u};
        *(LAS u32x2*)(size_t)(a + 4096) = au; SB; }
      ENDL(8); STAMP(28);
      mmy(); ENDM;
      bf16x8 ka[6]; f32x4 r6[6]; float ca, cc, o8[8];
      const __amdgpu_buffer_rsrc_t ro = mk_rsrc(ws + WS_O, 128 * 32 * 1024);
      const unsigned vo = (unsigned)((((g & 1) << 1) | (g >> 1)) * 256 + tok * 16) + ((p * 16 + wq * 4) * 32 + h) * 1024;
      auto ostep = [&](int m, int i) { const f32x4 z4 = {0.f, 0.f, 0.f, 0.f};
        if (i == 1) { __builtin_amdgcn_s_setprio(1);
#pragma unroll
          for (int k = 5; k >= 0; --k) { r6[k] = mfma(ka[k], eb[m][k & 1], z4); asm volatile("" : "+v"(r6[k])); }
          __builtin_amdgcn_s_setprio(0); }
        if (i == 2) { ca = rcp(r6[4][1]); cc = rcp(r6[5][1]); asm volatile("" : "+v"(ca), "+v"(cc)); }
        if (i == 3) { const float inv = rcp(opqf(r6[4][0] * ca + 1e-6f) + r6[5][0] * cc); ca = opqf(ca * inv); cc = opqf(cc * inv); asm volatile("" : "+v"(ca), "+v"(cc)); }
        if (i == 4 || i == 5) { _Pragma("unroll") for (int r = 0; r < 4; ++r) { const int d = i - 4; o8[4 * d + r] = opqf(r6[2 * d][r] * ca) + r6[2 * d + 1][r] * cc; asm volatile("" : "+v"(o8[4 * d + r])); } }
        if (i == 6) st16_sc1(swap16(pk2(o8[0], o8[1]), pk2(o8[2], o8[3]), pk2(o8[4], o8[5]), pk2(o8[6], o8[7])), ro, vo, m * 32768); };
#pragma unroll
      for (int i = 0; i < 6; ++i) ka[i] = lds128(kva + i * 1024);
      SB;
      loadh(c1, c1, c1, hA, 14, [&](auto I) { ostep(0, I()); }); ENDL(9); STAMP(29);
      mmy(); ENDM; loadh(c0, c1, c1, hB, 16, [&](auto I) { ostep(1, I()); }); ENDL(9); STAMP(30);
      mmy(); ENDM; loadh(c1, c1, c1, hA, 18, [&](auto I) { ostep(2, I()); }); ENDL(9);
      mmy(); ENDM; loadh(c0, c1, c1, hB, 20, [&](auto I) { ostep(3, I()); }); ENDL(9); STAMP(31);
      mmy(); ENDM; load(c1, c1, c1, hA, 22); WAIT_V(8);
      set_flag(flags + FL_O / 8 + (2 * p + (wq >> 1)) * 64 + h * 2 + (wq & 1), tag, lane); WAIT_L0; ENDM;
      mmy(); ENDM; load(c0, c1, c1, hB, 24); ENDL(8);
    }
    STAMP(18);
    gu64* po = flags + FL_O / 8 + rt * 64;
    mmy(); ENDM; if (G == 1) POLL_ISSUE(pv, po + lane); load(c1, c1, c1, hA, 25 + G); ENDL(8 + G);
    mmy(); ENDM;
    if (G == 0) { load(c0, c1, c1, hB, 27); ENDL(8); }
    else { WAIT_V(8); POLL_PIN(pv); if (!__all(pv == tag)) wait_flags<64, 1>(po, tag, tmo, lane); SB; load(c0, c1, c2, hB, 28); ENDL(6); }
    mmy(); ENDM; load(c1, c1, c2, hA, 29 + G); ENDL(6);
    mmy(); ENDM; STAMP(19); load(c0, c0, c2, hB, 31 + G); give(); if (G == 1) { take(); ENDL(10); } else ENDL(6);
    mms_(c1); ENDM; load(c1, c0, c2, hA, 33 + G); if (G == 0) take(); ENDL(10);
    gu64* fy = flags + FL_YX / 8 + b * 8 + G * 4 + wq;
    mms(); ENDM; load(c0, c0, c2, hB, 35 + G); if (G == 0) { ENDL(10); } else { WAIT_V(6); set_flag(fy, tag, lane); WAIT_L0; ENDM; }
    mms(); ENDM; load(c1, c0, c2, hA, 37 + G); WAIT_V(6); if (G == 0) set_flag(fy, tag, lane); WAIT_L0; ENDM;
    mms(); ENDM; load(c0, c0, c2, hB, 39 + G); ENDL(6);
    gu64* py_ = flags + FL_YX / 8 + (b ^ 1) * 8 + G * 4 + wq;
    mms(); ENDM; POLL_ISSUE(pv, py_); load(c1, c0, c2, hA, 41 + G); ENDL(7);
    STAMP(20);
    mms(); ENDM; if (G == 0) load(c0, c0, c2, hB, 43); else load(c0, c0, c0, hB, 0);
    WAIT_V(G == 0 ? 12 : 6); POLL_PIN(pv); if (!__all(pv == tag)) wait_flags<1, 1>(py_, tag, tmo, lane); SB;
    u32x4 py[4], xr[2];
    const unsigned vout = (rt * 128 + wi * 64 + 2 * G * 16 + tok) * 2048 + ct * 256 + kh * 128 + jj * 64 + (((g & 1) << 1) | (g >> 1)) * 16;
    const __amdgpu_buffer_rsrc_t rx = mk_rsrc(P.x, S_ * D_ * 2), rout = mk_rsrc(P.out, S_ * D_ * 2);
#pragma unroll
    for (int i = 0; i < 4; ++i) py[i] = ld16_sc1(ryx, lane * 16, (b ^ 1) * 32768 + (G * 4 + wq) * 4096 + i * 1024);
#pragma unroll
    for (int ml = 0; ml < 2; ++ml) xr[ml] = __builtin_amdgcn_raw_buffer_load_b128(rx, vout, ml * 16 * 2048, 0);
    SB; if (G == 0) ENDL(12); else ENDL(6);
    mms(); ENDM; load(c1, c0, c0, hA, 0); if (G == 0) WAIT_V(6); WAIT_L0; ENDM;
    mms(); MFMA_PAD; STAMP(21);
    if (G == 0) ENDM;
#pragma unroll
    for (int i = 0; i < 4; ++i) *(LAS f32x4*)(size_t)(exk + G * 16384 + i * 1024) = as[2 * (G ^ 1) + (i >> 1)][i & 1];
    WAIT_L0; ENDM; STAMP(22);
#pragma unroll
    for (int ml = 0; ml < 2; ++ml) {
      const u32x4 xs = swap16(xr[ml][0], xr[ml][1], xr[ml][2], xr[ml][3]); unsigned ow[4];
#pragma unroll
      for (int n = 0; n < 2; ++n) {
        const f32x4 y = acc[2 * G + ml][n] + __builtin_bit_cast(f32x4, py[ml * 2 + n]);
        const f32x4 s = as[2 * G + ml][n] + *(const LAS f32x4*)(size_t)(exk + (G ^ 1) * 16384 + (ml * 2 + n) * 1024);
        const f32x4 xv = {bflo(xs[2 * n]), bfhi(xs[2 * n]), bflo(xs[2 * n + 1]), bfhi(xs[2 * n + 1])}; f32x4 o;
#pragma unroll
        for (int r = 0; r < 4; ++r) o[r] = xv[r] + s[r] * (y[r] * rcp(1.0f + ex2(-LOG2E * y[r])));
        ow[2 * n] = pk2(o[0], o[1]); ow[2 * n + 1] = pk2(o[2], o[3]); }
      st16(swap16(ow[0], ow[1], ow[2], ow[3]), rout, vout, ml * 16 * 2048); }
    if (G == 0 && b == 0 && wq == 0) set_flag(flags + FL_EPOCH / 8, tag, lane);
    STAMP(23);
  }
};
#define P2RUN(p2, P, tag, eb) p2.run(P, tag, p, h, eb)

__global__ __launch_bounds__(512, 2) void k_fused(const u16* x, const u16* wqkv, const u16* wphi, const u16* wo, const u16* w1, const u16* b1, const u16* w2, char* ws, u16* out) {
  __shared__ __attribute__((aligned(16))) char smem[163840];
  const Ptrs P = {x, wqkv, wphi, wo, w1, b1, w2, ws, out};
  const unsigned lb = (unsigned)(size_t)(LAS char*)smem;
  const int tid = threadIdx.x, lane = tid & 63, w = __builtin_amdgcn_readfirstlane(tid >> 6), wx = w & 3;
  const int b = blockIdx.x, h = (b & 7) * 4 + ((b >> 3) & 3), p = b >> 5, odd = h & 1;
  const int tok = lane & 15, g = lane >> 4, q = (lane >> 2) & 3, pp = lane & 3;
  const unsigned ra0 = lb + L1_A + (wx * 64 + tok) * 128 + ((g ^ (tok >> 1)) << 4), ra1 = ra0 ^ 64;
  gu64* flags = (gu64*)(ws + WS_FLAG); gu64* tmo = flags + FL_TMO / 8;
  STAMP_DECL; STAMP(0);
  const int rot = p < 7 ? 2 * p : 0;
  auto kt = [&](int pos) { int t = pos + rot; t -= t >= 16 ? 14 : 0; return pos < 2 ? pos : t; };
  u64 ep; { gu64* pe = flags + FL_EPOCH / 8; POLL_ISSUE(ep, pe); }
  const __amdgpu_buffer_rsrc_t rfl = mk_rsrc(ws + WS_FLAG, 1u << 20);
  bf16x8 ones = {0x3F80, 0x3F80, 0x3F80, 0x3F80, 0x3F80, 0x3F80, 0x3F80, 0x3F80}; asm volatile("" : "+v"(ones));
  auto cs_publish = [&](const f32x4& c0, const f32x4& c1, int j0, bool two, u64 tag) {
    const f32x4 a = (tok & 2) ? c1 : c0; const u32x4 v = {__float_as_uint((tok & 1) ? a[2] : a[0]), (unsigned)tag, __float_as_uint((tok & 1) ? a[3] : a[1]), (unsigned)tag};
    if (tok < (two ? 4 : 2)) st16_sc1(v, rfl, ((j0 + 2 * (tok & 2)) * 16 + 4 * g + 2 * (tok & 1)) * 8, GR_CS + (h * 8 + p) * 1664);
    SB; };

  if (w < 4) {
    const u32x4 sa = mk_srd(x, S_ * D_ * 2);
    unsigned va0, va1;
    { const int r = lane >> 3; va0 = (p * 256 + wx * 64 + r) * 2048 + (((lane & 7) ^ (r >> 1)) << 4); va1 = va0 ^ 64; }
    const unsigned sl = lb + L1_A + wx * 8192;
    const unsigned rb0 = opq(lb + L1_BH + (8 * g + 4 * (g & 1) + q) * 224 + 8 * pp), rb1 = opq(lb + L1_BH + (8 * g + 4 * ((g & 1) ^ 1) + q) * 224 + 8 * pp);
    f32x4 acc[4][7], cs0, cs1;
    bf16x8 fa[2][4], fb[2][7];
    auto dmaA = [&](auto ST, auto J, unsigned sk) { constexpr int st = ST(), j = J(); if (j & 1) DMA(sa, va1, sk, j * 16384, sl, st * 32768 + j * 1024); else DMA(sa, va0, sk, j * 16384, sl, st * 32768 + j * 1024); };
    auto rdA = [&](auto ST, auto KS, auto M) { constexpr int st = ST(), ks = KS(), m = M(); fa[ks][m] = lds128((ks ? ra1 : ra0) + st * 32768 + m * 2048); SB; };
    auto rdB = [&](auto ST, auto KS, auto J) { constexpr int st = ST(), ks = KS(), j = J(); constexpr int o = st * 14336 + ks * 7168 + j * 32; fb[ks][j] = cat(ldstr(rb0 + o), ldstr(rb1 + o)); SB; };
    auto load = [&](auto ST, auto DO, unsigned sk) {
      constexpr int o = ST() ^ 1; constexpr bool d = DO();
      rdA(ST, IC<0>{}, IC<0>{}); rdB(ST, IC<0>{}, IC<0>{}); rdB(ST, IC<0>{}, IC<1>{}); if (d) dmaA(IC<o>{}, IC<0>{}, sk);
      rdA(ST, IC<0>{}, IC<1>{}); rdB(ST, IC<0>{}, IC<2>{}); rdB(ST, IC<0>{}, IC<3>{}); if (d) dmaA(IC<o>{}, IC<1>{}, sk);
      rdA(ST, IC<0>{}, IC<2>{}); rdB(ST, IC<0>{}, IC<4>{}); rdB(ST, IC<0>{}, IC<5>{}); if (d) dmaA(IC<o>{}, IC<2>{}, sk);
      rdA(ST, IC<0>{}, IC<3>{}); rdB(ST, IC<0>{}, IC<6>{}); rdB(ST, IC<1>{}, IC<0>{}); if (d) dmaA(IC<o>{}, IC<3>{}, sk);
      rdA(ST, IC<1>{}, IC<0>{}); rdB(ST, IC<1>{}, IC<1>{}); rdB(ST, IC<1>{}, IC<2>{}); if (d) dmaA(IC<o>{}, IC<4>{}, sk);
      rdA(ST, IC<1>{}, IC<1>{}); rdB(ST, IC<1>{}, IC<3>{}); rdB(ST, IC<1>{}, IC<4>{}); if (d) dmaA(IC<o>{}, IC<5>{}, sk);
      rdA(ST, IC<1>{}, IC<2>{}); rdB(ST, IC<1>{}, IC<5>{}); if (d) dmaA(IC<o>{}, IC<6>{}, sk);
      rdA(ST, IC<1>{}, IC<3>{}); rdB(ST, IC<1>{}, IC<6>{}); if (d) dmaA(IC<o>{}, IC<7>{}, sk);
    };
    auto mm_ = [&](auto F) {
#pragma unroll
      for (int m = 0; m < 4; ++m)
#pragma unroll
        for (int j = 0; j < 7; ++j) mk2<F()>(acc[m][j], fb[0][j], fa[0][m], fb[1][j], fa[1][m]);
      SB; };
    auto mm = [&]() { mm_(IC<0>{}); };
#define A8(ST, sk) dmaA(IC<ST>{}, IC<0>{}, sk); dmaA(IC<ST>{}, IC<1>{}, sk); dmaA(IC<ST>{}, IC<2>{}, sk); dmaA(IC<ST>{}, IC<3>{}, sk); dmaA(IC<ST>{}, IC<4>{}, sk); dmaA(IC<ST>{}, IC<5>{}, sk); dmaA(IC<ST>{}, IC<6>{}, sk); dmaA(IC<ST>{}, IC<7>{}, sk)
    A8(0, 0u);
    cs0 = zacc(); cs1 = zacc();
    auto colsum = [&](bool now) { colsum7(cs0, cs1, fb[0], fb[1], ones, now ? wx : 4); SB; };
    if (tid == 0) *(LAS u64*)(size_t)(lb + L1_CNT) = 0ull;
    STAMP(1);
    WAIT_V(0); POLL_PIN(ep); const u64 tag = uni64(ep) + 1;
    A8(1, 128u);
    WAIT_L0; BAR; SB; STAMP(4);
    load(IC<0>{}, IC<0>{}, 0u); WAIT_L0; BAR; SB; STAMP(5);
#pragma unroll
    for (int t = 0; t < 4; t += 2) {
      if (t == 0) mm_(IC<1>{}); else mm();
      colsum((p == 7) == (t == 0)); WAIT_V(0); BAR; SB;
      load(IC<1>{}, IC<1>{}, (unsigned)kt(t + 2) * 128u); WAIT_L0; BAR; SB;
      mm(); colsum((p == 7) == (t == 0)); WAIT_V(0); BAR; SB;
      load(IC<0>{}, IC<1>{}, (unsigned)kt(t + 3) * 128u); if (t == 2) cs_publish(cs0, cs1, wx, wx < 3, tag); WAIT_L0; BAR; SB;
    }
    for (int t = 4; t < 14; t += 2) {
      mm(); WAIT_V(0); BAR; SB;
      load(IC<1>{}, IC<1>{}, (unsigned)kt(t + 2) * 128u); WAIT_L0; BAR; SB;
      mm(); WAIT_V(0); BAR; SB;
      load(IC<0>{}, IC<1>{}, (unsigned)kt(t + 3) * 128u); WAIT_L0; BAR; SB;
    }
    STAMP(6);
    mm(); WAIT_V(0); BAR; SB;
    load(IC<1>{}, IC<0>{}, 0u);
    u32x2 bias[7];
#pragma unroll
    for (int j = 0; j < 7; ++j) { const int cb = 7 * h + (j < 6 ? j + odd : (odd ? 0 : 6)); bias[j] = *(const GAS u32x2*)((const GAS u16*)b1 + cb * 16 + 4 * g); }
    WAIT_L0; BAR; SB;
    mm(); BAR; MFMA_PAD; STAMP(7);
    f32x4 bl[7], cs[7]; float rl[4], rml[4];
#pragma unroll
    for (int j = 0; j < 7; ++j) { cs[j] = *(const LAS f32x4*)(size_t)(lb + L1_CS + j * 64 + g * 16); bl[j] = (f32x4){bflo(bias[j][0]), bfhi(bias[j][0]), bflo(bias[j][1]), bfhi(bias[j][1])} * LOG2E; asm volatile("" : "+v"(bl[j])); }
#pragma unroll
    for (int m = 0; m < 4; ++m) { const f32x2 st = *(const LAS f32x2*)(size_t)(lb + L1_ST + (wx * 64 + m * 16 + tok) * 8); rl[m] = st[0] * LOG2E; rml[m] = -rl[m] * st[1]; }
    SB;
    Phase2<0> p2; p2.init(lb, lane, wx, P);
    { const unsigned dump = lb + L1_DUMP; const int cu = b >> 3, a = (b & 7) >> 1;
      const int l2 = min(lane, 55) + 56 * (wx + 4 * cu);
      if (TOUCHW) TOUCH((const GAS char*)w2 + ((b & 1) * 1792 + a * 448) * 2048 + l2 * 128, dump);
      if (TOUCHW) TOUCH((const GAS char*)wo + (b * 64 + wx * 16 + (lane & 15)) * 128, dump); }
    const __amdgpu_buffer_rsrc_t ract = mk_rsrc(ws + WS_ACT, 128 * 112 * 1024);
    auto mish8 = [&](int m0, int j0, int m1, int j1) -> u32x4 {
      const f32x2 a[4] = {acc[m0][j0].xy, acc[m0][j0].zw, acc[m1][j1].xy, acc[m1][j1].zw}, c[4] = {cs[j0].xy, cs[j0].zw, cs[j1].xy, cs[j1].zw}, bb[4] = {bl[j0].xy, bl[j0].zw, bl[j1].xy, bl[j1].zw};
      f32x2 t[4], hs[4], e[4], d[4]; unsigned o[4];
#define ST4(stmt) _Pragma("unroll") for (int i = 0; i < 4; ++i) { stmt; } SB
      ST4(t[i] = c[i] * rml[i < 2 ? m0 : m1] + bb[i]);
      ST4(hs[i] = a[i] * rl[i < 2 ? m0 : m1] + t[i]);
      ST4(e[i] = opq2((f32x2){ex2(hs[i][0]), ex2(hs[i][1])}));
      ST4(t[i] = e[i] + 2.0f);
      ST4(d[i] = opq2(e[i] * t[i] + 2.0f));
      ST4(e[i] = opq2((f32x2){rcp(d[i][0]), rcp(d[i][1])}));
      ST4(t[i] = e[i] * (-2.0f / LOG2E) + 1.0f / LOG2E);
      ST4(d[i] = hs[i] * t[i]);
      ST4(o[i] = pk2(d[i][0], d[i][1]));
      return swap16(o[0], o[1], o[2], o[3]); };
    const unsigned vpair = (unsigned)((((g & 1) << 1) | (g >> 1)) * 256 + tok * 16), mb0 = p * 16 + wx * 4;
    auto pair = [&](auto J, auto M) { constexpr int j = J(), m = M();
      st16_sc1(mish8(m, j, m, j + 1), ract, vpair, ((mb0 + m) * 112 + ((7 * h + odd + j) >> 1)) * 1024); p2.dma1<j / 2, 4 + m>(); };
    gu64* fact = flags + FL_ACT / 8 + (((2 * p + (wx >> 1)) * 2 + (h >> 4)) * 32 + (h & 15) * 2 + (wx & 1)) * 16;
    STAMP(13);
    if (XP0) __builtin_amdgcn_s_setprio(2);
    pair(IC<0>{}, IC<0>{}); pair(IC<0>{}, IC<1>{}); pair(IC<0>{}, IC<2>{}); pair(IC<0>{}, IC<3>{});
    if (XP0) __builtin_amdgcn_s_setprio(0);
    STAMP(14);
    pair(IC<2>{}, IC<0>{}); pair(IC<2>{}, IC<1>{});
    STAMP(15);
    if (FLAGA == 0) { WAIT_V(5); set_flag(fact, tag, lane); } SB; STAMP(8);
    gu64* fa1 = flags + FL_ACT / 8 + (p2.rt * 2 + p2.kh) * 512; u64 pv;
    const unsigned vsing = (unsigned)((g & 1) * 112 * 1024 + (2 * odd + (g >> 1)) * 256 + tok * 16);
    auto single = [&](int m) {
      st16_sc1(mish8(m, 6, m + 1, 6), ract, vsing, ((mb0 + m) * 112 + ((7 * h + (odd ? 0 : 6)) >> 1)) * 1024); SB; };
    auto geta = [&]() { POLL_PIN(pv); STAMP(10); if (!__all(pv == tag)) wait_flags<32, 16>(fa1, tag, tmo, lane);
      SB; STAMP(11); p2.dma4<0, 0>(); p2.dma4<1, 0>(); p2.dma4<2, 0>(); };
    pair(IC<2>{}, IC<2>{}); pair(IC<2>{}, IC<3>{});
    if (FLAGA == 1) { WAIT_V(9); set_flag(fact, tag, lane); } SB;
    if (EARLYA == 0) {
      pair(IC<4>{}, IC<0>{}); pair(IC<4>{}, IC<1>{}); pair(IC<4>{}, IC<2>{}); pair(IC<4>{}, IC<3>{});
      POLL_ISSUE(pv, fa1 + (lane & 31) * 16); single(0); single(2);
      STAMP(9); WAIT_V(2); geta(); WAIT_V(12);
    } else {
      pair(IC<4>{}, IC<0>{}); POLL_ISSUE(pv, fa1 + (lane & 31) * 16); pair(IC<4>{}, IC<1>{}); pair(IC<4>{}, IC<2>{});
      WAIT_V(4); geta(); pair(IC<4>{}, IC<3>{}); single(0); single(2); STAMP(9); WAIT_V(0);
    }
    set_flag(fact + 131072 / 8, tag, lane); SB; STAMP(12);
    const bf16x8 none[4][2] = {};
    P2RUN(p2, P, tag, none);
  } else {
    const int srow = p * 256 + h * 8 + wx * 2; u32x4 xa[4];
    { const u32x4 sx = mk_srd(x, S_ * D_ * 2); const unsigned vx = srow * 2048 + lane * 16;
      asm volatile("buffer_load_dwordx4 %0, %4, %5, 0 offen\n\tbuffer_load_dwordx4 %1, %4, %5, 0 offen offset:1024\n\tbuffer_load_dwordx4 %2, %4, %5, 0 offen offset:2048\n\tbuffer_load_dwordx4 %3, %4, %5, 0 offen offset:3072"
                   : "=&v"(xa[0]), "=&v"(xa[1]), "=&v"(xa[2]), "=&v"(xa[3]) : "v"(vx), "s"(sx) : "memory"); SB; }
    const u32x4 sq = mk_srd(wqkv, D_ * 3 * D_ * 2), sh = mk_srd(w1, D_ * F_ * 2), sp = mk_srd(wphi, 32 * 32 * 32 * 2);
    unsigned vq, vh[4];
    { const int r = lane >> 2; vq = (wx * 16 + r) * 6144 + h * 64 + (((lane & 3) ^ (((r >> 3) & 1) << 1)) << 4); }
#pragma unroll
    for (int i = 0; i < 4; ++i) { const int o = (wx + 4 * i) * 1024 + lane * 16, rp = o / 224, c = (o - rp * 224) >> 4, k = rp ^ (((rp >> 3) & 1) << 2); int cs = c + 2 * odd; if (cs >= 14) cs -= 14;
      vh[i] = k * 7168 + h * 224 + cs * 16; }
    const unsigned slq = lb + L1_BQ + wx * 1024, slh = lb + L1_BH + wx * 1024;
    const unsigned rq0 = opq(lb + L1_BQ + (8 * g + q) * 64 + 32 * (g & 1) + 8 * pp), rq1 = opq(rq0 ^ 32);
    f32x4 acc[3][4][2], cs0, cs1;
    bf16x8 fa[2][4], fb[2][6];
    auto dmaQ = [&](auto ST, auto U, unsigned sk) { constexpr int st = ST(), u = U(); DMA(sq, vq, sk, u * 2048, slq, st * 12288 + u * 4096); };
    auto dmaH = [&](auto ST, auto I, unsigned sk) { constexpr int st = ST(), i = I(); if (i < 3 || wx < 2) DMA(sh, vh[i], sk, 0, slh, st * 14336 + i * 4096); };
    auto rdA = [&](auto ST, auto KS, auto M) { constexpr int st = ST(), ks = KS(), m = M(); fa[ks][m] = lds128((ks ? ra1 : ra0) + st * 32768 + m * 2048); SB; };
    auto rdB = [&](auto ST, auto KS, auto J) { constexpr int st = ST(), ks = KS(), j = J(); constexpr int o = st * 12288 + (j >> 1) * 4096 + ks * 2048; const unsigned a = (j & 1) ? rq1 : rq0;
      fb[ks][j] = cat(ldstr(a + o), ldstr(a + o + 256)); SB; };
    auto load = [&](auto ST, auto DQ, auto DH, int t) {
      constexpr int o = ST() ^ 1; constexpr bool dq = DQ(), dh = DH(); const unsigned skq = (unsigned)kt(t + 1) * (64 * 6144), skh = (unsigned)kt(t + 2) * (64 * 7168);
      rdA(ST, IC<0>{}, IC<0>{}); rdB(ST, IC<0>{}, IC<0>{}); rdB(ST, IC<0>{}, IC<1>{}); if (dq) dmaQ(IC<o>{}, IC<0>{}, skq);
      rdA(ST, IC<0>{}, IC<1>{}); rdB(ST, IC<0>{}, IC<2>{}); rdB(ST, IC<0>{}, IC<3>{}); if (dq) dmaQ(IC<o>{}, IC<1>{}, skq);
      rdA(ST, IC<0>{}, IC<2>{}); rdB(ST, IC<0>{}, IC<4>{}); rdB(ST, IC<0>{}, IC<5>{}); if (dq) dmaQ(IC<o>{}, IC<2>{}, skq);
      rdA(ST, IC<0>{}, IC<3>{}); rdB(ST, IC<1>{}, IC<0>{}); if (dh) dmaH(ST, IC<0>{}, skh);
      rdA(ST, IC<1>{}, IC<0>{}); rdB(ST, IC<1>{}, IC<1>{}); rdB(ST, IC<1>{}, IC<2>{}); if (dh) dmaH(ST, IC<1>{}, skh);
      rdA(ST, IC<1>{}, IC<1>{}); rdB(ST, IC<1>{}, IC<3>{}); if (dh) dmaH(ST, IC<2>{}, skh);
      rdA(ST, IC<1>{}, IC<2>{}); rdB(ST, IC<1>{}, IC<4>{});
      rdA(ST, IC<1>{}, IC<3>{}); rdB(ST, IC<1>{}, IC<5>{});
      if (dh) { if (wx < 2) { dmaH(ST, IC<3>{}, skh); WAIT_V(7); } else WAIT_V(6); }
    };
    auto mm_ = [&](auto F) {
#pragma unroll
      for (int m = 0; m < 4; ++m)
#pragma unroll
        for (int j = 0; j < 6; ++j) mk2<F()>(acc[j >> 1][m][j & 1], fb[0][j], fa[0][m], fb[1][j], fa[1][m]);
      SB; };
    auto mm = [&]() { mm_(IC<0>{}); };
#define H4(ST, sk) dmaH(IC<ST>{}, IC<0>{}, sk); dmaH(IC<ST>{}, IC<1>{}, sk); dmaH(IC<ST>{}, IC<2>{}, sk); dmaH(IC<ST>{}, IC<3>{}, sk)
#define Q3(ST, sk) dmaQ(IC<ST>{}, IC<0>{}, sk); dmaQ(IC<ST>{}, IC<1>{}, sk); dmaQ(IC<ST>{}, IC<2>{}, sk)
    H4(0, 0u); Q3(0, 0u);
    cs0 = zacc(); cs1 = zacc();
    auto colsum = [&](bool now) { const bf16x8 a[7] = {fb[0][0], fb[0][1], fb[0][2], fb[0][3], fb[0][4], fb[0][5], ones}, c[7] = {fb[1][0], fb[1][1], fb[1][2], fb[1][3], fb[1][4], fb[1][5], ones};
      colsum7(cs0, cs1, a, c, ones, now ? wx : 4); SB; };
    STAMP(1);
    if (wx < 2) WAIT_V(7); else WAIT_V(6);
    asm volatile("" : "+v"(xa[0]), "+v"(xa[1]), "+v"(xa[2]), "+v"(xa[3]), "+v"(ep));
    const u64 tag = uni64(ep) + 1;
    { float v[2][16], sm[2], sq_[2], mean[2];
#pragma unroll
      for (int r = 0; r < 2; ++r) { sm[r] = 0.f;
#pragma unroll
        for (int i = 0; i < 8; ++i) { const unsigned u = xa[2 * r + (i >> 2)][i & 3]; v[r][2 * i] = bflo(u); v[r][2 * i + 1] = bfhi(u); sm[r] += v[r][2 * i] + v[r][2 * i + 1]; } }
#pragma unroll
      for (int r = 0; r < 2; ++r) { mean[r] = wave_sum(sm[r]) * (1.0f / D_); sq_[r] = 0.f;
#pragma unroll
        for (int i = 0; i < 16; ++i) { const float d = v[r][i] - mean[r]; sq_[r] += d * d; } }
      const float r0 = __builtin_amdgcn_rsqf(wave_sum(sq_[0]) * (1.0f / D_) + 1e-5f), r1 = __builtin_amdgcn_rsqf(wave_sum(sq_[1]) * (1.0f / D_) + 1e-5f);
      const u32x4 gr = {__float_as_uint(lane ? mean[1] : mean[0]), (unsigned)tag, __float_as_uint(lane ? r1 : r0), (unsigned)tag};
      if (lane < 2) st16_sc1(gr, rfl, lane * 16, GR_ST + srow * 16);
      SB; }
    STAMP(2);
    WAIT_V(1);
    STAMP(3); BAR; SB; STAMP(4);
    H4(1, 64u * 7168u); Q3(1, 64u * 6144u);
    BAR; SB; STAMP(5);
    load(IC<0>{}, IC<0>{}, IC<1>{}, 0); WAIT_L0; BAR; SB;
    mm_(IC<1>{}); colsum(p == 7); WAIT_V(3); BAR; SB;
    load(IC<1>{}, IC<1>{}, IC<1>{}, 1); WAIT_L0; BAR; SB;
    mm(); colsum(p == 7); WAIT_V(3); BAR; SB;
    load(IC<0>{}, IC<1>{}, IC<1>{}, 2); WAIT_L0; BAR; SB;
    mm(); colsum(p != 7); WAIT_V(3); BAR; SB;
    load(IC<1>{}, IC<1>{}, IC<1>{}, 3); WAIT_L0; BAR; SB;
    mm(); colsum(p != 7); WAIT_V(3); BAR; MFMA_PAD;
    cs_publish(cs0, cs1, 7 + wx, wx < 2, tag);
    for (int t = 4; t < 14; t += 2) {
      load(IC<0>{}, IC<1>{}, IC<1>{}, t); WAIT_L0; BAR; SB;
      mm(); WAIT_V(3); BAR; SB;
      load(IC<1>{}, IC<1>{}, IC<1>{}, t + 1); WAIT_L0; BAR; SB;
      mm(); WAIT_V(3); BAR; SB;
    }
    STAMP(6);
    const unsigned vst = GR_ST + (p * 256 + wx * 64 + lane) * 16, vcs = GR_CS + h * (8 * 1664) + min(wx * 64 + lane, 103) * 16;
    const u32x4 sfl = mk_srd(ws + WS_FLAG, 1u << 20);
    u32x4 gs, gc[8]; LD16_ISSUE(gs, sfl, vst, 0u);
#pragma unroll
    for (int i = 0; i < 8; ++i) LD16_ISSUE(gc[i], sfl, vcs, i * 1664u);
    load(IC<0>{}, IC<1>{}, IC<0>{}, 14);
    { const unsigned vp = h * 2048 + (wx & 1) * 1024 + lane * 16, slp = lb + L1_WPHI + (wx & 1) * 1024; DMA(sp, vp, 0u, 0, slp, 0); }
    WAIT_V(13); WAIT_L0; BAR; SB;
    mm(); WAIT_V(0); BAR; SB;
    { auto stale = [&]() { unsigned d = differ(gs[1], gs[3], (unsigned)tag);
#pragma unroll
        for (int i = 0; i < 8; ++i) d |= differ(gc[i][1], gc[i][3], (unsigned)tag);
        return !__all(d == 0u); };
      asm volatile("" : "+v"(gs), "+v"(gc[0]), "+v"(gc[1]), "+v"(gc[2]), "+v"(gc[3]), "+v"(gc[4]), "+v"(gc[5]), "+v"(gc[6]), "+v"(gc[7]));
      if (__builtin_expect(stale(), 0)) {
#pragma nounroll
        for (unsigned spins = 0; spins < (1u << 16); ++spins) {
          asm volatile("buffer_load_dwordx4 %0, %1, %2, 0 offen sc1" : "+v"(gs) : "v"(vst), "s"(sfl) : "memory");
#pragma unroll
          for (int i = 0; i < 8; ++i) asm volatile("buffer_load_dwordx4 %0, %1, %2, %3 offen sc1" : "+v"(gc[i]) : "v"(vcs), "s"(sfl), "s"(i * 1664u) : "memory");
          asm volatile("s_waitcnt vmcnt(0)" : "+v"(gs), "+v"(gc[0]), "+v"(gc[1]), "+v"(gc[2]), "+v"(gc[3]), "+v"(gc[4]), "+v"(gc[5]), "+v"(gc[6]), "+v"(gc[7]) :: "memory");
          if (!stale()) break; } }
      f32x2 c = {__uint_as_float(gc[0][0]), __uint_as_float(gc[0][2])};
#pragma unroll
      for (int i = 1; i < 8; ++i) c = (f32x2){add1(c[0], __uint_as_float(gc[i][0])), add1(c[1], __uint_as_float(gc[i][2]))};
      *(LAS f32x2*)(size_t)(lb + L1_CS + min(wx * 64 + lane, 103) * 8) = c;
      *(LAS f32x2*)(size_t)(lb + L1_ST + (wx * 64 + lane) * 8) = (f32x2){__uint_as_float(gs[2]), __uint_as_float(gs[0])}; SB; }
    load(IC<1>{}, IC<0>{}, IC<0>{}, 15); WAIT_L0; BAR; SB;
    mm(); MFMA_PAD; STAMP(7);
    if (YPRIO) __builtin_amdgcn_s_setprio(1);
    {
    const int lane = opq(__lane_id()), tok = lane & 15, g = lane >> 4, q = (lane >> 2) & 3, pp = lane & 3;
    bf16x8 wp[2];
#pragma unroll
    for (int ff = 0; ff < 2; ++ff) { const unsigned a = lb + L1_WPHI + (4 * g + q) * 64 + ff * 32 + 8 * pp; wp[ff] = cat(ldstr(a), ldstr(a + 1024)); }
    f32x4 cs[6]; float rs[4], mean[4], rl[4];
#pragma unroll
    for (int j = 0; j < 6; ++j) cs[j] = *(const LAS f32x4*)(size_t)(lb + L1_CS + (7 + j) * 64 + g * 16);
#pragma unroll
    for (int m = 0; m < 4; ++m) { const f32x2 st = *(const LAS f32x2*)(size_t)(lb + L1_ST + (wx * 64 + m * 16 + tok) * 8); rs[m] = st[0]; mean[m] = opqf(-st[1]); rl[m] = st[0] * LOG2E; }
    const f32x4 zero4 = {0.f, 0.f, 0.f, 0.f};
    auto project = [&](int u, f32x4 (&pr)[4][2]) {
#pragma unroll
      for (int m = 0; m < 4; ++m) { const bf16x8 kb = pk8(acc[u][m][0] + cs[2 * u] * mean[m], acc[u][m][1] + cs[2 * u + 1] * mean[m]); pr[m][0] = mfma(wp[0], kb, zero4); pr[m][1] = mfma(wp[1], kb, zero4); }
      SB; };
    auto extrema = [&](const f32x4 (&pr)[4][2], float (&ma)[4], float (&mb)[4]) {
#pragma unroll
      for (int m = 0; m < 4; ++m) { ma[m] = vmax8(pr[m][0], pr[m][1]); mb[m] = vmax8(-pr[m][0], -pr[m][1]); }
      spread4(gather4(ma[0], mb[0], ma[1], mb[1], vmax), ma[0], mb[0], ma[1], mb[1]);
      spread4(gather4(ma[2], mb[2], ma[3], mb[3], vmax), ma[2], mb[2], ma[3], mb[3]);
#pragma unroll
      for (int m = 0; m < 4; ++m) { ma[m] *= -rl[m]; mb[m] *= -rl[m]; }
      SB; };
    auto expo = [&](int m, const f32x4 (&pr)[2], float ma, float mb, f32x4 (&ep)[2], f32x4 (&en)[2]) {
#pragma unroll
      for (int ff = 0; ff < 2; ++ff) { ep[ff] = pr[ff] * rl[m] + ma; en[ff] = pr[ff] * -rl[m] + mb; }
      SB;
#pragma unroll
      for (int ff = 0; ff < 2; ++ff)
#pragma unroll
        for (int r = 0; r < 4; ++r) { ep[ff][r] = ex2(ep[ff][r]); en[ff][r] = ex2(en[ff][r]); }
      SB; };
    { const unsigned swp = (tok & 1) | (((tok >> 2) & 1) << 1) | (((tok >> 1) & 1) << 2) | (tok & 8);
      const unsigned wph = lb + L1_PHI + (wx * 64 + tok) * 128 + ((g ^ swp) << 3), wv = lb + L1_V + (wx * 64 + tok) * 64 + ((g ^ (tok >> 1)) << 3);
      const unsigned wp0 = opq(wph), wp1 = opq(wph ^ 32), wp2 = opq(wph ^ 64), wp3 = opq(wph ^ 96), wv0 = opq(wv), wv1 = opq(wv ^ 32);
      auto put = [](unsigned a, f32x4 v) { *(LAS u32x2*)(size_t)a = (u32x2){pk2(v[0], v[1]), pk2(v[2], v[3])}; };
      f32x4 pr[4][2], ep[4][2], en[4][2]; float ma[4], mb[4];
      WAIT_L0; project(1, pr);
#pragma unroll
      for (int m = 0; m < 4; ++m) { put(wv0 + m * 1024, (acc[2][m][0] + cs[4] * mean[m]) * rs[m]); put(wv1 + m * 1024, (acc[2][m][1] + cs[5] * mean[m]) * rs[m]); }
      SB; extrema(pr, ma, mb);
#pragma unroll
      for (int m = 0; m < 4; ++m) expo(m, pr[m], ma[m], mb[m], ep[m], en[m]);
#pragma unroll
      for (int m = 0; m < 4; ++m) { const f32x4 a = ep[m][0] + ep[m][1], c = en[m][0] + en[m][1]; const f32x2 a2 = a.xy + a.zw, c2 = c.xy + c.zw; ma[m] = a2[0] + opqf(a2[1]); mb[m] = c2[0] + opqf(c2[1]); }
      auto fadd = [](float a, float c) { return a + c; };
      spread4(rcp(gather4(ma[0], mb[0], ma[1], mb[1], fadd)), ma[0], mb[0], ma[1], mb[1]);
      spread4(rcp(gather4(ma[2], mb[2], ma[3], mb[3], fadd)), ma[2], mb[2], ma[3], mb[3]);
      SB;
#pragma unroll
      for (int m = 0; m < 4; ++m) { ep[m][0] *= ma[m]; ep[m][1] *= ma[m]; en[m][0] *= mb[m]; en[m][1] *= mb[m]; SB;
        put(wp0 + m * 2048, ep[m][0]); put(wp1 + m * 2048, ep[m][1]); put(wp2 + m * 2048, en[m][0]); put(wp3 + m * 2048, en[m][1]); SB; } }
    STAMP(8);
    asm volatile("s_waitcnt lgkmcnt(0)" ::: "memory");
    if (lane == 0) { const unsigned one = 1, a = lb + L1_CNT; asm volatile("ds_add_u32 %0, %1" :: "v"(a), "v"(one) : "memory"); }
    bf16x8 eb[4][2];
    f32x4 pq[4][2]; float qa[4], qb[4];
    project(0, pq); extrema(pq, qa, qb);
    auto qpart = [&](int m0) {
#pragma unroll
      for (int m = m0; m < m0 + 2; ++m) { f32x4 ep[2], en[2]; expo(m, pq[m], qa[m], qb[m], ep, en); eb[m][0] = pk8(ep[0], ep[1]); eb[m][1] = pk8(en[0], en[1]); asm volatile("" : "+v"(eb[m][0]), "+v"(eb[m][1])); SB; } };
    lds_wait(lb + L1_CNT, 4u); SB; STAMP(9);
    { const bf16x8 ones = {0x3F80, 0x3F80, 0x3F80, 0x3F80, 0x3F80, 0x3F80, 0x3F80, 0x3F80};
      unsigned ap[2], av[2][2];
#pragma unroll
      for (int t = 0; t < 2; ++t) { const int tk = 8 * g + 4 * t + q, t15 = tk & 15; const unsigned sw = (t15 & 1) | (((t15 >> 2) & 1) << 1) | (((t15 >> 1) & 1) << 2) | (t15 & 8);
        ap[t] = opq(lb + L1_PHI + tk * 128 + (((4 * wx + pp) ^ sw) << 3));
        av[t][0] = opq(lb + L1_V + tk * 64 + ((pp ^ (t15 >> 1)) << 3)); av[t][1] = opq(av[t][0] ^ 32); }
      bf16x8 a[8], b0[8], b1_[8];
#pragma unroll
      for (int ks = 0; ks < 8; ++ks) { a[ks] = cat(ldstr(ap[0] + ks * 4096), ldstr(ap[1] + ks * 4096));
        b0[ks] = cat(ldstr(av[0][0] + ks * 2048), ldstr(av[1][0] + ks * 2048)); b1_[ks] = cat(ldstr(av[0][1] + ks * 2048), ldstr(av[1][1] + ks * 2048)); }
      SB; qpart(0); STAMP(12);
      f32x4 kv0 = zero4, kv1 = zero4, zz = zero4;
#pragma unroll
      for (int ks = 0; ks < 8; ++ks) { kv0 = mfma(a[ks], b0[ks], kv0); kv1 = mfma(a[ks], b1_[ks], kv1); zz = mfma(a[ks], ones, zz); }
      const __amdgpu_buffer_rsrc_t rkv = mk_rsrc(ws + WS_KVP, 3u << 20);
      st16_sc1(__builtin_bit_cast(u32x4, pk8(kv0, kv1)), rkv, lane * 16, (h * 8 + p) * 4096 + wx * 1024);
      if (tok == 0) st16_sc1(__builtin_bit_cast(u32x4, zz), rkv, g * 16, (unsigned)(WS_ZP - WS_KVP) + (h * 8 + p) * 256 + wx * 64);
      SB; STAMP(13); }
    qpart(2);
    SB; if (YPRIO) __builtin_amdgcn_s_setprio(0); STAMP(10); WAIT_V(0);
    set_flag(flags + (FL_KV + (h * 4 + wx) * 1024) / 8 + p * 16, tag, lane); STAMP(11);
    Phase2<1> p2; p2.init(lb, lane, wx, P);
    P2RUN(p2, P, tag, eb);
    }
  }
}
}

extern "C" void kernel_launch(void* const* d_in, const int* in_sizes, int n_in, void* d_out, int out_size, void* d_ws, size_t ws_size, hipStream_t stream) {
  k_fused<<<256, 512, 0, stream>>>((const u16*)d_in[0], (const u16*)d_in[1], (const u16*)d_in[2], (const u16*)d_in[3], (const u16*)d_in[5], (const u16*)d_in[6], (const u16*)d_in[7], (char*)d_ws, (u16*)d_out);
}
```

```cpp
#include <hip/hip_runtime.h>
#include <hip/hip_bf16.h>
#include <type_traits>

namespace {
#define LAS __attribute__((address_space(3)))
#define GAS __attribute__((address_space(1)))
typedef unsigned short u16;
typedef unsigned u32x4 __attribute__((ext_vector_type(4)));
typedef unsigned u32x2 __attribute__((ext_vector_type(2)));
typedef short bf16x8 __attribute__((ext_vector_type(8)));
typedef short s16x4 __attribute__((ext_vector_type(4)));
typedef float f32x4 __attribute__((ext_vector_type(4)));
typedef float f32x2 __attribute__((ext_vector_type(2)));
typedef __bf16 bf16x2_t __attribute__((ext_vector_type(2)));

constexpr int S_ = 2048, D_ = 1024, F_ = 3584;
constexpr size_t WS_FLAG = 0;
constexpr size_t WS_XN = 1u << 20;
constexpr size_t WS_ACT = WS_XN + (4u << 20);
constexpr size_t WS_O = WS_ACT + (15u << 20);
constexpr size_t WS_KVP = WS_O + (4u << 20);
constexpr size_t WS_ZP = WS_KVP + (2u << 20);
constexpr size_t WS_YX = WS_ZP + (1u << 20);
constexpr size_t WS_STAMP = 64u << 20;
#define STAMP_DECL
#define STAMP(i)
#define STAMP_OUT(wv)
constexpr unsigned FL_EPOCH = 0;
constexpr unsigned FL_O = 1024;
constexpr unsigned FL_YX = 16384;
constexpr unsigned FL_TMO = 61440;
constexpr unsigned GR_ST = 65536;
constexpr unsigned FL_KV = 131072;
constexpr unsigned FL_ACT = 262144;
constexpr unsigned GR_CS = 524288;
#define YPRIO 0
#define XP0 0
#define EARLYA 0
#define BLATE 6
#define TOUCH0 0
#define TOUCHW 1
constexpr float LOG2E = 1.4426950408889634f;

#define SB __builtin_amdgcn_sched_barrier(0)
#define BAR __builtin_amdgcn_s_barrier()
#define WAIT_L0 do { __builtin_amdgcn_s_waitcnt(0xC07F); SB; } while (0)
#define WAIT_V(n) asm volatile("s_waitcnt vmcnt(%0)" :: "i"(n) : "memory")
template <int N> using IC = std::integral_constant<int, N>;

__device__ __forceinline__ u32x4 mk_srd(const void* p, unsigned bytes) {
  const unsigned long long a = (unsigned long long)p; u32x4 r;
  r.x = __builtin_amdgcn_readfirstlane((unsigned)a); r.y = __builtin_amdgcn_readfirstlane((unsigned)(a >> 32)) & 0xffffu; r.z = bytes; r.w = 0x00020000u; return r; }
#define DMA(srd, voff, sk, KOFF, sl, LOFF) do { unsigned _t; asm volatile("s_add_u32 m0, %4, %5\n\ts_add_u32 %0, %3, %6\n\tbuffer_load_dwordx4 %1, %2, %0 offen lds" \
    : "=&s"(_t) : "v"(voff), "s"(srd), "s"(sk), "s"(sl), "i"(LOFF), "i"(KOFF) : "memory", "scc"); SB; } while (0)
#define DMA_SC1(srd, voff, sk, KOFF, sl, LOFF) do { unsigned _t; asm volatile("s_add_u32 m0, %4, %5\n\ts_add_u32 %0, %3, %6\n\tbuffer_load_dwordx4 %1, %2, %0 offen sc1 lds" \
    : "=&s"(_t) : "v"(voff), "s"(srd), "s"(sk), "s"(sl), "i"(LOFF), "i"(KOFF) : "memory", "scc"); SB; } while (0)
#define TOUCH(ptr, sl) do { asm volatile("s_mov_b32 m0, %1\n\ts_nop 0\n\tglobal_load_lds_dword %0, off" :: "v"(ptr), "s"(sl) : "memory"); SB; } while (0)
__device__ __forceinline__ bf16x8 lds128(unsigned a) { return *(const LAS bf16x8*)(size_t)a; }
__device__ __forceinline__ s16x4 ldstr(unsigned a) { return __builtin_amdgcn_ds_read_tr16_b64_v4i16((LAS s16x4*)(size_t)a); }
__device__ __forceinline__ bf16x8 cat(s16x4 lo, s16x4 hi) { return __builtin_shufflevector(lo, hi, 0, 1, 2, 3, 4, 5, 6, 7); }
__device__ __forceinline__ unsigned opq(unsigned x) { asm volatile("" : "+v"(x)); return x; }
__device__ __forceinline__ float opqf(float x) { asm("" : "+v"(x)); return x; }
__device__ __forceinline__ f32x2 opq2(f32x2 x) { asm("" : "+v"(x)); return x; }
__device__ __forceinline__ f32x4 mfma(bf16x8 a, bf16x8 b, f32x4 c) { return __builtin_amdgcn_mfma_f32_16x16x32_bf16(a, b, c, 0, 0, 0); }
__device__ __forceinline__ void mfma_acc(f32x4& d, bf16x8 a, bf16x8 b) { asm volatile("v_mfma_f32_16x16x32_bf16 %0, %1, %2, %0" : "+v"(d) : "v"(a), "v"(b)); }
__device__ __forceinline__ void mfma_k2(f32x4& d, bf16x8 a0, bf16x8 b0, bf16x8 a1, bf16x8 b1) {
  asm volatile("v_mfma_f32_16x16x32_bf16 %0, %1, %2, %0\n\tv_mfma_f32_16x16x32_bf16 %0, %3, %4, %0" : "+v"(d) : "v"(a0), "v"(b0), "v"(a1), "v"(b1)); }
__device__ __forceinline__ void mfma_k2_first(f32x4& d, bf16x8 a0, bf16x8 b0, bf16x8 a1, bf16x8 b1) {
  asm volatile("v_mfma_f32_16x16x32_bf16 %0, %1, %2, 0\n\tv_mfma_f32_16x16x32_bf16 %0, %3, %4, %0" : "=&v"(d) : "v"(a0), "v"(b0), "v"(a1), "v"(b1)); }
template <bool FIRST> __device__ __forceinline__ void mk2(f32x4& d, bf16x8 a0, bf16x8 b0, bf16x8 a1, bf16x8 b1) { if (FIRST) mfma_k2_first(d, a0, b0, a1, b1); else mfma_k2(d, a0, b0, a1, b1); }
__device__ __forceinline__ void mfma_k2x2(f32x4& d, f32x4& e, bf16x8 a0, bf16x8 c0, bf16x8 b0, bf16x8 a1, bf16x8 c1, bf16x8 b1) {
  asm volatile("v_mfma_f32_16x16x32_bf16 %0, %2, %4, %0\n\tv_mfma_f32_16x16x32_bf16 %1, %3, %4, %1\n\tv_mfma_f32_16x16x32_bf16 %0, %5, %7, %0\n\tv_mfma_f32_16x16x32_bf16 %1, %6, %7, %1"
               : "+v"(d), "+v"(e) : "v"(a0), "v"(c0), "v"(b0), "v"(a1), "v"(c1), "v"(b1)); }
__device__ __forceinline__ f32x4 zacc() { f32x4 z = {0.f, 0.f, 0.f, 0.f}; asm volatile("" : "+v"(z)); return z; }
#define CS_M(d, a) "v_mfma_f32_16x16x32_bf16 " d ", " a ", %[o], " d "\n\t"
__device__ __forceinline__ void colsum7(f32x4& c0, f32x4& c1, const bf16x8 (&a)[7], const bf16x8 (&b)[7], bf16x8 ones, unsigned sel) {
  asm volatile("s_cmp_lt_u32 %[s], 2\n\ts_cbranch_scc1 .Lcs01_%=\n\ts_cmp_eq_u32 %[s], 2\n\ts_cbranch_scc1 .Lcs2_%=\n\ts_cmp_eq_u32 %[s], 3\n\ts_cbranch_scc0 .Lcs9_%=\n\t"
               CS_M("%0", "%[a3]") CS_M("%0", "%[b3]") "s_branch .Lcs9_%=\n"
               ".Lcs2_%=:\n\t" CS_M("%0", "%[a2]") CS_M("%0", "%[b2]") CS_M("%1", "%[a6]") CS_M("%1", "%[b6]") "s_branch .Lcs9_%=\n"
               ".Lcs01_%=:\n\ts_cmp_eq_u32 %[s], 0\n\ts_cbranch_scc1 .Lcs0_%=\n\t" CS_M("%0", "%[a1]") CS_M("%0", "%[b1]") CS_M("%1", "%[a5]") CS_M("%1", "%[b5]") "s_branch .Lcs9_%=\n"
               ".Lcs0_%=:\n\t" CS_M("%0", "%[a0]") CS_M("%0", "%[b0]") CS_M("%1", "%[a4]") CS_M("%1", "%[b4]") "\n.Lcs9_%=:"
               : "+v"(c0), "+v"(c1) : [a0] "v"(a[0]), [a1] "v"(a[1]), [a2] "v"(a[2]), [a3] "v"(a[3]), [a4] "v"(a[4]), [a5] "v"(a[5]), [a6] "v"(a[6]),
                 [b0] "v"(b[0]), [b1] "v"(b[1]), [b2] "v"(b[2]), [b3] "v"(b[3]), [b4] "v"(b[4]), [b5] "v"(b[5]), [b6] "v"(b[6]), [o] "v"(ones), [s] "s"(sel) : "scc");
}
#define MFMA_PAD do { asm volatile("s_nop 7\n\ts_nop 1" ::: "memory"); SB; } while (0)
__device__ __forceinline__ unsigned pk2(float lo, float hi) { const f32x2 v = {lo, hi}; return __builtin_bit_cast(unsigned, __builtin_convertvector(v, bf16x2_t)); }
__device__ __forceinline__ bf16x8 pk8(f32x4 a, f32x4 b) { const u32x4 u = {pk2(a[0], a[1]), pk2(a[2], a[3]), pk2(b[0], b[1]), pk2(b[2], b[3])}; return __builtin_bit_cast(bf16x8, u); }
__device__ __forceinline__ float bflo(unsigned u) { return __uint_as_float(u << 16); }
__device__ __forceinline__ float bfhi(unsigned u) { return __uint_as_float(u & 0xffff0000u); }
__device__ __forceinline__ float ex2(float x) { return __builtin_amdgcn_exp2f(x); }
__device__ __forceinline__ float rcp(float x) { return __builtin_amdgcn_rcpf(x); }
__device__ __forceinline__ u32x4 swap16(unsigned a0, unsigned a1, unsigned b0, unsigned b1) {
  const auto p = __builtin_amdgcn_permlane16_swap(a0, b0, false, false), q = __builtin_amdgcn_permlane16_swap(a1, b1, false, false);
  return (u32x4){p[0], q[0], p[1], q[1]}; }
__device__ __forceinline__ float vmax(float a, float b) { return __builtin_elementwise_maximum(a, b); }
__device__ __forceinline__ float vmax8(f32x4 a, f32x4 c) { return vmax(vmax(vmax(vmax(a[0], a[1]), a[2]), vmax(vmax(a[3], c[0]), c[1])), vmax(c[2], c[3])); }
__device__ __forceinline__ void sw32(float& a, float& b) { const auto p = __builtin_amdgcn_permlane32_swap(__float_as_uint(a), __float_as_uint(b), false, false); a = __uint_as_float(p[0]); b = __uint_as_float(p[1]); }
__device__ __forceinline__ void sw16(float& a, float& b) { const auto p = __builtin_amdgcn_permlane16_swap(__float_as_uint(a), __float_as_uint(b), false, false); a = __uint_as_float(p[0]); b = __uint_as_float(p[1]); }
template <class Op> __device__ __forceinline__ float gather4(float a0, float a1, float b0, float b1, Op op) { sw32(a0, a1); sw32(b0, b1); float c = op(a0, a1), d = op(b0, b1); sw16(c, d); return op(c, d); }
__device__ __forceinline__ void spread4(float d, float& a0, float& a1, float& b0, float& b1) { a0 = d; b0 = d; sw16(a0, b0); a1 = a0; b1 = b0; sw32(a0, a1); sw32(b0, b1); }
template <class Op> __device__ __forceinline__ float red4(float v, Op op) {
  const unsigned u = __float_as_uint(v); const auto p = __builtin_amdgcn_permlane32_swap(u, u, false, false);
  const float a = op(__uint_as_float(p[0]), __uint_as_float(p[1])); const unsigned ua = __float_as_uint(a);
  const auto q = __builtin_amdgcn_permlane16_swap(ua, ua, false, false); return op(__uint_as_float(q[0]), __uint_as_float(q[1])); }
__device__ __forceinline__ void st16_sc1(u32x4 v, __amdgpu_buffer_rsrc_t r, unsigned voff, unsigned soff) { __builtin_amdgcn_raw_buffer_store_b128(v, r, voff + soff, 0, 16); }
__device__ __forceinline__ void st16(u32x4 v, __amdgpu_buffer_rsrc_t r, unsigned voff, unsigned soff) { __builtin_amdgcn_raw_buffer_store_b128(v, r, voff + soff, 0, 0); }
__device__ __forceinline__ u32x4 ld16_sc1(__amdgpu_buffer_rsrc_t r, unsigned voff, unsigned soff) { return __builtin_amdgcn_raw_buffer_load_b128(r, voff, soff, 16); }
__device__ __forceinline__ __amdgpu_buffer_rsrc_t mk_rsrc(const void* p, unsigned bytes) { return __builtin_amdgcn_make_buffer_rsrc((void*)p, 0, bytes, 0x00020000); }
#define RLX_AGENT __ATOMIC_RELAXED, __HIP_MEMORY_SCOPE_AGENT
typedef unsigned long long u64;
typedef GAS u64 gu64;
__device__ __forceinline__ void set_flag(gu64* w, u64 tag, int lane) { if (lane == 0) __hip_atomic_store(w, tag, RLX_AGENT); }
template <int N, int STRIDE> __device__ __forceinline__ void wait_flags(gu64* w, u64 tag, gu64* tmo, int lane) {
#pragma nounroll
  for (unsigned spins = 0;; ++spins) {
    u64 v = tag; if (N >= 64 || lane < N) v = __hip_atomic_load(w + lane * STRIDE, RLX_AGENT);
    if (__all(v == tag)) break;
    if (spins > (1u << 16)) { if (lane == 0) __hip_atomic_store(tmo, tag, RLX_AGENT); break; }
    __builtin_amdgcn_s_sleep(2);
  }
  __builtin_amdgcn_fence(__ATOMIC_ACQUIRE, "wavefront");
}
__device__ __forceinline__ u64 uni64(u64 v) { return ((u64)(unsigned)__builtin_amdgcn_readfirstlane((unsigned)(v >> 32)) << 32) | (unsigned)__builtin_amdgcn_readfirstlane((unsigned)v); }
__device__ __forceinline__ void lds_wait(unsigned addr, unsigned target) {
  for (;;) { unsigned v; asm volatile("ds_read_b32 %0, %1\n\ts_waitcnt lgkmcnt(0)" : "=v"(v) : "v"(addr) : "memory");
    if ((unsigned)__builtin_amdgcn_readfirstlane(v) >= target) break; }
}
#define POLL_ISSUE(dst, ptr) do { asm volatile("global_load_dwordx2 %0, %1, off sc1" : "=v"(dst) : "v"(ptr) : "memory"); SB; } while (0)
#define LD16_ISSUE(dst, srd, voff, soff) do { asm volatile("buffer_load_dwordx4 %0, %1, %2, %3 offen sc1" : "=v"(dst) : "v"(voff), "s"(srd), "s"(soff) : "memory"); SB; } while (0)
#define POLL_PIN(dst) asm volatile("" : "+v"(dst))
__device__ __forceinline__ float add1(float a, float b) { float r; asm("v_add_f32 %0, %1, %2" : "=v"(r) : "v"(a), "v"(b)); return r; }
__device__ __forceinline__ float mul1(float a, float b) { float r; asm("v_mul_f32 %0, %1, %2" : "=v"(r) : "v"(a), "v"(b)); return r; }
__device__ __forceinline__ float fma1(float a, float b, float c) { float r; asm("v_fma_f32 %0, %1, %2, %3" : "=v"(r) : "v"(a), "v"(b), "v"(c)); return r; }
__device__ __forceinline__ f32x4 add4(f32x4 a, u32x4 b) { return (f32x4){add1(a[0], __uint_as_float(b[0])), add1(a[1], __uint_as_float(b[1])), add1(a[2], __uint_as_float(b[2])), add1(a[3], __uint_as_float(b[3]))}; }
__device__ __forceinline__ unsigned differ(unsigned a, unsigned b, unsigned t) { unsigned r; asm("v_bitop3_b32 %0, %1, %2, %3 bitop3:0x7e" : "=v"(r) : "v"(a), "v"(b), "s"(t)); return r; }
template <int C> __device__ __forceinline__ float dpp(float v) { return __int_as_float(__builtin_amdgcn_update_dpp(0, __float_as_int(v), C, 0xf, 0xf, true)); }
__device__ __forceinline__ float wave_sum(float v) { v += dpp<0x128>(v); v += dpp<0x124>(v); v += dpp<0x122>(v); v += dpp<0x121>(v); return red4(v, [](float a, float c) { return a + c; }); }
__device__ __forceinline__ void lds_sync(unsigned addr, unsigned target, int lane) {
  asm volatile("s_waitcnt lgkmcnt(0)" ::: "memory");
  if (lane == 0) { const unsigned one = 1; asm volatile("ds_add_u32 %0, %1" :: "v"(addr), "v"(one) : "memory"); }
  for (;;) { unsigned v; asm volatile("ds_read_b32 %0, %1\n\ts_waitcnt lgkmcnt(0)" : "=v"(v) : "v"(addr) : "memory");
    if ((unsigned)__builtin_amdgcn_readfirstlane(v) >= target) break; }
}

constexpr unsigned L1_A = 0;
constexpr unsigned L1_BQ = 65536;
constexpr unsigned L1_BH = 90112;
constexpr unsigned L1_WPHI = 118784;
constexpr unsigned L1_CNT = 120832;
constexpr unsigned L1_DUMP = 121088;
constexpr unsigned L1_BIAS = 121344;
constexpr unsigned L1_CS = 128000;
constexpr unsigned L1_ST = 129024;
constexpr unsigned L1_V = 98304;
constexpr unsigned L1_PHI = 131072;
constexpr unsigned L2_EX = 131072;

struct Ptrs { const u16 *x, *wqkv, *wphi, *wo, *w1, *b1, *w2; char* ws; u16* out; };

template <int G> struct Phase2 {
  unsigned lb; int lane, wq, b, kh, rt, ct;
  u32x4 sact, so, sw2, swo;
  unsigned vaa, vao, vb0, vb1, vs0, vs1, sla, sls;
  struct Off { unsigned a0, a1, b; };
  __device__ __forceinline__ Off off_y(int U) const { const int s0 = kh * 56 + (U < 24 ? 7 * (U & 7) + (U >> 3) : 14 * U - 333), s1 = s0 + (U < 24 ? 4 : 7); return {s0 * 1024u, s1 * 1024u, (wq >> 1 ? s1 : s0) * 65536u}; }
  __device__ __forceinline__ Off off_s(int U) const { return {(U - 28) * 2048u, (U - 28) * 2048u + 1024u, (U - 28) * 131072u}; }
  __device__ __forceinline__ void init(unsigned lb_, int lane_, int wq_, const Ptrs& P) {
    lb = lb_; lane = lane_; wq = wq_; b = blockIdx.x; kh = b & 1; rt = ((b & 7) >> 1) * 4 + ((b >> 3) & 3); ct = b >> 5;
    sact = mk_srd(P.ws + WS_ACT, 128 * 112 * 1024); so = mk_srd(P.ws + WS_O, 128 * 32 * 1024); sw2 = mk_srd(P.w2, F_ * D_ * 2); swo = mk_srd(P.wo, D_ * D_ * 2);
    vaa = lane * 16 + (rt * 8 + wq * 2) * (112 * 1024); vao = lane * 16 + (rt * 8 + wq * 2) * (32 * 1024);
    { const int r = lane >> 4, c = lane & 15; vb0 = ((wq & 1) * 16 + r) * 2048 + ct * 256 + (((c >> 1) ^ r) << 5) + ((c & 1) << 4); vb1 = vb0 ^ 128; }
    { const int r = lane >> 3, c = lane & 7; vs0 = (wq * 16 + r) * 2048 + ct * 256 + kh * 128 + (((c >> 1) ^ ((r >> 1) & 1)) << 5) + ((c & 1) << 4); vs1 = vs0 ^ 64; }
    sla = lb + wq * 4096; sls = lb + wq * 2048;
  }
  template <int LO, bool Y, int I> __device__ __forceinline__ void dma(const Off& o) {
    if (I < 4) { const unsigned sk = (I & 1) ? o.a1 : o.a0; if (Y) DMA_SC1(sact, vaa, sk, (I >> 1) * 112 * 1024, sla, LO + I * 1024); else DMA_SC1(so, vao, sk, (I >> 1) * 32 * 1024, sla, LO + I * 1024); }
    else if (Y) { constexpr int j = I - 4; if (j >> 1) DMA(sw2, vb1, o.b, j * 8192, sla, LO + 16384 + j * 1024); else DMA(sw2, vb0, o.b, j * 8192, sla, LO + 16384 + j * 1024); }
    else if (I < 6) { constexpr int j = I - 4; if (j) DMA(swo, vs1, o.b, j * 16384, sls, LO + 16384 + j * 1024); else DMA(swo, vs0, o.b, j * 16384, sls, LO + 16384 + j * 1024); } }
  template <int U, int I> __device__ __forceinline__ void dma1() { dma<(U & 1) * 65536 + ((U >> 1) & 1) * 32768, true, I>(off_y(U)); }
  template <int U, int I0> __device__ __forceinline__ void dma4() { dma1<U, I0>(); dma1<U, I0 + 1>(); dma1<U, I0 + 2>(); dma1<U, I0 + 3>(); }

  __device__ __forceinline__ void run(const Ptrs& P, u64 tag, int p, int h, const bf16x8 (&eb)[4][2]) {
    char* ws = P.ws;
    const int tok = lane & 15, g = lane >> 4, q = (lane >> 2) & 3, pp = lane & 3, wi = wq >> 1, jj = wq & 1;
    const unsigned ra = opq(lb + G * 65536 + wi * 8192 + lane * 16);
    unsigned rb[4], rs[2];
#pragma unroll
    for (int n = 0; n < 4; ++n) { const int f = 4 * (kh ^ (n >> 1)) + 2 * jj + (n & 1); rb[n] = opq(lb + G * 65536 + 16384 + (8 * g + q) * 256 + ((f ^ (q | ((g & 1) << 2))) << 5) + 8 * pp); }
#pragma unroll
    for (int n = 0; n < 2; ++n) { const int f = 2 * jj + n; rs[n] = opq(lb + G * 65536 + 16384 + (8 * g + q) * 128 + ((f ^ ((q >> 1) | ((g & 1) << 1))) << 5) + 8 * pp); }
    f32x4 acc[4][4], as[4][2];
    bf16x8 fa[2][4], fb[2][4];
    auto rdA = [&](auto HR, auto I) { constexpr int i = I(); fa[i >> 2][i & 3] = lds128(ra + HR() * 32768 + (i & 3) * 2048 + (i >> 2) * 1024); SB; };
    auto rdB = [&](auto HR, auto Y, auto I) { constexpr int i = I(), ks = i >> 2, n = i & 3; constexpr bool y = Y();
      if (y) { constexpr int o = HR() * 32768 + ks * 8192; fb[ks][n] = cat(ldstr(rb[n] + o), ldstr(rb[n] + o + 1024)); SB; }
      else if (n < 2) { constexpr int o = HR() * 32768 + ks * 4096; fb[ks][n] = cat(ldstr(rs[n & 1] + o), ldstr(rs[n & 1] + o + 512)); SB; } };
    auto loadh = [&](auto HR, auto YR, auto MD, auto HD, int U, auto hook) { constexpr int md = MD(), LO = (G ^ 1) * 65536 + HD() * 32768; constexpr bool d = md != 0, y = md == 1;
      const Off o = y ? off_y(U) : off_s(U);
      rdA(HR, IC<0>{}); rdB(HR, YR, IC<0>{}); if (d) dma<LO, y, 0>(o); hook(IC<0>{}); SB;
      rdA(HR, IC<1>{}); rdB(HR, YR, IC<1>{}); if (d) dma<LO, y, 4>(o); hook(IC<1>{}); SB;
      rdA(HR, IC<2>{}); rdB(HR, YR, IC<2>{}); if (d) dma<LO, y, 1>(o); hook(IC<2>{}); SB;
      rdA(HR, IC<3>{}); rdB(HR, YR, IC<3>{}); if (d) dma<LO, y, 5>(o); hook(IC<3>{}); SB;
      rdA(HR, IC<4>{}); rdB(HR, YR, IC<4>{}); if (d) dma<LO, y, 2>(o); hook(IC<4>{}); SB;
      rdA(HR, IC<5>{}); rdB(HR, YR, IC<5>{}); if (d) dma<LO, y, 6>(o); hook(IC<5>{}); SB;
      rdA(HR, IC<6>{}); rdB(HR, YR, IC<6>{}); if (d) dma<LO, y, 3>(o); hook(IC<6>{}); SB;
      rdA(HR, IC<7>{}); rdB(HR, YR, IC<7>{}); hook(IC<7>{}); SB; if (d) dma<LO, y, 7>(o); };
    auto load = [&](auto HR, auto YR, auto MD, auto HD, int U) { loadh(HR, YR, MD, HD, U, [](auto) {}); };
    auto mmy_ = [&](auto F) {
#pragma unroll
      for (int m = 0; m < 4; ++m)
#pragma unroll
        for (int n = 0; n < 4; ++n) mk2<F()>(acc[m][n], fb[0][n], fa[0][m], fb[1][n], fa[1][m]);
      SB; };
    auto mms_ = [&](auto F) {
#pragma unroll
      for (int m = 0; m < 4; ++m)
#pragma unroll
        for (int n = 0; n < 2; ++n) mk2<F()>(as[m][n], fb[0][n], fa[0][m], fb[1][n], fa[1][m]);
      SB; };
    auto mmy = [&]() { mmy_(IC<0>{}); }; auto mms = [&]() { mms_(IC<0>{}); };
#define ENDL(n) do { WAIT_V(n); WAIT_L0; BAR; SB; } while (0)
#define ENDM do { BAR; SB; } while (0)
    IC<0> c0; IC<1> c1; IC<2> c2; IC<G> hA; IC<G ^ 1> hB;
    gu64* flags = (gu64*)(ws + WS_FLAG); gu64* tmo = flags + FL_TMO / 8;
    const __amdgpu_buffer_rsrc_t ryx = mk_rsrc(ws + WS_YX, 256 * 32768);
    const unsigned exk = lb + L2_EX + wq * 4096 + lane * 16, exs = lb + 24576 + (wq >> 1) * 32768 + (wq & 1) * 4096 + lane * 16;
    f32x4 tk[4];
    auto give = [&](auto K) { constexpr int k = K();
#pragma unroll
      for (int i = 0; i < 4; ++i) *(LAS f32x4*)(size_t)((k ? exs + G * 65536 : exk + G * 16384) + i * 1024) = acc[2 * (G ^ 1) + (i >> 1)][2 * k + (i & 1)];
      SB; };
    auto take_rd = [&](auto K) { constexpr int k = K();
#pragma unroll
      for (int i = 0; i < 4; ++i) tk[i] = *(const LAS f32x4*)(size_t)((k ? exs + (G ^ 1) * 65536 : exk + (G ^ 1) * 16384) + i * 1024);
      SB; };
    auto take = [&](auto K) { constexpr int k = K();
#pragma unroll
      for (int i = 0; i < 4; ++i) { f32x4& a = acc[2 * G + (i >> 1)][2 * k + (i & 1)]; a += tk[i]; if (k) st16_sc1(__builtin_bit_cast(u32x4, a), ryx, lane * 16, b * 32768 + (G * 4 + wq) * 4096 + i * 1024); else asm volatile("" : "+v"(a)); }
      SB; };
    u64 pv;
    if (G == 0) {
      WAIT_V(EARLYA ? 1 : 9); BAR; SB; STAMP(16);
      load(c0, c1, c1, c1, 3); ENDL(13);
      mmy_(c1); WAIT_V(8); ENDM; STAMP(17);
      load(c1, c1, c1, hA, 5); ENDL(8);
      mmy(); ENDM; load(c0, c1, c1, hB, 7); ENDL(8);
      for (int T = 4; T < 20; T += 4) {
        mmy(); ENDM; load(c1, c1, c1, hA, T + 5); ENDL(8);
        mmy(); ENDM; load(c0, c1, c1, hB, T + 7); ENDL(8);
      }
    } else {
      BAR; SB; STAMP(16); BAR; SB;
      load(c0, c1, c1, c0, 4); ENDL(8);
      mmy_(c1); ENDM; STAMP(17);
      gu64* fa2 = flags + (FL_ACT + 131072) / 8 + (rt * 2 + kh) * 512; gu64* fkv = flags + (FL_KV + (h * 4 + wq) * 1024) / 8;
      POLL_ISSUE(pv, lane < 32 ? fa2 + lane * 16 : fkv + (lane & 7) * 16);
      load(c1, c1, c1, hA, 6); ENDL(9);
      mmy(); ENDM;
      WAIT_V(8); POLL_PIN(pv); if (!__all(pv == tag)) { wait_flags<32, 16>(fa2, tag, tmo, lane); wait_flags<8, 16>(fkv, tag, tmo, lane); } SB;
      const u32x4 skv = mk_srd(ws + WS_KVP, 3u << 20);
      u32x4 t[8], tz; LD16_ISSUE(tz, skv, h * 2048 + (lane & 7) * 256 + wq * 64 + g * 16, (unsigned)(WS_ZP - WS_KVP));
      { const unsigned vkv = h * 32768 + wq * 1024 + lane * 16;
#pragma unroll
        for (int i = 0; i < 8; ++i) LD16_ISSUE(t[i], skv, vkv, i * 4096u); }
      load(c0, c1, c1, hB, 8); ENDL(17);
      mmy(); ENDM;
      WAIT_V(8); asm volatile("" : "+v"(t[0]), "+v"(t[1]), "+v"(t[2]), "+v"(t[3]), "+v"(t[4]), "+v"(t[5]), "+v"(t[6]), "+v"(t[7]), "+v"(tz)); SB;
      float sm[8];
      auto addp = [&](int i, int c) { sm[2 * c] = add1(sm[2 * c], bflo(t[i][c])); sm[2 * c + 1] = add1(sm[2 * c + 1], bfhi(t[i][c])); asm volatile("" : "+v"(sm[2 * c]), "+v"(sm[2 * c + 1])); };
      loadh(c1, c1, c1, hA, 10, [&](auto I) { constexpr int i = I();
        if (i == 0) { _Pragma("unroll") for (int c = 0; c < 4; ++c) { sm[2 * c] = bflo(t[0][c]); sm[2 * c + 1] = bfhi(t[0][c]); asm volatile("" : "+v"(sm[2 * c]), "+v"(sm[2 * c + 1])); } }
        else if (i < 7) { addp((i + 1) >> 1, 2 * (~i & 1)); addp((i + 1) >> 1, 2 * (~i & 1) + 1); }
        else { _Pragma("unroll") for (int r = 0; r < 4; ++r) { float z = __uint_as_float(tz[r]); z += dpp<0xB1>(z); z = opqf(z); z += dpp<0x4E>(z); z = opqf(z); z += dpp<0x141>(z); tz[r] = __float_as_uint(opqf(z)); } asm volatile("" : "+v"(tz)); } });
      ENDL(8);
      mmy(); ENDM;
      const unsigned kva = opq(lb + L2_EX + lane * 16);
      loadh(c0, c1, c1, hB, 12, [&](auto I) { constexpr int i = I(); addp(4 + (i >> 1), 2 * (i & 1)); addp(4 + (i >> 1), 2 * (i & 1) + 1); });
      { const unsigned a = kva + (wq >> 1) * 1024 + (wq & 1) * 8;
        *(LAS u32x2*)(size_t)a = (u32x2){pk2(sm[0], sm[1]), pk2(sm[2], sm[3])}; *(LAS u32x2*)(size_t)(a + 2048) = (u32x2){pk2(sm[4], sm[5]), pk2(sm[6], sm[7])};
        u32x2 au = {pk2(__uint_as_float(tz[0]), __uint_as_float(tz[1])), pk2(__uint_as_float(tz[2]), __uint_as_float(tz[3]))};
        if ((lane & 3) == 1) au = (u32x2){0x3F803F80u, 0x3F803F80u}; else if ((lane & 3) != 0) au = (u32x2){0u, 0u};
        *(LAS u32x2*)(size_t)(a + 4096) = au; SB; }
      ENDL(8);
      mmy(); ENDM;
      bf16x8 ka[6]; f32x4 r6[6]; float ca, cc, o8[8];
      const __amdgpu_buffer_rsrc_t ro = mk_rsrc(ws + WS_O, 128 * 32 * 1024);
      const unsigned vo = (unsigned)((((g & 1) << 1) | (g >> 1)) * 256 + tok * 16) + ((p * 16 + wq * 4) * 32 + h) * 1024;
      auto ostep = [&](int m, int i) { const f32x4 z4 = {0.f, 0.f, 0.f, 0.f};
        if (i == 1) { __builtin_amdgcn_s_setprio(1);
#pragma unroll
          for (int k = 5; k >= 0; --k) { r6[k] = mfma(ka[k], eb[m][k & 1], z4); asm volatile("" : "+v"(r6[k])); }
          __builtin_amdgcn_s_setprio(0); }
        if (i == 2) { ca = rcp(r6[4][1]); cc = rcp(r6[5][1]); asm volatile("" : "+v"(ca), "+v"(cc)); }
        if (i == 3) { const float inv = rcp(opqf(r6[4][0] * ca + 1e-6f) + r6[5][0] * cc); ca = opqf(ca * inv); cc = opqf(cc * inv); asm volatile("" : "+v"(ca), "+v"(cc)); }
        if (i == 4 || i == 5) { _Pragma("unroll") for (int r = 0; r < 4; ++r) { const int d = i - 4; o8[4 * d + r] = opqf(r6[2 * d][r] * ca) + r6[2 * d + 1][r] * cc; asm volatile("" : "+v"(o8[4 * d + r])); } }
        if (i == 6) st16_sc1(swap16(pk2(o8[0], o8[1]), pk2(o8[2], o8[3]), pk2(o8[4], o8[5]), pk2(o8[6], o8[7])), ro, vo, m * 32768); };
#pragma unroll
      for (int i = 0; i < 6; ++i) ka[i] = lds128(kva + i * 1024);
      SB;
      loadh(c1, c1, c1, hA, 14, [&](auto I) { ostep(0, I()); }); ENDL(9);
      mmy(); ENDM; loadh(c0, c1, c1, hB, 16, [&](auto I) { ostep(1, I()); }); ENDL(9);
      mmy(); ENDM; loadh(c1, c1, c1, hA, 18, [&](auto I) { ostep(2, I()); }); ENDL(9);
      mmy(); ENDM; loadh(c0, c1, c1, hB, 20, [&](auto I) { ostep(3, I()); }); ENDL(9);
      mmy(); ENDM; load(c1, c1, c1, hA, 22); WAIT_V(8);
      set_flag(flags + FL_O / 8 + (2 * p + (wq >> 1)) * 64 + h * 2 + (wq & 1), tag, lane); WAIT_L0; ENDM;
      mmy(); ENDM; load(c0, c1, c1, hB, 24); ENDL(8);
    }
    STAMP(18);
    gu64* po = flags + FL_O / 8 + rt * 64;
    mmy(); ENDM; if (G == 1) POLL_ISSUE(pv, po + lane); load(c1, c1, c1, hA, 25 + G); ENDL(8 + G);
    mmy(); ENDM;
    if (G == 0) { load(c0, c1, c1, hB, 27); ENDL(8); }
    else { WAIT_V(8); POLL_PIN(pv); if (!__all(pv == tag)) wait_flags<64, 1>(po, tag, tmo, lane); SB; load(c0, c1, c2, hB, 28); ENDL(6); }
    mmy(); ENDM; load(c1, c1, c2, hA, 29 + G); ENDL(6);
    gu64* fy = flags + FL_YX / 8 + b * 8 + G * 4 + wq;
    mmy(); ENDM; STAMP(19); load(c0, c0, c2, hB, 31 + G); ENDL(6); STAMP(25);
    if (G == 0) {
      mms_(c1); give(c1); WAIT_L0; ENDM; load(c1, c0, c2, hA, 33); ENDL(6); STAMP(26);
      mms(); take_rd(c1); take(c1); WAIT_L0; ENDM; load(c0, c0, c2, hB, 35); ENDL(10); STAMP(27);
      mms(); give(c0); WAIT_L0; ENDM; load(c1, c0, c2, hA, 37); WAIT_V(6); set_flag(fy, tag, lane); WAIT_L0; ENDM; STAMP(28);
    } else {
      mms_(c1); take_rd(c1); give(c1); take(c1); WAIT_L0; ENDM; load(c1, c0, c2, hA, 34); ENDL(10); STAMP(26);
      mms(); give(c0); WAIT_L0; ENDM; load(c0, c0, c2, hB, 36); WAIT_V(6); set_flag(fy, tag, lane); WAIT_L0; ENDM; STAMP(27);
      mms(); take_rd(c0); take(c0); WAIT_L0; ENDM; load(c1, c0, c2, hA, 38); ENDL(6); STAMP(28);
    }
    u32x4 py[4], xr[2];
    const unsigned vout = (rt * 128 + wi * 64 + 2 * G * 16 + tok) * 2048 + ct * 256 + kh * 128 + jj * 64 + (((g & 1) << 1) | (g >> 1)) * 16;
    mms(); if (G == 0) { take_rd(c0); take(c0); WAIT_L0; } ENDM; { const u32x4 sx = mk_srd(P.x, S_ * D_ * 2); LD16_ISSUE(xr[0], sx, vout, 0u); LD16_ISSUE(xr[1], sx, vout, 32768u); } load(c0, c0, c2, hB, 39 + G); ENDL(8); STAMP(29);
    gu64* py_ = flags + FL_YX / 8 + (b ^ 1) * 8 + G * 4 + wq;
    mms(); ENDM; if (G == 1) POLL_ISSUE(pv, py_); load(c1, c0, c2, hA, 41 + G); ENDL(6 + G); STAMP(20);
    auto fetch = [&]() { WAIT_V(6); POLL_PIN(pv); if (!__all(pv == tag)) wait_flags<1, 1>(py_, tag, tmo, lane); SB;
      const u32x4 syx = mk_srd(ws + WS_YX, 256 * 32768); const unsigned vy = (b ^ 1) * 32768 + (G * 4 + wq) * 4096 + lane * 16;
#pragma unroll
      for (int i = 0; i < 4; ++i) LD16_ISSUE(py[i], syx, vy, i * 1024u);
      };
    f32x2 gt[8], xv[8];
    auto unpack = [&](auto I) { constexpr int i = I();
      if (i == 0) asm volatile("" : "+v"(xr[0]), "+v"(xr[1]));
      if constexpr (i < 2) { const u32x4 xs = swap16(xr[i][0], xr[i][1], xr[i][2], xr[i][3]);
        _Pragma("unroll") for (int c = 0; c < 4; ++c) { xv[4 * i + c] = (f32x2){bflo(xs[c]), bfhi(xs[c])}; asm volatile("" : "+v"(xv[4 * i + c])); } } };
    auto gate = [&]() { WAIT_V(0); asm volatile("" : "+v"(py[0]), "+v"(py[1]), "+v"(py[2]), "+v"(py[3])); SB; f32x2 y[8], e[8];
#define ST8(stmt) _Pragma("unroll") for (int i = 0; i < 8; ++i) { stmt; } SB
      ST8(const f32x4 a = acc[2 * G + (i >> 2)][(i >> 1) & 1]; const f32x4 c = __builtin_bit_cast(f32x4, py[i >> 1]); y[i] = (i & 1) ? a.zw + c.zw : a.xy + c.xy);
      ST8(e[i] = opq2(y[i] * -LOG2E));
      ST8(e[i] = opq2((f32x2){ex2(e[i][0]), ex2(e[i][1])}));
      ST8(e[i] = opq2(e[i] + 1.0f));
      ST8(e[i] = opq2((f32x2){rcp(e[i][0]), rcp(e[i][1])}));
      ST8(gt[i] = y[i] * e[i]; asm volatile("" : "+v"(gt[i]))); };
    auto pass = [&]() {
#pragma unroll
      for (int i = 0; i < 4; ++i) *(LAS f32x4*)(size_t)(exk + G * 16384 + i * 1024) = as[2 * (G ^ 1) + (i >> 1)][i & 1];
      SB; };
    if (G == 0) {
      mms(); ENDM; POLL_ISSUE(pv, py_); load(c0, c0, c2, hB, 43); ENDL(7); STAMP(30);
      mms(); ENDM; fetch(); loadh(c1, c0, c0, hA, 0, unpack); ENDL(4); STAMP(31);
      mms(); MFMA_PAD; STAMP(21); ENDM;
      pass(); gate();
    } else {
      mms(); ENDM; fetch(); loadh(c0, c0, c0, hB, 0, unpack); ENDL(4); STAMP(30);
      mms(); ENDM; load(c1, c0, c0, hA, 0); gate(); WAIT_L0; ENDM; STAMP(31);
      mms(); MFMA_PAD; STAMP(21);
      pass();
    }
    WAIT_L0; ENDM; STAMP(22);
    const __amdgpu_buffer_rsrc_t rout = mk_rsrc(P.out, S_ * D_ * 2);
#pragma unroll
    for (int i = 0; i < 4; ++i) tk[i] = *(const LAS f32x4*)(size_t)(exk + (G ^ 1) * 16384 + i * 1024);
#pragma unroll
    for (int ml = 0; ml < 2; ++ml) { unsigned ow[4];
#pragma unroll
      for (int n = 0; n < 2; ++n) { const int i = ml * 2 + n; const f32x4 s = as[2 * G + ml][n] + tk[i];
        const f32x2 o0 = s.xy * gt[2 * i] + xv[2 * i], o1 = s.zw * gt[2 * i + 1] + xv[2 * i + 1];
        ow[2 * n] = pk2(o0[0], o0[1]); ow[2 * n + 1] = pk2(o1[0], o1[1]); }
      st16(swap16(ow[0], ow[1], ow[2], ow[3]), rout, vout, ml * 16 * 2048); }
    if (G == 0 && b == 0 && wq == 0) set_flag(flags + FL_EPOCH / 8, tag, lane);
    STAMP(23);
  }
};
#define P2RUN(p2, P, tag, eb) p2.run(P, tag, p, h, eb)

__global__ __launch_bounds__(512, 2) void k_fused(const u16* x, const u16* wqkv, const u16* wphi, const u16* wo, const u16* w1, const u16* b1, const u16* w2, char* ws, u16* out) {
  __shared__ __attribute__((aligned(16))) char smem[163840];
  const Ptrs P = {x, wqkv, wphi, wo, w1, b1, w2, ws, out};
  const unsigned lb = (unsigned)(size_t)(LAS char*)smem;
  const int tid = threadIdx.x, lane = tid & 63, w = __builtin_amdgcn_readfirstlane(tid >> 6), wx = w & 3;
  const int b = blockIdx.x, h = (b & 7) * 4 + ((b >> 3) & 3), p = b >> 5, odd = h & 1;
  const int tok = lane & 15, g = lane >> 4, q = (lane >> 2) & 3, pp = lane & 3;
  const unsigned ra0 = lb + L1_A + (wx * 64 + tok) * 128 + ((g ^ (tok >> 1)) << 4), ra1 = ra0 ^ 64;
  gu64* flags = (gu64*)(ws + WS_FLAG); gu64* tmo = flags + FL_TMO / 8;
  STAMP_DECL; STAMP(0);
  const int rot = p < 7 ? 2 * p : 0;
  auto kt = [&](int pos) { int t = pos + rot; t -= t >= 16 ? 14 : 0; return pos < 2 ? pos : t; };
  u64 ep; { gu64* pe = flags + FL_EPOCH / 8; POLL_ISSUE(ep, pe); }
  const __amdgpu_buffer_rsrc_t rfl = mk_rsrc(ws + WS_FLAG, 1u << 20);
  bf16x8 ones = {0x3F80, 0x3F80, 0x3F80, 0x3F80, 0x3F80, 0x3F80, 0x3F80, 0x3F80}; asm volatile("" : "+v"(ones));
  auto cs_publish = [&](const f32x4& c0, const f32x4& c1, int j0, bool two, u64 tag) {
    const f32x4 a = (tok & 2) ? c1 : c0; const u32x4 v = {__float_as_uint((tok & 1) ? a[2] : a[0]), (unsigned)tag, __float_as_uint((tok & 1) ? a[3] : a[1]), (unsigned)tag};
    if (tok < (two ? 4 : 2)) st16_sc1(v, rfl, ((j0 + 2 * (tok & 2)) * 16 + 4 * g + 2 * (tok & 1)) * 8, GR_CS + (h * 8 + p) * 1664);
    SB; };

  if (w < 4) {
    const u32x4 sa = mk_srd(x, S_ * D_ * 2);
    unsigned va0, va1;
    { const int r = lane >> 3; va0 = (p * 256 + wx * 64 + r) * 2048 + (((lane & 7) ^ (r >> 1)) << 4); va1 = va0 ^ 64; }
    const unsigned sl = lb + L1_A + wx * 8192;
    const unsigned rb0 = opq(lb + L1_BH + (8 * g + 4 * (g & 1) + q) * 224 + 8 * pp), rb1 = opq(lb + L1_BH + (8 * g + 4 * ((g & 1) ^ 1) + q) * 224 + 8 * pp);
    f32x4 acc[4][7], cs0, cs1;
    bf16x8 fa[2][4], fb[2][7];
    auto dmaA = [&](auto ST, auto J, unsigned sk) { constexpr int st = ST(), j = J(); if (j & 1) DMA(sa, va1, sk, j * 16384, sl, st * 32768 + j * 1024); else DMA(sa, va0, sk, j * 16384, sl, st * 32768 + j * 1024); };
    auto rdA = [&](auto ST, auto KS, auto M) { constexpr int st = ST(), ks = KS(), m = M(); fa[ks][m] = lds128((ks ? ra1 : ra0) + st * 32768 + m * 2048); SB; };
    auto rdB = [&](auto ST, auto KS, auto J) { constexpr int st = ST(), ks = KS(), j = J(); constexpr int o = st * 14336 + ks * 7168 + j * 32; fb[ks][j] = cat(ldstr(rb0 + o), ldstr(rb1 + o)); SB; };
    auto load = [&](auto ST, auto DO, unsigned sk) {
      constexpr int o = ST() ^ 1; constexpr bool d = DO();
      rdA(ST, IC<0>{}, IC<0>{}); rdB(ST, IC<0>{}, IC<0>{}); rdB(ST, IC<0>{}, IC<1>{}); if (d) dmaA(IC<o>{}, IC<0>{}, sk);
      rdA(ST, IC<0>{}, IC<1>{}); rdB(ST, IC<0>{}, IC<2>{}); rdB(ST, IC<0>{}, IC<3>{}); if (d) dmaA(IC<o>{}, IC<1>{}, sk);
      rdA(ST, IC<0>{}, IC<2>{}); rdB(ST, IC<0>{}, IC<4>{}); rdB(ST, IC<0>{}, IC<5>{}); if (d) dmaA(IC<o>{}, IC<2>{}, sk);
      rdA(ST, IC<0>{}, IC<3>{}); rdB(ST, IC<0>{}, IC<6>{}); rdB(ST, IC<1>{}, IC<0>{}); if (d) dmaA(IC<o>{}, IC<3>{}, sk);
      rdA(ST, IC<1>{}, IC<0>{}); rdB(ST, IC<1>{}, IC<1>{}); rdB(ST, IC<1>{}, IC<2>{}); if (d) dmaA(IC<o>{}, IC<4>{}, sk);
      rdA(ST, IC<1>{}, IC<1>{}); rdB(ST, IC<1>{}, IC<3>{}); rdB(ST, IC<1>{}, IC<4>{}); if (d) dmaA(IC<o>{}, IC<5>{}, sk);
      rdA(ST, IC<1>{}, IC<2>{}); rdB(ST, IC<1>{}, IC<5>{}); if (d) dmaA(IC<o>{}, IC<6>{}, sk);
      rdA(ST, IC<1>{}, IC<3>{}); rdB(ST, IC<1>{}, IC<6>{}); if (d) dmaA(IC<o>{}, IC<7>{}, sk);
    };
    auto mm_ = [&](auto F) {
#pragma unroll
      for (int m = 0; m < 4; ++m)
#pragma unroll
        for (int j = 0; j < 7; ++j) mk2<F()>(acc[m][j], fb[0][j], fa[0][m], fb[1][j], fa[1][m]);
      SB; };
    auto mm = [&]() { mm_(IC<0>{}); };
#define A8(ST, sk) dmaA(IC<ST>{}, IC<0>{}, sk); dmaA(IC<ST>{}, IC<1>{}, sk); dmaA(IC<ST>{}, IC<2>{}, sk); dmaA(IC<ST>{}, IC<3>{}, sk); dmaA(IC<ST>{}, IC<4>{}, sk); dmaA(IC<ST>{}, IC<5>{}, sk); dmaA(IC<ST>{}, IC<6>{}, sk); dmaA(IC<ST>{}, IC<7>{}, sk)
    if (wx == 0 && TOUCH0) { const int l = min(lane, 51), ts = l / 26, rem = l - ts * 26, rr = rem / 13, li = rem - rr * 13, k = ts * 64 + (b >> 3) * 2 + rr;
      TOUCH(li < 6 ? (const GAS char*)wqkv + k * 6144 + (li >> 1) * 2048 + (b & 7) * 256 + (li & 1) * 128 : (const GAS char*)w1 + k * 7168 + (b & 7) * 896 + (li - 6) * 128, lb + L1_DUMP); }
    if (wx == 1 && TOUCH0) TOUCH((const GAS char*)x + (p * 256 + ((b >> 3) & 3) * 64 + lane) * 2048 + (b & 1) * 128, lb + L1_DUMP);
    A8(0, 0u);
    if (wx == 0) asm volatile("s_mov_b32 m0, %2\n\ts_nop 0\n\tbuffer_load_dword %0, %1, 0 offen lds" :: "v"(h * 224 + lane * 4), "s"(mk_srd(b1, F_ * 2)), "s"(lb + L1_BIAS) : "memory");
    cs0 = zacc(); cs1 = zacc();
    auto colsum = [&](bool now) { colsum7(cs0, cs1, fb[0], fb[1], ones, now ? wx : 4); SB; };
    if (tid == 0) *(LAS u64*)(size_t)(lb + L1_CNT) = 0ull;
    STAMP(1);
    WAIT_V(0); POLL_PIN(ep); const u64 tag = uni64(ep) + 1;
    A8(1, 128u);
    WAIT_L0; BAR; SB; STAMP(4);
    load(IC<0>{}, IC<0>{}, 0u); WAIT_L0; BAR; SB; STAMP(5);
#pragma unroll
    for (int t = 0; t < 4; t += 2) {
      if (t == 0) mm_(IC<1>{}); else mm();
      colsum((p == 7) == (t == 0)); WAIT_V(0); BAR; SB;
      load(IC<1>{}, IC<1>{}, (unsigned)kt(t + 2) * 128u); WAIT_L0; BAR; SB;
      mm(); colsum((p == 7) == (t == 0)); WAIT_V(0); BAR; SB;
      load(IC<0>{}, IC<1>{}, (unsigned)kt(t + 3) * 128u); if (t == 2) cs_publish(cs0, cs1, wx, wx < 3, tag); WAIT_L0; BAR; SB;
    }
    for (int t = 4; t < 14; t += 2) {
      mm(); WAIT_V(0); BAR; SB;
      load(IC<1>{}, IC<1>{}, (unsigned)kt(t + 2) * 128u); WAIT_L0; BAR; SB;
      mm(); WAIT_V(0); BAR; SB;
      load(IC<0>{}, IC<1>{}, (unsigned)kt(t + 3) * 128u); WAIT_L0; BAR; SB;
    }
    STAMP(6);
    mm(); WAIT_V(0); BAR; SB;
    u32x2 bias[7];
#pragma unroll
    for (int j = 0; j < 7; ++j) bias[j] = *(const LAS u32x2*)(size_t)(lb + L1_BIAS + (j < 6 ? j + odd : (odd ? 0 : 6)) * 32 + g * 8);
    SB;
    { const unsigned dump = lb + L1_DUMP; const int cu = b >> 3, a = (b & 7) >> 1;
      const int l2 = min(lane, 55) + 56 * (wx + 4 * cu);
      if (TOUCHW) TOUCH((const GAS char*)w2 + ((b & 1) * 1792 + a * 448) * 2048 + l2 * 128, dump);
      if (TOUCHW) TOUCH((const GAS char*)wo + (b * 64 + wx * 16 + (lane & 15)) * 128, dump); }
    load(IC<1>{}, IC<0>{}, 0u);
    f32x4 bl[7];
#pragma unroll
    for (int j = 0; j < 7; ++j) { bl[j] = (f32x4){opqf(bflo(bias[j][0]) * LOG2E), opqf(bfhi(bias[j][0]) * LOG2E), opqf(bflo(bias[j][1]) * LOG2E), opqf(bfhi(bias[j][1]) * LOG2E)}; asm volatile("" : "+v"(bl[j])); }
    WAIT_L0; BAR; SB;
    mm(); BAR; MFMA_PAD; STAMP(7);
    f32x4 cs[7]; float rl[4], rml[4];
#pragma unroll
    for (int j = 0; j < 7; ++j) cs[j] = *(const LAS f32x4*)(size_t)(lb + L1_CS + j * 64 + g * 16);
#pragma unroll
    for (int m = 0; m < 4; ++m) { const f32x2 st = *(const LAS f32x2*)(size_t)(lb + L1_ST + (wx * 64 + m * 16 + tok) * 8); rl[m] = opqf(st[0] * LOG2E); rml[m] = opqf(-rl[m] * st[1]); }
    SB;
    Phase2<0> p2; p2.init(lb, lane, wx, P);
    const __amdgpu_buffer_rsrc_t ract = mk_rsrc(ws + WS_ACT, 128 * 112 * 1024);
    auto mish8 = [&](int m0, int j0, int m1, int j1) -> u32x4 {
      const f32x2 a[4] = {acc[m0][j0].xy, acc[m0][j0].zw, acc[m1][j1].xy, acc[m1][j1].zw}, c[4] = {cs[j0].xy, cs[j0].zw, cs[j1].xy, cs[j1].zw}, bb[4] = {bl[j0].xy, bl[j0].zw, bl[j1].xy, bl[j1].zw};
      f32x2 t[4], hs[4], e[4], d[4]; unsigned o[4];
#define ST4(stmt) _Pragma("unroll") for (int i = 0; i < 4; ++i) { stmt; } SB
      ST4(t[i] = c[i] * rml[i < 2 ? m0 : m1] + bb[i]);
      ST4(hs[i] = a[i] * rl[i < 2 ? m0 : m1] + t[i]);
      ST4(e[i] = opq2((f32x2){ex2(hs[i][0]), ex2(hs[i][1])}));
      ST4(t[i] = e[i] + 2.0f);
      ST4(d[i] = opq2(e[i] * t[i] + 2.0f));
      ST4(e[i] = opq2((f32x2){rcp(d[i][0]), rcp(d[i][1])}));
      ST4(t[i] = e[i] * (-2.0f / LOG2E) + 1.0f / LOG2E);
      ST4(d[i] = hs[i] * t[i]);
      ST4(o[i] = pk2(d[i][0], d[i][1]));
      return swap16(o[0], o[1], o[2], o[3]); };
    const unsigned vpair = (unsigned)((((g & 1) << 1) | (g >> 1)) * 256 + tok * 16), mb0 = p * 16 + wx * 4;
    auto pair = [&](auto J, auto M) { constexpr int j = J(), m = M();
      st16_sc1(mish8(m, j, m, j + 1), ract, vpair, ((mb0 + m) * 112 + ((7 * h + odd + j) >> 1)) * 1024); SB;
      constexpr int n = 2 * j + m - BLATE; if constexpr (n >= 0 && n < 6) { p2.dma1<n / 2, 4 + 2 * (n & 1)>(); p2.dma1<n / 2, 5 + 2 * (n & 1)>(); } };
    gu64* fact = flags + FL_ACT / 8 + (((2 * p + (wx >> 1)) * 2 + (h >> 4)) * 32 + (h & 15) * 2 + (wx & 1)) * 16;
    STAMP(13);
    if (XP0) __builtin_amdgcn_s_setprio(2);
    pair(IC<0>{}, IC<0>{}); pair(IC<0>{}, IC<1>{}); pair(IC<0>{}, IC<2>{}); pair(IC<0>{}, IC<3>{});
    if (XP0) __builtin_amdgcn_s_setprio(0);
    STAMP(14);
    pair(IC<2>{}, IC<0>{}); pair(IC<2>{}, IC<1>{});
    STAMP(15);
    WAIT_V(2); set_flag(fact, tag, lane); SB; STAMP(8);
    gu64* fa1 = flags + FL_ACT / 8 + (p2.rt * 2 + p2.kh) * 512; u64 pv;
    const unsigned vsing = (unsigned)((g & 1) * 112 * 1024 + (2 * odd + (g >> 1)) * 256 + tok * 16);
    auto single = [&](int m) {
      st16_sc1(mish8(m, 6, m + 1, 6), ract, vsing, ((mb0 + m) * 112 + ((7 * h + (odd ? 0 : 6)) >> 1)) * 1024); SB; };
    auto geta = [&]() { POLL_PIN(pv); STAMP(10); if (!__all(pv == tag)) wait_flags<32, 16>(fa1, tag, tmo, lane);
      SB; STAMP(11); p2.dma4<0, 0>(); p2.dma4<1, 0>(); p2.dma4<2, 0>(); };
    pair(IC<2>{}, IC<2>{}); pair(IC<2>{}, IC<3>{});
    if (EARLYA == 0) {
      pair(IC<4>{}, IC<0>{}); pair(IC<4>{}, IC<1>{}); pair(IC<4>{}, IC<2>{}); pair(IC<4>{}, IC<3>{});
      POLL_ISSUE(pv, fa1 + (lane & 31) * 16); single(0); single(2);
      STAMP(9); WAIT_V(2); geta(); WAIT_V(12);
    } else {
      pair(IC<4>{}, IC<0>{}); POLL_ISSUE(pv, fa1 + (lane & 31) * 16); pair(IC<4>{}, IC<1>{}); pair(IC<4>{}, IC<2>{});
      WAIT_V(4); geta(); pair(IC<4>{}, IC<3>{}); single(0); single(2); STAMP(9); WAIT_V(0);
    }
    set_flag(fact + 131072 / 8, tag, lane); SB; STAMP(12);
    const bf16x8 none[4][2] = {};
    P2RUN(p2, P, tag, none);
  } else {
    const int srow = p * 256 + h * 8 + wx * 2; u32x4 xa[4];
    { const u32x4 sx = mk_srd(x, S_ * D_ * 2); const unsigned vx = srow * 2048 + lane * 16;
      asm volatile("buffer_load_dwordx4 %0, %4, %5, 0 offen\n\tbuffer_load_dwordx4 %1, %4, %5, 0 offen offset:1024\n\tbuffer_load_dwordx4 %2, %4, %5, 0 offen offset:2048\n\tbuffer_load_dwordx4 %3, %4, %5, 0 offen offset:3072"
                   : "=&v"(xa[0]), "=&v"(xa[1]), "=&v"(xa[2]), "=&v"(xa[3]) : "v"(vx), "s"(sx) : "memory"); SB; }
    const u32x4 sq = mk_srd(wqkv, D_ * 3 * D_ * 2), sh = mk_srd(w1, D_ * F_ * 2), sp = mk_srd(wphi, 32 * 32 * 32 * 2);
    unsigned vq, vh[4];
    { const int r = lane >> 2; vq = (wx * 16 + r) * 6144 + h * 64 + (((lane & 3) ^ (((r >> 3) & 1) << 1)) << 4); }
#pragma unroll
    for (int i = 0; i < 4; ++i) { const int o = (wx + 4 * i) * 1024 + lane * 16, rp = o / 224, c = (o - rp * 224) >> 4, k = rp ^ (((rp >> 3) & 1) << 2); int cs = c + 2 * odd; if (cs >= 14) cs -= 14;
      vh[i] = k * 7168 + h * 224 + cs * 16; }
    const unsigned slq = lb + L1_BQ + wx * 1024, slh = lb + L1_BH + wx * 1024;
    const unsigned rq0 = opq(lb + L1_BQ + (8 * g + q) * 64 + 32 * (g & 1) + 8 * pp), rq1 = opq(rq0 ^ 32);
    f32x4 acc[3][4][2], cs0, cs1;
    bf16x8 fa[2][4], fb[2][6];
    auto dmaQ = [&](auto ST, auto U, unsigned sk) { constexpr int st = ST(), u = U(); DMA(sq, vq, sk, u * 2048, slq, st * 12288 + u * 4096); };
    auto dmaH = [&](auto ST, auto I, unsigned sk) { constexpr int st = ST(), i = I(); if (i < 3 || wx < 2) DMA(sh, vh[i], sk, 0, slh, st * 14336 + i * 4096); };
    auto rdA = [&](auto ST, auto KS, auto M) { constexpr int st = ST(), ks = KS(), m = M(); fa[ks][m] = lds128((ks ? ra1 : ra0) + st * 32768 + m * 2048); SB; };
    auto rdB = [&](auto ST, auto KS, auto J) { constexpr int st = ST(), ks = KS(), j = J(); constexpr int o = st * 12288 + (j >> 1) * 4096 + ks * 2048; const unsigned a = (j & 1) ? rq1 : rq0;
      fb[ks][j] = cat(ldstr(a + o), ldstr(a + o + 256)); SB; };
    auto load = [&](auto ST, auto DQ, auto DH, int t) {
      constexpr int o = ST() ^ 1; constexpr bool dq = DQ(), dh = DH(); const unsigned skq = (unsigned)kt(t + 1) * (64 * 6144), skh = (unsigned)kt(t + 2) * (64 * 7168);
      rdA(ST, IC<0>{}, IC<0>{}); rdB(ST, IC<0>{}, IC<0>{}); rdB(ST, IC<0>{}, IC<1>{}); if (dq) dmaQ(IC<o>{}, IC<0>{}, skq);
      rdA(ST, IC<0>{}, IC<1>{}); rdB(ST, IC<0>{}, IC<2>{}); rdB(ST, IC<0>{}, IC<3>{}); if (dq) dmaQ(IC<o>{}, IC<1>{}, skq);
      rdA(ST, IC<0>{}, IC<2>{}); rdB(ST, IC<0>{}, IC<4>{}); rdB(ST, IC<0>{}, IC<5>{}); if (dq) dmaQ(IC<o>{}, IC<2>{}, skq);
      rdA(ST, IC<0>{}, IC<3>{}); rdB(ST, IC<1>{}, IC<0>{}); if (dh) dmaH(ST, IC<0>{}, skh);
      rdA(ST, IC<1>{}, IC<0>{}); rdB(ST, IC<1>{}, IC<1>{}); rdB(ST, IC<1>{}, IC<2>{}); if (dh) dmaH(ST, IC<1>{}, skh);
      rdA(ST, IC<1>{}, IC<1>{}); rdB(ST, IC<1>{}, IC<3>{}); if (dh) dmaH(ST, IC<2>{}, skh);
      rdA(ST, IC<1>{}, IC<2>{}); rdB(ST, IC<1>{}, IC<4>{});
      rdA(ST, IC<1>{}, IC<3>{}); rdB(ST, IC<1>{}, IC<5>{});
      if (dh) { if (wx < 2) { dmaH(ST, IC<3>{}, skh); WAIT_V(7); } else WAIT_V(6); }
    };
    auto mm_ = [&](auto F) {
#pragma unroll
      for (int m = 0; m < 4; ++m)
#pragma unroll
        for (int j = 0; j < 6; ++j) mk2<F()>(acc[j >> 1][m][j & 1], fb[0][j], fa[0][m], fb[1][j], fa[1][m]);
      SB; };
    auto mm = [&]() { mm_(IC<0>{}); };
#define H4(ST, sk) dmaH(IC<ST>{}, IC<0>{}, sk); dmaH(IC<ST>{}, IC<1>{}, sk); dmaH(IC<ST>{}, IC<2>{}, sk); dmaH(IC<ST>{}, IC<3>{}, sk)
#define Q3(ST, sk) dmaQ(IC<ST>{}, IC<0>{}, sk); dmaQ(IC<ST>{}, IC<1>{}, sk); dmaQ(IC<ST>{}, IC<2>{}, sk)
    H4(0, 0u); Q3(0, 0u);
    cs0 = zacc(); cs1 = zacc();
    auto colsum = [&](bool now) { const bf16x8 a[7] = {fb[0][0], fb[0][1], fb[0][2], fb[0][3], fb[0][4], fb[0][5], ones}, c[7] = {fb[1][0], fb[1][1], fb[1][2], fb[1][3], fb[1][4], fb[1][5], ones};
      colsum7(cs0, cs1, a, c, ones, now ? wx : 4); SB; };
    STAMP(1);
    if (wx < 2) WAIT_V(7); else WAIT_V(6);
    asm volatile("" : "+v"(xa[0]), "+v"(xa[1]), "+v"(xa[2]), "+v"(xa[3]), "+v"(ep));
    const u64 tag = uni64(ep) + 1;
    { float v[2][16], sm[2], sq_[2], mean[2];
#pragma unroll
      for (int r = 0; r < 2; ++r) { sm[r] = 0.f;
#pragma unroll
        for (int i = 0; i < 8; ++i) { const unsigned u = xa[2 * r + (i >> 2)][i & 3]; v[r][2 * i] = bflo(u); v[r][2 * i + 1] = bfhi(u); sm[r] += v[r][2 * i] + v[r][2 * i + 1]; } }
#pragma unroll
      for (int r = 0; r < 2; ++r) { mean[r] = wave_sum(sm[r]) * (1.0f / D_); sq_[r] = 0.f;
#pragma unroll
        for (int i = 0; i < 16; ++i) { const float d = v[r][i] - mean[r]; sq_[r] += d * d; } }
      const float r0 = __builtin_amdgcn_rsqf(wave_sum(sq_[0]) * (1.0f / D_) + 1e-5f), r1 = __builtin_amdgcn_rsqf(wave_sum(sq_[1]) * (1.0f / D_) + 1e-5f);
      const u32x4 gr = {__float_as_uint(lane ? mean[1] : mean[0]), (unsigned)tag, __float_as_uint(lane ? r1 : r0), (unsigned)tag};
      if (lane < 2) st16_sc1(gr, rfl, lane * 16, GR_ST + srow * 16);
      SB; }
    STAMP(2);
    WAIT_V(1);
    STAMP(3); BAR; SB; STAMP(4);
    if (wx < 2) { const unsigned vp = h * 2048 + wx * 1024 + lane * 16, slp = lb + L1_WPHI + wx * 1024; DMA(sp, vp, 0u, 0, slp, 0); }
    H4(1, 64u * 7168u); Q3(1, 64u * 6144u);
    BAR; SB; STAMP(5);
    load(IC<0>{}, IC<0>{}, IC<1>{}, 0); WAIT_L0; BAR; SB;
    mm_(IC<1>{}); colsum(p == 7); WAIT_V(3); BAR; SB;
    load(IC<1>{}, IC<1>{}, IC<1>{}, 1); WAIT_L0; BAR; SB;
    mm(); colsum(p == 7); WAIT_V(3); BAR; SB;
    load(IC<0>{}, IC<1>{}, IC<1>{}, 2); WAIT_L0; BAR; SB;
    mm(); colsum(p != 7); WAIT_V(3); BAR; SB;
    load(IC<1>{}, IC<1>{}, IC<1>{}, 3); WAIT_L0; BAR; SB;
    mm(); colsum(p != 7); WAIT_V(3); BAR; MFMA_PAD;
    cs_publish(cs0, cs1, 7 + wx, wx < 2, tag);
    for (int t = 4; t < 14; t += 2) {
      load(IC<0>{}, IC<1>{}, IC<1>{}, t); WAIT_L0; BAR; SB;
      mm(); WAIT_V(3); BAR; SB;
      load(IC<1>{}, IC<1>{}, IC<1>{}, t + 1); WAIT_L0; BAR; SB;
      mm(); WAIT_V(3); BAR; SB;
    }
    STAMP(6);
    const unsigned vst = GR_ST + (p * 256 + wx * 64 + lane) * 16, vcs = GR_CS + h * (8 * 1664) + min(wx * 64 + lane, 103) * 16;
    const u32x4 sfl = mk_srd(ws + WS_FLAG, 1u << 20);
    u32x4 gs, gc[8]; LD16_ISSUE(gs, sfl, vst, 0u);
#pragma unroll
    for (int i = 0; i < 8; ++i) LD16_ISSUE(gc[i], sfl, vcs, i * 1664u);
    load(IC<0>{}, IC<1>{}, IC<0>{}, 14);
    WAIT_V(12); WAIT_L0; BAR; SB;
    mm(); WAIT_V(0); BAR; SB;
    load(IC<1>{}, IC<0>{}, IC<0>{}, 15);
    { auto stale = [&]() { unsigned d = differ(gs[1], gs[3], (unsigned)tag);
#pragma unroll
        for (int i = 0; i < 8; ++i) d |= differ(gc[i][1], gc[i][3], (unsigned)tag);
        return !__all(d == 0u); };
      asm volatile("" : "+v"(gs), "+v"(gc[0]), "+v"(gc[1]), "+v"(gc[2]), "+v"(gc[3]), "+v"(gc[4]), "+v"(gc[5]), "+v"(gc[6]), "+v"(gc[7]));
      if (__builtin_expect(stale(), 0)) {
#pragma nounroll
        for (unsigned spins = 0; spins < (1u << 16); ++spins) {
          asm volatile("buffer_load_dwordx4 %0, %1, %2, 0 offen sc1" : "+v"(gs) : "v"(vst), "s"(sfl) : "memory");
#pragma unroll
          for (int i = 0; i < 8; ++i) asm volatile("buffer_load_dwordx4 %0, %1, %2, %3 offen sc1" : "+v"(gc[i]) : "v"(vcs), "s"(sfl), "s"(i * 1664u) : "memory");
          asm volatile("s_waitcnt vmcnt(0)" : "+v"(gs), "+v"(gc[0]), "+v"(gc[1]), "+v"(gc[2]), "+v"(gc[3]), "+v"(gc[4]), "+v"(gc[5]), "+v"(gc[6]), "+v"(gc[7]) :: "memory");
          if (!stale()) break; } }
      f32x2 c = {__uint_as_float(gc[0][0]), __uint_as_float(gc[0][2])};
#pragma unroll
      for (int i = 1; i < 8; ++i) c = (f32x2){add1(c[0], __uint_as_float(gc[i][0])), add1(c[1], __uint_as_float(gc[i][2]))};
      *(LAS f32x2*)(size_t)(lb + L1_CS + min(wx * 64 + lane, 103) * 8) = c;
      *(LAS f32x2*)(size_t)(lb + L1_ST + (wx * 64 + lane) * 8) = (f32x2){__uint_as_float(gs[2]), __uint_as_float(gs[0])}; SB; }
    WAIT_L0; BAR; SB;
    mm(); MFMA_PAD; STAMP(7);
    if (YPRIO) __builtin_amdgcn_s_setprio(1);
    {
    const int lane = opq(__lane_id()), tok = lane & 15, g = lane >> 4, q = (lane >> 2) & 3, pp = lane & 3;
    bf16x8 wp[2];
#pragma unroll
    for (int ff = 0; ff < 2; ++ff) { const unsigned a = lb + L1_WPHI + (4 * g + q) * 64 + ff * 32 + 8 * pp; wp[ff] = cat(ldstr(a), ldstr(a + 1024)); }
    f32x4 cs[6]; float rs[4], mean[4], rl[4];
#pragma unroll
    for (int j = 0; j < 6; ++j) cs[j] = *(const LAS f32x4*)(size_t)(lb + L1_CS + (7 + j) * 64 + g * 16);
#pragma unroll
    for (int m = 0; m < 4; ++m) { const f32x2 st = *(const LAS f32x2*)(size_t)(lb + L1_ST + (wx * 64 + m * 16 + tok) * 8); rs[m] = st[0]; mean[m] = opqf(-st[1]); rl[m] = st[0] * LOG2E; }
    const f32x4 zero4 = {0.f, 0.f, 0.f, 0.f};
    auto project = [&](int u, f32x4 (&pr)[4][2]) {
#pragma unroll
      for (int m = 0; m < 4; ++m) { const bf16x8 kb = pk8(acc[u][m][0] + cs[2 * u] * mean[m], acc[u][m][1] + cs[2 * u + 1] * mean[m]); pr[m][0] = mfma(wp[0], kb, zero4); pr[m][1] = mfma(wp[1], kb, zero4); }
      SB; };
    auto extrema = [&](const f32x4 (&pr)[4][2], float (&ma)[4], float (&mb)[4]) {
#pragma unroll
      for (int m = 0; m < 4; ++m) { ma[m] = vmax8(pr[m][0], pr[m][1]); mb[m] = vmax8(-pr[m][0], -pr[m][1]); }
      spread4(gather4(ma[0], mb[0], ma[1], mb[1], vmax), ma[0], mb[0], ma[1], mb[1]);
      spread4(gather4(ma[2], mb[2], ma[3], mb[3], vmax), ma[2], mb[2], ma[3], mb[3]);
#pragma unroll
      for (int m = 0; m < 4; ++m) { ma[m] *= -rl[m]; mb[m] *= -rl[m]; }
      SB; };
    auto expo = [&](int m, const f32x4 (&pr)[2], float ma, float mb, f32x4 (&ep)[2], f32x4 (&en)[2]) {
#pragma unroll
      for (int ff = 0; ff < 2; ++ff) { ep[ff] = pr[ff] * rl[m] + ma; en[ff] = pr[ff] * -rl[m] + mb; }
      SB;
#pragma unroll
      for (int ff = 0; ff < 2; ++ff)
#pragma unroll
        for (int r = 0; r < 4; ++r) { ep[ff][r] = ex2(ep[ff][r]); en[ff][r] = ex2(en[ff][r]); }
      SB; };
    { const unsigned swp = (tok & 1) | (((tok >> 2) & 1) << 1) | (((tok >> 1) & 1) << 2) | (tok & 8);
      const unsigned wph = lb + L1_PHI + (wx * 64 + tok) * 128 + ((g ^ swp) << 3), wv = lb + L1_V + (wx * 64 + tok) * 64 + ((g ^ (tok >> 1)) << 3);
      const unsigned wp0 = opq(wph), wp1 = opq(wph ^ 32), wp2 = opq(wph ^ 64), wp3 = opq(wph ^ 96), wv0 = opq(wv), wv1 = opq(wv ^ 32);
      auto put = [](unsigned a, f32x4 v) { *(LAS u32x2*)(size_t)a = (u32x2){pk2(v[0], v[1]), pk2(v[2], v[3])}; };
      f32x4 pr[4][2], ep[4][2], en[4][2]; float ma[4], mb[4];
      WAIT_L0; project(1, pr);
#pragma unroll
      for (int m = 0; m < 4; ++m) { put(wv0 + m * 1024, (acc[2][m][0] + cs[4] * mean[m]) * rs[m]); put(wv1 + m * 1024, (acc[2][m][1] + cs[5] * mean[m]) * rs[m]); }
      SB; extrema(pr, ma, mb);
#pragma unroll
      for (int m = 0; m < 4; ++m) expo(m, pr[m], ma[m], mb[m], ep[m], en[m]);
#pragma unroll
      for (int m = 0; m < 4; ++m) { const f32x4 a = ep[m][0] + ep[m][1], c = en[m][0] + en[m][1]; const f32x2 a2 = a.xy + a.zw, c2 = c.xy + c.zw; ma[m] = a2[0] + opqf(a2[1]); mb[m] = c2[0] + opqf(c2[1]); }
      auto fadd = [](float a, float c) { return a + c; };
      spread4(rcp(gather4(ma[0], mb[0], ma[1], mb[1], fadd)), ma[0], mb[0], ma[1], mb[1]);
      spread4(rcp(gather4(ma[2], mb[2], ma[3], mb[3], fadd)), ma[2], mb[2], ma[3], mb[3]);
      SB;
#pragma unroll
      for (int m = 0; m < 4; ++m) { ep[m][0] *= ma[m]; ep[m][1] *= ma[m]; en[m][0] *= mb[m]; en[m][1] *= mb[m]; SB;
        put(wp0 + m * 2048, ep[m][0]); put(wp1 + m * 2048, ep[m][1]); put(wp2 + m * 2048, en[m][0]); put(wp3 + m * 2048, en[m][1]); SB; } }
    STAMP(8);
    asm volatile("s_waitcnt lgkmcnt(0)" ::: "memory");
    if (lane == 0) { const unsigned one = 1, a = lb + L1_CNT; asm volatile("ds_add_u32 %0, %1" :: "v"(a), "v"(one) : "memory"); }
    bf16x8 eb[4][2];
    f32x4 pq[4][2]; float qa[4], qb[4];
    project(0, pq); extrema(pq, qa, qb);
    auto qpart = [&](int m0) {
#pragma unroll
      for (int m = m0; m < m0 + 2; ++m) { f32x4 ep[2], en[2]; expo(m, pq[m], qa[m], qb[m], ep, en); eb[m][0] = pk8(ep[0], ep[1]); eb[m][1] = pk8(en[0], en[1]); asm volatile("" : "+v"(eb[m][0]), "+v"(eb[m][1])); SB; } };
    lds_wait(lb + L1_CNT, 4u); SB; STAMP(9);
    { const bf16x8 ones = {0x3F80, 0x3F80, 0x3F80, 0x3F80, 0x3F80, 0x3F80, 0x3F80, 0x3F80};
      unsigned ap[2], av[2][2];
#pragma unroll
      for (int t = 0; t < 2; ++t) { const int tk = 8 * g + 4 * t + q, t15 = tk & 15; const unsigned sw = (t15 & 1) | (((t15 >> 2) & 1) << 1) | (((t15 >> 1) & 1) << 2) | (t15 & 8);
        ap[t] = opq(lb + L1_PHI + tk * 128 + (((4 * wx + pp) ^ sw) << 3));
        av[t][0] = opq(lb + L1_V + tk * 64 + ((pp ^ (t15 >> 1)) << 3)); av[t][1] = opq(av[t][0] ^ 32); }
      bf16x8 a[8], b0[8], b1_[8];
#pragma unroll
      for (int ks = 0; ks < 8; ++ks) { a[ks] = cat(ldstr(ap[0] + ks * 4096), ldstr(ap[1] + ks * 4096));
        b0[ks] = cat(ldstr(av[0][0] + ks * 2048), ldstr(av[1][0] + ks * 2048)); b1_[ks] = cat(ldstr(av[0][1] + ks * 2048), ldstr(av[1][1] + ks * 2048)); }
      SB; qpart(0); STAMP(12);
      f32x4 kv0 = zero4, kv1 = zero4, zz = zero4;
#pragma unroll
      for (int ks = 0; ks < 8; ++ks) { kv0 = mfma(a[ks], b0[ks], kv0); kv1 = mfma(a[ks], b1_[ks], kv1); zz = mfma(a[ks], ones, zz); }
      const __amdgpu_buffer_rsrc_t rkv = mk_rsrc(ws + WS_KVP, 3u << 20);
      st16_sc1(__builtin_bit_cast(u32x4, pk8(kv0, kv1)), rkv, lane * 16, (h * 8 + p) * 4096 + wx * 1024);
      if (tok == 0) st16_sc1(__builtin_bit_cast(u32x4, zz), rkv, g * 16, (unsigned)(WS_ZP - WS_KVP) + (h * 8 + p) * 256 + wx * 64);
      SB; STAMP(13); }
    qpart(2);
    SB; if (YPRIO) __builtin_amdgcn_s_setprio(0); STAMP(10); WAIT_V(0);
    set_flag(flags + (FL_KV + (h * 4 + wx) * 1024) / 8 + p * 16, tag, lane); STAMP(11);
    Phase2<1> p2; p2.init(lb, lane, wx, P);
    P2RUN(p2, P, tag, eb);
    }
  }
}
}

extern "C" void kernel_launch(void* const* d_in, const int* in_sizes, int n_in, void* d_out, int out_size, void* d_ws, size_t ws_size, hipStream_t stream) {
  k_fused<<<256, 512, 0, stream>>>((const u16*)d_in[0], (const u16*)d_in[1], (const u16*)d_in[2], (const u16*)d_in[3], (const u16*)d_in[5], (const u16*)d_in[6], (const u16*)d_in[7], (char*)d_ws, (u16*)d_out);
}
```

```cpp
#define SA 0
#define SBB 0
#define CLK "s_memrealtime"
#define MULN 0
#define MULD 1
#include <hip/hip_runtime.h>
#include <hip/hip_bf16.h>
#include <type_traits>

namespace {
#define LAS __attribute__((address_space(3)))
#define GAS __attribute__((address_space(1)))
typedef unsigned short u16;
typedef unsigned u32x4 __attribute__((ext_vector_type(4)));
typedef unsigned u32x2 __attribute__((ext_vector_type(2)));
typedef short bf16x8 __attribute__((ext_vector_type(8)));
typedef short s16x4 __attribute__((ext_vector_type(4)));
typedef float f32x4 __attribute__((ext_vector_type(4)));
typedef float f32x2 __attribute__((ext_vector_type(2)));
typedef __bf16 bf16x2_t __attribute__((ext_vector_type(2)));

constexpr int S_ = 2048, D_ = 1024, F_ = 3584;
constexpr size_t WS_FLAG = 0;
constexpr size_t WS_XN = 1u << 20;
constexpr size_t WS_ACT = WS_XN + (4u << 20);
constexpr size_t WS_O = WS_ACT + (15u << 20);
constexpr size_t WS_KVP = WS_O + (4u << 20);
constexpr size_t WS_ZP = WS_KVP + (2u << 20);
constexpr size_t WS_YX = WS_ZP + (1u << 20);
constexpr size_t WS_STAMP = 64u << 20;
#define DIAG 1
constexpr int NSTAMP = 32;
#define STAMP_DECL unsigned st_c = 0, st_r = 0
#define STAMP(i) do { if ((i) == SA || (i) == SBB) { unsigned long long c_; __builtin_amdgcn_sched_barrier(0); asm volatile(CLK " %0\n\ts_waitcnt lgkmcnt(0)" : "=s"(c_) :: "memory"); \
  if ((i) == SA) st_c = (unsigned)c_; else st_r = (unsigned)c_; asm volatile("" : "+v"(st_c), "+v"(st_r)); __builtin_amdgcn_sched_barrier(0); } } while (0)
#define STAMP_OUT(wv) do { if ((wv) == 0 && blockIdx.x < 8) { const unsigned d_ = __builtin_amdgcn_readfirstlane(st_r - st_c) * MULN / MULD; const unsigned long long t0_ = __builtin_amdgcn_s_memrealtime(); \
  while (__builtin_amdgcn_s_memrealtime() - t0_ < d_ && d_ < 100000u) __builtin_amdgcn_s_sleep(1); } } while (0)
constexpr unsigned FL_EPOCH = 0;
constexpr unsigned FL_O = 1024;
constexpr unsigned FL_YX = 16384;
constexpr unsigned FL_TMO = 61440;
constexpr unsigned GR_ST = 65536;
constexpr unsigned FL_KV = 131072;
constexpr unsigned FL_ACT = 262144;
constexpr unsigned GR_CS = 524288;
#define YPRIO 0
#define XP0 0
#define EARLYA 0
#define BLATE 6
#define TOUCH0 0
#define TOUCHW 1
constexpr float LOG2E = 1.4426950408889634f;

#define SB __builtin_amdgcn_sched_barrier(0)
#define BAR __builtin_amdgcn_s_barrier()
#define WAIT_L0 do { __builtin_amdgcn_s_waitcnt(0xC07F); SB; } while (0)
#define WAIT_V(n) asm volatile("s_waitcnt vmcnt(%0)" :: "i"(n) : "memory")
template <int N> using IC = std::integral_constant<int, N>;

__device__ __forceinline__ u32x4 mk_srd(const void* p, unsigned bytes) {
  const unsigned long long a = (unsigned long long)p; u32x4 r;
  r.x = __builtin_amdgcn_readfirstlane((unsigned)a); r.y = __builtin_amdgcn_readfirstlane((unsigned)(a >> 32)) & 0xffffu; r.z = bytes; r.w = 0x00020000u; return r; }
#define DMA(srd, voff, sk, KOFF, sl, LOFF) do { unsigned _t; asm volatile("s_add_u32 m0, %4, %5\n\ts_add_u32 %0, %3, %6\n\tbuffer_load_dwordx4 %1, %2, %0 offen lds" \
    : "=&s"(_t) : "v"(voff), "s"(srd), "s"(sk), "s"(sl), "i"(LOFF), "i"(KOFF) : "memory", "scc"); SB; } while (0)
#define DMA_SC1(srd, voff, sk, KOFF, sl, LOFF) do { unsigned _t; asm volatile("s_add_u32 m0, %4, %5\n\ts_add_u32 %0, %3, %6\n\tbuffer_load_dwordx4 %1, %2, %0 offen sc1 lds" \
    : "=&s"(_t) : "v"(voff), "s"(srd), "s"(sk), "s"(sl), "i"(LOFF), "i"(KOFF) : "memory", "scc"); SB; } while (0)
#define TOUCH(ptr, sl) do { asm volatile("s_mov_b32 m0, %1\n\ts_nop 0\n\tglobal_load_lds_dword %0, off" :: "v"(ptr), "s"(sl) : "memory"); SB; } while (0)
__device__ __forceinline__ bf16x8 lds128(unsigned a) { return *(const LAS bf16x8*)(size_t)a; }
__device__ __forceinline__ s16x4 ldstr(unsigned a) { return __builtin_amdgcn_ds_read_tr16_b64_v4i16((LAS s16x4*)(size_t)a); }
__device__ __forceinline__ bf16x8 cat(s16x4 lo, s16x4 hi) { return __builtin_shufflevector(lo, hi, 0, 1, 2, 3, 4, 5, 6, 7); }
__device__ __forceinline__ unsigned opq(unsigned x) { asm volatile("" : "+v"(x)); return x; }
__device__ __forceinline__ float opqf(float x) { asm("" : "+v"(x)); return x; }
__device__ __forceinline__ f32x2 opq2(f32x2 x) { asm("" : "+v"(x)); return x; }
__device__ __forceinline__ f32x4 mfma(bf16x8 a, bf16x8 b, f32x4 c) { return __builtin_amdgcn_mfma_f32_16x16x32_bf16(a, b, c, 0, 0, 0); }
__device__ __forceinline__ void mfma_acc(f32x4& d, bf16x8 a, bf16x8 b) { asm volatile("v_mfma_f32_16x16x32_bf16 %0, %1, %2, %0" : "+v"(d) : "v"(a), "v"(b)); }
__device__ __forceinline__ void mfma_k2(f32x4& d, bf16x8 a0, bf16x8 b0, bf16x8 a1, bf16x8 b1) {
  asm volatile("v_mfma_f32_16x16x32_bf16 %0, %1, %2, %0\n\tv_mfma_f32_16x16x32_bf16 %0, %3, %4, %0" : "+v"(d) : "v"(a0), "v"(b0), "v"(a1), "v"(b1)); }
__device__ __forceinline__ void mfma_k2_first(f32x4& d, bf16x8 a0, bf16x8 b0, bf16x8 a1, bf16x8 b1) {
  asm volatile("v_mfma_f32_16x16x32_bf16 %0, %1, %2, 0\n\tv_mfma_f32_16x16x32_bf16 %0, %3, %4, %0" : "=&v"(d) : "v"(a0), "v"(b0), "v"(a1), "v"(b1)); }
template <bool FIRST> __device__ __forceinline__ void mk2(f32x4& d, bf16x8 a0, bf16x8 b0, bf16x8 a1, bf16x8 b1) { if (FIRST) mfma_k2_first(d, a0, b0, a1, b1); else mfma_k2(d, a0, b0, a1, b1); }
__device__ __forceinline__ void mfma_k2x2(f32x4& d, f32x4& e, bf16x8 a0, bf16x8 c0, bf16x8 b0, bf16x8 a1, bf16x8 c1, bf16x8 b1) {
  asm volatile("v_mfma_f32_16x16x32_bf16 %0, %2, %4, %0\n\tv_mfma_f32_16x16x32_bf16 %1, %3, %4, %1\n\tv_mfma_f32_16x16x32_bf16 %0, %5, %7, %0\n\tv_mfma_f32_16x16x32_bf16 %1, %6, %7, %1"
               : "+v"(d), "+v"(e) : "v"(a0), "v"(c0), "v"(b0), "v"(a1), "v"(c1), "v"(b1)); }
__device__ __forceinline__ f32x4 zacc() { f32x4 z = {0.f, 0.f, 0.f, 0.f}; asm volatile("" : "+v"(z)); return z; }
#define CS_M(d, a) "v_mfma_f32_16x16x32_bf16 " d ", " a ", %[o], " d "\n\t"
__device__ __forceinline__ void colsum7(f32x4& c0, f32x4& c1, const bf16x8 (&a)[7], const bf16x8 (&b)[7], bf16x8 ones, unsigned sel) {
  asm volatile("s_cmp_lt_u32 %[s], 2\n\ts_cbranch_scc1 .Lcs01_%=\n\ts_cmp_eq_u32 %[s], 2\n\ts_cbranch_scc1 .Lcs2_%=\n\ts_cmp_eq_u32 %[s], 3\n\ts_cbranch_scc0 .Lcs9_%=\n\t"
               CS_M("%0", "%[a3]") CS_M("%0", "%[b3]") "s_branch .Lcs9_%=\n"
               ".Lcs2_%=:\n\t" CS_M("%0", "%[a2]") CS_M("%0", "%[b2]") CS_M("%1", "%[a6]") CS_M("%1", "%[b6]") "s_branch .Lcs9_%=\n"
               ".Lcs01_%=:\n\ts_cmp_eq_u32 %[s], 0\n\ts_cbranch_scc1 .Lcs0_%=\n\t" CS_M("%0", "%[a1]") CS_M("%0", "%[b1]") CS_M("%1", "%[a5]") CS_M("%1", "%[b5]") "s_branch .Lcs9_%=\n"
               ".Lcs0_%=:\n\t" CS_M("%0", "%[a0]") CS_M("%0", "%[b0]") CS_M("%1", "%[a4]") CS_M("%1", "%[b4]") "\n.Lcs9_%=:"
               : "+v"(c0), "+v"(c1) : [a0] "v"(a[0]), [a1] "v"(a[1]), [a2] "v"(a[2]), [a3] "v"(a[3]), [a4] "v"(a[4]), [a5] "v"(a[5]), [a6] "v"(a[6]),
                 [b0] "v"(b[0]), [b1] "v"(b[1]), [b2] "v"(b[2]), [b3] "v"(b[3]), [b4] "v"(b[4]), [b5] "v"(b[5]), [b6] "v"(b[6]), [o] "v"(ones), [s] "s"(sel) : "scc");
}
#define MFMA_PAD do { asm volatile("s_nop 7\n\ts_nop 1" ::: "memory"); SB; } while (0)
__device__ __forceinline__ unsigned pk2(float lo, float hi) { const f32x2 v = {lo, hi}; return __builtin_bit_cast(unsigned, __builtin_convertvector(v, bf16x2_t)); }
__device__ __forceinline__ bf16x8 pk8(f32x4 a, f32x4 b) { const u32x4 u = {pk2(a[0], a[1]), pk2(a[2], a[3]), pk2(b[0], b[1]), pk2(b[2], b[3])}; return __builtin_bit_cast(bf16x8, u); }
__device__ __forceinline__ float bflo(unsigned u) { return __uint_as_float(u << 16); }
__device__ __forceinline__ float bfhi(unsigned u) { return __uint_as_float(u & 0xffff0000u); }
__device__ __forceinline__ float ex2(float x) { return __builtin_amdgcn_exp2f(x); }
__device__ __forceinline__ float rcp(float x) { return __builtin_amdgcn_rcpf(x); }
__device__ __forceinline__ u32x4 swap16(unsigned a0, unsigned a1, unsigned b0, unsigned b1) {
  const auto p = __builtin_amdgcn_permlane16_swap(a0, b0, false, false), q = __builtin_amdgcn_permlane16_swap(a1, b1, false, false);
  return (u32x4){p[0], q[0], p[1], q[1]}; }
__device__ __forceinline__ float vmax(float a, float b) { return __builtin_elementwise_maximum(a, b); }
__device__ __forceinline__ float vmax8(f32x4 a, f32x4 c) { return vmax(vmax(vmax(vmax(a[0], a[1]), a[2]), vmax(vmax(a[3], c[0]), c[1])), vmax(c[2], c[3])); }
__device__ __forceinline__ void sw32(float& a, float& b) { const auto p = __builtin_amdgcn_permlane32_swap(__float_as_uint(a), __float_as_uint(b), false, false); a = __uint_as_float(p[0]); b = __uint_as_float(p[1]); }
__device__ __forceinline__ void sw16(float& a, float& b) { const auto p = __builtin_amdgcn_permlane16_swap(__float_as_uint(a), __float_as_uint(b), false, false); a = __uint_as_float(p[0]); b = __uint_as_float(p[1]); }
template <class Op> __device__ __forceinline__ float gather4(float a0, float a1, float b0, float b1, Op op) { sw32(a0, a1); sw32(b0, b1); float c = op(a0, a1), d = op(b0, b1); sw16(c, d); return op(c, d); }
__device__ __forceinline__ void spread4(float d, float& a0, float& a1, float& b0, float& b1) { a0 = d; b0 = d; sw16(a0, b0); a1 = a0; b1 = b0; sw32(a0, a1); sw32(b0, b1); }
template <class Op> __device__ __forceinline__ float red4(float v, Op op) {
  const unsigned u = __float_as_uint(v); const auto p = __builtin_amdgcn_permlane32_swap(u, u, false, false);
  const float a = op(__uint_as_float(p[0]), __uint_as_float(p[1])); const unsigned ua = __float_as_uint(a);
  const auto q = __builtin_amdgcn_permlane16_swap(ua, ua, false, false); return op(__uint_as_float(q[0]), __uint_as_float(q[1])); }
__device__ __forceinline__ void st16_sc1(u32x4 v, __amdgpu_buffer_rsrc_t r, unsigned voff, unsigned soff) { __builtin_amdgcn_raw_buffer_store_b128(v, r, voff + soff, 0, 16); }
__device__ __forceinline__ void st16(u32x4 v, __amdgpu_buffer_rsrc_t r, unsigned voff, unsigned soff) { __builtin_amdgcn_raw_buffer_store_b128(v, r, voff + soff, 0, 0); }
__device__ __forceinline__ u32x4 ld16_sc1(__amdgpu_buffer_rsrc_t r, unsigned voff, unsigned soff) { return __builtin_amdgcn_raw_buffer_load_b128(r, voff, soff, 16); }
__device__ __forceinline__ __amdgpu_buffer_rsrc_t mk_rsrc(const void* p, unsigned bytes) { return __builtin_amdgcn_make_buffer_rsrc((void*)p, 0, bytes, 0x00020000); }
#define RLX_AGENT __ATOMIC_RELAXED, __HIP_MEMORY_SCOPE_AGENT
typedef unsigned long long u64;
typedef GAS u64 gu64;
__device__ __forceinline__ void set_flag(gu64* w, u64 tag, int lane) { if (lane == 0) __hip_atomic_store(w, tag, RLX_AGENT); }
template <int N, int STRIDE> __device__ __forceinline__ void wait_flags(gu64* w, u64 tag, gu64* tmo, int lane) {
#pragma nounroll
  for (unsigned spins = 0;; ++spins) {
    u64 v = tag; if (N >= 64 || lane < N) v = __hip_atomic_load(w + lane * STRIDE, RLX_AGENT);
    if (__all(v == tag)) break;
    if (spins > (1u << 16)) { if (lane == 0) __hip_atomic_store(tmo, tag, RLX_AGENT); break; }
    __builtin_amdgcn_s_sleep(2);
  }
  __builtin_amdgcn_fence(__ATOMIC_ACQUIRE, "wavefront");
}
__device__ __forceinline__ u64 uni64(u64 v) { return ((u64)(unsigned)__builtin_amdgcn_readfirstlane((unsigned)(v >> 32)) << 32) | (unsigned)__builtin_amdgcn_readfirstlane((unsigned)v); }
__device__ __forceinline__ void lds_wait(unsigned addr, unsigned target) {
  for (;;) { unsigned v; asm volatile("ds_read_b32 %0, %1\n\ts_waitcnt lgkmcnt(0)" : "=v"(v) : "v"(addr) : "memory");
    if ((unsigned)__builtin_amdgcn_readfirstlane(v) >= target) break; }
}
#define POLL_ISSUE(dst, ptr) do { asm volatile("global_load_dwordx2 %0, %1, off sc1" : "=v"(dst) : "v"(ptr) : "memory"); SB; } while (0)
#define LD16_ISSUE(dst, srd, voff, soff) do { asm volatile("buffer_load_dwordx4 %0, %1, %2, %3 offen sc1" : "=v"(dst) : "v"(voff), "s"(srd), "s"(soff) : "memory"); SB; } while (0)
#define POLL_PIN(dst) asm volatile("" : "+v"(dst))
__device__ __forceinline__ float add1(float a, float b) { float r; asm("v_add_f32 %0, %1, %2" : "=v"(r) : "v"(a), "v"(b)); return r; }
__device__ __forceinline__ float mul1(float a, float b) { float r; asm("v_mul_f32 %0, %1, %2" : "=v"(r) : "v"(a), "v"(b)); return r; }
__device__ __forceinline__ float fma1(float a, float b, float c) { float r; asm("v_fma_f32 %0, %1, %2, %3" : "=v"(r) : "v"(a), "v"(b), "v"(c)); return r; }
__device__ __forceinline__ f32x4 add4(f32x4 a, u32x4 b) { return (f32x4){add1(a[0], __uint_as_float(b[0])), add1(a[1], __uint_as_float(b[1])), add1(a[2], __uint_as_float(b[2])), add1(a[3], __uint_as_float(b[3]))}; }
__device__ __forceinline__ unsigned differ(unsigned a, unsigned b, unsigned t) { unsigned r; asm("v_bitop3_b32 %0, %1, %2, %3 bitop3:0x7e" : "=v"(r) : "v"(a), "v"(b), "s"(t)); return r; }
template <int C> __device__ __forceinline__ float dpp(float v) { return __int_as_float(__builtin_amdgcn_update_dpp(0, __float_as_int(v), C, 0xf, 0xf, true)); }
__device__ __forceinline__ float wave_sum(float v) { v += dpp<0x128>(v); v += dpp<0x124>(v); v += dpp<0x122>(v); v += dpp<0x121>(v); return red4(v, [](float a, float c) { return a + c; }); }
__device__ __forceinline__ void lds_sync(unsigned addr, unsigned target, int lane) {
  asm volatile("s_waitcnt lgkmcnt(0)" ::: "memory");
  if (lane == 0) { const unsigned one = 1; asm volatile("ds_add_u32 %0, %1" :: "v"(addr), "v"(one) : "memory"); }
  for (;;) { unsigned v; asm volatile("ds_read_b32 %0, %1\n\ts_waitcnt lgkmcnt(0)" : "=v"(v) : "v"(addr) : "memory");
    if ((unsigned)__builtin_amdgcn_readfirstlane(v) >= target) break; }
}

constexpr unsigned L1_A = 0;
constexpr unsigned L1_BQ = 65536;
constexpr unsigned L1_BH = 90112;
constexpr unsigned L1_WPHI = 118784;
constexpr unsigned L1_CNT = 120832;
constexpr unsigned L1_DUMP = 121088;
constexpr unsigned L1_BIAS = 121344;
constexpr unsigned L1_CS = 128000;
constexpr unsigned L1_ST = 129024;
constexpr unsigned L1_V = 98304;
constexpr unsigned L1_PHI = 131072;
constexpr unsigned L2_EX = 131072;

struct Ptrs { const u16 *x, *wqkv, *wphi, *wo, *w1, *b1, *w2; char* ws; u16* out; };

template <int G> struct Phase2 {
  unsigned lb; int lane, wq, b, kh, rt, ct;
  u32x4 sact, so, sw2, swo;
  unsigned vaa, vao, vb0, vb1, vs0, vs1, sla, sls;
  struct Off { unsigned a0, a1, b; };
  __device__ __forceinline__ Off off_y(int U) const { const int s0 = kh * 56 + (U < 24 ? 7 * (U & 7) + (U >> 3) : 14 * U - 333), s1 = s0 + (U < 24 ? 4 : 7); return {s0 * 1024u, s1 * 1024u, (wq >> 1 ? s1 : s0) * 65536u}; }
  __device__ __forceinline__ Off off_s(int U) const { return {(U - 28) * 2048u, (U - 28) * 2048u + 1024u, (U - 28) * 131072u}; }
  __device__ __forceinline__ void init(unsigned lb_, int lane_, int wq_, const Ptrs& P) {
    lb = lb_; lane = lane_; wq = wq_; b = blockIdx.x; kh = b & 1; rt = ((b & 7) >> 1) * 4 + ((b >> 3) & 3); ct = b >> 5;
    sact = mk_srd(P.ws + WS_ACT, 128 * 112 * 1024); so = mk_srd(P.ws + WS_O, 128 * 32 * 1024); sw2 = mk_srd(P.w2, F_ * D_ * 2); swo = mk_srd(P.wo, D_ * D_ * 2);
    vaa = lane * 16 + (rt * 8 + wq * 2) * (112 * 1024); vao = lane * 16 + (rt * 8 + wq * 2) * (32 * 1024);
    { const int r = lane >> 4, c = lane & 15; vb0 = ((wq & 1) * 16 + r) * 2048 + ct * 256 + (((c >> 1) ^ r) << 5) + ((c & 1) << 4); vb1 = vb0 ^ 128; }
    { const int r = lane >> 3, c = lane & 7; vs0 = (wq * 16 + r) * 2048 + ct * 256 + kh * 128 + (((c >> 1) ^ ((r >> 1) & 1)) << 5) + ((c & 1) << 4); vs1 = vs0 ^ 64; }
    sla = lb + wq * 4096; sls = lb + wq * 2048;
  }
  template <int LO, bool Y, int I> __device__ __forceinline__ void dma(const Off& o) {
    if (I < 4) { const unsigned sk = (I & 1) ? o.a1 : o.a0; if (Y) DMA_SC1(sact, vaa, sk, (I >> 1) * 112 * 1024, sla, LO + I * 1024); else DMA_SC1(so, vao, sk, (I >> 1) * 32 * 1024, sla, LO + I * 1024); }
    else if (Y) { constexpr int j = I - 4; if (j >> 1) DMA(sw2, vb1, o.b, j * 8192, sla, LO + 16384 + j * 1024); else DMA(sw2, vb0, o.b, j * 8192, sla, LO + 16384 + j * 1024); }
    else if (I < 6) { constexpr int j = I - 4; if (j) DMA(swo, vs1, o.b, j * 16384, sls, LO + 16384 + j * 1024); else DMA(swo, vs0, o.b, j * 16384, sls, LO + 16384 + j * 1024); } }
  template <int U, int I> __device__ __forceinline__ void dma1() { dma<(U & 1) * 65536 + ((U >> 1) & 1) * 32768, true, I>(off_y(U)); }
  template <int U, int I0> __device__ __forceinline__ void dma4() { dma1<U, I0>(); dma1<U, I0 + 1>(); dma1<U, I0 + 2>(); dma1<U, I0 + 3>(); }

  __device__ __forceinline__ void run(const Ptrs& P, u64 tag, int p, int h, const bf16x8 (&eb)[4][2], unsigned& st_c, unsigned& st_r) {
    char* ws = P.ws;
    const int tok = lane & 15, g = lane >> 4, q = (lane >> 2) & 3, pp = lane & 3, wi = wq >> 1, jj = wq & 1;
    const unsigned ra = opq(lb + G * 65536 + wi * 8192 + lane * 16);
    unsigned rb[4], rs[2];
#pragma unroll
    for (int n = 0; n < 4; ++n) { const int f = 4 * (kh ^ (n >> 1)) + 2 * jj + (n & 1); rb[n] = opq(lb + G * 65536 + 16384 + (8 * g + q) * 256 + ((f ^ (q | ((g & 1) << 2))) << 5) + 8 * pp); }
#pragma unroll
    for (int n = 0; n < 2; ++n) { const int f = 2 * jj + n; rs[n] = opq(lb + G * 65536 + 16384 + (8 * g + q) * 128 + ((f ^ ((q >> 1) | ((g & 1) << 1))) << 5) + 8 * pp); }
    f32x4 acc[4][4], as[4][2];
    bf16x8 fa[2][4], fb[2][4];
    auto rdA = [&](auto HR, auto I) { constexpr int i = I(); fa[i >> 2][i & 3] = lds128(ra + HR() * 32768 + (i & 3) * 2048 + (i >> 2) * 1024); SB; };
    auto rdB = [&](auto HR, auto Y, auto I) { constexpr int i = I(), ks = i >> 2, n = i & 3; constexpr bool y = Y();
      if (y) { constexpr int o = HR() * 32768 + ks * 8192; fb[ks][n] = cat(ldstr(rb[n] + o), ldstr(rb[n] + o + 1024)); SB; }
      else if (n < 2) { constexpr int o = HR() * 32768 + ks * 4096; fb[ks][n] = cat(ldstr(rs[n & 1] + o), ldstr(rs[n & 1] + o + 512)); SB; } };
    auto loadh = [&](auto HR, auto YR, auto MD, auto HD, int U, auto hook) { constexpr int md = MD(), LO = (G ^ 1) * 65536 + HD() * 32768; constexpr bool d = md != 0, y = md == 1;
      const Off o = y ? off_y(U) : off_s(U);
      rdA(HR, IC<0>{}); rdB(HR, YR, IC<0>{}); if (d) dma<LO, y, 0>(o); hook(IC<0>{}); SB;
      rdA(HR, IC<1>{}); rdB(HR, YR, IC<1>{}); if (d) dma<LO, y, 4>(o); hook(IC<1>{}); SB;
      rdA(HR, IC<2>{}); rdB(HR, YR, IC<2>{}); if (d) dma<LO, y, 1>(o); hook(IC<2>{}); SB;
      rdA(HR, IC<3>{}); rdB(HR, YR, IC<3>{}); if (d) dma<LO, y, 5>(o); hook(IC<3>{}); SB;
      rdA(HR, IC<4>{}); rdB(HR, YR, IC<4>{}); if (d) dma<LO, y, 2>(o); hook(IC<4>{}); SB;
      rdA(HR, IC<5>{}); rdB(HR, YR, IC<5>{}); if (d) dma<LO, y, 6>(o); hook(IC<5>{}); SB;
      rdA(HR, IC<6>{}); rdB(HR, YR, IC<6>{}); if (d) dma<LO, y, 3>(o); hook(IC<6>{}); SB;
      rdA(HR, IC<7>{}); rdB(HR, YR, IC<7>{}); hook(IC<7>{}); SB; if (d) dma<LO, y, 7>(o); };
    auto load = [&](auto HR, auto YR, auto MD, auto HD, int U) { loadh(HR, YR, MD, HD, U, [](auto) {}); };
    auto mmy_ = [&](auto F) {
#pragma unroll
      for (int m = 0; m < 4; ++m)
#pragma unroll
        for (int n = 0; n < 4; ++n) mk2<F()>(acc[m][n], fb[0][n], fa[0][m], fb[1][n], fa[1][m]);
      SB; };
    auto mms_ = [&](auto F) {
#pragma unroll
      for (int m = 0; m < 4; ++m)
#pragma unroll
        for (int n = 0; n < 2; ++n) mk2<F()>(as[m][n], fb[0][n], fa[0][m], fb[1][n], fa[1][m]);
      SB; };
    auto mmy = [&]() { mmy_(IC<0>{}); }; auto mms = [&]() { mms_(IC<0>{}); };
#define ENDL(n) do { WAIT_V(n); WAIT_L0; BAR; SB; } while (0)
#define ENDM do { BAR; SB; } while (0)
    IC<0> c0; IC<1> c1; IC<2> c2; IC<G> hA; IC<G ^ 1> hB;
    gu64* flags = (gu64*)(ws + WS_FLAG); gu64* tmo = flags + FL_TMO / 8;
    const __amdgpu_buffer_rsrc_t ryx = mk_rsrc(ws + WS_YX, 256 * 32768);
    const unsigned exk = lb + L2_EX + wq * 4096 + lane * 16, exs = lb + 24576 + (wq >> 1) * 32768 + (wq & 1) * 4096 + lane * 16;
    f32x4 tk[4];
    auto give = [&](auto K) { constexpr int k = K();
#pragma unroll
      for (int i = 0; i < 4; ++i) *(LAS f32x4*)(size_t)((k ? exs + G * 65536 : exk + G * 16384) + i * 1024) = acc[2 * (G ^ 1) + (i >> 1)][2 * k + (i & 1)];
      SB; };
    auto take_rd = [&](auto K) { constexpr int k = K();
#pragma unroll
      for (int i = 0; i < 4; ++i) tk[i] = *(const LAS f32x4*)(size_t)((k ? exs + (G ^ 1) * 65536 : exk + (G ^ 1) * 16384) + i * 1024);
      SB; };
    auto take = [&](auto K) { constexpr int k = K();
#pragma unroll
      for (int i = 0; i < 4; ++i) { f32x4& a = acc[2 * G + (i >> 1)][2 * k + (i & 1)]; a += tk[i]; if (k) st16_sc1(__builtin_bit_cast(u32x4, a), ryx, lane * 16, b * 32768 + (G * 4 + wq) * 4096 + i * 1024); else asm volatile("" : "+v"(a)); }
      SB; };
    u64 pv;
    if (G == 0) {
      WAIT_V(EARLYA ? 1 : 9); BAR; SB; STAMP(16);
      load(c0, c1, c1, c1, 3); ENDL(13);
      mmy_(c1); WAIT_V(8); ENDM; STAMP(17);
      load(c1, c1, c1, hA, 5); ENDL(8);
      mmy(); ENDM; load(c0, c1, c1, hB, 7); ENDL(8);
      for (int T = 4; T < 20; T += 4) {
        mmy(); ENDM; load(c1, c1, c1, hA, T + 5); ENDL(8);
        mmy(); ENDM; load(c0, c1, c1, hB, T + 7); ENDL(8);
      }
    } else {
      BAR; SB; STAMP(16); BAR; SB;
      load(c0, c1, c1, c0, 4); ENDL(8);
      mmy_(c1); ENDM; STAMP(17);
      gu64* fa2 = flags + (FL_ACT + 131072) / 8 + (rt * 2 + kh) * 512; gu64* fkv = flags + (FL_KV + (h * 4 + wq) * 1024) / 8;
      POLL_ISSUE(pv, lane < 32 ? fa2 + lane * 16 : fkv + (lane & 7) * 16);
      load(c1, c1, c1, hA, 6); ENDL(9);
      mmy(); ENDM;
      WAIT_V(8); POLL_PIN(pv); if (!__all(pv == tag)) { wait_flags<32, 16>(fa2, tag, tmo, lane); wait_flags<8, 16>(fkv, tag, tmo, lane); } SB;
      const u32x4 skv = mk_srd(ws + WS_KVP, 3u << 20);
      u32x4 t[8], tz; LD16_ISSUE(tz, skv, h * 2048 + (lane & 7) * 256 + wq * 64 + g * 16, (unsigned)(WS_ZP - WS_KVP));
      { const unsigned vkv = h * 32768 + wq * 1024 + lane * 16;
#pragma unroll
        for (int i = 0; i < 8; ++i) LD16_ISSUE(t[i], skv, vkv, i * 4096u); }
      load(c0, c1, c1, hB, 8); ENDL(17);
      mmy(); ENDM;
      WAIT_V(8); asm volatile("" : "+v"(t[0]), "+v"(t[1]), "+v"(t[2]), "+v"(t[3]), "+v"(t[4]), "+v"(t[5]), "+v"(t[6]), "+v"(t[7]), "+v"(tz)); SB;
      float sm[8];
      auto addp = [&](int i, int c) { sm[2 * c] = add1(sm[2 * c], bflo(t[i][c])); sm[2 * c + 1] = add1(sm[2 * c + 1], bfhi(t[i][c])); asm volatile("" : "+v"(sm[2 * c]), "+v"(sm[2 * c + 1])); };
      loadh(c1, c1, c1, hA, 10, [&](auto I) { constexpr int i = I();
        if (i == 0) { _Pragma("unroll") for (int c = 0; c < 4; ++c) { sm[2 * c] = bflo(t[0][c]); sm[2 * c + 1] = bfhi(t[0][c]); asm volatile("" : "+v"(sm[2 * c]), "+v"(sm[2 * c + 1])); } }
        else if (i < 7) { addp((i + 1) >> 1, 2 * (~i & 1)); addp((i + 1) >> 1, 2 * (~i & 1) + 1); }
        else { _Pragma("unroll") for (int r = 0; r < 4; ++r) { float z = __uint_as_float(tz[r]); z += dpp<0xB1>(z); z = opqf(z); z += dpp<0x4E>(z); z = opqf(z); z += dpp<0x141>(z); tz[r] = __float_as_uint(opqf(z)); } asm volatile("" : "+v"(tz)); } });
      ENDL(8);
      mmy(); ENDM;
      const unsigned kva = opq(lb + L2_EX + lane * 16);
      loadh(c0, c1, c1, hB, 12, [&](auto I) { constexpr int i = I(); addp(4 + (i >> 1), 2 * (i & 1)); addp(4 + (i >> 1), 2 * (i & 1) + 1); });
      { const unsigned a = kva + (wq >> 1) * 1024 + (wq & 1) * 8;
        *(LAS u32x2*)(size_t)a = (u32x2){pk2(sm[0], sm[1]), pk2(sm[2], sm[3])}; *(LAS u32x2*)(size_t)(a + 2048) = (u32x2){pk2(sm[4], sm[5]), pk2(sm[6], sm[7])};
        u32x2 au = {pk2(__uint_as_float(tz[0]), __uint_as_float(tz[1])), pk2(__uint_as_float(tz[2]), __uint_as_float(tz[3]))};
        if ((lane & 3) == 1) au = (u32x2){0x3F803F80u, 0x3F803F80u}; else if ((lane & 3) != 0) au = (u32x2){0u, 0u};
        *(LAS u32x2*)(size_t)(a + 4096) = au; SB; }
      ENDL(8);
      mmy(); ENDM;
      bf16x8 ka[6]; f32x4 r6[6]; float ca, cc, o8[8];
      const __amdgpu_buffer_rsrc_t ro = mk_rsrc(ws + WS_O, 128 * 32 * 1024);
      const unsigned vo = (unsigned)((((g & 1) << 1) | (g >> 1)) * 256 + tok * 16) + ((p * 16 + wq * 4) * 32 + h) * 1024;
      auto ostep = [&](int m, int i) { const f32x4 z4 = {0.f, 0.f, 0.f, 0.f};
        if (i == 1) { __builtin_amdgcn_s_setprio(1);
#pragma unroll
          for (int k = 5; k >= 0; --k) { r6[k] = mfma(ka[k], eb[m][k & 1], z4); asm volatile("" : "+v"(r6[k])); }
          __builtin_amdgcn_s_setprio(0); }
        if (i == 2) { ca = rcp(r6[4][1]); cc = rcp(r6[5][1]); asm volatile("" : "+v"(ca), "+v"(cc)); }
        if (i == 3) { const float inv = rcp(opqf(r6[4][0] * ca + 1e-6f) + r6[5][0] * cc); ca = opqf(ca * inv); cc = opqf(cc * inv); asm volatile("" : "+v"(ca), "+v"(cc)); }
        if (i == 4 || i == 5) { _Pragma("unroll") for (int r = 0; r < 4; ++r) { const int d = i - 4; o8[4 * d + r] = opqf(r6[2 * d][r] * ca) + r6[2 * d + 1][r] * cc; asm volatile("" : "+v"(o8[4 * d + r])); } }
        if (i == 6) st16_sc1(swap16(pk2(o8[0], o8[1]), pk2(o8[2], o8[3]), pk2(o8[4], o8[5]), pk2(o8[6], o8[7])), ro, vo, m * 32768); };
#pragma unroll
      for (int i = 0; i < 6; ++i) ka[i] = lds128(kva + i * 1024);
      SB;
      loadh(c1, c1, c1, hA, 14, [&](auto I) { ostep(0, I()); }); ENDL(9);
      mmy(); ENDM; loadh(c0, c1, c1, hB, 16, [&](auto I) { ostep(1, I()); }); ENDL(9);
      mmy(); ENDM; loadh(c1, c1, c1, hA, 18, [&](auto I) { ostep(2, I()); }); ENDL(9);
      mmy(); ENDM; loadh(c0, c1, c1, hB, 20, [&](auto I) { ostep(3, I()); }); ENDL(9);
      mmy(); ENDM; load(c1, c1, c1, hA, 22); WAIT_V(8);
      set_flag(flags + FL_O / 8 + (2 * p + (wq >> 1)) * 64 + h * 2 + (wq & 1), tag, lane); WAIT_L0; ENDM;
      mmy(); ENDM; load(c0, c1, c1, hB, 24); ENDL(8);
    }
    STAMP(18);
    gu64* po = flags + FL_O / 8 + rt * 64;
    mmy(); ENDM; if (G == 1) POLL_ISSUE(pv, po + lane); load(c1, c1, c1, hA, 25 + G); ENDL(8 + G);
    mmy(); ENDM;
    if (G == 0) { load(c0, c1, c1, hB, 27); ENDL(8); }
    else { WAIT_V(8); POLL_PIN(pv); if (!__all(pv == tag)) wait_flags<64, 1>(po, tag, tmo, lane); SB; load(c0, c1, c2, hB, 28); ENDL(6); }
    mmy(); ENDM; load(c1, c1, c2, hA, 29 + G); ENDL(6);
    gu64* fy = flags + FL_YX / 8 + b * 8 + G * 4 + wq;
    mmy(); ENDM; STAMP(19); load(c0, c0, c2, hB, 31 + G); ENDL(6); STAMP(25);
    if (G == 0) {
      mms_(c1); give(c1); WAIT_L0; ENDM; load(c1, c0, c2, hA, 33); ENDL(6); STAMP(26);
      mms(); take_rd(c1); take(c1); WAIT_L0; ENDM; load(c0, c0, c2, hB, 35); ENDL(10); STAMP(27);
      mms(); give(c0); WAIT_L0; ENDM; load(c1, c0, c2, hA, 37); WAIT_V(6); set_flag(fy, tag, lane); WAIT_L0; ENDM; STAMP(28);
    } else {
      mms_(c1); take_rd(c1); give(c1); take(c1); WAIT_L0; ENDM; load(c1, c0, c2, hA, 34); ENDL(10); STAMP(26);
      mms(); give(c0); WAIT_L0; ENDM; load(c0, c0, c2, hB, 36); WAIT_V(6); set_flag(fy, tag, lane); WAIT_L0; ENDM; STAMP(27);
      mms(); take_rd(c0); take(c0); WAIT_L0; ENDM; load(c1, c0, c2, hA, 38); ENDL(6); STAMP(28);
    }
    u32x4 py[4], xr[2];
    const unsigned vout = (rt * 128 + wi * 64 + 2 * G * 16 + tok) * 2048 + ct * 256 + kh * 128 + jj * 64 + (((g & 1) << 1) | (g >> 1)) * 16;
    mms(); if (G == 0) { take_rd(c0); take(c0); WAIT_L0; } ENDM; { const u32x4 sx = mk_srd(P.x, S_ * D_ * 2); LD16_ISSUE(xr[0], sx, vout, 0u); LD16_ISSUE(xr[1], sx, vout, 32768u); } load(c0, c0, c2, hB, 39 + G); ENDL(8); STAMP(29);
    gu64* py_ = flags + FL_YX / 8 + (b ^ 1) * 8 + G * 4 + wq;
    mms(); ENDM; if (G == 1) POLL_ISSUE(pv, py_); load(c1, c0, c2, hA, 41 + G); ENDL(6 + G); STAMP(20);
    auto fetch = [&]() { WAIT_V(6); POLL_PIN(pv); if (!__all(pv == tag)) wait_flags<1, 1>(py_, tag, tmo, lane); SB;
      const u32x4 syx = mk_srd(ws + WS_YX, 256 * 32768); const unsigned vy = (b ^ 1) * 32768 + (G * 4 + wq) * 4096 + lane * 16;
#pragma unroll
      for (int i = 0; i < 4; ++i) LD16_ISSUE(py[i], syx, vy, i * 1024u);
      };
    f32x2 gt[8], xv[8];
    auto unpack = [&](auto I) { constexpr int i = I();
      if (i == 0) asm volatile("" : "+v"(xr[0]), "+v"(xr[1]));
      if constexpr (i < 2) { const u32x4 xs = swap16(xr[i][0], xr[i][1], xr[i][2], xr[i][3]);
        _Pragma("unroll") for (int c = 0; c < 4; ++c) { xv[4 * i + c] = (f32x2){bflo(xs[c]), bfhi(xs[c])}; asm volatile("" : "+v"(xv[4 * i + c])); } } };
    auto gate = [&]() { WAIT_V(0); asm volatile("" : "+v"(py[0]), "+v"(py[1]), "+v"(py[2]), "+v"(py[3])); SB; f32x2 y[8], e[8];
#define ST8(stmt) _Pragma("unroll") for (int i = 0; i < 8; ++i) { stmt; } SB
      ST8(const f32x4 a = acc[2 * G + (i >> 2)][(i >> 1) & 1]; const f32x4 c = __builtin_bit_cast(f32x4, py[i >> 1]); y[i] = (i & 1) ? a.zw + c.zw : a.xy + c.xy);
      ST8(e[i] = opq2(y[i] * -LOG2E));
      ST8(e[i] = opq2((f32x2){ex2(e[i][0]), ex2(e[i][1])}));
      ST8(e[i] = opq2(e[i] + 1.0f));
      ST8(e[i] = opq2((f32x2){rcp(e[i][0]), rcp(e[i][1])}));
      ST8(gt[i] = y[i] * e[i]; asm volatile("" : "+v"(gt[i]))); };
    auto pass = [&]() {
#pragma unroll
      for (int i = 0; i < 4; ++i) *(LAS f32x4*)(size_t)(exk + G * 16384 + i * 1024) = as[2 * (G ^ 1) + (i >> 1)][i & 1];
      SB; };
    if (G == 0) {
      mms(); ENDM; POLL_ISSUE(pv, py_); load(c0, c0, c2, hB, 43); ENDL(7); STAMP(30);
      mms(); ENDM; fetch(); loadh(c1, c0, c0, hA, 0, unpack); ENDL(4); STAMP(31);
      mms(); MFMA_PAD; STAMP(21); ENDM;
      pass(); gate();
    } else {
      mms(); ENDM; fetch(); loadh(c0, c0, c0, hB, 0, unpack); ENDL(4); STAMP(30);
      mms(); ENDM; load(c1, c0, c0, hA, 0); gate(); WAIT_L0; ENDM; STAMP(31);
      mms(); MFMA_PAD; STAMP(21);
      pass();
    }
    WAIT_L0; ENDM; STAMP(22);
    const __amdgpu_buffer_rsrc_t rout = mk_rsrc(P.out, S_ * D_ * 2);
#pragma unroll
    for (int i = 0; i < 4; ++i) tk[i] = *(const LAS f32x4*)(size_t)(exk + (G ^ 1) * 16384 + i * 1024);
#pragma unroll
    for (int ml = 0; ml < 2; ++ml) { unsigned ow[4];
#pragma unroll
      for (int n = 0; n < 2; ++n) { const int i = ml * 2 + n; const f32x4 s = as[2 * G + ml][n] + tk[i];
        const f32x2 o0 = s.xy * gt[2 * i] + xv[2 * i], o1 = s.zw * gt[2 * i + 1] + xv[2 * i + 1];
        ow[2 * n] = pk2(o0[0], o0[1]); ow[2 * n + 1] = pk2(o1[0], o1[1]); }
      st16(swap16(ow[0], ow[1], ow[2], ow[3]), rout, vout, ml * 16 * 2048); }
    if (G == 0 && b == 0 && wq == 0) set_flag(flags + FL_EPOCH / 8, tag, lane);
    STAMP(23);
    WAIT_V(0); STAMP(24); STAMP_OUT(G * 4 + wq);
  }
};
#define P2RUN(p2, P, tag, eb) p2.run(P, tag, p, h, eb, st_c, st_r)

__global__ __launch_bounds__(512, 2) void k_fused(const u16* x, const u16* wqkv, const u16* wphi, const u16* wo, const u16* w1, const u16* b1, const u16* w2, char* ws, u16* out) {
  __shared__ __attribute__((aligned(16))) char smem[163840];
  const Ptrs P = {x, wqkv, wphi, wo, w1, b1, w2, ws, out};
  const unsigned lb = (unsigned)(size_t)(LAS char*)smem;
  const int tid = threadIdx.x, lane = tid & 63, w = __builtin_amdgcn_readfirstlane(tid >> 6), wx = w & 3;
  const int b = blockIdx.x, h = (b & 7) * 4 + ((b >> 3) & 3), p = b >> 5, odd = h & 1;
  const int tok = lane & 15, g = lane >> 4, q = (lane >> 2) & 3, pp = lane & 3;
  const unsigned ra0 = lb + L1_A + (wx * 64 + tok) * 128 + ((g ^ (tok >> 1)) << 4), ra1 = ra0 ^ 64;
  gu64* flags = (gu64*)(ws + WS_FLAG); gu64* tmo = flags + FL_TMO / 8;
  STAMP_DECL; STAMP(0);
  const int rot = p < 7 ? 2 * p : 0;
  auto kt = [&](int pos) { int t = pos + rot; t -= t >= 16 ? 14 : 0; return pos < 2 ? pos : t; };
  u64 ep; { gu64* pe = flags + FL_EPOCH / 8; POLL_ISSUE(ep, pe); }
  const __amdgpu_buffer_rsrc_t rfl = mk_rsrc(ws + WS_FLAG, 1u << 20);
  bf16x8 ones = {0x3F80, 0x3F80, 0x3F80, 0x3F80, 0x3F80, 0x3F80, 0x3F80, 0x3F80}; asm volatile("" : "+v"(ones));
  auto cs_publish = [&](const f32x4& c0, const f32x4& c1, int j0, bool two, u64 tag) {
    const f32x4 a = (tok & 2) ? c1 : c0; const u32x4 v = {__float_as_uint((tok & 1) ? a[2] : a[0]), (unsigned)tag, __float_as_uint((tok & 1) ? a[3] : a[1]), (unsigned)tag};
    if (tok < (two ? 4 : 2)) st16_sc1(v, rfl, ((j0 + 2 * (tok & 2)) * 16 + 4 * g + 2 * (tok & 1)) * 8, GR_CS + (h * 8 + p) * 1664);
    SB; };

  if (w < 4) {
    const u32x4 sa = mk_srd(x, S_ * D_ * 2);
    unsigned va0, va1;
    { const int r = lane >> 3; va0 = (p * 256 + wx * 64 + r) * 2048 + (((lane & 7) ^ (r >> 1)) << 4); va1 = va0 ^ 64; }
    const unsigned sl = lb + L1_A + wx * 8192;
    const unsigned rb0 = opq(lb + L1_BH + (8 * g + 4 * (g & 1) + q) * 224 + 8 * pp), rb1 = opq(lb + L1_BH + (8 * g + 4 * ((g & 1) ^ 1) + q) * 224 + 8 * pp);
    f32x4 acc[4][7], cs0, cs1;
    bf16x8 fa[2][4], fb[2][7];
    auto dmaA = [&](auto ST, auto J, unsigned sk) { constexpr int st = ST(), j = J(); if (j & 1) DMA(sa, va1, sk, j * 16384, sl, st * 32768 + j * 1024); else DMA(sa, va0, sk, j * 16384, sl, st * 32768 + j * 1024); };
    auto rdA = [&](auto ST, auto KS, auto M) { constexpr int st = ST(), ks = KS(), m = M(); fa[ks][m] = lds128((ks ? ra1 : ra0) + st * 32768 + m * 2048); SB; };
    auto rdB = [&](auto ST, auto KS, auto J) { constexpr int st = ST(), ks = KS(), j = J(); constexpr int o = st * 14336 + ks * 7168 + j * 32; fb[ks][j] = cat(ldstr(rb0 + o), ldstr(rb1 + o)); SB; };
    auto load = [&](auto ST, auto DO, unsigned sk) {
      constexpr int o = ST() ^ 1; constexpr bool d = DO();
      rdA(ST, IC<0>{}, IC<0>{}); rdB(ST, IC<0>{}, IC<0>{}); rdB(ST, IC<0>{}, IC<1>{}); if (d) dmaA(IC<o>{}, IC<0>{}, sk);
      rdA(ST, IC<0>{}, IC<1>{}); rdB(ST, IC<0>{}, IC<2>{}); rdB(ST, IC<0>{}, IC<3>{}); if (d) dmaA(IC<o>{}, IC<1>{}, sk);
      rdA(ST, IC<0>{}, IC<2>{}); rdB(ST, IC<0>{}, IC<4>{}); rdB(ST, IC<0>{}, IC<5>{}); if (d) dmaA(IC<o>{}, IC<2>{}, sk);
      rdA(ST, IC<0>{}, IC<3>{}); rdB(ST, IC<0>{}, IC<6>{}); rdB(ST, IC<1>{}, IC<0>{}); if (d) dmaA(IC<o>{}, IC<3>{}, sk);
      rdA(ST, IC<1>{}, IC<0>{}); rdB(ST, IC<1>{}, IC<1>{}); rdB(ST, IC<1>{}, IC<2>{}); if (d) dmaA(IC<o>{}, IC<4>{}, sk);
      rdA(ST, IC<1>{}, IC<1>{}); rdB(ST, IC<1>{}, IC<3>{}); rdB(ST, IC<1>{}, IC<4>{}); if (d) dmaA(IC<o>{}, IC<5>{}, sk);
      rdA(ST, IC<1>{}, IC<2>{}); rdB(ST, IC<1>{}, IC<5>{}); if (d) dmaA(IC<o>{}, IC<6>{}, sk);
      rdA(ST, IC<1>{}, IC<3>{}); rdB(ST, IC<1>{}, IC<6>{}); if (d) dmaA(IC<o>{}, IC<7>{}, sk);
    };
    auto mm_ = [&](auto F) {
#pragma unroll
      for (int m = 0; m < 4; ++m)
#pragma unroll
        for (int j = 0; j < 7; ++j) mk2<F()>(acc[m][j], fb[0][j], fa[0][m], fb[1][j], fa[1][m]);
      SB; };
    auto mm = [&]() { mm_(IC<0>{}); };
#define A8(ST, sk) dmaA(IC<ST>{}, IC<0>{}, sk); dmaA(IC<ST>{}, IC<1>{}, sk); dmaA(IC<ST>{}, IC<2>{}, sk); dmaA(IC<ST>{}, IC<3>{}, sk); dmaA(IC<ST>{}, IC<4>{}, sk); dmaA(IC<ST>{}, IC<5>{}, sk); dmaA(IC<ST>{}, IC<6>{}, sk); dmaA(IC<ST>{}, IC<7>{}, sk)
    if (wx == 0 && TOUCH0) { const int l = min(lane, 51), ts = l / 26, rem = l - ts * 26, rr = rem / 13, li = rem - rr * 13, k = ts * 64 + (b >> 3) * 2 + rr;
      TOUCH(li < 6 ? (const GAS char*)wqkv + k * 6144 + (li >> 1) * 2048 + (b & 7) * 256 + (li & 1) * 128 : (const GAS char*)w1 + k * 7168 + (b & 7) * 896 + (li - 6) * 128, lb + L1_DUMP); }
    if (wx == 1 && TOUCH0) TOUCH((const GAS char*)x + (p * 256 + ((b >> 3) & 3) * 64 + lane) * 2048 + (b & 1) * 128, lb + L1_DUMP);
    A8(0, 0u);
    if (wx == 0) asm volatile("s_mov_b32 m0, %2\n\ts_nop 0\n\tbuffer_load_dword %0, %1, 0 offen lds" :: "v"(h * 224 + lane * 4), "s"(mk_srd(b1, F_ * 2)), "s"(lb + L1_BIAS) : "memory");
    cs0 = zacc(); cs1 = zacc();
    auto colsum = [&](bool now) { colsum7(cs0, cs1, fb[0], fb[1], ones, now ? wx : 4); SB; };
    if (tid == 0) *(LAS u64*)(size_t)(lb + L1_CNT) = 0ull;
    STAMP(1);
    WAIT_V(0); POLL_PIN(ep); const u64 tag = uni64(ep) + 1;
    A8(1, 128u);
    WAIT_L0; BAR; SB; STAMP(4);
    load(IC<0>{}, IC<0>{}, 0u); WAIT_L0; BAR; SB; STAMP(5);
#pragma unroll
    for (int t = 0; t < 4; t += 2) {
      if (t == 0) mm_(IC<1>{}); else mm();
      colsum((p == 7) == (t == 0)); WAIT_V(0); BAR; SB;
      load(IC<1>{}, IC<1>{}, (unsigned)kt(t + 2) * 128u); WAIT_L0; BAR; SB;
      mm(); colsum((p == 7) == (t == 0)); WAIT_V(0); BAR; SB;
      load(IC<0>{}, IC<1>{}, (unsigned)kt(t + 3) * 128u); if (t == 2) cs_publish(cs0, cs1, wx, wx < 3, tag); WAIT_L0; BAR; SB;
    }
    for (int t = 4; t < 14; t += 2) {
      mm(); WAIT_V(0); BAR; SB;
      load(IC<1>{}, IC<1>{}, (unsigned)kt(t + 2) * 128u); WAIT_L0; BAR; SB;
      mm(); WAIT_V(0); BAR; SB;
      load(IC<0>{}, IC<1>{}, (unsigned)kt(t + 3) * 128u); WAIT_L0; BAR; SB;
    }
    STAMP(6);
    mm(); WAIT_V(0); BAR; SB;
    u32x2 bias[7];
#pragma unroll
    for (int j = 0; j < 7; ++j) bias[j] = *(const LAS u32x2*)(size_t)(lb + L1_BIAS + (j < 6 ? j + odd : (odd ? 0 : 6)) * 32 + g * 8);
    SB;
    { const unsigned dump = lb + L1_DUMP; const int cu = b >> 3, a = (b & 7) >> 1;
      const int l2 = min(lane, 55) + 56 * (wx + 4 * cu);
      if (TOUCHW) TOUCH((const GAS char*)w2 + ((b & 1) * 1792 + a * 448) * 2048 + l2 * 128, dump);
      if (TOUCHW) TOUCH((const GAS char*)wo + (b * 64 + wx * 16 + (lane & 15)) * 128, dump); }
    load(IC<1>{}, IC<0>{}, 0u);
    f32x4 bl[7];
#pragma unroll
    for (int j = 0; j < 7; ++j) { bl[j] = (f32x4){opqf(bflo(bias[j][0]) * LOG2E), opqf(bfhi(bias[j][0]) * LOG2E), opqf(bflo(bias[j][1]) * LOG2E), opqf(bfhi(bias[j][1]) * LOG2E)}; asm volatile("" : "+v"(bl[j])); }
    WAIT_L0; BAR; SB;
    mm(); BAR; MFMA_PAD; STAMP(7);
    f32x4 cs[7]; float rl[4], rml[4];
#pragma unroll
    for (int j = 0; j < 7; ++j) cs[j] = *(const LAS f32x4*)(size_t)(lb + L1_CS + j * 64 + g * 16);
#pragma unroll
    for (int m = 0; m < 4; ++m) { const f32x2 st = *(const LAS f32x2*)(size_t)(lb + L1_ST + (wx * 64 + m * 16 + tok) * 8); rl[m] = opqf(st[0] * LOG2E); rml[m] = opqf(-rl[m] * st[1]); }
    SB;
    Phase2<0> p2; p2.init(lb, lane, wx, P);
    const __amdgpu_buffer_rsrc_t ract = mk_rsrc(ws + WS_ACT, 128 * 112 * 1024);
    auto mish8 = [&](int m0, int j0, int m1, int j1) -> u32x4 {
      const f32x2 a[4] = {acc[m0][j0].xy, acc[m0][j0].zw, acc[m1][j1].xy, acc[m1][j1].zw}, c[4] = {cs[j0].xy, cs[j0].zw, cs[j1].xy, cs[j1].zw}, bb[4] = {bl[j0].xy, bl[j0].zw, bl[j1].xy, bl[j1].zw};
      f32x2 t[4], hs[4], e[4], d[4]; unsigned o[4];
#define ST4(stmt) _Pragma("unroll") for (int i = 0; i < 4; ++i) { stmt; } SB
      ST4(t[i] = c[i] * rml[i < 2 ? m0 : m1] + bb[i]);
      ST4(hs[i] = a[i] * rl[i < 2 ? m0 : m1] + t[i]);
      ST4(e[i] = opq2((f32x2){ex2(hs[i][0]), ex2(hs[i][1])}));
      ST4(t[i] = e[i] + 2.0f);
      ST4(d[i] = opq2(e[i] * t[i] + 2.0f));
      ST4(e[i] = opq2((f32x2){rcp(d[i][0]), rcp(d[i][1])}));
      ST4(t[i] = e[i] * (-2.0f / LOG2E) + 1.0f / LOG2E);
      ST4(d[i] = hs[i] * t[i]);
      ST4(o[i] = pk2(d[i][0], d[i][1]));
      return swap16(o[0], o[1], o[2], o[3]); };
    const unsigned vpair = (unsigned)((((g & 1) << 1) | (g >> 1)) * 256 + tok * 16), mb0 = p * 16 + wx * 4;
    auto pair = [&](auto J, auto M) { constexpr int j = J(), m = M();
      st16_sc1(mish8(m, j, m, j + 1), ract, vpair, ((mb0 + m) * 112 + ((7 * h + odd + j) >> 1)) * 1024); SB;
      constexpr int n = 2 * j + m - BLATE; if constexpr (n >= 0 && n < 6) { p2.dma1<n / 2, 4 + 2 * (n & 1)>(); p2.dma1<n / 2, 5 + 2 * (n & 1)>(); } };
    gu64* fact = flags + FL_ACT / 8 + (((2 * p + (wx >> 1)) * 2 + (h >> 4)) * 32 + (h & 15) * 2 + (wx & 1)) * 16;
    STAMP(13);
    if (XP0) __builtin_amdgcn_s_setprio(2);
    pair(IC<0>{}, IC<0>{}); pair(IC<0>{}, IC<1>{}); pair(IC<0>{}, IC<2>{}); pair(IC<0>{}, IC<3>{});
    if (XP0) __builtin_amdgcn_s_setprio(0);
    STAMP(14);
    pair(IC<2>{}, IC<0>{}); pair(IC<2>{}, IC<1>{});
    STAMP(15);
    WAIT_V(2); set_flag(fact, tag, lane); SB; STAMP(8);
    gu64* fa1 = flags + FL_ACT / 8 + (p2.rt * 2 + p2.kh) * 512; u64 pv;
    const unsigned vsing = (unsigned)((g & 1) * 112 * 1024 + (2 * odd + (g >> 1)) * 256 + tok * 16);
    auto single = [&](int m) {
      st16_sc1(mish8(m, 6, m + 1, 6), ract, vsing, ((mb0 + m) * 112 + ((7 * h + (odd ? 0 : 6)) >> 1)) * 1024); SB; };
    auto geta = [&]() { POLL_PIN(pv); STAMP(10); if (!__all(pv == tag)) wait_flags<32, 16>(fa1, tag, tmo, lane);
      SB; STAMP(11); p2.dma4<0, 0>(); p2.dma4<1, 0>(); p2.dma4<2, 0>(); };
    pair(IC<2>{}, IC<2>{}); pair(IC<2>{}, IC<3>{});
    if (EARLYA == 0) {
      pair(IC<4>{}, IC<0>{}); pair(IC<4>{}, IC<1>{}); pair(IC<4>{}, IC<2>{}); pair(IC<4>{}, IC<3>{});
      POLL_ISSUE(pv, fa1 + (lane & 31) * 16); single(0); single(2);
      STAMP(9); WAIT_V(2); geta(); WAIT_V(12);
    } else {
      pair(IC<4>{}, IC<0>{}); POLL_ISSUE(pv, fa1 + (lane & 31) * 16); pair(IC<4>{}, IC<1>{}); pair(IC<4>{}, IC<2>{});
      WAIT_V(4); geta(); pair(IC<4>{}, IC<3>{}); single(0); single(2); STAMP(9); WAIT_V(0);
    }
    set_flag(fact + 131072 / 8, tag, lane); SB; STAMP(12);
    const bf16x8 none[4][2] = {};
    P2RUN(p2, P, tag, none);
  } else {
    const int srow = p * 256 + h * 8 + wx * 2; u32x4 xa[4];
    { const u32x4 sx = mk_srd(x, S_ * D_ * 2); const unsigned vx = srow * 2048 + lane * 16;
      asm volatile("buffer_load_dwordx4 %0, %4, %5, 0 offen\n\tbuffer_load_dwordx4 %1, %4, %5, 0 offen offset:1024\n\tbuffer_load_dwordx4 %2, %4, %5, 0 offen offset:2048\n\tbuffer_load_dwordx4 %3, %4, %5, 0 offen offset:3072"
                   : "=&v"(xa[0]), "=&v"(xa[1]), "=&v"(xa[2]), "=&v"(xa[3]) : "v"(vx), "s"(sx) : "memory"); SB; }
    const u32x4 sq = mk_srd(wqkv, D_ * 3 * D_ * 2), sh = mk_srd(w1, D_ * F_ * 2), sp = mk_srd(wphi, 32 * 32 * 32 * 2);
    unsigned vq, vh[4];
    { const int r = lane >> 2; vq = (wx * 16 + r) * 6144 + h * 64 + (((lane & 3) ^ (((r >> 3) & 1) << 1)) << 4); }
#pragma unroll
    for (int i = 0; i < 4; ++i) { const int o = (wx + 4 * i) * 1024 + lane * 16, rp = o / 224, c = (o - rp * 224) >> 4, k = rp ^ (((rp >> 3) & 1) << 2); int cs = c + 2 * odd; if (cs >= 14) cs -= 14;
      vh[i] = k * 7168 + h * 224 + cs * 16; }
    const unsigned slq = lb + L1_BQ + wx * 1024, slh = lb + L1_BH + wx * 1024;
    const unsigned rq0 = opq(lb + L1_BQ + (8 * g + q) * 64 + 32 * (g & 1) + 8 * pp), rq1 = opq(rq0 ^ 32);
    f32x4 acc[3][4][2], cs0, cs1;
    bf16x8 fa[2][4], fb[2][6];
    auto dmaQ = [&](auto ST, auto U, unsigned sk) { constexpr int st = ST(), u = U(); DMA(sq, vq, sk, u * 2048, slq, st * 12288 + u * 4096); };
    auto dmaH = [&](auto ST, auto I, unsigned sk) { constexpr int st = ST(), i = I(); if (i < 3 || wx < 2) DMA(sh, vh[i], sk, 0, slh, st * 14336 + i * 4096); };
    auto rdA = [&](auto ST, auto KS, auto M) { constexpr int st = ST(), ks = KS(), m = M(); fa[ks][m] = lds128((ks ? ra1 : ra0) + st * 32768 + m * 2048); SB; };
    auto rdB = [&](auto ST, auto KS, auto J) { constexpr int st = ST(), ks = KS(), j = J(); constexpr int o = st * 12288 + (j >> 1) * 4096 + ks * 2048; const unsigned a = (j & 1) ? rq1 : rq0;
      fb[ks][j] = cat(ldstr(a + o), ldstr(a + o + 256)); SB; };
    auto load = [&](auto ST, auto DQ, auto DH, int t) {
      constexpr int o = ST() ^ 1; constexpr bool dq = DQ(), dh = DH(); const unsigned skq = (unsigned)kt(t + 1) * (64 * 6144), skh = (unsigned)kt(t + 2) * (64 * 7168);
      rdA(ST, IC<0>{}, IC<0>{}); rdB(ST, IC<0>{}, IC<0>{}); rdB(ST, IC<0>{}, IC<1>{}); if (dq) dmaQ(IC<o>{}, IC<0>{}, skq);
      rdA(ST, IC<0>{}, IC<1>{}); rdB(ST, IC<0>{}, IC<2>{}); rdB(ST, IC<0>{}, IC<3>{}); if (dq) dmaQ(IC<o>{}, IC<1>{}, skq);
      rdA(ST, IC<0>{}, IC<2>{}); rdB(ST, IC<0>{}, IC<4>{}); rdB(ST, IC<0>{}, IC<5>{}); if (dq) dmaQ(IC<o>{}, IC<2>{}, skq);
      rdA(ST, IC<0>{}, IC<3>{}); rdB(ST, IC<1>{}, IC<0>{}); if (dh) dmaH(ST, IC<0>{}, skh);
      rdA(ST, IC<1>{}, IC<0>{}); rdB(ST, IC<1>{}, IC<1>{}); rdB(ST, IC<1>{}, IC<2>{}); if (dh) dmaH(ST, IC<1>{}, skh);
      rdA(ST, IC<1>{}, IC<1>{}); rdB(ST, IC<1>{}, IC<3>{}); if (dh) dmaH(ST, IC<2>{}, skh);
      rdA(ST, IC<1>{}, IC<2>{}); rdB(ST, IC<1>{}, IC<4>{});
      rdA(ST, IC<1>{}, IC<3>{}); rdB(ST, IC<1>{}, IC<5>{});
      if (dh) { if (wx < 2) { dmaH(ST, IC<3>{}, skh); WAIT_V(7); } else WAIT_V(6); }
    };
    auto mm_ = [&](auto F) {
#pragma unroll
      for (int m = 0; m < 4; ++m)
#pragma unroll
        for (int j = 0; j < 6; ++j) mk2<F()>(acc[j >> 1][m][j & 1], fb[0][j], fa[0][m], fb[1][j], fa[1][m]);
      SB; };
    auto mm = [&]() { mm_(IC<0>{}); };
#define H4(ST, sk) dmaH(IC<ST>{}, IC<0>{}, sk); dmaH(IC<ST>{}, IC<1>{}, sk); dmaH(IC<ST>{}, IC<2>{}, sk); dmaH(IC<ST>{}, IC<3>{}, sk)
#define Q3(ST, sk) dmaQ(IC<ST>{}, IC<0>{}, sk); dmaQ(IC<ST>{}, IC<1>{}, sk); dmaQ(IC<ST>{}, IC<2>{}, sk)
    H4(0, 0u); Q3(0, 0u);
    cs0 = zacc(); cs1 = zacc();
    auto colsum = [&](bool now) { const bf16x8 a[7] = {fb[0][0], fb[0][1], fb[0][2], fb[0][3], fb[0][4], fb[0][5], ones}, c[7] = {fb[1][0], fb[1][1], fb[1][2], fb[1][3], fb[1][4], fb[1][5], ones};
      colsum7(cs0, cs1, a, c, ones, now ? wx : 4); SB; };
    STAMP(1);
    if (wx < 2) WAIT_V(7); else WAIT_V(6);
    asm volatile("" : "+v"(xa[0]), "+v"(xa[1]), "+v"(xa[2]), "+v"(xa[3]), "+v"(ep));
    const u64 tag = uni64(ep) + 1;
    { float v[2][16], sm[2], sq_[2], mean[2];
#pragma unroll
      for (int r = 0; r < 2; ++r) { sm[r] = 0.f;
#pragma unroll
        for (int i = 0; i < 8; ++i) { const unsigned u = xa[2 * r + (i >> 2)][i & 3]; v[r][2 * i] = bflo(u); v[r][2 * i + 1] = bfhi(u); sm[r] += v[r][2 * i] + v[r][2 * i + 1]; } }
#pragma unroll
      for (int r = 0; r < 2; ++r) { mean[r] = wave_sum(sm[r]) * (1.0f / D_); sq_[r] = 0.f;
#pragma unroll
        for (int i = 0; i < 16; ++i) { const float d = v[r][i] - mean[r]; sq_[r] += d * d; } }
      const float r0 = __builtin_amdgcn_rsqf(wave_sum(sq_[0]) * (1.0f / D_) + 1e-5f), r1 = __builtin_amdgcn_rsqf(wave_sum(sq_[1]) * (1.0f / D_) + 1e-5f);
      const u32x4 gr = {__float_as_uint(lane ? mean[1] : mean[0]), (unsigned)tag, __float_as_uint(lane ? r1 : r0), (unsigned)tag};
      if (lane < 2) st16_sc1(gr, rfl, lane * 16, GR_ST + srow * 16);
      SB; }
    STAMP(2);
    WAIT_V(1);
    STAMP(3); BAR; SB; STAMP(4);
    if (wx < 2) { const unsigned vp = h * 2048 + wx * 1024 + lane * 16, slp = lb + L1_WPHI + wx * 1024; DMA(sp, vp, 0u, 0, slp, 0); }
    H4(1, 64u * 7168u); Q3(1, 64u * 6144u);
    BAR; SB; STAMP(5);
    load(IC<0>{}, IC<0>{}, IC<1>{}, 0); WAIT_L0; BAR; SB;
    mm_(IC<1>{}); colsum(p == 7); WAIT_V(3); BAR; SB;
    load(IC<1>{}, IC<1>{}, IC<1>{}, 1); WAIT_L0; BAR; SB;
    mm(); colsum(p == 7); WAIT_V(3); BAR; SB;
    load(IC<0>{}, IC<1>{}, IC<1>{}, 2); WAIT_L0; BAR; SB;
    mm(); colsum(p != 7); WAIT_V(3); BAR; SB;
    load(IC<1>{}, IC<1>{}, IC<1>{}, 3); WAIT_L0; BAR; SB;
    mm(); colsum(p != 7); WAIT_V(3); BAR; MFMA_PAD;
    cs_publish(cs0, cs1, 7 + wx, wx < 2, tag);
    for (int t = 4; t < 14; t += 2) {
      load(IC<0>{}, IC<1>{}, IC<1>{}, t); WAIT_L0; BAR; SB;
      mm(); WAIT_V(3); BAR; SB;
      load(IC<1>{}, IC<1>{}, IC<1>{}, t + 1); WAIT_L0; BAR; SB;
      mm(); WAIT_V(3); BAR; SB;
    }
    STAMP(6);
    const unsigned vst = GR_ST + (p * 256 + wx * 64 + lane) * 16, vcs = GR_CS + h * (8 * 1664) + min(wx * 64 + lane, 103) * 16;
    const u32x4 sfl = mk_srd(ws + WS_FLAG, 1u << 20);
    u32x4 gs, gc[8]; LD16_ISSUE(gs, sfl, vst, 0u);
#pragma unroll
    for (int i = 0; i < 8; ++i) LD16_ISSUE(gc[i], sfl, vcs, i * 1664u);
    load(IC<0>{}, IC<1>{}, IC<0>{}, 14);
    WAIT_V(12); WAIT_L0; BAR; SB;
    mm(); WAIT_V(0); BAR; SB;
    load(IC<1>{}, IC<0>{}, IC<0>{}, 15);
    { auto stale = [&]() { unsigned d = differ(gs[1], gs[3], (unsigned)tag);
#pragma unroll
        for (int i = 0; i < 8; ++i) d |= differ(gc[i][1], gc[i][3], (unsigned)tag);
        return !__all(d == 0u); };
      asm volatile("" : "+v"(gs), "+v"(gc[0]), "+v"(gc[1]), "+v"(gc[2]), "+v"(gc[3]), "+v"(gc[4]), "+v"(gc[5]), "+v"(gc[6]), "+v"(gc[7]));
      if (__builtin_expect(stale(), 0)) {
#pragma nounroll
        for (unsigned spins = 0; spins < (1u << 16); ++spins) {
          asm volatile("buffer_load_dwordx4 %0, %1, %2, 0 offen sc1" : "+v"(gs) : "v"(vst), "s"(sfl) : "memory");
#pragma unroll
          for (int i = 0; i < 8; ++i) asm volatile("buffer_load_dwordx4 %0, %1, %2, %3 offen sc1" : "+v"(gc[i]) : "v"(vcs), "s"(sfl), "s"(i * 1664u) : "memory");
          asm volatile("s_waitcnt vmcnt(0)" : "+v"(gs), "+v"(gc[0]), "+v"(gc[1]), "+v"(gc[2]), "+v"(gc[3]), "+v"(gc[4]), "+v"(gc[5]), "+v"(gc[6]), "+v"(gc[7]) :: "memory");
          if (!stale()) break; } }
      f32x2 c = {__uint_as_float(gc[0][0]), __uint_as_float(gc[0][2])};
#pragma unroll
      for (int i = 1; i < 8; ++i) c = (f32x2){add1(c[0], __uint_as_float(gc[i][0])), add1(c[1], __uint_as_float(gc[i][2]))};
      *(LAS f32x2*)(size_t)(lb + L1_CS + min(wx * 64 + lane, 103) * 8) = c;
      *(LAS f32x2*)(size_t)(lb + L1_ST + (wx * 64 + lane) * 8) = (f32x2){__uint_as_float(gs[2]), __uint_as_float(gs[0])}; SB; }
    WAIT_L0; BAR; SB;
    mm(); MFMA_PAD; STAMP(7);
    if (YPRIO) __builtin_amdgcn_s_setprio(1);
    {
    const int lane = opq(__lane_id()), tok = lane & 15, g = lane >> 4, q = (lane >> 2) & 3, pp = lane & 3;
    bf16x8 wp[2];
#pragma unroll
    for (int ff = 0; ff < 2; ++ff) { const unsigned a = lb + L1_WPHI + (4 * g + q) * 64 + ff * 32 + 8 * pp; wp[ff] = cat(ldstr(a), ldstr(a + 1024)); }
    f32x4 cs[6]; float rs[4], mean[4], rl[4];
#pragma unroll
    for (int j = 0; j < 6; ++j) cs[j] = *(const LAS f32x4*)(size_t)(lb + L1_CS + (7 + j) * 64 + g * 16);
#pragma unroll
    for (int m = 0; m < 4; ++m) { const f32x2 st = *(const LAS f32x2*)(size_t)(lb + L1_ST + (wx * 64 + m * 16 + tok) * 8); rs[m] = st[0]; mean[m] = opqf(-st[1]); rl[m] = st[0] * LOG2E; }
    const f32x4 zero4 = {0.f, 0.f, 0.f, 0.f};
    auto project = [&](int u, f32x4 (&pr)[4][2]) {
#pragma unroll
      for (int m = 0; m < 4; ++m) { const bf16x8 kb = pk8(acc[u][m][0] + cs[2 * u] * mean[m], acc[u][m][1] + cs[2 * u + 1] * mean[m]); pr[m][0] = mfma(wp[0], kb, zero4); pr[m][1] = mfma(wp[1], kb, zero4); }
      SB; };
    auto extrema = [&](const f32x4 (&pr)[4][2], float (&ma)[4], float (&mb)[4]) {
#pragma unroll
      for (int m = 0; m < 4; ++m) { ma[m] = vmax8(pr[m][0], pr[m][1]); mb[m] = vmax8(-pr[m][0], -pr[m][1]); }
      spread4(gather4(ma[0], mb[0], ma[1], mb[1], vmax), ma[0], mb[0], ma[1], mb[1]);
      spread4(gather4(ma[2], mb[2], ma[3], mb[3], vmax), ma[2], mb[2], ma[3], mb[3]);
#pragma unroll
      for (int m = 0; m < 4; ++m) { ma[m] *= -rl[m]; mb[m] *= -rl[m]; }
      SB; };
    auto expo = [&](int m, const f32x4 (&pr)[2], float ma, float mb, f32x4 (&ep)[2], f32x4 (&en)[2]) {
#pragma unroll
      for (int ff = 0; ff < 2; ++ff) { ep[ff] = pr[ff] * rl[m] + ma; en[ff] = pr[ff] * -rl[m] + mb; }
      SB;
#pragma unroll
      for (int ff = 0; ff < 2; ++ff)
#pragma unroll
        for (int r = 0; r < 4; ++r) { ep[ff][r] = ex2(ep[ff][r]); en[ff][r] = ex2(en[ff][r]); }
      SB; };
    { const unsigned swp = (tok & 1) | (((tok >> 2) & 1) << 1) | (((tok >> 1) & 1) << 2) | (tok & 8);
      const unsigned wph = lb + L1_PHI + (wx * 64 + tok) * 128 + ((g ^ swp) << 3), wv = lb + L1_V + (wx * 64 + tok) * 64 + ((g ^ (tok >> 1)) << 3);
      const unsigned wp0 = opq(wph), wp1 = opq(wph ^ 32), wp2 = opq(wph ^ 64), wp3 = opq(wph ^ 96), wv0 = opq(wv), wv1 = opq(wv ^ 32);
      auto put = [](unsigned a, f32x4 v) { *(LAS u32x2*)(size_t)a = (u32x2){pk2(v[0], v[1]), pk2(v[2], v[3])}; };
      f32x4 pr[4][2], ep[4][2], en[4][2]; float ma[4], mb[4];
      WAIT_L0; project(1, pr);
#pragma unroll
      for (int m = 0; m < 4; ++m) { put(wv0 + m * 1024, (acc[2][m][0] + cs[4] * mean[m]) * rs[m]); put(wv1 + m * 1024, (acc[2][m][1] + cs[5] * mean[m]) * rs[m]); }
      SB; extrema(pr, ma, mb);
#pragma unroll
      for (int m = 0; m < 4; ++m) expo(m, pr[m], ma[m], mb[m], ep[m], en[m]);
#pragma unroll
      for (int m = 0; m < 4; ++m) { const f32x4 a = ep[m][0] + ep[m][1], c = en[m][0] + en[m][1]; const f32x2 a2 = a.xy + a.zw, c2 = c.xy + c.zw; ma[m] = a2[0] + opqf(a2[1]); mb[m] = c2[0] + opqf(c2[1]); }
      auto fadd = [](float a, float c) { return a + c; };
      spread4(rcp(gather4(ma[0], mb[0], ma[1], mb[1], fadd)), ma[0], mb[0], ma[1], mb[1]);
      spread4(rcp(gather4(ma[2], mb[2], ma[3], mb[3], fadd)), ma[2], mb[2], ma[3], mb[3]);
      SB;
#pragma unroll
      for (int m = 0; m < 4; ++m) { ep[m][0] *= ma[m]; ep[m][1] *= ma[m]; en[m][0] *= mb[m]; en[m][1] *= mb[m]; SB;
        put(wp0 + m * 2048, ep[m][0]); put(wp1 + m * 2048, ep[m][1]); put(wp2 + m * 2048, en[m][0]); put(wp3 + m * 2048, en[m][1]); SB; } }
    STAMP(8);
    asm volatile("s_waitcnt lgkmcnt(0)" ::: "memory");
    if (lane == 0) { const unsigned one = 1, a = lb + L1_CNT; asm volatile("ds_add_u32 %0, %1" :: "v"(a), "v"(one) : "memory"); }
    bf16x8 eb[4][2];
    f32x4 pq[4][2]; float qa[4], qb[4];
    project(0, pq); extrema(pq, qa, qb);
    auto qpart = [&](int m0) {
#pragma unroll
      for (int m = m0; m < m0 + 2; ++m) { f32x4 ep[2], en[2]; expo(m, pq[m], qa[m], qb[m], ep, en); eb[m][0] = pk8(ep[0], ep[1]); eb[m][1] = pk8(en[0], en[1]); asm volatile("" : "+v"(eb[m][0]), "+v"(eb[m][1])); SB; } };
    lds_wait(lb + L1_CNT, 4u); SB; STAMP(9);
    { const bf16x8 ones = {0x3F80, 0x3F80, 0x3F80, 0x3F80, 0x3F80, 0x3F80, 0x3F80, 0x3F80};
      unsigned ap[2], av[2][2];
#pragma unroll
      for (int t = 0; t < 2; ++t) { const int tk = 8 * g + 4 * t + q, t15 = tk & 15; const unsigned sw = (t15 & 1) | (((t15 >> 2) & 1) << 1) | (((t15 >> 1) & 1) << 2) | (t15 & 8);
        ap[t] = opq(lb + L1_PHI + tk * 128 + (((4 * wx + pp) ^ sw) << 3));
        av[t][0] = opq(lb + L1_V + tk * 64 + ((pp ^ (t15 >> 1)) << 3)); av[t][1] = opq(av[t][0] ^ 32); }
      bf16x8 a[8], b0[8], b1_[8];
#pragma unroll
      for (int ks = 0; ks < 8; ++ks) { a[ks] = cat(ldstr(ap[0] + ks * 4096), ldstr(ap[1] + ks * 4096));
        b0[ks] = cat(ldstr(av[0][0] + ks * 2048), ldstr(av[1][0] + ks * 2048)); b1_[ks] = cat(ldstr(av[0][1] + ks * 2048), ldstr(av[1][1] + ks * 2048)); }
      SB; qpart(0); STAMP(12);
      f32x4 kv0 = zero4, kv1 = zero4, zz = zero4;
#pragma unroll
      for (int ks = 0; ks < 8; ++ks) { kv0 = mfma(a[ks], b0[ks], kv0); kv1 = mfma(a[ks], b1_[ks], kv1); zz = mfma(a[ks], ones, zz); }
      const __amdgpu_buffer_rsrc_t rkv = mk_rsrc(ws + WS_KVP, 3u << 20);
      st16_sc1(__builtin_bit_cast(u32x4, pk8(kv0, kv1)), rkv, lane * 16, (h * 8 + p) * 4096 + wx * 1024);
      if (tok == 0) st16_sc1(__builtin_bit_cast(u32x4, zz), rkv, g * 16, (unsigned)(WS_ZP - WS_KVP) + (h * 8 + p) * 256 + wx * 64);
      SB; STAMP(13); }
    qpart(2);
    SB; if (YPRIO) __builtin_amdgcn_s_setprio(0); STAMP(10); WAIT_V(0);
    set_flag(flags + (FL_KV + (h * 4 + wx) * 1024) / 8 + p * 16, tag, lane); STAMP(11);
    Phase2<1> p2; p2.init(lb, lane, wx, P);
    P2RUN(p2, P, tag, eb);
    }
  }
}
}

extern "C" void kernel_launch(void* const* d_in, const int* in_sizes, int n_in, void* d_out, int out_size, void* d_ws, size_t ws_size, hipStream_t stream) {
  k_fused<<<256, 512, 0, stream>>>((const u16*)d_in[0], (const u16*)d_in[1], (const u16*)d_in[2], (const u16*)d_in[3], (const u16*)d_in[5], (const u16*)d_in[6], (const u16*)d_in[7], (char*)d_ws, (u16*)d_out);
}
```

```cpp
#define PFCODE 1
#define PFTLB 0
#include <hip/hip_runtime.h>
#include <hip/hip_bf16.h>
#include <type_traits>

namespace {
#define LAS __attribute__((address_space(3)))
#define GAS __attribute__((address_space(1)))
typedef unsigned short u16;
typedef unsigned u32x4 __attribute__((ext_vector_type(4)));
typedef unsigned u32x2 __attribute__((ext_vector_type(2)));
typedef short bf16x8 __attribute__((ext_vector_type(8)));
typedef short s16x4 __attribute__((ext_vector_type(4)));
typedef float f32x4 __attribute__((ext_vector_type(4)));
typedef float f32x2 __attribute__((ext_vector_type(2)));
typedef __bf16 bf16x2_t __attribute__((ext_vector_type(2)));

constexpr int S_ = 2048, D_ = 1024, F_ = 3584;
constexpr size_t WS_FLAG = 0;
constexpr size_t WS_XN = 1u << 20;
constexpr size_t WS_ACT = WS_XN + (4u << 20);
constexpr size_t WS_O = WS_ACT + (15u << 20);
constexpr size_t WS_KVP = WS_O + (4u << 20);
constexpr size_t WS_ZP = WS_KVP + (2u << 20);
constexpr size_t WS_YX = WS_ZP + (1u << 20);
constexpr size_t WS_STAMP = 64u << 20;
#define STAMP_DECL
#define STAMP(i)
#define STAMP_OUT(wv)
constexpr unsigned FL_EPOCH = 0;
constexpr unsigned FL_O = 1024;
constexpr unsigned FL_YX = 16384;
constexpr unsigned FL_TMO = 61440;
constexpr unsigned GR_ST = 65536;
constexpr unsigned FL_KV = 131072;
constexpr unsigned FL_ACT = 262144;
constexpr unsigned GR_CS = 524288;
#define YPRIO 0
#define XP0 0
#define EARLYA 0
#define BLATE 6
#define TOUCH0 0
#define TOUCHW 1
constexpr float LOG2E = 1.4426950408889634f;

#define SB __builtin_amdgcn_sched_barrier(0)
#define BAR __builtin_amdgcn_s_barrier()
#define WAIT_L0 do { __builtin_amdgcn_s_waitcnt(0xC07F); SB; } while (0)
#define WAIT_V(n) asm volatile("s_waitcnt vmcnt(%0)" :: "i"(n) : "memory")
template <int N> using IC = std::integral_constant<int, N>;

__device__ __forceinline__ u32x4 mk_srd(const void* p, unsigned bytes) {
  const unsigned long long a = (unsigned long long)p; u32x4 r;
  r.x = __builtin_amdgcn_readfirstlane((unsigned)a); r.y = __builtin_amdgcn_readfirstlane((unsigned)(a >> 32)) & 0xffffu; r.z = bytes; r.w = 0x00020000u; return r; }
#define DMA(srd, voff, sk, KOFF, sl, LOFF) do { unsigned _t; asm volatile("s_add_u32 m0, %4, %5\n\ts_add_u32 %0, %3, %6\n\tbuffer_load_dwordx4 %1, %2, %0 offen lds" \
    : "=&s"(_t) : "v"(voff), "s"(srd), "s"(sk), "s"(sl), "i"(LOFF), "i"(KOFF) : "memory", "scc"); SB; } while (0)
#define DMA_SC1(srd, voff, sk, KOFF, sl, LOFF) do { unsigned _t; asm volatile("s_add_u32 m0, %4, %5\n\ts_add_u32 %0, %3, %6\n\tbuffer_load_dwordx4 %1, %2, %0 offen sc1 lds" \
    : "=&s"(_t) : "v"(voff), "s"(srd), "s"(sk), "s"(sl), "i"(LOFF), "i"(KOFF) : "memory", "scc"); SB; } while (0)
#define TOUCH(ptr, sl) do { asm volatile("s_mov_b32 m0, %1\n\ts_nop 0\n\tglobal_load_lds_dword %0, off" :: "v"(ptr), "s"(sl) : "memory"); SB; } while (0)
__device__ __forceinline__ bf16x8 lds128(unsigned a) { return *(const LAS bf16x8*)(size_t)a; }
__device__ __forceinline__ s16x4 ldstr(unsigned a) { return __builtin_amdgcn_ds_read_tr16_b64_v4i16((LAS s16x4*)(size_t)a); }
__device__ __forceinline__ bf16x8 cat(s16x4 lo, s16x4 hi) { return __builtin_shufflevector(lo, hi, 0, 1, 2, 3, 4, 5, 6, 7); }
__device__ __forceinline__ unsigned opq(unsigned x) { asm volatile("" : "+v"(x)); return x; }
__device__ __forceinline__ float opqf(float x) { asm("" : "+v"(x)); return x; }
__device__ __forceinline__ f32x2 opq2(f32x2 x) { asm("" : "+v"(x)); return x; }
__device__ __forceinline__ f32x4 mfma(bf16x8 a, bf16x8 b, f32x4 c) { return __builtin_amdgcn_mfma_f32_16x16x32_bf16(a, b, c, 0, 0, 0); }
__device__ __forceinline__ void mfma_acc(f32x4& d, bf16x8 a, bf16x8 b) { asm volatile("v_mfma_f32_16x16x32_bf16 %0, %1, %2, %0" : "+v"(d) : "v"(a), "v"(b)); }
__device__ __forceinline__ void mfma_k2(f32x4& d, bf16x8 a0, bf16x8 b0, bf16x8 a1, bf16x8 b1) {
  asm volatile("v_mfma_f32_16x16x32_bf16 %0, %1, %2, %0\n\tv_mfma_f32_16x16x32_bf16 %0, %3, %4, %0" : "+v"(d) : "v"(a0), "v"(b0), "v"(a1), "v"(b1)); }
__device__ __forceinline__ void mfma_k2_first(f32x4& d, bf16x8 a0, bf16x8 b0, bf16x8 a1, bf16x8 b1) {
  asm volatile("v_mfma_f32_16x16x32_bf16 %0, %1, %2, 0\n\tv_mfma_f32_16x16x32_bf16 %0, %3, %4, %0" : "=&v"(d) : "v"(a0), "v"(b0), "v"(a1), "v"(b1)); }
template <bool FIRST> __device__ __forceinline__ void mk2(f32x4& d, bf16x8 a0, bf16x8 b0, bf16x8 a1, bf16x8 b1) { if (FIRST) mfma_k2_first(d, a0, b0, a1, b1); else mfma_k2(d, a0, b0, a1, b1); }
__device__ __forceinline__ void mfma_k2x2(f32x4& d, f32x4& e, bf16x8 a0, bf16x8 c0, bf16x8 b0, bf16x8 a1, bf16x8 c1, bf16x8 b1) {
  asm volatile("v_mfma_f32_16x16x32_bf16 %0, %2, %4, %0\n\tv_mfma_f32_16x16x32_bf16 %1, %3, %4, %1\n\tv_mfma_f32_16x16x32_bf16 %0, %5, %7, %0\n\tv_mfma_f32_16x16x32_bf16 %1, %6, %7, %1"
               : "+v"(d), "+v"(e) : "v"(a0), "v"(c0), "v"(b0), "v"(a1), "v"(c1), "v"(b1)); }
__device__ __forceinline__ f32x4 zacc() { f32x4 z = {0.f, 0.f, 0.f, 0.f}; asm volatile("" : "+v"(z)); return z; }
#define CS_M(d, a) "v_mfma_f32_16x16x32_bf16 " d ", " a ", %[o], " d "\n\t"
__device__ __forceinline__ void colsum7(f32x4& c0, f32x4& c1, const bf16x8 (&a)[7], const bf16x8 (&b)[7], bf16x8 ones, unsigned sel) {
  asm volatile("s_cmp_lt_u32 %[s], 2\n\ts_cbranch_scc1 .Lcs01_%=\n\ts_cmp_eq_u32 %[s], 2\n\ts_cbranch_scc1 .Lcs2_%=\n\ts_cmp_eq_u32 %[s], 3\n\ts_cbranch_scc0 .Lcs9_%=\n\t"
               CS_M("%0", "%[a3]") CS_M("%0", "%[b3]") "s_branch .Lcs9_%=\n"
               ".Lcs2_%=:\n\t" CS_M("%0", "%[a2]") CS_M("%0", "%[b2]") CS_M("%1", "%[a6]") CS_M("%1", "%[b6]") "s_branch .Lcs9_%=\n"
               ".Lcs01_%=:\n\ts_cmp_eq_u32 %[s], 0\n\ts_cbranch_scc1 .Lcs0_%=\n\t" CS_M("%0", "%[a1]") CS_M("%0", "%[b1]") CS_M("%1", "%[a5]") CS_M("%1", "%[b5]") "s_branch .Lcs9_%=\n"
               ".Lcs0_%=:\n\t" CS_M("%0", "%[a0]") CS_M("%0", "%[b0]") CS_M("%1", "%[a4]") CS_M("%1", "%[b4]") "\n.Lcs9_%=:"
               : "+v"(c0), "+v"(c1) : [a0] "v"(a[0]), [a1] "v"(a[1]), [a2] "v"(a[2]), [a3] "v"(a[3]), [a4] "v"(a[4]), [a5] "v"(a[5]), [a6] "v"(a[6]),
                 [b0] "v"(b[0]), [b1] "v"(b[1]), [b2] "v"(b[2]), [b3] "v"(b[3]), [b4] "v"(b[4]), [b5] "v"(b[5]), [b6] "v"(b[6]), [o] "v"(ones), [s] "s"(sel) : "scc");
}
#define MFMA_PAD do { asm volatile("s_nop 7\n\ts_nop 1" ::: "memory"); SB; } while (0)
__device__ __forceinline__ unsigned pk2(float lo, float hi) { const f32x2 v = {lo, hi}; return __builtin_bit_cast(unsigned, __builtin_convertvector(v, bf16x2_t)); }
__device__ __forceinline__ bf16x8 pk8(f32x4 a, f32x4 b) { const u32x4 u = {pk2(a[0], a[1]), pk2(a[2], a[3]), pk2(b[0], b[1]), pk2(b[2], b[3])}; return __builtin_bit_cast(bf16x8, u); }
__device__ __forceinline__ float bflo(unsigned u) { return __uint_as_float(u << 16); }
__device__ __forceinline__ float bfhi(unsigned u) { return __uint_as_float(u & 0xffff0000u); }
__device__ __forceinline__ float ex2(float x) { return __builtin_amdgcn_exp2f(x); }
__device__ __forceinline__ float rcp(float x) { return __builtin_amdgcn_rcpf(x); }
__device__ __forceinline__ u32x4 swap16(unsigned a0, unsigned a1, unsigned b0, unsigned b1) {
  const auto p = __builtin_amdgcn_permlane16_swap(a0, b0, false, false), q = __builtin_amdgcn_permlane16_swap(a1, b1, false, false);
  return (u32x4){p[0], q[0], p[1], q[1]}; }
__device__ __forceinline__ float vmax(float a, float b) { return __builtin_elementwise_maximum(a, b); }
__device__ __forceinline__ float vmax8(f32x4 a, f32x4 c) { return vmax(vmax(vmax(vmax(a[0], a[1]), a[2]), vmax(vmax(a[3], c[0]), c[1])), vmax(c[2], c[3])); }
__device__ __forceinline__ void sw32(float& a, float& b) { const auto p = __builtin_amdgcn_permlane32_swap(__float_as_uint(a), __float_as_uint(b), false, false); a = __uint_as_float(p[0]); b = __uint_as_float(p[1]); }
__device__ __forceinline__ void sw16(float& a, float& b) { const auto p = __builtin_amdgcn_permlane16_swap(__float_as_uint(a), __float_as_uint(b), false, false); a = __uint_as_float(p[0]); b = __uint_as_float(p[1]); }
template <class Op> __device__ __forceinline__ float gather4(float a0, float a1, float b0, float b1, Op op) { sw32(a0, a1); sw32(b0, b1); float c = op(a0, a1), d = op(b0, b1); sw16(c, d); return op(c, d); }
__device__ __forceinline__ void spread4(float d, float& a0, float& a1, float& b0, float& b1) { a0 = d; b0 = d; sw16(a0, b0); a1 = a0; b1 = b0; sw32(a0, a1); sw32(b0, b1); }
template <class Op> __device__ __forceinline__ float red4(float v, Op op) {
  const unsigned u = __float_as_uint(v); const auto p = __builtin_amdgcn_permlane32_swap(u, u, false, false);
  const float a = op(__uint_as_float(p[0]), __uint_as_float(p[1])); const unsigned ua = __float_as_uint(a);
  const auto q = __builtin_amdgcn_permlane16_swap(ua, ua, false, false); return op(__uint_as_float(q[0]), __uint_as_float(q[1])); }
__device__ __forceinline__ void st16_sc1(u32x4 v, __amdgpu_buffer_rsrc_t r, unsigned voff, unsigned soff) { __builtin_amdgcn_raw_buffer_store_b128(v, r, voff + soff, 0, 16); }
__device__ __forceinline__ void st16(u32x4 v, __amdgpu_buffer_rsrc_t r, unsigned voff, unsigned soff) { __builtin_amdgcn_raw_buffer_store_b128(v, r, voff + soff, 0, 0); }
__device__ __forceinline__ u32x4 ld16_sc1(__amdgpu_buffer_rsrc_t r, unsigned voff, unsigned soff) { return __builtin_amdgcn_raw_buffer_load_b128(r, voff, soff, 16); }
__device__ __forceinline__ __amdgpu_buffer_rsrc_t mk_rsrc(const void* p, unsigned bytes) { return __builtin_amdgcn_make_buffer_rsrc((void*)p, 0, bytes, 0x00020000); }
#define RLX_AGENT __ATOMIC_RELAXED, __HIP_MEMORY_SCOPE_AGENT
typedef unsigned long long u64;
typedef GAS u64 gu64;
__device__ __forceinline__ void set_flag(gu64* w, u64 tag, int lane) { if (lane == 0) __hip_atomic_store(w, tag, RLX_AGENT); }
template <int N, int STRIDE> __device__ __forceinline__ void wait_flags(gu64* w, u64 tag, gu64* tmo, int lane) {
#pragma nounroll
  for (unsigned spins = 0;; ++spins) {
    u64 v = tag; if (N >= 64 || lane < N) v = __hip_atomic_load(w + lane * STRIDE, RLX_AGENT);
    if (__all(v == tag)) break;
    if (spins > (1u << 16)) { if (lane == 0) __hip_atomic_store(tmo, tag, RLX_AGENT); break; }
    __builtin_amdgcn_s_sleep(2);
  }
  __builtin_amdgcn_fence(__ATOMIC_ACQUIRE, "wavefront");
}
__device__ __forceinline__ u64 uni64(u64 v) { return ((u64)(unsigned)__builtin_amdgcn_readfirstlane((unsigned)(v >> 32)) << 32) | (unsigned)__builtin_amdgcn_readfirstlane((unsigned)v); }
__device__ __forceinline__ void lds_wait(unsigned addr, unsigned target) {
  for (;;) { unsigned v; asm volatile("ds_read_b32 %0, %1\n\ts_waitcnt lgkmcnt(0)" : "=v"(v) : "v"(addr) : "memory");
    if ((unsigned)__builtin_amdgcn_readfirstlane(v) >= target) break; }
}
#define POLL_ISSUE(dst, ptr) do { asm volatile("global_load_dwordx2 %0, %1, off sc1" : "=v"(dst) : "v"(ptr) : "memory"); SB; } while (0)
#define LD16_ISSUE(dst, srd, voff, soff) do { asm volatile("buffer_load_dwordx4 %0, %1, %2, %3 offen sc1" : "=v"(dst) : "v"(voff), "s"(srd), "s"(soff) : "memory"); SB; } while (0)
#define POLL_PIN(dst) asm volatile("" : "+v"(dst))
__device__ __forceinline__ float add1(float a, float b) { float r; asm("v_add_f32 %0, %1, %2" : "=v"(r) : "v"(a), "v"(b)); return r; }
__device__ __forceinline__ float mul1(float a, float b) { float r; asm("v_mul_f32 %0, %1, %2" : "=v"(r) : "v"(a), "v"(b)); return r; }
__device__ __forceinline__ float fma1(float a, float b, float c) { float r; asm("v_fma_f32 %0, %1, %2, %3" : "=v"(r) : "v"(a), "v"(b), "v"(c)); return r; }
__device__ __forceinline__ f32x4 add4(f32x4 a, u32x4 b) { return (f32x4){add1(a[0], __uint_as_float(b[0])), add1(a[1], __uint_as_float(b[1])), add1(a[2], __uint_as_float(b[2])), add1(a[3], __uint_as_float(b[3]))}; }
__device__ __forceinline__ unsigned differ(unsigned a, unsigned b, unsigned t) { unsigned r; asm("v_bitop3_b32 %0, %1, %2, %3 bitop3:0x7e" : "=v"(r) : "v"(a), "v"(b), "s"(t)); return r; }
template <int C> __device__ __forceinline__ float dpp(float v) { return __int_as_float(__builtin_amdgcn_update_dpp(0, __float_as_int(v), C, 0xf, 0xf, true)); }
__device__ __forceinline__ float wave_sum(float v) { v += dpp<0x128>(v); v += dpp<0x124>(v); v += dpp<0x122>(v); v += dpp<0x121>(v); return red4(v, [](float a, float c) { return a + c; }); }
__device__ __forceinline__ void lds_sync(unsigned addr, unsigned target, int lane) {
  asm volatile("s_waitcnt lgkmcnt(0)" ::: "memory");
  if (lane == 0) { const unsigned one = 1; asm volatile("ds_add_u32 %0, %1" :: "v"(addr), "v"(one) : "memory"); }
  for (;;) { unsigned v; asm volatile("ds_read_b32 %0, %1\n\ts_waitcnt lgkmcnt(0)" : "=v"(v) : "v"(addr) : "memory");
    if ((unsigned)__builtin_amdgcn_readfirstlane(v) >= target) break; }
}

constexpr unsigned L1_A = 0;
constexpr unsigned L1_BQ = 65536;
constexpr unsigned L1_BH = 90112;
constexpr unsigned L1_WPHI = 118784;
constexpr unsigned L1_CNT = 120832;
constexpr unsigned L1_DUMP = 121088;
constexpr unsigned L1_BIAS = 121344;
constexpr unsigned L1_CS = 128000;
constexpr unsigned L1_ST = 129024;
constexpr unsigned L1_V = 98304;
constexpr unsigned L1_PHI = 131072;
constexpr unsigned L2_EX = 131072;

struct Ptrs { const u16 *x, *wqkv, *wphi, *wo, *w1, *b1, *w2; char* ws; u16* out; };

template <int G> struct Phase2 {
  unsigned lb; int lane, wq, b, kh, rt, ct;
  u32x4 sact, so, sw2, swo;
  unsigned vaa, vao, vb0, vb1, vs0, vs1, sla, sls;
  struct Off { unsigned a0, a1, b; };
  __device__ __forceinline__ Off off_y(int U) const { const int s0 = kh * 56 + (U < 24 ? 7 * (U & 7) + (U >> 3) : 14 * U - 333), s1 = s0 + (U < 24 ? 4 : 7); return {s0 * 1024u, s1 * 1024u, (wq >> 1 ? s1 : s0) * 65536u}; }
  __device__ __forceinline__ Off off_s(int U) const { return {(U - 28) * 2048u, (U - 28) * 2048u + 1024u, (U - 28) * 131072u}; }
  __device__ __forceinline__ void init(unsigned lb_, int lane_, int wq_, const Ptrs& P) {
    lb = lb_; lane = lane_; wq = wq_; b = blockIdx.x; kh = b & 1; rt = ((b & 7) >> 1) * 4 + ((b >> 3) & 3); ct = b >> 5;
    sact = mk_srd(P.ws + WS_ACT, 128 * 112 * 1024); so = mk_srd(P.ws + WS_O, 128 * 32 * 1024); sw2 = mk_srd(P.w2, F_ * D_ * 2); swo = mk_srd(P.wo, D_ * D_ * 2);
    vaa = lane * 16 + (rt * 8 + wq * 2) * (112 * 1024); vao = lane * 16 + (rt * 8 + wq * 2) * (32 * 1024);
    { const int r = lane >> 4, c = lane & 15; vb0 = ((wq & 1) * 16 + r) * 2048 + ct * 256 + (((c >> 1) ^ r) << 5) + ((c & 1) << 4); vb1 = vb0 ^ 128; }
    { const int r = lane >> 3, c = lane & 7; vs0 = (wq * 16 + r) * 2048 + ct * 256 + kh * 128 + (((c >> 1) ^ ((r >> 1) & 1)) << 5) + ((c & 1) << 4); vs1 = vs0 ^ 64; }
    sla = lb + wq * 4096; sls = lb + wq * 2048;
  }
  template <int LO, bool Y, int I> __device__ __forceinline__ void dma(const Off& o) {
    if (I < 4) { const unsigned sk = (I & 1) ? o.a1 : o.a0; if (Y) DMA_SC1(sact, vaa, sk, (I >> 1) * 112 * 1024, sla, LO + I * 1024); else DMA_SC1(so, vao, sk, (I >> 1) * 32 * 1024, sla, LO + I * 1024); }
    else if (Y) { constexpr int j = I - 4; if (j >> 1) DMA(sw2, vb1, o.b, j * 8192, sla, LO + 16384 + j * 1024); else DMA(sw2, vb0, o.b, j * 8192, sla, LO + 16384 + j * 1024); }
    else if (I < 6) { constexpr int j = I - 4; if (j) DMA(swo, vs1, o.b, j * 16384, sls, LO + 16384 + j * 1024); else DMA(swo, vs0, o.b, j * 16384, sls, LO + 16384 + j * 1024); } }
  template <int U, int I> __device__ __forceinline__ void dma1() { dma<(U & 1) * 65536 + ((U >> 1) & 1) * 32768, true, I>(off_y(U)); }
  template <int U, int I0> __device__ __forceinline__ void dma4() { dma1<U, I0>(); dma1<U, I0 + 1>(); dma1<U, I0 + 2>(); dma1<U, I0 + 3>(); }

  __device__ __forceinline__ void run(const Ptrs& P, u64 tag, int p, int h, const bf16x8 (&eb)[4][2]) {
    char* ws = P.ws;
    const int tok = lane & 15, g = lane >> 4, q = (lane >> 2) & 3, pp = lane & 3, wi = wq >> 1, jj = wq & 1;
    const unsigned ra = opq(lb + G * 65536 + wi * 8192 + lane * 16);
    unsigned rb[4], rs[2];
#pragma unroll
    for (int n = 0; n < 4; ++n) { const int f = 4 * (kh ^ (n >> 1)) + 2 * jj + (n & 1); rb[n] = opq(lb + G * 65536 + 16384 + (8 * g + q) * 256 + ((f ^ (q | ((g & 1) << 2))) << 5) + 8 * pp); }
#pragma unroll
    for (int n = 0; n < 2; ++n) { const int f = 2 * jj + n; rs[n] = opq(lb + G * 65536 + 16384 + (8 * g + q) * 128 + ((f ^ ((q >> 1) | ((g & 1) << 1))) << 5) + 8 * pp); }
    f32x4 acc[4][4], as[4][2];
    bf16x8 fa[2][4], fb[2][4];
    auto rdA = [&](auto HR, auto I) { constexpr int i = I(); fa[i >> 2][i & 3] = lds128(ra + HR() * 32768 + (i & 3) * 2048 + (i >> 2) * 1024); SB; };
    auto rdB = [&](auto HR, auto Y, auto I) { constexpr int i = I(), ks = i >> 2, n = i & 3; constexpr bool y = Y();
      if (y) { constexpr int o = HR() * 32768 + ks * 8192; fb[ks][n] = cat(ldstr(rb[n] + o), ldstr(rb[n] + o + 1024)); SB; }
      else if (n < 2) { constexpr int o = HR() * 32768 + ks * 4096; fb[ks][n] = cat(ldstr(rs[n & 1] + o), ldstr(rs[n & 1] + o + 512)); SB; } };
    auto loadh = [&](auto HR, auto YR, auto MD, auto HD, int U, auto hook) { constexpr int md = MD(), LO = (G ^ 1) * 65536 + HD() * 32768; constexpr bool d = md != 0, y = md == 1;
      const Off o = y ? off_y(U) : off_s(U);
      rdA(HR, IC<0>{}); rdB(HR, YR, IC<0>{}); if (d) dma<LO, y, 0>(o); hook(IC<0>{}); SB;
      rdA(HR, IC<1>{}); rdB(HR, YR, IC<1>{}); if (d) dma<LO, y, 4>(o); hook(IC<1>{}); SB;
      rdA(HR, IC<2>{}); rdB(HR, YR, IC<2>{}); if (d) dma<LO, y, 1>(o); hook(IC<2>{}); SB;
      rdA(HR, IC<3>{}); rdB(HR, YR, IC<3>{}); if (d) dma<LO, y, 5>(o); hook(IC<3>{}); SB;
      rdA(HR, IC<4>{}); rdB(HR, YR, IC<4>{}); if (d) dma<LO, y, 2>(o); hook(IC<4>{}); SB;
      rdA(HR, IC<5>{}); rdB(HR, YR, IC<5>{}); if (d) dma<LO, y, 6>(o); hook(IC<5>{}); SB;
      rdA(HR, IC<6>{}); rdB(HR, YR, IC<6>{}); if (d) dma<LO, y, 3>(o); hook(IC<6>{}); SB;
      rdA(HR, IC<7>{}); rdB(HR, YR, IC<7>{}); hook(IC<7>{}); SB; if (d) dma<LO, y, 7>(o); };
    auto load = [&](auto HR, auto YR, auto MD, auto HD, int U) { loadh(HR, YR, MD, HD, U, [](auto) {}); };
    auto mmy_ = [&](auto F) {
#pragma unroll
      for (int m = 0; m < 4; ++m)
#pragma unroll
        for (int n = 0; n < 4; ++n) mk2<F()>(acc[m][n], fb[0][n], fa[0][m], fb[1][n], fa[1][m]);
      SB; };
    auto mms_ = [&](auto F) {
#pragma unroll
      for (int m = 0; m < 4; ++m)
#pragma unroll
        for (int n = 0; n < 2; ++n) mk2<F()>(as[m][n], fb[0][n], fa[0][m], fb[1][n], fa[1][m]);
      SB; };
    auto mmy = [&]() { mmy_(IC<0>{}); }; auto mms = [&]() { mms_(IC<0>{}); };
#define ENDL(n) do { WAIT_V(n); WAIT_L0; BAR; SB; } while (0)
#define ENDM do { BAR; SB; } while (0)
    IC<0> c0; IC<1> c1; IC<2> c2; IC<G> hA; IC<G ^ 1> hB;
    gu64* flags = (gu64*)(ws + WS_FLAG); gu64* tmo = flags + FL_TMO / 8;
    const __amdgpu_buffer_rsrc_t ryx = mk_rsrc(ws + WS_YX, 256 * 32768);
    const unsigned exk = lb + L2_EX + wq * 4096 + lane * 16, exs = lb + 24576 + (wq >> 1) * 32768 + (wq & 1) * 4096 + lane * 16;
    f32x4 tk[4];
    auto give = [&](auto K) { constexpr int k = K();
#pragma unroll
      for (int i = 0; i < 4; ++i) *(LAS f32x4*)(size_t)((k ? exs + G * 65536 : exk + G * 16384) + i * 1024) = acc[2 * (G ^ 1) + (i >> 1)][2 * k + (i & 1)];
      SB; };
    auto take_rd = [&](auto K) { constexpr int k = K();
#pragma unroll
      for (int i = 0; i < 4; ++i) tk[i] = *(const LAS f32x4*)(size_t)((k ? exs + (G ^ 1) * 65536 : exk + (G ^ 1) * 16384) + i * 1024);
      SB; };
    auto take = [&](auto K) { constexpr int k = K();
#pragma unroll
      for (int i = 0; i < 4; ++i) { f32x4& a = acc[2 * G + (i >> 1)][2 * k + (i & 1)]; a += tk[i]; if (k) st16_sc1(__builtin_bit_cast(u32x4, a), ryx, lane * 16, b * 32768 + (G * 4 + wq) * 4096 + i * 1024); else asm volatile("" : "+v"(a)); }
      SB; };
    u64 pv;
    if (G == 0) {
      WAIT_V(EARLYA ? 1 : 9); BAR; SB; STAMP(16);
      load(c0, c1, c1, c1, 3); ENDL(13);
      mmy_(c1); WAIT_V(8); ENDM; STAMP(17);
      load(c1, c1, c1, hA, 5); ENDL(8);
      mmy(); ENDM; load(c0, c1, c1, hB, 7); ENDL(8);
      for (int T = 4; T < 20; T += 4) {
        mmy(); ENDM; load(c1, c1, c1, hA, T + 5); ENDL(8);
        mmy(); ENDM; load(c0, c1, c1, hB, T + 7); ENDL(8);
      }
    } else {
      BAR; SB; STAMP(16); BAR; SB;
      load(c0, c1, c1, c0, 4); ENDL(8);
      mmy_(c1); ENDM; STAMP(17);
      gu64* fa2 = flags + (FL_ACT + 131072) / 8 + (rt * 2 + kh) * 512; gu64* fkv = flags + (FL_KV + (h * 4 + wq) * 1024) / 8;
      POLL_ISSUE(pv, lane < 32 ? fa2 + lane * 16 : fkv + (lane & 7) * 16);
      load(c1, c1, c1, hA, 6); ENDL(9);
      mmy(); ENDM;
      WAIT_V(8); POLL_PIN(pv); if (!__all(pv == tag)) { wait_flags<32, 16>(fa2, tag, tmo, lane); wait_flags<8, 16>(fkv, tag, tmo, lane); } SB;
      const u32x4 skv = mk_srd(ws + WS_KVP, 3u << 20);
      u32x4 t[8], tz; LD16_ISSUE(tz, skv, h * 2048 + (lane & 7) * 256 + wq * 64 + g * 16, (unsigned)(WS_ZP - WS_KVP));
      { const unsigned vkv = h * 32768 + wq * 1024 + lane * 16;
#pragma unroll
        for (int i = 0; i < 8; ++i) LD16_ISSUE(t[i], skv, vkv, i * 4096u); }
      load(c0, c1, c1, hB, 8); ENDL(17);
      mmy(); ENDM;
      WAIT_V(8); asm volatile("" : "+v"(t[0]), "+v"(t[1]), "+v"(t[2]), "+v"(t[3]), "+v"(t[4]), "+v"(t[5]), "+v"(t[6]), "+v"(t[7]), "+v"(tz)); SB;
      float sm[8];
      auto addp = [&](int i, int c) { sm[2 * c] = add1(sm[2 * c], bflo(t[i][c])); sm[2 * c + 1] = add1(sm[2 * c + 1], bfhi(t[i][c])); asm volatile("" : "+v"(sm[2 * c]), "+v"(sm[2 * c + 1])); };
      loadh(c1, c1, c1, hA, 10, [&](auto I) { constexpr int i = I();
        if (i == 0) { _Pragma("unroll") for (int c = 0; c < 4; ++c) { sm[2 * c] = bflo(t[0][c]); sm[2 * c + 1] = bfhi(t[0][c]); asm volatile("" : "+v"(sm[2 * c]), "+v"(sm[2 * c + 1])); } }
        else if (i < 7) { addp((i + 1) >> 1, 2 * (~i & 1)); addp((i + 1) >> 1, 2 * (~i & 1) + 1); }
        else { _Pragma("unroll") for (int r = 0; r < 4; ++r) { float z = __uint_as_float(tz[r]); z += dpp<0xB1>(z); z = opqf(z); z += dpp<0x4E>(z); z = opqf(z); z += dpp<0x141>(z); tz[r] = __float_as_uint(opqf(z)); } asm volatile("" : "+v"(tz)); } });
      ENDL(8);
      mmy(); ENDM;
      const unsigned kva = opq(lb + L2_EX + lane * 16);
      loadh(c0, c1, c1, hB, 12, [&](auto I) { constexpr int i = I(); addp(4 + (i >> 1), 2 * (i & 1)); addp(4 + (i >> 1), 2 * (i & 1) + 1); });
      { const unsigned a = kva + (wq >> 1) * 1024 + (wq & 1) * 8;
        *(LAS u32x2*)(size_t)a = (u32x2){pk2(sm[0], sm[1]), pk2(sm[2], sm[3])}; *(LAS u32x2*)(size_t)(a + 2048) = (u32x2){pk2(sm[4], sm[5]), pk2(sm[6], sm[7])};
        u32x2 au = {pk2(__uint_as_float(tz[0]), __uint_as_float(tz[1])), pk2(__uint_as_float(tz[2]), __uint_as_float(tz[3]))};
        if ((lane & 3) == 1) au = (u32x2){0x3F803F80u, 0x3F803F80u}; else if ((lane & 3) != 0) au = (u32x2){0u, 0u};
        *(LAS u32x2*)(size_t)(a + 4096) = au; SB; }
      ENDL(8);
      mmy(); ENDM;
      bf16x8 ka[6]; f32x4 r6[6]; float ca, cc, o8[8];
      const __amdgpu_buffer_rsrc_t ro = mk_rsrc(ws + WS_O, 128 * 32 * 1024);
      const unsigned vo = (unsigned)((((g & 1) << 1) | (g >> 1)) * 256 + tok * 16) + ((p * 16 + wq * 4) * 32 + h) * 1024;
      auto ostep = [&](int m, int i) { const f32x4 z4 = {0.f, 0.f, 0.f, 0.f};
        if (i == 1) { __builtin_amdgcn_s_setprio(1);
#pragma unroll
          for (int k = 5; k >= 0; --k) { r6[k] = mfma(ka[k], eb[m][k & 1], z4); asm volatile("" : "+v"(r6[k])); }
          __builtin_amdgcn_s_setprio(0); }
        if (i == 2) { ca = rcp(r6[4][1]); cc = rcp(r6[5][1]); asm volatile("" : "+v"(ca), "+v"(cc)); }
        if (i == 3) { const float inv = rcp(opqf(r6[4][0] * ca + 1e-6f) + r6[5][0] * cc); ca = opqf(ca * inv); cc = opqf(cc * inv); asm volatile("" : "+v"(ca), "+v"(cc)); }
        if (i == 4 || i == 5) { _Pragma("unroll") for (int r = 0; r < 4; ++r) { const int d = i - 4; o8[4 * d + r] = opqf(r6[2 * d][r] * ca) + r6[2 * d + 1][r] * cc; asm volatile("" : "+v"(o8[4 * d + r])); } }
        if (i == 6) st16_sc1(swap16(pk2(o8[0], o8[1]), pk2(o8[2], o8[3]), pk2(o8[4], o8[5]), pk2(o8[6], o8[7])), ro, vo, m * 32768); };
#pragma unroll
      for (int i = 0; i < 6; ++i) ka[i] = lds128(kva + i * 1024);
      SB;
      loadh(c1, c1, c1, hA, 14, [&](auto I) { ostep(0, I()); }); ENDL(9);
      mmy(); ENDM; loadh(c0, c1, c1, hB, 16, [&](auto I) { ostep(1, I()); }); ENDL(9);
      mmy(); ENDM; loadh(c1, c1, c1, hA, 18, [&](auto I) { ostep(2, I()); }); ENDL(9);
      mmy(); ENDM; loadh(c0, c1, c1, hB, 20, [&](auto I) { ostep(3, I()); }); ENDL(9);
      mmy(); ENDM; load(c1, c1, c1, hA, 22); WAIT_V(8);
      set_flag(flags + FL_O / 8 + (2 * p + (wq >> 1)) * 64 + h * 2 + (wq & 1), tag, lane); WAIT_L0; ENDM;
      mmy(); ENDM; load(c0, c1, c1, hB, 24); ENDL(8);
    }
    STAMP(18);
    gu64* po = flags + FL_O / 8 + rt * 64;
    mmy(); ENDM; if (G == 1) POLL_ISSUE(pv, po + lane); load(c1, c1, c1, hA, 25 + G); ENDL(8 + G);
    mmy(); ENDM;
    if (G == 0) { load(c0, c1, c1, hB, 27); ENDL(8); }
    else { WAIT_V(8); POLL_PIN(pv); if (!__all(pv == tag)) wait_flags<64, 1>(po, tag, tmo, lane); SB; load(c0, c1, c2, hB, 28); ENDL(6); }
    mmy(); ENDM; load(c1, c1, c2, hA, 29 + G); ENDL(6);
    gu64* fy = flags + FL_YX / 8 + b * 8 + G * 4 + wq;
    mmy(); ENDM; STAMP(19); load(c0, c0, c2, hB, 31 + G); ENDL(6); STAMP(25);
    if (G == 0) {
      mms_(c1); give(c1); WAIT_L0; ENDM; load(c1, c0, c2, hA, 33); ENDL(6); STAMP(26);
      mms(); take_rd(c1); take(c1); WAIT_L0; ENDM; load(c0, c0, c2, hB, 35); ENDL(10); STAMP(27);
      mms(); give(c0); WAIT_L0; ENDM; load(c1, c0, c2, hA, 37); WAIT_V(6); set_flag(fy, tag, lane); WAIT_L0; ENDM; STAMP(28);
    } else {
      mms_(c1); take_rd(c1); give(c1); take(c1); WAIT_L0; ENDM; load(c1, c0, c2, hA, 34); ENDL(10); STAMP(26);
      mms(); give(c0); WAIT_L0; ENDM; load(c0, c0, c2, hB, 36); WAIT_V(6); set_flag(fy, tag, lane); WAIT_L0; ENDM; STAMP(27);
      mms(); take_rd(c0); take(c0); WAIT_L0; ENDM; load(c1, c0, c2, hA, 38); ENDL(6); STAMP(28);
    }
    u32x4 py[4], xr[2];
    const unsigned vout = (rt * 128 + wi * 64 + 2 * G * 16 + tok) * 2048 + ct * 256 + kh * 128 + jj * 64 + (((g & 1) << 1) | (g >> 1)) * 16;
    mms(); if (G == 0) { take_rd(c0); take(c0); WAIT_L0; } ENDM; { const u32x4 sx = mk_srd(P.x, S_ * D_ * 2); LD16_ISSUE(xr[0], sx, vout, 0u); LD16_ISSUE(xr[1], sx, vout, 32768u); } load(c0, c0, c2, hB, 39 + G); ENDL(8); STAMP(29);
    gu64* py_ = flags + FL_YX / 8 + (b ^ 1) * 8 + G * 4 + wq;
    mms(); ENDM; if (G == 1) POLL_ISSUE(pv, py_); load(c1, c0, c2, hA, 41 + G); ENDL(6 + G); STAMP(20);
    auto fetch = [&]() { WAIT_V(6); POLL_PIN(pv); if (!__all(pv == tag)) wait_flags<1, 1>(py_, tag, tmo, lane); SB;
      const u32x4 syx = mk_srd(ws + WS_YX, 256 * 32768); const unsigned vy = (b ^ 1) * 32768 + (G * 4 + wq) * 4096 + lane * 16;
#pragma unroll
      for (int i = 0; i < 4; ++i) LD16_ISSUE(py[i], syx, vy, i * 1024u);
      };
    f32x2 gt[8], xv[8];
    auto unpack = [&](auto I) { constexpr int i = I();
      if (i == 0) asm volatile("" : "+v"(xr[0]), "+v"(xr[1]));
      if constexpr (i < 2) { const u32x4 xs = swap16(xr[i][0], xr[i][1], xr[i][2], xr[i][3]);
        _Pragma("unroll") for (int c = 0; c < 4; ++c) { xv[4 * i + c] = (f32x2){bflo(xs[c]), bfhi(xs[c])}; asm volatile("" : "+v"(xv[4 * i + c])); } } };
    auto gate = [&]() { WAIT_V(0); asm volatile("" : "+v"(py[0]), "+v"(py[1]), "+v"(py[2]), "+v"(py[3])); SB; f32x2 y[8], e[8];
#define ST8(stmt) _Pragma("unroll") for (int i = 0; i < 8; ++i) { stmt; } SB
      ST8(const f32x4 a = acc[2 * G + (i >> 2)][(i >> 1) & 1]; const f32x4 c = __builtin_bit_cast(f32x4, py[i >> 1]); y[i] = (i & 1) ? a.zw + c.zw : a.xy + c.xy);
      ST8(e[i] = opq2(y[i] * -LOG2E));
      ST8(e[i] = opq2((f32x2){ex2(e[i][0]), ex2(e[i][1])}));
      ST8(e[i] = opq2(e[i] + 1.0f));
      ST8(e[i] = opq2((f32x2){rcp(e[i][0]), rcp(e[i][1])}));
      ST8(gt[i] = y[i] * e[i]; asm volatile("" : "+v"(gt[i]))); };
    auto pass = [&]() {
#pragma unroll
      for (int i = 0; i < 4; ++i) *(LAS f32x4*)(size_t)(exk + G * 16384 + i * 1024) = as[2 * (G ^ 1) + (i >> 1)][i & 1];
      SB; };
    if (G == 0) {
      mms(); ENDM; POLL_ISSUE(pv, py_); load(c0, c0, c2, hB, 43); ENDL(7); STAMP(30);
      mms(); ENDM; fetch(); loadh(c1, c0, c0, hA, 0, unpack); ENDL(4); STAMP(31);
      mms(); MFMA_PAD; STAMP(21); ENDM;
      pass(); gate();
    } else {
      mms(); ENDM; fetch(); loadh(c0, c0, c0, hB, 0, unpack); ENDL(4); STAMP(30);
      mms(); ENDM; load(c1, c0, c0, hA, 0); gate(); WAIT_L0; ENDM; STAMP(31);
      mms(); MFMA_PAD; STAMP(21);
      pass();
    }
    WAIT_L0; ENDM; STAMP(22);
    const __amdgpu_buffer_rsrc_t rout = mk_rsrc(P.out, S_ * D_ * 2);
#pragma unroll
    for (int i = 0; i < 4; ++i) tk[i] = *(const LAS f32x4*)(size_t)(exk + (G ^ 1) * 16384 + i * 1024);
#pragma unroll
    for (int ml = 0; ml < 2; ++ml) { unsigned ow[4];
#pragma unroll
      for (int n = 0; n < 2; ++n) { const int i = ml * 2 + n; const f32x4 s = as[2 * G + ml][n] + tk[i];
        const f32x2 o0 = s.xy * gt[2 * i] + xv[2 * i], o1 = s.zw * gt[2 * i + 1] + xv[2 * i + 1];
        ow[2 * n] = pk2(o0[0], o0[1]); ow[2 * n + 1] = pk2(o1[0], o1[1]); }
      st16(swap16(ow[0], ow[1], ow[2], ow[3]), rout, vout, ml * 16 * 2048); }
    if (G == 0 && b == 0 && wq == 0) set_flag(flags + FL_EPOCH / 8, tag, lane);
    if (G == 0) asm volatile(".Lend_g0:"); else asm volatile(".Lend_g1:");
    STAMP(23);
  }
};
#define P2RUN(p2, P, tag, eb) p2.run(P, tag, p, h, eb)

__global__ __launch_bounds__(512, 2) void k_fused(const u16* x, const u16* wqkv, const u16* wphi, const u16* wo, const u16* w1, const u16* b1, const u16* w2, char* ws, u16* out) {
  __shared__ __attribute__((aligned(16))) char smem[163840];
  const Ptrs P = {x, wqkv, wphi, wo, w1, b1, w2, ws, out};
  const unsigned lb = (unsigned)(size_t)(LAS char*)smem;
  const int tid = threadIdx.x, lane = tid & 63, w = __builtin_amdgcn_readfirstlane(tid >> 6), wx = w & 3;
  const int b = blockIdx.x, h = (b & 7) * 4 + ((b >> 3) & 3), p = b >> 5, odd = h & 1;
  const int tok = lane & 15, g = lane >> 4, q = (lane >> 2) & 3, pp = lane & 3;
  const unsigned ra0 = lb + L1_A + (wx * 64 + tok) * 128 + ((g ^ (tok >> 1)) << 4), ra1 = ra0 ^ 64;
  gu64* flags = (gu64*)(ws + WS_FLAG); gu64* tmo = flags + FL_TMO / 8;
  STAMP_DECL; STAMP(0);
  const int rot = p < 7 ? 2 * p : 0;
  auto kt = [&](int pos) { int t = pos + rot; t -= t >= 16 ? 14 : 0; return pos < 2 ? pos : t; };
  u64 ep; { gu64* pe = flags + FL_EPOCH / 8; POLL_ISSUE(ep, pe); }
  if (PFCODE && w == 3) {
    u64 c0; unsigned len; asm volatile("s_getpc_b64 %0\n.Lpc_here:\n\ts_mov_b32 %1, .Lend_g0-.Lpc_here\n\ts_max_u32 %1, %1, .Lend_g1-.Lpc_here" : "=s"(c0), "=s"(len) :: "scc");
    const unsigned o = min(((b >> 3) * 16 + (lane & 15)) * 128u, len - 4) & ~3u; TOUCH((const GAS char*)c0 + o, lb + L1_DUMP); }
  if (PFTLB && w == 2) {
    const int kh = b & 1, rt = ((b & 7) >> 1) * 4 + ((b >> 3) & 3); const GAS char* a;
    if (PFTLB == 1) a = lane < 24 ? (const GAS char*)ws + (lane << 21) : lane < 26 ? (const GAS char*)out + ((lane - 24) << 21) : lane < 30 ? (const GAS char*)w2 + ((lane - 26) << 21) : (const GAS char*)wo;
    else a = lane < 16 ? (const GAS char*)ws + WS_ACT + ((p * 16 + lane) * 112 + 7 * h / 2) * 1024 : lane < 24 ? (const GAS char*)ws + WS_ACT + ((rt * 8 + lane - 16) * 112 + kh * 56) * 1024
           : lane < 32 ? (const GAS char*)ws + WS_O + (p * 16 + (lane - 24) * 2) * 32768 : lane < 36 ? (const GAS char*)ws + WS_O + (rt * 8 + (lane - 32) * 2) * 32768
           : lane == 36 ? (const GAS char*)ws + WS_KVP + h * 32768 : lane == 37 ? (const GAS char*)ws + WS_ZP : lane == 38 ? (const GAS char*)ws + WS_YX + (b >> 1) * 65536
           : lane < 43 ? (const GAS char*)out + (rt * 128 + (lane - 39) * 32) * 2048 : lane < 48 ? (const GAS char*)ws + (lane - 43) * 131072 : (const GAS char*)ws + WS_ACT + ((rt * 8 + (lane & 7)) * 112 + kh * 56 + 55) * 1024;
    TOUCH(a, lb + L1_DUMP); }
  const __amdgpu_buffer_rsrc_t rfl = mk_rsrc(ws + WS_FLAG, 1u << 20);
  bf16x8 ones = {0x3F80, 0x3F80, 0x3F80, 0x3F80, 0x3F80, 0x3F80, 0x3F80, 0x3F80}; asm volatile("" : "+v"(ones));
  auto cs_publish = [&](const f32x4& c0, const f32x4& c1, int j0, bool two, u64 tag) {
    const f32x4 a = (tok & 2) ? c1 : c0; const u32x4 v = {__float_as_uint((tok & 1) ? a[2] : a[0]), (unsigned)tag, __float_as_uint((tok & 1) ? a[3] : a[1]), (unsigned)tag};
    if (tok < (two ? 4 : 2)) st16_sc1(v, rfl, ((j0 + 2 * (tok & 2)) * 16 + 4 * g + 2 * (tok & 1)) * 8, GR_CS + (h * 8 + p) * 1664);
    SB; };

  if (w < 4) {
    const u32x4 sa = mk_srd(x, S_ * D_ * 2);
    unsigned va0, va1;
    { const int r = lane >> 3; va0 = (p * 256 + wx * 64 + r) * 2048 + (((lane & 7) ^ (r >> 1)) << 4); va1 = va0 ^ 64; }
    const unsigned sl = lb + L1_A + wx * 8192;
    const unsigned rb0 = opq(lb + L1_BH + (8 * g + 4 * (g & 1) + q) * 224 + 8 * pp), rb1 = opq(lb + L1_BH + (8 * g + 4 * ((g & 1) ^ 1) + q) * 224 + 8 * pp);
    f32x4 acc[4][7], cs0, cs1;
    bf16x8 fa[2][4], fb[2][7];
    auto dmaA = [&](auto ST, auto J, unsigned sk) { constexpr int st = ST(), j = J(); if (j & 1) DMA(sa, va1, sk, j * 16384, sl, st * 32768 + j * 1024); else DMA(sa, va0, sk, j * 16384, sl, st * 32768 + j * 1024); };
    auto rdA = [&](auto ST, auto KS, auto M) { constexpr int st = ST(), ks = KS(), m = M(); fa[ks][m] = lds128((ks ? ra1 : ra0) + st * 32768 + m * 2048); SB; };
    auto rdB = [&](auto ST, auto KS, auto J) { constexpr int st = ST(), ks = KS(), j = J(); constexpr int o = st * 14336 + ks * 7168 + j * 32; fb[ks][j] = cat(ldstr(rb0 + o), ldstr(rb1 + o)); SB; };
    auto load = [&](auto ST, auto DO, unsigned sk) {
      constexpr int o = ST() ^ 1; constexpr bool d = DO();
      rdA(ST, IC<0>{}, IC<0>{}); rdB(ST, IC<0>{}, IC<0>{}); rdB(ST, IC<0>{}, IC<1>{}); if (d) dmaA(IC<o>{}, IC<0>{}, sk);
      rdA(ST, IC<0>{}, IC<1>{}); rdB(ST, IC<0>{}, IC<2>{}); rdB(ST, IC<0>{}, IC<3>{}); if (d) dmaA(IC<o>{}, IC<1>{}, sk);
      rdA(ST, IC<0>{}, IC<2>{}); rdB(ST, IC<0>{}, IC<4>{}); rdB(ST, IC<0>{}, IC<5>{}); if (d) dmaA(IC<o>{}, IC<2>{}, sk);
      rdA(ST, IC<0>{}, IC<3>{}); rdB(ST, IC<0>{}, IC<6>{}); rdB(ST, IC<1>{}, IC<0>{}); if (d) dmaA(IC<o>{}, IC<3>{}, sk);
      rdA(ST, IC<1>{}, IC<0>{}); rdB(ST, IC<1>{}, IC<1>{}); rdB(ST, IC<1>{}, IC<2>{}); if (d) dmaA(IC<o>{}, IC<4>{}, sk);
      rdA(ST, IC<1>{}, IC<1>{}); rdB(ST, IC<1>{}, IC<3>{}); rdB(ST, IC<1>{}, IC<4>{}); if (d) dmaA(IC<o>{}, IC<5>{}, sk);
      rdA(ST, IC<1>{}, IC<2>{}); rdB(ST, IC<1>{}, IC<5>{}); if (d) dmaA(IC<o>{}, IC<6>{}, sk);
      rdA(ST, IC<1>{}, IC<3>{}); rdB(ST, IC<1>{}, IC<6>{}); if (d) dmaA(IC<o>{}, IC<7>{}, sk);
    };
    auto mm_ = [&](auto F) {
#pragma unroll
      for (int m = 0; m < 4; ++m)
#pragma unroll
        for (int j = 0; j < 7; ++j) mk2<F()>(acc[m][j], fb[0][j], fa[0][m], fb[1][j], fa[1][m]);
      SB; };
    auto mm = [&]() { mm_(IC<0>{}); };
#define A8(ST, sk) dmaA(IC<ST>{}, IC<0>{}, sk); dmaA(IC<ST>{}, IC<1>{}, sk); dmaA(IC<ST>{}, IC<2>{}, sk); dmaA(IC<ST>{}, IC<3>{}, sk); dmaA(IC<ST>{}, IC<4>{}, sk); dmaA(IC<ST>{}, IC<5>{}, sk); dmaA(IC<ST>{}, IC<6>{}, sk); dmaA(IC<ST>{}, IC<7>{}, sk)
    if (wx == 0 && TOUCH0) { const int l = min(lane, 51), ts = l / 26, rem = l - ts * 26, rr = rem / 13, li = rem - rr * 13, k = ts * 64 + (b >> 3) * 2 + rr;
      TOUCH(li < 6 ? (const GAS char*)wqkv + k * 6144 + (li >> 1) * 2048 + (b & 7) * 256 + (li & 1) * 128 : (const GAS char*)w1 + k * 7168 + (b & 7) * 896 + (li - 6) * 128, lb + L1_DUMP); }
    if (wx == 1 && TOUCH0) TOUCH((const GAS char*)x + (p * 256 + ((b >> 3) & 3) * 64 + lane) * 2048 + (b & 1) * 128, lb + L1_DUMP);
    A8(0, 0u);
    if (wx == 0) asm volatile("s_mov_b32 m0, %2\n\ts_nop 0\n\tbuffer_load_dword %0, %1, 0 offen lds" :: "v"(h * 224 + lane * 4), "s"(mk_srd(b1, F_ * 2)), "s"(lb + L1_BIAS) : "memory");
    cs0 = zacc(); cs1 = zacc();
    auto colsum = [&](bool now) { colsum7(cs0, cs1, fb[0], fb[1], ones, now ? wx : 4); SB; };
    if (tid == 0) *(LAS u64*)(size_t)(lb + L1_CNT) = 0ull;
    STAMP(1);
    WAIT_V(0); POLL_PIN(ep); const u64 tag = uni64(ep) + 1;
    A8(1, 128u);
    WAIT_L0; BAR; SB; STAMP(4);
    load(IC<0>{}, IC<0>{}, 0u); WAIT_L0; BAR; SB; STAMP(5);
#pragma unroll
    for (int t = 0; t < 4; t += 2) {
      if (t == 0) mm_(IC<1>{}); else mm();
      colsum((p == 7) == (t == 0)); WAIT_V(0); BAR; SB;
      load(IC<1>{}, IC<1>{}, (unsigned)kt(t + 2) * 128u); WAIT_L0; BAR; SB;
      mm(); colsum((p == 7) == (t == 0)); WAIT_V(0); BAR; SB;
      load(IC<0>{}, IC<1>{}, (unsigned)kt(t + 3) * 128u); if (t == 2) cs_publish(cs0, cs1, wx, wx < 3, tag); WAIT_L0; BAR; SB;
    }
    for (int t = 4; t < 14; t += 2) {
      mm(); WAIT_V(0); BAR; SB;
      load(IC<1>{}, IC<1>{}, (unsigned)kt(t + 2) * 128u); WAIT_L0; BAR; SB;
      mm(); WAIT_V(0); BAR; SB;
      load(IC<0>{}, IC<1>{}, (unsigned)kt(t + 3) * 128u); WAIT_L0; BAR; SB;
    }
    STAMP(6);
    mm(); WAIT_V(0); BAR; SB;
    u32x2 bias[7];
#pragma unroll
    for (int j = 0; j < 7; ++j) bias[j] = *(const LAS u32x2*)(size_t)(lb + L1_BIAS + (j < 6 ? j + odd : (odd ? 0 : 6)) * 32 + g * 8);
    SB;
    { const unsigned dump = lb + L1_DUMP; const int cu = b >> 3, a = (b & 7) >> 1;
      const int l2 = min(lane, 55) + 56 * (wx + 4 * cu);
      if (TOUCHW) TOUCH((const GAS char*)w2 + ((b & 1) * 1792 + a * 448) * 2048 + l2 * 128, dump);
      if (TOUCHW) TOUCH((const GAS char*)wo + (b * 64 + wx * 16 + (lane & 15)) * 128, dump); }
    load(IC<1>{}, IC<0>{}, 0u);
    f32x4 bl[7];
#pragma unroll
    for (int j = 0; j < 7; ++j) { bl[j] = (f32x4){opqf(bflo(bias[j][0]) * LOG2E), opqf(bfhi(bias[j][0]) * LOG2E), opqf(bflo(bias[j][1]) * LOG2E), opqf(bfhi(bias[j][1]) * LOG2E)}; asm volatile("" : "+v"(bl[j])); }
    WAIT_L0; BAR; SB;
    mm(); BAR; MFMA_PAD; STAMP(7);
    f32x4 cs[7]; float rl[4], rml[4];
#pragma unroll
    for (int j = 0; j < 7; ++j) cs[j] = *(const LAS f32x4*)(size_t)(lb + L1_CS + j * 64 + g * 16);
#pragma unroll
    for (int m = 0; m < 4; ++m) { const f32x2 st = *(const LAS f32x2*)(size_t)(lb + L1_ST + (wx * 64 + m * 16 + tok) * 8); rl[m] = opqf(st[0] * LOG2E); rml[m] = opqf(-rl[m] * st[1]); }
    SB;
    Phase2<0> p2; p2.init(lb, lane, wx, P);
    const __amdgpu_buffer_rsrc_t ract = mk_rsrc(ws + WS_ACT, 128 * 112 * 1024);
    auto mish8 = [&](int m0, int j0, int m1, int j1) -> u32x4 {
      const f32x2 a[4] = {acc[m0][j0].xy, acc[m0][j0].zw, acc[m1][j1].xy, acc[m1][j1].zw}, c[4] = {cs[j0].xy, cs[j0].zw, cs[j1].xy, cs[j1].zw}, bb[4] = {bl[j0].xy, bl[j0].zw, bl[j1].xy, bl[j1].zw};
      f32x2 t[4], hs[4], e[4], d[4]; unsigned o[4];
#define ST4(stmt) _Pragma("unroll") for (int i = 0; i < 4; ++i) { stmt; } SB
      ST4(t[i] = c[i] * rml[i < 2 ? m0 : m1] + bb[i]);
      ST4(hs[i] = a[i] * rl[i < 2 ? m0 : m1] + t[i]);
      ST4(e[i] = opq2((f32x2){ex2(hs[i][0]), ex2(hs[i][1])}));
      ST4(t[i] = e[i] + 2.0f);
      ST4(d[i] = opq2(e[i] * t[i] + 2.0f));
      ST4(e[i] = opq2((f32x2){rcp(d[i][0]), rcp(d[i][1])}));
      ST4(t[i] = e[i] * (-2.0f / LOG2E) + 1.0f / LOG2E);
      ST4(d[i] = hs[i] * t[i]);
      ST4(o[i] = pk2(d[i][0], d[i][1]));
      return swap16(o[0], o[1], o[2], o[3]); };
    const unsigned vpair = (unsigned)((((g & 1) << 1) | (g >> 1)) * 256 + tok * 16), mb0 = p * 16 + wx * 4;
    auto pair = [&](auto J, auto M) { constexpr int j = J(), m = M();
      st16_sc1(mish8(m, j, m, j + 1), ract, vpair, ((mb0 + m) * 112 + ((7 * h + odd + j) >> 1)) * 1024); SB;
      constexpr int n = 2 * j + m - BLATE; if constexpr (n >= 0 && n < 6) { p2.dma1<n / 2, 4 + 2 * (n & 1)>(); p2.dma1<n / 2, 5 + 2 * (n & 1)>(); } };
    gu64* fact = flags + FL_ACT / 8 + (((2 * p + (wx >> 1)) * 2 + (h >> 4)) * 32 + (h & 15) * 2 + (wx & 1)) * 16;
    STAMP(13);
    if (XP0) __builtin_amdgcn_s_setprio(2);
    pair(IC<0>{}, IC<0>{}); pair(IC<0>{}, IC<1>{}); pair(IC<0>{}, IC<2>{}); pair(IC<0>{}, IC<3>{});
    if (XP0) __builtin_amdgcn_s_setprio(0);
    STAMP(14);
    pair(IC<2>{}, IC<0>{}); pair(IC<2>{}, IC<1>{});
    STAMP(15);
    WAIT_V(2); set_flag(fact, tag, lane); SB; STAMP(8);
    gu64* fa1 = flags + FL_ACT / 8 + (p2.rt * 2 + p2.kh) * 512; u64 pv;
    const unsigned vsing = (unsigned)((g & 1) * 112 * 1024 + (2 * odd + (g >> 1)) * 256 + tok * 16);
    auto single = [&](int m) {
      st16_sc1(mish8(m, 6, m + 1, 6), ract, vsing, ((mb0 + m) * 112 + ((7 * h + (odd ? 0 : 6)) >> 1)) * 1024); SB; };
    auto geta = [&]() { POLL_PIN(pv); STAMP(10); if (!__all(pv == tag)) wait_flags<32, 16>(fa1, tag, tmo, lane);
      SB; STAMP(11); p2.dma4<0, 0>(); p2.dma4<1, 0>(); p2.dma4<2, 0>(); };
    pair(IC<2>{}, IC<2>{}); pair(IC<2>{}, IC<3>{});
    if (EARLYA == 0) {
      pair(IC<4>{}, IC<0>{}); pair(IC<4>{}, IC<1>{}); pair(IC<4>{}, IC<2>{}); pair(IC<4>{}, IC<3>{});
      POLL_ISSUE(pv, fa1 + (lane & 31) * 16); single(0); single(2);
      STAMP(9); WAIT_V(2); geta(); WAIT_V(12);
    } else {
      pair(IC<4>{}, IC<0>{}); POLL_ISSUE(pv, fa1 + (lane & 31) * 16); pair(IC<4>{}, IC<1>{}); pair(IC<4>{}, IC<2>{});
      WAIT_V(4); geta(); pair(IC<4>{}, IC<3>{}); single(0); single(2); STAMP(9); WAIT_V(0);
    }
    set_flag(fact + 131072 / 8, tag, lane); SB; STAMP(12);
    const bf16x8 none[4][2] = {};
    P2RUN(p2, P, tag, none);
  } else {
    const int srow = p * 256 + h * 8 + wx * 2; u32x4 xa[4];
    { const u32x4 sx = mk_srd(x, S_ * D_ * 2); const unsigned vx = srow * 2048 + lane * 16;
      asm volatile("buffer_load_dwordx4 %0, %4, %5, 0 offen\n\tbuffer_load_dwordx4 %1, %4, %5, 0 offen offset:1024\n\tbuffer_load_dwordx4 %2, %4, %5, 0 offen offset:2048\n\tbuffer_load_dwordx4 %3, %4, %5, 0 offen offset:3072"
                   : "=&v"(xa[0]), "=&v"(xa[1]), "=&v"(xa[2]), "=&v"(xa[3]) : "v"(vx), "s"(sx) : "memory"); SB; }
    const u32x4 sq = mk_srd(wqkv, D_ * 3 * D_ * 2), sh = mk_srd(w1, D_ * F_ * 2), sp = mk_srd(wphi, 32 * 32 * 32 * 2);
    unsigned vq, vh[4];
    { const int r = lane >> 2; vq = (wx * 16 + r) * 6144 + h * 64 + (((lane & 3) ^ (((r >> 3) & 1) << 1)) << 4); }
#pragma unroll
    for (int i = 0; i < 4; ++i) { const int o = (wx + 4 * i) * 1024 + lane * 16, rp = o / 224, c = (o - rp * 224) >> 4, k = rp ^ (((rp >> 3) & 1) << 2); int cs = c + 2 * odd; if (cs >= 14) cs -= 14;
      vh[i] = k * 7168 + h * 224 + cs * 16; }
    const unsigned slq = lb + L1_BQ + wx * 1024, slh = lb + L1_BH + wx * 1024;
    const unsigned rq0 = opq(lb + L1_BQ + (8 * g + q) * 64 + 32 * (g & 1) + 8 * pp), rq1 = opq(rq0 ^ 32);
    f32x4 acc[3][4][2], cs0, cs1;
    bf16x8 fa[2][4], fb[2][6];
    auto dmaQ = [&](auto ST, auto U, unsigned sk) { constexpr int st = ST(), u = U(); DMA(sq, vq, sk, u * 2048, slq, st * 12288 + u * 4096); };
    auto dmaH = [&](auto ST, auto I, unsigned sk) { constexpr int st = ST(), i = I(); if (i < 3 || wx < 2) DMA(sh, vh[i], sk, 0, slh, st * 14336 + i * 4096); };
    auto rdA = [&](auto ST, auto KS, auto M) { constexpr int st = ST(), ks = KS(), m = M(); fa[ks][m] = lds128((ks ? ra1 : ra0) + st * 32768 + m * 2048); SB; };
    auto rdB = [&](auto ST, auto KS, auto J) { constexpr int st = ST(), ks = KS(), j = J(); constexpr int o = st * 12288 + (j >> 1) * 4096 + ks * 2048; const unsigned a = (j & 1) ? rq1 : rq0;
      fb[ks][j] = cat(ldstr(a + o), ldstr(a + o + 256)); SB; };
    auto load = [&](auto ST, auto DQ, auto DH, int t) {
      constexpr int o = ST() ^ 1; constexpr bool dq = DQ(), dh = DH(); const unsigned skq = (unsigned)kt(t + 1) * (64 * 6144), skh = (unsigned)kt(t + 2) * (64 * 7168);
      rdA(ST, IC<0>{}, IC<0>{}); rdB(ST, IC<0>{}, IC<0>{}); rdB(ST, IC<0>{}, IC<1>{}); if (dq) dmaQ(IC<o>{}, IC<0>{}, skq);
      rdA(ST, IC<0>{}, IC<1>{}); rdB(ST, IC<0>{}, IC<2>{}); rdB(ST, IC<0>{}, IC<3>{}); if (dq) dmaQ(IC<o>{}, IC<1>{}, skq);
      rdA(ST, IC<0>{}, IC<2>{}); rdB(ST, IC<0>{}, IC<4>{}); rdB(ST, IC<0>{}, IC<5>{}); if (dq) dmaQ(IC<o>{}, IC<2>{}, skq);
      rdA(ST, IC<0>{}, IC<3>{}); rdB(ST, IC<1>{}, IC<0>{}); if (dh) dmaH(ST, IC<0>{}, skh);
      rdA(ST, IC<1>{}, IC<0>{}); rdB(ST, IC<1>{}, IC<1>{}); rdB(ST, IC<1>{}, IC<2>{}); if (dh) dmaH(ST, IC<1>{}, skh);
      rdA(ST, IC<1>{}, IC<1>{}); rdB(ST, IC<1>{}, IC<3>{}); if (dh) dmaH(ST, IC<2>{}, skh);
      rdA(ST, IC<1>{}, IC<2>{}); rdB(ST, IC<1>{}, IC<4>{});
      rdA(ST, IC<1>{}, IC<3>{}); rdB(ST, IC<1>{}, IC<5>{});
      if (dh) { if (wx < 2) { dmaH(ST, IC<3>{}, skh); WAIT_V(7); } else WAIT_V(6); }
    };
    auto mm_ = [&](auto F) {
#pragma unroll
      for (int m = 0; m < 4; ++m)
#pragma unroll
        for (int j = 0; j < 6; ++j) mk2<F()>(acc[j >> 1][m][j & 1], fb[0][j], fa[0][m], fb[1][j], fa[1][m]);
      SB; };
    auto mm = [&]() { mm_(IC<0>{}); };
#define H4(ST, sk) dmaH(IC<ST>{}, IC<0>{}, sk); dmaH(IC<ST>{}, IC<1>{}, sk); dmaH(IC<ST>{}, IC<2>{}, sk); dmaH(IC<ST>{}, IC<3>{}, sk)
#define Q3(ST, sk) dmaQ(IC<ST>{}, IC<0>{}, sk); dmaQ(IC<ST>{}, IC<1>{}, sk); dmaQ(IC<ST>{}, IC<2>{}, sk)
    H4(0, 0u); Q3(0, 0u);
    cs0 = zacc(); cs1 = zacc();
    auto colsum = [&](bool now) { const bf16x8 a[7] = {fb[0][0], fb[0][1], fb[0][2], fb[0][3], fb[0][4], fb[0][5], ones}, c[7] = {fb[1][0], fb[1][1], fb[1][2], fb[1][3], fb[1][4], fb[1][5], ones};
      colsum7(cs0, cs1, a, c, ones, now ? wx : 4); SB; };
    STAMP(1);
    if (wx < 2) WAIT_V(7); else WAIT_V(6);
    asm volatile("" : "+v"(xa[0]), "+v"(xa[1]), "+v"(xa[2]), "+v"(xa[3]), "+v"(ep));
    const u64 tag = uni64(ep) + 1;
    { float v[2][16], sm[2], sq_[2], mean[2];
#pragma unroll
      for (int r = 0; r < 2; ++r) { sm[r] = 0.f;
#pragma unroll
        for (int i = 0; i < 8; ++i) { const unsigned u = xa[2 * r + (i >> 2)][i & 3]; v[r][2 * i] = bflo(u); v[r][2 * i + 1] = bfhi(u); sm[r] += v[r][2 * i] + v[r][2 * i + 1]; } }
#pragma unroll
      for (int r = 0; r < 2; ++r) { mean[r] = wave_sum(sm[r]) * (1.0f / D_); sq_[r] = 0.f;
#pragma unroll
        for (int i = 0; i < 16; ++i) { const float d = v[r][i] - mean[r]; sq_[r] += d * d; } }
      const float r0 = __builtin_amdgcn_rsqf(wave_sum(sq_[0]) * (1.0f / D_) + 1e-5f), r1 = __builtin_amdgcn_rsqf(wave_sum(sq_[1]) * (1.0f / D_) + 1e-5f);
      const u32x4 gr = {__float_as_uint(lane ? mean[1] : mean[0]), (unsigned)tag, __float_as_uint(lane ? r1 : r0), (unsigned)tag};
      if (lane < 2) st16_sc1(gr, rfl, lane * 16, GR_ST + srow * 16);
      SB; }
    STAMP(2);
    WAIT_V(1);
    STAMP(3); BAR; SB; STAMP(4);
    if (wx < 2) { const unsigned vp = h * 2048 + wx * 1024 + lane * 16, slp = lb + L1_WPHI + wx * 1024; DMA(sp, vp, 0u, 0, slp, 0); }
    H4(1, 64u * 7168u); Q3(1, 64u * 6144u);
    BAR; SB; STAMP(5);
    load(IC<0>{}, IC<0>{}, IC<1>{}, 0); WAIT_L0; BAR; SB;
    mm_(IC<1>{}); colsum(p == 7); WAIT_V(3); BAR; SB;
    load(IC<1>{}, IC<1>{}, IC<1>{}, 1); WAIT_L0; BAR; SB;
    mm(); colsum(p == 7); WAIT_V(3); BAR; SB;
    load(IC<0>{}, IC<1>{}, IC<1>{}, 2); WAIT_L0; BAR; SB;
    mm(); colsum(p != 7); WAIT_V(3); BAR; SB;
    load(IC<1>{}, IC<1>{}, IC<1>{}, 3); WAIT_L0; BAR; SB;
    mm(); colsum(p != 7); WAIT_V(3); BAR; MFMA_PAD;
    cs_publish(cs0, cs1, 7 + wx, wx < 2, tag);
    for (int t = 4; t < 14; t += 2) {
      load(IC<0>{}, IC<1>{}, IC<1>{}, t); WAIT_L0; BAR; SB;
      mm(); WAIT_V(3); BAR; SB;
      load(IC<1>{}, IC<1>{}, IC<1>{}, t + 1); WAIT_L0; BAR; SB;
      mm(); WAIT_V(3); BAR; SB;
    }
    STAMP(6);
    const unsigned vst = GR_ST + (p * 256 + wx * 64 + lane) * 16, vcs = GR_CS + h * (8 * 1664) + min(wx * 64 + lane, 103) * 16;
    const u32x4 sfl = mk_srd(ws + WS_FLAG, 1u << 20);
    u32x4 gs, gc[8]; LD16_ISSUE(gs, sfl, vst, 0u);
#pragma unroll
    for (int i = 0; i < 8; ++i) LD16_ISSUE(gc[i], sfl, vcs, i * 1664u);
    load(IC<0>{}, IC<1>{}, IC<0>{}, 14);
    WAIT_V(12); WAIT_L0; BAR; SB;
    mm(); WAIT_V(0); BAR; SB;
    load(IC<1>{}, IC<0>{}, IC<0>{}, 15);
    { auto stale = [&]() { unsigned d = differ(gs[1], gs[3], (unsigned)tag);
#pragma unroll
        for (int i = 0; i < 8; ++i) d |= differ(gc[i][1], gc[i][3], (unsigned)tag);
        return !__all(d == 0u); };
      asm volatile("" : "+v"(gs), "+v"(gc[0]), "+v"(gc[1]), "+v"(gc[2]), "+v"(gc[3]), "+v"(gc[4]), "+v"(gc[5]), "+v"(gc[6]), "+v"(gc[7]));
      if (__builtin_expect(stale(), 0)) {
#pragma nounroll
        for (unsigned spins = 0; spins < (1u << 16); ++spins) {
          asm volatile("buffer_load_dwordx4 %0, %1, %2, 0 offen sc1" : "+v"(gs) : "v"(vst), "s"(sfl) : "memory");
#pragma unroll
          for (int i = 0; i < 8; ++i) asm volatile("buffer_load_dwordx4 %0, %1, %2, %3 offen sc1" : "+v"(gc[i]) : "v"(vcs), "s"(sfl), "s"(i * 1664u) : "memory");
          asm volatile("s_waitcnt vmcnt(0)" : "+v"(gs), "+v"(gc[0]), "+v"(gc[1]), "+v"(gc[2]), "+v"(gc[3]), "+v"(gc[4]), "+v"(gc[5]), "+v"(gc[6]), "+v"(gc[7]) :: "memory");
          if (!stale()) break; } }
      f32x2 c = {__uint_as_float(gc[0][0]), __uint_as_float(gc[0][2])};
#pragma unroll
      for (int i = 1; i < 8; ++i) c = (f32x2){add1(c[0], __uint_as_float(gc[i][0])), add1(c[1], __uint_as_float(gc[i][2]))};
      *(LAS f32x2*)(size_t)(lb + L1_CS + min(wx * 64 + lane, 103) * 8) = c;
      *(LAS f32x2*)(size_t)(lb + L1_ST + (wx * 64 + lane) * 8) = (f32x2){__uint_as_float(gs[2]), __uint_as_float(gs[0])}; SB; }
    WAIT_L0; BAR; SB;
    mm(); MFMA_PAD; STAMP(7);
    if (YPRIO) __builtin_amdgcn_s_setprio(1);
    {
    const int lane = opq(__lane_id()), tok = lane & 15, g = lane >> 4, q = (lane >> 2) & 3, pp = lane & 3;
    bf16x8 wp[2];
#pragma unroll
    for (int ff = 0; ff < 2; ++ff) { const unsigned a = lb + L1_WPHI + (4 * g + q) * 64 + ff * 32 + 8 * pp; wp[ff] = cat(ldstr(a), ldstr(a + 1024)); }
    f32x4 cs[6]; float rs[4], mean[4], rl[4];
#pragma unroll
    for (int j = 0; j < 6; ++j) cs[j] = *(const LAS f32x4*)(size_t)(lb + L1_CS + (7 + j) * 64 + g * 16);
#pragma unroll
    for (int m = 0; m < 4; ++m) { const f32x2 st = *(const LAS f32x2*)(size_t)(lb + L1_ST + (wx * 64 + m * 16 + tok) * 8); rs[m] = st[0]; mean[m] = opqf(-st[1]); rl[m] = st[0] * LOG2E; }
    const f32x4 zero4 = {0.f, 0.f, 0.f, 0.f};
    auto project = [&](int u, f32x4 (&pr)[4][2]) {
#pragma unroll
      for (int m = 0; m < 4; ++m) { const bf16x8 kb = pk8(acc[u][m][0] + cs[2 * u] * mean[m], acc[u][m][1] + cs[2 * u + 1] * mean[m]); pr[m][0] = mfma(wp[0], kb, zero4); pr[m][1] = mfma(wp[1], kb, zero4); }
      SB; };
    auto extrema = [&](const f32x4 (&pr)[4][2], float (&ma)[4], float (&mb)[4]) {
#pragma unroll
      for (int m = 0; m < 4; ++m) { ma[m] = vmax8(pr[m][0], pr[m][1]); mb[m] = vmax8(-pr[m][0], -pr[m][1]); }
      spread4(gather4(ma[0], mb[0], ma[1], mb[1], vmax), ma[0], mb[0], ma[1], mb[1]);
      spread4(gather4(ma[2], mb[2], ma[3], mb[3], vmax), ma[2], mb[2], ma[3], mb[3]);
#pragma unroll
      for (int m = 0; m < 4; ++m) { ma[m] *= -rl[m]; mb[m] *= -rl[m]; }
      SB; };
    auto expo = [&](int m, const f32x4 (&pr)[2], float ma, float mb, f32x4 (&ep)[2], f32x4 (&en)[2]) {
#pragma unroll
      for (int ff = 0; ff < 2; ++ff) { ep[ff] = pr[ff] * rl[m] + ma; en[ff] = pr[ff] * -rl[m] + mb; }
      SB;
#pragma unroll
      for (int ff = 0; ff < 2; ++ff)
#pragma unroll
        for (int r = 0; r < 4; ++r) { ep[ff][r] = ex2(ep[ff][r]); en[ff][r] = ex2(en[ff][r]); }
      SB; };
    { const unsigned swp = (tok & 1) | (((tok >> 2) & 1) << 1) | (((tok >> 1) & 1) << 2) | (tok & 8);
      const unsigned wph = lb + L1_PHI + (wx * 64 + tok) * 128 + ((g ^ swp) << 3), wv = lb + L1_V + (wx * 64 + tok) * 64 + ((g ^ (tok >> 1)) << 3);
      const unsigned wp0 = opq(wph), wp1 = opq(wph ^ 32), wp2 = opq(wph ^ 64), wp3 = opq(wph ^ 96), wv0 = opq(wv), wv1 = opq(wv ^ 32);
      auto put = [](unsigned a, f32x4 v) { *(LAS u32x2*)(size_t)a = (u32x2){pk2(v[0], v[1]), pk2(v[2], v[3])}; };
      f32x4 pr[4][2], ep[4][2], en[4][2]; float ma[4], mb[4];
      WAIT_L0; project(1, pr);
#pragma unroll
      for (int m = 0; m < 4; ++m) { put(wv0 + m * 1024, (acc[2][m][0] + cs[4] * mean[m]) * rs[m]); put(wv1 + m * 1024, (acc[2][m][1] + cs[5] * mean[m]) * rs[m]); }
      SB; extrema(pr, ma, mb);
#pragma unroll
      for (int m = 0; m < 4; ++m) expo(m, pr[m], ma[m], mb[m], ep[m], en[m]);
#pragma unroll
      for (int m = 0; m < 4; ++m) { const f32x4 a = ep[m][0] + ep[m][1], c = en[m][0] + en[m][1]; const f32x2 a2 = a.xy + a.zw, c2 = c.xy + c.zw; ma[m] = a2[0] + opqf(a2[1]); mb[m] = c2[0] + opqf(c2[1]); }
      auto fadd = [](float a, float c) { return a + c; };
      spread4(rcp(gather4(ma[0], mb[0], ma[1], mb[1], fadd)), ma[0], mb[0], ma[1], mb[1]);
      spread4(rcp(gather4(ma[2], mb[2], ma[3], mb[3], fadd)), ma[2], mb[2], ma[3], mb[3]);
      SB;
#pragma unroll
      for (int m = 0; m < 4; ++m) { ep[m][0] *= ma[m]; ep[m][1] *= ma[m]; en[m][0] *= mb[m]; en[m][1] *= mb[m]; SB;
        put(wp0 + m * 2048, ep[m][0]); put(wp1 + m * 2048, ep[m][1]); put(wp2 + m * 2048, en[m][0]); put(wp3 + m * 2048, en[m][1]); SB; } }
    STAMP(8);
    asm volatile("s_waitcnt lgkmcnt(0)" ::: "memory");
    if (lane == 0) { const unsigned one = 1, a = lb + L1_CNT; asm volatile("ds_add_u32 %0, %1" :: "v"(a), "v"(one) : "memory"); }
    bf16x8 eb[4][2];
    f32x4 pq[4][2]; float qa[4], qb[4];
    project(0, pq); extrema(pq, qa, qb);
    auto qpart = [&](int m0) {
#pragma unroll
      for (int m = m0; m < m0 + 2; ++m) { f32x4 ep[2], en[2]; expo(m, pq[m], qa[m], qb[m], ep, en); eb[m][0] = pk8(ep[0], ep[1]); eb[m][1] = pk8(en[0], en[1]); asm volatile("" : "+v"(eb[m][0]), "+v"(eb[m][1])); SB; } };
    lds_wait(lb + L1_CNT, 4u); SB; STAMP(9);
    { const bf16x8 ones = {0x3F80, 0x3F80, 0x3F80, 0x3F80, 0x3F80, 0x3F80, 0x3F80, 0x3F80};
      unsigned ap[2], av[2][2];
#pragma unroll
      for (int t = 0; t < 2; ++t) { const int tk = 8 * g + 4 * t + q, t15 = tk & 15; const unsigned sw = (t15 & 1) | (((t15 >> 2) & 1) << 1) | (((t15 >> 1) & 1) << 2) | (t15 & 8);
        ap[t] = opq(lb + L1_PHI + tk * 128 + (((4 * wx + pp) ^ sw) << 3));
        av[t][0] = opq(lb + L1_V + tk * 64 + ((pp ^ (t15 >> 1)) << 3)); av[t][1] = opq(av[t][0] ^ 32); }
      bf16x8 a[8], b0[8], b1_[8];
#pragma unroll
      for (int ks = 0; ks < 8; ++ks) { a[ks] = cat(ldstr(ap[0] + ks * 4096), ldstr(ap[1] + ks * 4096));
        b0[ks] = cat(ldstr(av[0][0] + ks * 2048), ldstr(av[1][0] + ks * 2048)); b1_[ks] = cat(ldstr(av[0][1] + ks * 2048), ldstr(av[1][1] + ks * 2048)); }
      SB; qpart(0); STAMP(12);
      f32x4 kv0 = zero4, kv1 = zero4, zz = zero4;
#pragma unroll
      for (int ks = 0; ks < 8; ++ks) { kv0 = mfma(a[ks], b0[ks], kv0); kv1 = mfma(a[ks], b1_[ks], kv1); zz = mfma(a[ks], ones, zz); }
      const __amdgpu_buffer_rsrc_t rkv = mk_rsrc(ws + WS_KVP, 3u << 20);
      st16_sc1(__builtin_bit_cast(u32x4, pk8(kv0, kv1)), rkv, lane * 16, (h * 8 + p) * 4096 + wx * 1024);
      if (tok == 0) st16_sc1(__builtin_bit_cast(u32x4, zz), rkv, g * 16, (unsigned)(WS_ZP - WS_KVP) + (h * 8 + p) * 256 + wx * 64);
      SB; STAMP(13); }
    qpart(2);
    SB; if (YPRIO) __builtin_amdgcn_s_setprio(0); STAMP(10); WAIT_V(0);
    set_flag(flags + (FL_KV + (h * 4 + wx) * 1024) / 8 + p * 16, tag, lane); STAMP(11);
    Phase2<1> p2; p2.init(lb, lane, wx, P);
    P2RUN(p2, P, tag, eb);
    }
  }
}
}

extern "C" void kernel_launch(void* const* d_in, const int* in_sizes, int n_in, void* d_out, int out_size, void* d_ws, size_t ws_size, hipStream_t stream) {
  k_fused<<<256, 512, 0, stream>>>((const u16*)d_in[0], (const u16*)d_in[1], (const u16*)d_in[2], (const u16*)d_in[3], (const u16*)d_in[5], (const u16*)d_in[6], (const u16*)d_in[7], (char*)d_ws, (u16*)d_out);
}
```

```cpp
#define PFCODE 7
#include <hip/hip_runtime.h>
#include <hip/hip_bf16.h>
#include <type_traits>

namespace {
#define LAS __attribute__((address_space(3)))
#define GAS __attribute__((address_space(1)))
typedef unsigned short u16;
typedef unsigned u32x4 __attribute__((ext_vector_type(4)));
typedef unsigned u32x2 __attribute__((ext_vector_type(2)));
typedef short bf16x8 __attribute__((ext_vector_type(8)));
typedef short s16x4 __attribute__((ext_vector_type(4)));
typedef float f32x4 __attribute__((ext_vector_type(4)));
typedef float f32x2 __attribute__((ext_vector_type(2)));
typedef __bf16 bf16x2_t __attribute__((ext_vector_type(2)));

constexpr int S_ = 2048, D_ = 1024, F_ = 3584;
constexpr size_t WS_FLAG = 0;
constexpr size_t WS_XN = 1u << 20;
constexpr size_t WS_ACT = WS_XN + (4u << 20);
constexpr size_t WS_O = WS_ACT + (15u << 20);
constexpr size_t WS_KVP = WS_O + (4u << 20);
constexpr size_t WS_ZP = WS_KVP + (2u << 20);
constexpr size_t WS_YX = WS_ZP + (1u << 20);
constexpr size_t WS_STAMP = 64u << 20;
#define STAMP_DECL
#define STAMP(i)
#define STAMP_OUT(wv)
constexpr unsigned FL_EPOCH = 0;
constexpr unsigned FL_O = 1024;
constexpr unsigned FL_YX = 16384;
constexpr unsigned FL_TMO = 61440;
constexpr unsigned GR_ST = 65536;
constexpr unsigned FL_KV = 131072;
constexpr unsigned FL_ACT = 262144;
constexpr unsigned GR_CS = 524288;
#define YPRIO 0
#define XP0 0
#define EARLYA 0
#define BLATE 6
#define TOUCH0 0
#define TOUCHW 1
constexpr float LOG2E = 1.4426950408889634f;

#define SB __builtin_amdgcn_sched_barrier(0)
#define BAR __builtin_amdgcn_s_barrier()
#define WAIT_L0 do { __builtin_amdgcn_s_waitcnt(0xC07F); SB; } while (0)
#define WAIT_V(n) asm volatile("s_waitcnt vmcnt(%0)" :: "i"(n) : "memory")
template <int N> using IC = std::integral_constant<int, N>;

__device__ __forceinline__ u32x4 mk_srd(const void* p, unsigned bytes) {
  const unsigned long long a = (unsigned long long)p; u32x4 r;
  r.x = __builtin_amdgcn_readfirstlane((unsigned)a); r.y = __builtin_amdgcn_readfirstlane((unsigned)(a >> 32)) & 0xffffu; r.z = bytes; r.w = 0x00020000u; return r; }
#define DMA(srd, voff, sk, KOFF, sl, LOFF) do { unsigned _t; asm volatile("s_add_u32 m0, %4, %5\n\ts_add_u32 %0, %3, %6\n\tbuffer_load_dwordx4 %1, %2, %0 offen lds" \
    : "=&s"(_t) : "v"(voff), "s"(srd), "s"(sk), "s"(sl), "i"(LOFF), "i"(KOFF) : "memory", "scc"); SB; } while (0)
#define DMA_SC1(srd, voff, sk, KOFF, sl, LOFF) do { unsigned _t; asm volatile("s_add_u32 m0, %4, %5\n\ts_add_u32 %0, %3, %6\n\tbuffer_load_dwordx4 %1, %2, %0 offen sc1 lds" \
    : "=&s"(_t) : "v"(voff), "s"(srd), "s"(sk), "s"(sl), "i"(LOFF), "i"(KOFF) : "memory", "scc"); SB; } while (0)
#define TOUCH(ptr, sl) do { asm volatile("s_mov_b32 m0, %1\n\ts_nop 0\n\tglobal_load_lds_dword %0, off" :: "v"(ptr), "s"(sl) : "memory"); SB; } while (0)
__device__ __forceinline__ bf16x8 lds128(unsigned a) { return *(const LAS bf16x8*)(size_t)a; }
__device__ __forceinline__ s16x4 ldstr(unsigned a) { return __builtin_amdgcn_ds_read_tr16_b64_v4i16((LAS s16x4*)(size_t)a); }
__device__ __forceinline__ bf16x8 cat(s16x4 lo, s16x4 hi) { return __builtin_shufflevector(lo, hi, 0, 1, 2, 3, 4, 5, 6, 7); }
__device__ __forceinline__ unsigned opq(unsigned x) { asm volatile("" : "+v"(x)); return x; }
__device__ __forceinline__ float opqf(float x) { asm("" : "+v"(x)); return x; }
__device__ __forceinline__ f32x2 opq2(f32x2 x) { asm("" : "+v"(x)); return x; }
__device__ __forceinline__ f32x4 mfma(bf16x8 a, bf16x8 b, f32x4 c) { return __builtin_amdgcn_mfma_f32_16x16x32_bf16(a, b, c, 0, 0, 0); }
__device__ __forceinline__ void mfma_acc(f32x4& d, bf16x8 a, bf16x8 b) { asm volatile("v_mfma_f32_16x16x32_bf16 %0, %1, %2, %0" : "+v"(d) : "v"(a), "v"(b)); }
__device__ __forceinline__ void mfma_k2(f32x4& d, bf16x8 a0, bf16x8 b0, bf16x8 a1, bf16x8 b1) {
  asm volatile("v_mfma_f32_16x16x32_bf16 %0, %1, %2, %0\n\tv_mfma_f32_16x16x32_bf16 %0, %3, %4, %0" : "+v"(d) : "v"(a0), "v"(b0), "v"(a1), "v"(b1)); }
__device__ __forceinline__ void mfma_k2_first(f32x4& d, bf16x8 a0, bf16x8 b0, bf16x8 a1, bf16x8 b1) {
  asm volatile("v_mfma_f32_16x16x32_bf16 %0, %1, %2, 0\n\tv_mfma_f32_16x16x32_bf16 %0, %3, %4, %0" : "=&v"(d) : "v"(a0), "v"(b0), "v"(a1), "v"(b1)); }
template <bool FIRST> __device__ __forceinline__ void mk2(f32x4& d, bf16x8 a0, bf16x8 b0, bf16x8 a1, bf16x8 b1) { if (FIRST) mfma_k2_first(d, a0, b0, a1, b1); else mfma_k2(d, a0, b0, a1, b1); }
__device__ __forceinline__ void mfma_k2x2(f32x4& d, f32x4& e, bf16x8 a0, bf16x8 c0, bf16x8 b0, bf16x8 a1, bf16x8 c1, bf16x8 b1) {
  asm volatile("v_mfma_f32_16x16x32_bf16 %0, %2, %4, %0\n\tv_mfma_f32_16x16x32_bf16 %1, %3, %4, %1\n\tv_mfma_f32_16x16x32_bf16 %0, %5, %7, %0\n\tv_mfma_f32_16x16x32_bf16 %1, %6, %7, %1"
               : "+v"(d), "+v"(e) : "v"(a0), "v"(c0), "v"(b0), "v"(a1), "v"(c1), "v"(b1)); }
__device__ __forceinline__ f32x4 zacc() { f32x4 z = {0.f, 0.f, 0.f, 0.f}; asm volatile("" : "+v"(z)); return z; }
#define CS_M(d, a) "v_mfma_f32_16x16x32_bf16 " d ", " a ", %[o], " d "\n\t"
__device__ __forceinline__ void colsum7(f32x4& c0, f32x4& c1, const bf16x8 (&a)[7], const bf16x8 (&b)[7], bf16x8 ones, unsigned sel) {
  asm volatile("s_cmp_lt_u32 %[s], 2\n\ts_cbranch_scc1 .Lcs01_%=\n\ts_cmp_eq_u32 %[s], 2\n\ts_cbranch_scc1 .Lcs2_%=\n\ts_cmp_eq_u32 %[s], 3\n\ts_cbranch_scc0 .Lcs9_%=\n\t"
               CS_M("%0", "%[a3]") CS_M("%0", "%[b3]") "s_branch .Lcs9_%=\n"
               ".Lcs2_%=:\n\t" CS_M("%0", "%[a2]") CS_M("%0", "%[b2]") CS_M("%1", "%[a6]") CS_M("%1", "%[b6]") "s_branch .Lcs9_%=\n"
               ".Lcs01_%=:\n\ts_cmp_eq_u32 %[s], 0\n\ts_cbranch_scc1 .Lcs0_%=\n\t" CS_M("%0", "%[a1]") CS_M("%0", "%[b1]") CS_M("%1", "%[a5]") CS_M("%1", "%[b5]") "s_branch .Lcs9_%=\n"
               ".Lcs0_%=:\n\t" CS_M("%0", "%[a0]") CS_M("%0", "%[b0]") CS_M("%1", "%[a4]") CS_M("%1", "%[b4]") "\n.Lcs9_%=:"
               : "+v"(c0), "+v"(c1) : [a0] "v"(a[0]), [a1] "v"(a[1]), [a2] "v"(a[2]), [a3] "v"(a[3]), [a4] "v"(a[4]), [a5] "v"(a[5]), [a6] "v"(a[6]),
                 [b0] "v"(b[0]), [b1] "v"(b[1]), [b2] "v"(b[2]), [b3] "v"(b[3]), [b4] "v"(b[4]), [b5] "v"(b[5]), [b6] "v"(b[6]), [o] "v"(ones), [s] "s"(sel) : "scc");
}
#define MFMA_PAD do { asm volatile("s_nop 7\n\ts_nop 1" ::: "memory"); SB; } while (0)
__device__ __forceinline__ unsigned pk2(float lo, float hi) { const f32x2 v = {lo, hi}; return __builtin_bit_cast(unsigned, __builtin_convertvector(v, bf16x2_t)); }
__device__ __forceinline__ bf16x8 pk8(f32x4 a, f32x4 b) { const u32x4 u = {pk2(a[0], a[1]), pk2(a[2], a[3]), pk2(b[0], b[1]), pk2(b[2], b[3])}; return __builtin_bit_cast(bf16x8, u); }
__device__ __forceinline__ float bflo(unsigned u) { return __uint_as_float(u << 16); }
__device__ __forceinline__ float bfhi(unsigned u) { return __uint_as_float(u & 0xffff0000u); }
__device__ __forceinline__ float ex2(float x) { return __builtin_amdgcn_exp2f(x); }
__device__ __forceinline__ float rcp(float x) { return __builtin_amdgcn_rcpf(x); }
__device__ __forceinline__ u32x4 swap16(unsigned a0, unsigned a1, unsigned b0, unsigned b1) {
  const auto p = __builtin_amdgcn_permlane16_swap(a0, b0, false, false), q = __builtin_amdgcn_permlane16_swap(a1, b1, false, false);
  return (u32x4){p[0], q[0], p[1], q[1]}; }
__device__ __forceinline__ float vmax(float a, float b) { return __builtin_elementwise_maximum(a, b); }
__device__ __forceinline__ float vmax8(f32x4 a, f32x4 c) { return vmax(vmax(vmax(vmax(a[0], a[1]), a[2]), vmax(vmax(a[3], c[0]), c[1])), vmax(c[2], c[3])); }
__device__ __forceinline__ void sw32(float& a, float& b) { const auto p = __builtin_amdgcn_permlane32_swap(__float_as_uint(a), __float_as_uint(b), false, false); a = __uint_as_float(p[0]); b = __uint_as_float(p[1]); }
__device__ __forceinline__ void sw16(float& a, float& b) { const auto p = __builtin_amdgcn_permlane16_swap(__float_as_uint(a), __float_as_uint(b), false, false); a = __uint_as_float(p[0]); b = __uint_as_float(p[1]); }
template <class Op> __device__ __forceinline__ float gather4(float a0, float a1, float b0, float b1, Op op) { sw32(a0, a1); sw32(b0, b1); float c = op(a0, a1), d = op(b0, b1); sw16(c, d); return op(c, d); }
__device__ __forceinline__ void spread4(float d, float& a0, float& a1, float& b0, float& b1) { a0 = d; b0 = d; sw16(a0, b0); a1 = a0; b1 = b0; sw32(a0, a1); sw32(b0, b1); }
template <class Op> __device__ __forceinline__ float red4(float v, Op op) {
  const unsigned u = __float_as_uint(v); const auto p = __builtin_amdgcn_permlane32_swap(u, u, false, false);
  const float a = op(__uint_as_float(p[0]), __uint_as_float(p[1])); const unsigned ua = __float_as_uint(a);
  const auto q = __builtin_amdgcn_permlane16_swap(ua, ua, false, false); return op(__uint_as_float(q[0]), __uint_as_float(q[1])); }
__device__ __forceinline__ void st16_sc1(u32x4 v, __amdgpu_buffer_rsrc_t r, unsigned voff, unsigned soff) { __builtin_amdgcn_raw_buffer_store_b128(v, r, voff + soff, 0, 16); }
__device__ __forceinline__ void st16(u32x4 v, __amdgpu_buffer_rsrc_t r, unsigned voff, unsigned soff) { __builtin_amdgcn_raw_buffer_store_b128(v, r, voff + soff, 0, 0); }
__device__ __forceinline__ u32x4 ld16_sc1(__amdgpu_buffer_rsrc_t r, unsigned voff, unsigned soff) { return __builtin_amdgcn_raw_buffer_load_b128(r, voff, soff, 16); }
__device__ __forceinline__ __amdgpu_buffer_rsrc_t mk_rsrc(const void* p, unsigned bytes) { return __builtin_amdgcn_make_buffer_rsrc((void*)p, 0, bytes, 0x00020000); }
#define RLX_AGENT __ATOMIC_RELAXED, __HIP_MEMORY_SCOPE_AGENT
typedef unsigned long long u64;
typedef GAS u64 gu64;
__device__ __forceinline__ void set_flag(gu64* w, u64 tag, int lane) { if (lane == 0) __hip_atomic_store(w, tag, RLX_AGENT); }
template <int N, int STRIDE> __device__ __forceinline__ void wait_flags(gu64* w, u64 tag, gu64* tmo, int lane) {
#pragma nounroll
  for (unsigned spins = 0;; ++spins) {
    u64 v = tag; if (N >= 64 || lane < N) v = __hip_atomic_load(w + lane * STRIDE, RLX_AGENT);
    if (__all(v == tag)) break;
    if (spins > (1u << 16)) { if (lane == 0) __hip_atomic_store(tmo, tag, RLX_AGENT); break; }
    __builtin_amdgcn_s_sleep(2);
  }
  __builtin_amdgcn_fence(__ATOMIC_ACQUIRE, "wavefront");
}
__device__ __forceinline__ u64 uni64(u64 v) { return ((u64)(unsigned)__builtin_amdgcn_readfirstlane((unsigned)(v >> 32)) << 32) | (unsigned)__builtin_amdgcn_readfirstlane((unsigned)v); }
__device__ __forceinline__ void lds_wait(unsigned addr, unsigned target) {
  for (;;) { unsigned v; asm volatile("ds_read_b32 %0, %1\n\ts_waitcnt lgkmcnt(0)" : "=v"(v) : "v"(addr) : "memory");
    if ((unsigned)__builtin_amdgcn_readfirstlane(v) >= target) break; }
}
#define POLL_ISSUE(dst, ptr) do { asm volatile("global_load_dwordx2 %0, %1, off sc1" : "=v"(dst) : "v"(ptr) : "memory"); SB; } while (0)
#define LD16_ISSUE(dst, srd, voff, soff) do { asm volatile("buffer_load_dwordx4 %0, %1, %2, %3 offen sc1" : "=v"(dst) : "v"(voff), "s"(srd), "s"(soff) : "memory"); SB; } while (0)
#define POLL_PIN(dst) asm volatile("" : "+v"(dst))
__device__ __forceinline__ float add1(float a, float b) { float r; asm("v_add_f32 %0, %1, %2" : "=v"(r) : "v"(a), "v"(b)); return r; }
__device__ __forceinline__ float mul1(float a, float b) { float r; asm("v_mul_f32 %0, %1, %2" : "=v"(r) : "v"(a), "v"(b)); return r; }
__device__ __forceinline__ float fma1(float a, float b, float c) { float r; asm("v_fma_f32 %0, %1, %2, %3" : "=v"(r) : "v"(a), "v"(b), "v"(c)); return r; }
__device__ __forceinline__ f32x4 add4(f32x4 a, u32x4 b) { return (f32x4){add1(a[0], __uint_as_float(b[0])), add1(a[1], __uint_as_float(b[1])), add1(a[2], __uint_as_float(b[2])), add1(a[3], __uint_as_float(b[3]))}; }
__device__ __forceinline__ unsigned differ(unsigned a, unsigned b, unsigned t) { unsigned r; asm("v_bitop3_b32 %0, %1, %2, %3 bitop3:0x7e" : "=v"(r) : "v"(a), "v"(b), "s"(t)); return r; }
template <int C> __device__ __forceinline__ float dpp(float v) { return __int_as_float(__builtin_amdgcn_update_dpp(0, __float_as_int(v), C, 0xf, 0xf, true)); }
__device__ __forceinline__ float wave_sum(float v) { v += dpp<0x128>(v); v += dpp<0x124>(v); v += dpp<0x122>(v); v += dpp<0x121>(v); return red4(v, [](float a, float c) { return a + c; }); }
__device__ __forceinline__ void lds_sync(unsigned addr, unsigned target, int lane) {
  asm volatile("s_waitcnt lgkmcnt(0)" ::: "memory");
  if (lane == 0) { const unsigned one = 1; asm volatile("ds_add_u32 %0, %1" :: "v"(addr), "v"(one) : "memory"); }
  for (;;) { unsigned v; asm volatile("ds_read_b32 %0, %1\n\ts_waitcnt lgkmcnt(0)" : "=v"(v) : "v"(addr) : "memory");
    if ((unsigned)__builtin_amdgcn_readfirstlane(v) >= target) break; }
}

constexpr unsigned L1_A = 0;
constexpr unsigned L1_BQ = 65536;
constexpr unsigned L1_BH = 90112;
constexpr unsigned L1_WPHI = 118784;
constexpr unsigned L1_CNT = 120832;
constexpr unsigned L1_DUMP = 121088;
constexpr unsigned L1_BIAS = 121344;
constexpr unsigned L1_CS = 128000;
constexpr unsigned L1_ST = 129024;
constexpr unsigned L1_V = 98304;
constexpr unsigned L1_PHI = 131072;
constexpr unsigned L2_EX = 131072;

__device__ __forceinline__ void touch_code(unsigned dump, int lane, int wx) {
  u64 c0; unsigned back, len;
  asm volatile("s_getpc_b64 %0\n.Lpc_%=:\n\ts_mov_b32 %1, .Lpc_%=-.Lcode_begin\n\ts_mov_b32 %2, .Lend_g0-.Lcode_begin\n\ts_max_u32 %2, %2, .Lend_g1-.Lcode_begin" : "=s"(c0), "=s"(back), "=s"(len) :: "scc");
  const unsigned o = min((((blockIdx.x >> 3) * 4 + wx) * 4 + (lane & 3)) * 128u, len) & ~3u; TOUCH((const GAS char*)(c0 - back) + o, dump); }

struct Ptrs { const u16 *x, *wqkv, *wphi, *wo, *w1, *b1, *w2; char* ws; u16* out; };

template <int G> struct Phase2 {
  unsigned lb; int lane, wq, b, kh, rt, ct;
  u32x4 sact, so, sw2, swo;
  unsigned vaa, vao, vb0, vb1, vs0, vs1, sla, sls;
  struct Off { unsigned a0, a1, b; };
  __device__ __forceinline__ Off off_y(int U) const { const int s0 = kh * 56 + (U < 24 ? 7 * (U & 7) + (U >> 3) : 14 * U - 333), s1 = s0 + (U < 24 ? 4 : 7); return {s0 * 1024u, s1 * 1024u, (wq >> 1 ? s1 : s0) * 65536u}; }
  __device__ __forceinline__ Off off_s(int U) const { return {(U - 28) * 2048u, (U - 28) * 2048u + 1024u, (U - 28) * 131072u}; }
  __device__ __forceinline__ void init(unsigned lb_, int lane_, int wq_, const Ptrs& P) {
    lb = lb_; lane = lane_; wq = wq_; b = blockIdx.x; kh = b & 1; rt = ((b & 7) >> 1) * 4 + ((b >> 3) & 3); ct = b >> 5;
    sact = mk_srd(P.ws + WS_ACT, 128 * 112 * 1024); so = mk_srd(P.ws + WS_O, 128 * 32 * 1024); sw2 = mk_srd(P.w2, F_ * D_ * 2); swo = mk_srd(P.wo, D_ * D_ * 2);
    vaa = lane * 16 + (rt * 8 + wq * 2) * (112 * 1024); vao = lane * 16 + (rt * 8 + wq * 2) * (32 * 1024);
    { const int r = lane >> 4, c = lane & 15; vb0 = ((wq & 1) * 16 + r) * 2048 + ct * 256 + (((c >> 1) ^ r) << 5) + ((c & 1) << 4); vb1 = vb0 ^ 128; }
    { const int r = lane >> 3, c = lane & 7; vs0 = (wq * 16 + r) * 2048 + ct * 256 + kh * 128 + (((c >> 1) ^ ((r >> 1) & 1)) << 5) + ((c & 1) << 4); vs1 = vs0 ^ 64; }
    sla = lb + wq * 4096; sls = lb + wq * 2048;
  }
  template <int LO, bool Y, int I> __device__ __forceinline__ void dma(const Off& o) {
    if (I < 4) { const unsigned sk = (I & 1) ? o.a1 : o.a0; if (Y) DMA_SC1(sact, vaa, sk, (I >> 1) * 112 * 1024, sla, LO + I * 1024); else DMA_SC1(so, vao, sk, (I >> 1) * 32 * 1024, sla, LO + I * 1024); }
    else if (Y) { constexpr int j = I - 4; if (j >> 1) DMA(sw2, vb1, o.b, j * 8192, sla, LO + 16384 + j * 1024); else DMA(sw2, vb0, o.b, j * 8192, sla, LO + 16384 + j * 1024); }
    else if (I < 6) { constexpr int j = I - 4; if (j) DMA(swo, vs1, o.b, j * 16384, sls, LO + 16384 + j * 1024); else DMA(swo, vs0, o.b, j * 16384, sls, LO + 16384 + j * 1024); } }
  template <int U, int I> __device__ __forceinline__ void dma1() { dma<(U & 1) * 65536 + ((U >> 1) & 1) * 32768, true, I>(off_y(U)); }
  template <int U, int I0> __device__ __forceinline__ void dma4() { dma1<U, I0>(); dma1<U, I0 + 1>(); dma1<U, I0 + 2>(); dma1<U, I0 + 3>(); }

  __device__ __forceinline__ void run(const Ptrs& P, u64 tag, int p, int h, const bf16x8 (&eb)[4][2]) {
    char* ws = P.ws;
    const int tok = lane & 15, g = lane >> 4, q = (lane >> 2) & 3, pp = lane & 3, wi = wq >> 1, jj = wq & 1;
    const unsigned ra = opq(lb + G * 65536 + wi * 8192 + lane * 16);
    unsigned rb[4], rs[2];
#pragma unroll
    for (int n = 0; n < 4; ++n) { const int f = 4 * (kh ^ (n >> 1)) + 2 * jj + (n & 1); rb[n] = opq(lb + G * 65536 + 16384 + (8 * g + q) * 256 + ((f ^ (q | ((g & 1) << 2))) << 5) + 8 * pp); }
#pragma unroll
    for (int n = 0; n < 2; ++n) { const int f = 2 * jj + n; rs[n] = opq(lb + G * 65536 + 16384 + (8 * g + q) * 128 + ((f ^ ((q >> 1) | ((g & 1) << 1))) << 5) + 8 * pp); }
    f32x4 acc[4][4], as[4][2];
    bf16x8 fa[2][4], fb[2][4];
    auto rdA = [&](auto HR, auto I) { constexpr int i = I(); fa[i >> 2][i & 3] = lds128(ra + HR() * 32768 + (i & 3) * 2048 + (i >> 2) * 1024); SB; };
    auto rdB = [&](auto HR, auto Y, auto I) { constexpr int i = I(), ks = i >> 2, n = i & 3; constexpr bool y = Y();
      if (y) { constexpr int o = HR() * 32768 + ks * 8192; fb[ks][n] = cat(ldstr(rb[n] + o), ldstr(rb[n] + o + 1024)); SB; }
      else if (n < 2) { constexpr int o = HR() * 32768 + ks * 4096; fb[ks][n] = cat(ldstr(rs[n & 1] + o), ldstr(rs[n & 1] + o + 512)); SB; } };
    auto loadh = [&](auto HR, auto YR, auto MD, auto HD, int U, auto hook) { constexpr int md = MD(), LO = (G ^ 1) * 65536 + HD() * 32768; constexpr bool d = md != 0, y = md == 1;
      const Off o = y ? off_y(U) : off_s(U);
      rdA(HR, IC<0>{}); rdB(HR, YR, IC<0>{}); if (d) dma<LO, y, 0>(o); hook(IC<0>{}); SB;
      rdA(HR, IC<1>{}); rdB(HR, YR, IC<1>{}); if (d) dma<LO, y, 4>(o); hook(IC<1>{}); SB;
      rdA(HR, IC<2>{}); rdB(HR, YR, IC<2>{}); if (d) dma<LO, y, 1>(o); hook(IC<2>{}); SB;
      rdA(HR, IC<3>{}); rdB(HR, YR, IC<3>{}); if (d) dma<LO, y, 5>(o); hook(IC<3>{}); SB;
      rdA(HR, IC<4>{}); rdB(HR, YR, IC<4>{}); if (d) dma<LO, y, 2>(o); hook(IC<4>{}); SB;
      rdA(HR, IC<5>{}); rdB(HR, YR, IC<5>{}); if (d) dma<LO, y, 6>(o); hook(IC<5>{}); SB;
      rdA(HR, IC<6>{}); rdB(HR, YR, IC<6>{}); if (d) dma<LO, y, 3>(o); hook(IC<6>{}); SB;
      rdA(HR, IC<7>{}); rdB(HR, YR, IC<7>{}); hook(IC<7>{}); SB; if (d) dma<LO, y, 7>(o); };
    auto load = [&](auto HR, auto YR, auto MD, auto HD, int U) { loadh(HR, YR, MD, HD, U, [](auto) {}); };
    auto mmy_ = [&](auto F) {
#pragma unroll
      for (int m = 0; m < 4; ++m)
#pragma unroll
        for (int n = 0; n < 4; ++n) mk2<F()>(acc[m][n], fb[0][n], fa[0][m], fb[1][n], fa[1][m]);
      SB; };
    auto mms_ = [&](auto F) {
#pragma unroll
      for (int m = 0; m < 4; ++m)
#pragma unroll
        for (int n = 0; n < 2; ++n) mk2<F()>(as[m][n], fb[0][n], fa[0][m], fb[1][n], fa[1][m]);
      SB; };
    auto mmy = [&]() { mmy_(IC<0>{}); }; auto mms = [&]() { mms_(IC<0>{}); };
#define ENDL(n) do { WAIT_V(n); WAIT_L0; BAR; SB; } while (0)
#define ENDM do { BAR; SB; } while (0)
    IC<0> c0; IC<1> c1; IC<2> c2; IC<G> hA; IC<G ^ 1> hB;
    gu64* flags = (gu64*)(ws + WS_FLAG); gu64* tmo = flags + FL_TMO / 8;
    const __amdgpu_buffer_rsrc_t ryx = mk_rsrc(ws + WS_YX, 256 * 32768);
    const unsigned exk = lb + L2_EX + wq * 4096 + lane * 16, exs = lb + 24576 + (wq >> 1) * 32768 + (wq & 1) * 4096 + lane * 16;
    f32x4 tk[4];
    auto give = [&](auto K) { constexpr int k = K();
#pragma unroll
      for (int i = 0; i < 4; ++i) *(LAS f32x4*)(size_t)((k ? exs + G * 65536 : exk + G * 16384) + i * 1024) = acc[2 * (G ^ 1) + (i >> 1)][2 * k + (i & 1)];
      SB; };
    auto take_rd = [&](auto K) { constexpr int k = K();
#pragma unroll
      for (int i = 0; i < 4; ++i) tk[i] = *(const LAS f32x4*)(size_t)((k ? exs + (G ^ 1) * 65536 : exk + (G ^ 1) * 16384) + i * 1024);
      SB; };
    auto take = [&](auto K) { constexpr int k = K();
#pragma unroll
      for (int i = 0; i < 4; ++i) { f32x4& a = acc[2 * G + (i >> 1)][2 * k + (i & 1)]; a += tk[i]; if (k) st16_sc1(__builtin_bit_cast(u32x4, a), ryx, lane * 16, b * 32768 + (G * 4 + wq) * 4096 + i * 1024); else asm volatile("" : "+v"(a)); }
      SB; };
    u64 pv;
    if (G == 0) {
      WAIT_V(EARLYA ? 1 : 9); BAR; SB; STAMP(16);
      load(c0, c1, c1, c1, 3); ENDL(13);
      mmy_(c1); WAIT_V(8); ENDM; STAMP(17);
      load(c1, c1, c1, hA, 5); ENDL(8);
      mmy(); ENDM; load(c0, c1, c1, hB, 7); ENDL(8);
      for (int T = 4; T < 20; T += 4) {
        mmy(); ENDM; load(c1, c1, c1, hA, T + 5); ENDL(8);
        mmy(); ENDM; load(c0, c1, c1, hB, T + 7); ENDL(8);
      }
    } else {
      BAR; SB; STAMP(16); BAR; SB;
      load(c0, c1, c1, c0, 4); ENDL(8);
      mmy_(c1); ENDM; STAMP(17);
      gu64* fa2 = flags + (FL_ACT + 131072) / 8 + (rt * 2 + kh) * 512; gu64* fkv = flags + (FL_KV + (h * 4 + wq) * 1024) / 8;
      POLL_ISSUE(pv, lane < 32 ? fa2 + lane * 16 : fkv + (lane & 7) * 16);
      load(c1, c1, c1, hA, 6); ENDL(9);
      mmy(); ENDM;
      WAIT_V(8); POLL_PIN(pv); if (!__all(pv == tag)) { wait_flags<32, 16>(fa2, tag, tmo, lane); wait_flags<8, 16>(fkv, tag, tmo, lane); } SB;
      const u32x4 skv = mk_srd(ws + WS_KVP, 3u << 20);
      u32x4 t[8], tz; LD16_ISSUE(tz, skv, h * 2048 + (lane & 7) * 256 + wq * 64 + g * 16, (unsigned)(WS_ZP - WS_KVP));
      { const unsigned vkv = h * 32768 + wq * 1024 + lane * 16;
#pragma unroll
        for (int i = 0; i < 8; ++i) LD16_ISSUE(t[i], skv, vkv, i * 4096u); }
      load(c0, c1, c1, hB, 8); ENDL(17);
      mmy(); ENDM;
      WAIT_V(8); asm volatile("" : "+v"(t[0]), "+v"(t[1]), "+v"(t[2]), "+v"(t[3]), "+v"(t[4]), "+v"(t[5]), "+v"(t[6]), "+v"(t[7]), "+v"(tz)); SB;
      float sm[8];
      auto addp = [&](int i, int c) { sm[2 * c] = add1(sm[2 * c], bflo(t[i][c])); sm[2 * c + 1] = add1(sm[2 * c + 1], bfhi(t[i][c])); asm volatile("" : "+v"(sm[2 * c]), "+v"(sm[2 * c + 1])); };
      loadh(c1, c1, c1, hA, 10, [&](auto I) { constexpr int i = I();
        if (i == 0) { _Pragma("unroll") for (int c = 0; c < 4; ++c) { sm[2 * c] = bflo(t[0][c]); sm[2 * c + 1] = bfhi(t[0][c]); asm volatile("" : "+v"(sm[2 * c]), "+v"(sm[2 * c + 1])); } }
        else if (i < 7) { addp((i + 1) >> 1, 2 * (~i & 1)); addp((i + 1) >> 1, 2 * (~i & 1) + 1); }
        else { _Pragma("unroll") for (int r = 0; r < 4; ++r) { float z = __uint_as_float(tz[r]); z += dpp<0xB1>(z); z = opqf(z); z += dpp<0x4E>(z); z = opqf(z); z += dpp<0x141>(z); tz[r] = __float_as_uint(opqf(z)); } asm volatile("" : "+v"(tz)); } });
      ENDL(8);
      mmy(); ENDM;
      const unsigned kva = opq(lb + L2_EX + lane * 16);
      loadh(c0, c1, c1, hB, 12, [&](auto I) { constexpr int i = I(); addp(4 + (i >> 1), 2 * (i & 1)); addp(4 + (i >> 1), 2 * (i & 1) + 1); });
      { const unsigned a = kva + (wq >> 1) * 1024 + (wq & 1) * 8;
        *(LAS u32x2*)(size_t)a = (u32x2){pk2(sm[0], sm[1]), pk2(sm[2], sm[3])}; *(LAS u32x2*)(size_t)(a + 2048) = (u32x2){pk2(sm[4], sm[5]), pk2(sm[6], sm[7])};
        u32x2 au = {pk2(__uint_as_float(tz[0]), __uint_as_float(tz[1])), pk2(__uint_as_float(tz[2]), __uint_as_float(tz[3]))};
        if ((lane & 3) == 1) au = (u32x2){0x3F803F80u, 0x3F803F80u}; else if ((lane & 3) != 0) au = (u32x2){0u, 0u};
        *(LAS u32x2*)(size_t)(a + 4096) = au; SB; }
      ENDL(8);
      mmy(); ENDM;
      bf16x8 ka[6]; f32x4 r6[6]; float ca, cc, o8[8];
      const __amdgpu_buffer_rsrc_t ro = mk_rsrc(ws + WS_O, 128 * 32 * 1024);
      const unsigned vo = (unsigned)((((g & 1) << 1) | (g >> 1)) * 256 + tok * 16) + ((p * 16 + wq * 4) * 32 + h) * 1024;
      auto ostep = [&](int m, int i) { const f32x4 z4 = {0.f, 0.f, 0.f, 0.f};
        if (i == 1) { __builtin_amdgcn_s_setprio(1);
#pragma unroll
          for (int k = 5; k >= 0; --k) { r6[k] = mfma(ka[k], eb[m][k & 1], z4); asm volatile("" : "+v"(r6[k])); }
          __builtin_amdgcn_s_setprio(0); }
        if (i == 2) { ca = rcp(r6[4][1]); cc = rcp(r6[5][1]); asm volatile("" : "+v"(ca), "+v"(cc)); }
        if (i == 3) { const float inv = rcp(opqf(r6[4][0] * ca + 1e-6f) + r6[5][0] * cc); ca = opqf(ca * inv); cc = opqf(cc * inv); asm volatile("" : "+v"(ca), "+v"(cc)); }
        if (i == 4 || i == 5) { _Pragma("unroll") for (int r = 0; r < 4; ++r) { const int d = i - 4; o8[4 * d + r] = opqf(r6[2 * d][r] * ca) + r6[2 * d + 1][r] * cc; asm volatile("" : "+v"(o8[4 * d + r])); } }
        if (i == 6) st16_sc1(swap16(pk2(o8[0], o8[1]), pk2(o8[2], o8[3]), pk2(o8[4], o8[5]), pk2(o8[6], o8[7])), ro, vo, m * 32768); };
#pragma unroll
      for (int i = 0; i < 6; ++i) ka[i] = lds128(kva + i * 1024);
      SB;
      loadh(c1, c1, c1, hA, 14, [&](auto I) { ostep(0, I()); }); ENDL(9);
      mmy(); ENDM; loadh(c0, c1, c1, hB, 16, [&](auto I) { ostep(1, I()); }); ENDL(9);
      mmy(); ENDM; loadh(c1, c1, c1, hA, 18, [&](auto I) { ostep(2, I()); }); ENDL(9);
      mmy(); ENDM; loadh(c0, c1, c1, hB, 20, [&](auto I) { ostep(3, I()); }); ENDL(9);
      mmy(); ENDM; load(c1, c1, c1, hA, 22); WAIT_V(8);
      set_flag(flags + FL_O / 8 + (2 * p + (wq >> 1)) * 64 + h * 2 + (wq & 1), tag, lane); WAIT_L0; ENDM;
      mmy(); ENDM; load(c0, c1, c1, hB, 24); ENDL(8);
    }
    STAMP(18);
    gu64* po = flags + FL_O / 8 + rt * 64;
    mmy(); ENDM; if (G == 1) POLL_ISSUE(pv, po + lane); else if (PFCODE & 4) touch_code(lb + L2_EX + 8192, lane, wq); load(c1, c1, c1, hA, 25 + G); ENDL((G == 1 || (PFCODE & 4)) ? 9 : 8);
    mmy(); ENDM;
    if (G == 0) { load(c0, c1, c1, hB, 27); ENDL(8); }
    else { WAIT_V(8); POLL_PIN(pv); if (!__all(pv == tag)) wait_flags<64, 1>(po, tag, tmo, lane); SB; load(c0, c1, c2, hB, 28); ENDL(6); }
    mmy(); ENDM; load(c1, c1, c2, hA, 29 + G); ENDL(6);
    gu64* fy = flags + FL_YX / 8 + b * 8 + G * 4 + wq;
    mmy(); ENDM; STAMP(19); load(c0, c0, c2, hB, 31 + G); ENDL(6); STAMP(25);
    if (G == 0) {
      mms_(c1); give(c1); WAIT_L0; ENDM; load(c1, c0, c2, hA, 33); ENDL(6); STAMP(26);
      mms(); take_rd(c1); take(c1); WAIT_L0; ENDM; load(c0, c0, c2, hB, 35); ENDL(10); STAMP(27);
      mms(); give(c0); WAIT_L0; ENDM; load(c1, c0, c2, hA, 37); WAIT_V(6); set_flag(fy, tag, lane); WAIT_L0; ENDM; STAMP(28);
    } else {
      mms_(c1); take_rd(c1); give(c1); take(c1); WAIT_L0; ENDM; load(c1, c0, c2, hA, 34); ENDL(10); STAMP(26);
      mms(); give(c0); WAIT_L0; ENDM; load(c0, c0, c2, hB, 36); WAIT_V(6); set_flag(fy, tag, lane); WAIT_L0; ENDM; STAMP(27);
      mms(); take_rd(c0); take(c0); WAIT_L0; ENDM; load(c1, c0, c2, hA, 38); ENDL(6); STAMP(28);
    }
    u32x4 py[4], xr[2];
    const unsigned vout = (rt * 128 + wi * 64 + 2 * G * 16 + tok) * 2048 + ct * 256 + kh * 128 + jj * 64 + (((g & 1) << 1) | (g >> 1)) * 16;
    mms(); if (G == 0) { take_rd(c0); take(c0); WAIT_L0; } ENDM; { const u32x4 sx = mk_srd(P.x, S_ * D_ * 2); LD16_ISSUE(xr[0], sx, vout, 0u); LD16_ISSUE(xr[1], sx, vout, 32768u); } load(c0, c0, c2, hB, 39 + G); ENDL(8); STAMP(29);
    gu64* py_ = flags + FL_YX / 8 + (b ^ 1) * 8 + G * 4 + wq;
    mms(); ENDM; if (G == 1) POLL_ISSUE(pv, py_); load(c1, c0, c2, hA, 41 + G); ENDL(6 + G); STAMP(20);
    auto fetch = [&]() { WAIT_V(6); POLL_PIN(pv); if (!__all(pv == tag)) wait_flags<1, 1>(py_, tag, tmo, lane); SB;
      const u32x4 syx = mk_srd(ws + WS_YX, 256 * 32768); const unsigned vy = (b ^ 1) * 32768 + (G * 4 + wq) * 4096 + lane * 16;
#pragma unroll
      for (int i = 0; i < 4; ++i) LD16_ISSUE(py[i], syx, vy, i * 1024u);
      };
    f32x2 gt[8], xv[8];
    auto unpack = [&](auto I) { constexpr int i = I();
      if (i == 0) asm volatile("" : "+v"(xr[0]), "+v"(xr[1]));
      if constexpr (i < 2) { const u32x4 xs = swap16(xr[i][0], xr[i][1], xr[i][2], xr[i][3]);
        _Pragma("unroll") for (int c = 0; c < 4; ++c) { xv[4 * i + c] = (f32x2){bflo(xs[c]), bfhi(xs[c])}; asm volatile("" : "+v"(xv[4 * i + c])); } } };
    auto gate = [&]() { WAIT_V(0); asm volatile("" : "+v"(py[0]), "+v"(py[1]), "+v"(py[2]), "+v"(py[3])); SB; f32x2 y[8], e[8];
#define ST8(stmt) _Pragma("unroll") for (int i = 0; i < 8; ++i) { stmt; } SB
      ST8(const f32x4 a = acc[2 * G + (i >> 2)][(i >> 1) & 1]; const f32x4 c = __builtin_bit_cast(f32x4, py[i >> 1]); y[i] = (i & 1) ? a.zw + c.zw : a.xy + c.xy);
      ST8(e[i] = opq2(y[i] * -LOG2E));
      ST8(e[i] = opq2((f32x2){ex2(e[i][0]), ex2(e[i][1])}));
      ST8(e[i] = opq2(e[i] + 1.0f));
      ST8(e[i] = opq2((f32x2){rcp(e[i][0]), rcp(e[i][1])}));
      ST8(gt[i] = y[i] * e[i]; asm volatile("" : "+v"(gt[i]))); };
    auto pass = [&]() {
#pragma unroll
      for (int i = 0; i < 4; ++i) *(LAS f32x4*)(size_t)(exk + G * 16384 + i * 1024) = as[2 * (G ^ 1) + (i >> 1)][i & 1];
      SB; };
    if (G == 0) {
      mms(); ENDM; POLL_ISSUE(pv, py_); load(c0, c0, c2, hB, 43); ENDL(7); STAMP(30);
      mms(); ENDM; fetch(); loadh(c1, c0, c0, hA, 0, unpack); ENDL(4); STAMP(31);
      mms(); MFMA_PAD; STAMP(21); ENDM;
      pass(); gate();
    } else {
      mms(); ENDM; fetch(); loadh(c0, c0, c0, hB, 0, unpack); ENDL(4); STAMP(30);
      mms(); ENDM; load(c1, c0, c0, hA, 0); gate(); WAIT_L0; ENDM; STAMP(31);
      mms(); MFMA_PAD; STAMP(21);
      pass();
    }
    WAIT_L0; ENDM; STAMP(22);
    const __amdgpu_buffer_rsrc_t rout = mk_rsrc(P.out, S_ * D_ * 2);
#pragma unroll
    for (int i = 0; i < 4; ++i) tk[i] = *(const LAS f32x4*)(size_t)(exk + (G ^ 1) * 16384 + i * 1024);
#pragma unroll
    for (int ml = 0; ml < 2; ++ml) { unsigned ow[4];
#pragma unroll
      for (int n = 0; n < 2; ++n) { const int i = ml * 2 + n; const f32x4 s = as[2 * G + ml][n] + tk[i];
        const f32x2 o0 = s.xy * gt[2 * i] + xv[2 * i], o1 = s.zw * gt[2 * i + 1] + xv[2 * i + 1];
        ow[2 * n] = pk2(o0[0], o0[1]); ow[2 * n + 1] = pk2(o1[0], o1[1]); }
      st16(swap16(ow[0], ow[1], ow[2], ow[3]), rout, vout, ml * 16 * 2048); }
    if (G == 0 && b == 0 && wq == 0) set_flag(flags + FL_EPOCH / 8, tag, lane);
    if (G == 0) asm volatile(".Lend_g0:"); else asm volatile(".Lend_g1:");
    STAMP(23);
  }
};
#define P2RUN(p2, P, tag, eb) p2.run(P, tag, p, h, eb)

__global__ __launch_bounds__(512, 2) void k_fused(const u16* x, const u16* wqkv, const u16* wphi, const u16* wo, const u16* w1, const u16* b1, const u16* w2, char* ws, u16* out) {
  __shared__ __attribute__((aligned(16))) char smem[163840];
  const Ptrs P = {x, wqkv, wphi, wo, w1, b1, w2, ws, out};
  const unsigned lb = (unsigned)(size_t)(LAS char*)smem;
  const int tid = threadIdx.x, lane = tid & 63, w = __builtin_amdgcn_readfirstlane(tid >> 6), wx = w & 3;
  const int b = blockIdx.x, h = (b & 7) * 4 + ((b >> 3) & 3), p = b >> 5, odd = h & 1;
  const int tok = lane & 15, g = lane >> 4, q = (lane >> 2) & 3, pp = lane & 3;
  const unsigned ra0 = lb + L1_A + (wx * 64 + tok) * 128 + ((g ^ (tok >> 1)) << 4), ra1 = ra0 ^ 64;
  gu64* flags = (gu64*)(ws + WS_FLAG); gu64* tmo = flags + FL_TMO / 8;
  asm volatile(".Lcode_begin:");
  STAMP_DECL; STAMP(0);
  const int rot = p < 7 ? 2 * p : 0;
  auto kt = [&](int pos) { int t = pos + rot; t -= t >= 16 ? 14 : 0; return pos < 2 ? pos : t; };
  u64 ep; { gu64* pe = flags + FL_EPOCH / 8; POLL_ISSUE(ep, pe); }
  if ((PFCODE & 1) && w < 4) touch_code(lb + L1_DUMP, lane, wx);
  const __amdgpu_buffer_rsrc_t rfl = mk_rsrc(ws + WS_FLAG, 1u << 20);
  bf16x8 ones = {0x3F80, 0x3F80, 0x3F80, 0x3F80, 0x3F80, 0x3F80, 0x3F80, 0x3F80}; asm volatile("" : "+v"(ones));
  auto cs_publish = [&](const f32x4& c0, const f32x4& c1, int j0, bool two, u64 tag) {
    const f32x4 a = (tok & 2) ? c1 : c0; const u32x4 v = {__float_as_uint((tok & 1) ? a[2] : a[0]), (unsigned)tag, __float_as_uint((tok & 1) ? a[3] : a[1]), (unsigned)tag};
    if (tok < (two ? 4 : 2)) st16_sc1(v, rfl, ((j0 + 2 * (tok & 2)) * 16 + 4 * g + 2 * (tok & 1)) * 8, GR_CS + (h * 8 + p) * 1664);
    SB; };

  if (w < 4) {
    const u32x4 sa = mk_srd(x, S_ * D_ * 2);
    unsigned va0, va1;
    { const int r = lane >> 3; va0 = (p * 256 + wx * 64 + r) * 2048 + (((lane & 7) ^ (r >> 1)) << 4); va1 = va0 ^ 64; }
    const unsigned sl = lb + L1_A + wx * 8192;
    const unsigned rb0 = opq(lb + L1_BH + (8 * g + 4 * (g & 1) + q) * 224 + 8 * pp), rb1 = opq(lb + L1_BH + (8 * g + 4 * ((g & 1) ^ 1) + q) * 224 + 8 * pp);
    f32x4 acc[4][7], cs0, cs1;
    bf16x8 fa[2][4], fb[2][7];
    auto dmaA = [&](auto ST, auto J, unsigned sk) { constexpr int st = ST(), j = J(); if (j & 1) DMA(sa, va1, sk, j * 16384, sl, st * 32768 + j * 1024); else DMA(sa, va0, sk, j * 16384, sl, st * 32768 + j * 1024); };
    auto rdA = [&](auto ST, auto KS, auto M) { constexpr int st = ST(), ks = KS(), m = M(); fa[ks][m] = lds128((ks ? ra1 : ra0) + st * 32768 + m * 2048); SB; };
    auto rdB = [&](auto ST, auto KS, auto J) { constexpr int st = ST(), ks = KS(), j = J(); constexpr int o = st * 14336 + ks * 7168 + j * 32; fb[ks][j] = cat(ldstr(rb0 + o), ldstr(rb1 + o)); SB; };
    auto load = [&](auto ST, auto DO, unsigned sk) {
      constexpr int o = ST() ^ 1; constexpr bool d = DO();
      rdA(ST, IC<0>{}, IC<0>{}); rdB(ST, IC<0>{}, IC<0>{}); rdB(ST, IC<0>{}, IC<1>{}); if (d) dmaA(IC<o>{}, IC<0>{}, sk);
      rdA(ST, IC<0>{}, IC<1>{}); rdB(ST, IC<0>{}, IC<2>{}); rdB(ST, IC<0>{}, IC<3>{}); if (d) dmaA(IC<o>{}, IC<1>{}, sk);
      rdA(ST, IC<0>{}, IC<2>{}); rdB(ST, IC<0>{}, IC<4>{}); rdB(ST, IC<0>{}, IC<5>{}); if (d) dmaA(IC<o>{}, IC<2>{}, sk);
      rdA(ST, IC<0>{}, IC<3>{}); rdB(ST, IC<0>{}, IC<6>{}); rdB(ST, IC<1>{}, IC<0>{}); if (d) dmaA(IC<o>{}, IC<3>{}, sk);
      rdA(ST, IC<1>{}, IC<0>{}); rdB(ST, IC<1>{}, IC<1>{}); rdB(ST, IC<1>{}, IC<2>{}); if (d) dmaA(IC<o>{}, IC<4>{}, sk);
      rdA(ST, IC<1>{}, IC<1>{}); rdB(ST, IC<1>{}, IC<3>{}); rdB(ST, IC<1>{}, IC<4>{}); if (d) dmaA(IC<o>{}, IC<5>{}, sk);
      rdA(ST, IC<1>{}, IC<2>{}); rdB(ST, IC<1>{}, IC<5>{}); if (d) dmaA(IC<o>{}, IC<6>{}, sk);
      rdA(ST, IC<1>{}, IC<3>{}); rdB(ST, IC<1>{}, IC<6>{}); if (d) dmaA(IC<o>{}, IC<7>{}, sk);
    };
    auto mm_ = [&](auto F) {
#pragma unroll
      for (int m = 0; m < 4; ++m)
#pragma unroll
        for (int j = 0; j < 7; ++j) mk2<F()>(acc[m][j], fb[0][j], fa[0][m], fb[1][j], fa[1][m]);
      SB; };
    auto mm = [&]() { mm_(IC<0>{}); };
#define A8(ST, sk) dmaA(IC<ST>{}, IC<0>{}, sk); dmaA(IC<ST>{}, IC<1>{}, sk); dmaA(IC<ST>{}, IC<2>{}, sk); dmaA(IC<ST>{}, IC<3>{}, sk); dmaA(IC<ST>{}, IC<4>{}, sk); dmaA(IC<ST>{}, IC<5>{}, sk); dmaA(IC<ST>{}, IC<6>{}, sk); dmaA(IC<ST>{}, IC<7>{}, sk)
    if (wx == 0 && TOUCH0) { const int l = min(lane, 51), ts = l / 26, rem = l - ts * 26, rr = rem / 13, li = rem - rr * 13, k = ts * 64 + (b >> 3) * 2 + rr;
      TOUCH(li < 6 ? (const GAS char*)wqkv + k * 6144 + (li >> 1) * 2048 + (b & 7) * 256 + (li & 1) * 128 : (const GAS char*)w1 + k * 7168 + (b & 7) * 896 + (li - 6) * 128, lb + L1_DUMP); }
    if (wx == 1 && TOUCH0) TOUCH((const GAS char*)x + (p * 256 + ((b >> 3) & 3) * 64 + lane) * 2048 + (b & 1) * 128, lb + L1_DUMP);
    A8(0, 0u);
    if (wx == 0) asm volatile("s_mov_b32 m0, %2\n\ts_nop 0\n\tbuffer_load_dword %0, %1, 0 offen lds" :: "v"(h * 224 + lane * 4), "s"(mk_srd(b1, F_ * 2)), "s"(lb + L1_BIAS) : "memory");
    cs0 = zacc(); cs1 = zacc();
    auto colsum = [&](bool now) { colsum7(cs0, cs1, fb[0], fb[1], ones, now ? wx : 4); SB; };
    if (tid == 0) *(LAS u64*)(size_t)(lb + L1_CNT) = 0ull;
    STAMP(1);
    WAIT_V(0); POLL_PIN(ep); const u64 tag = uni64(ep) + 1;
    A8(1, 128u);
    WAIT_L0; BAR; SB; STAMP(4);
    load(IC<0>{}, IC<0>{}, 0u); WAIT_L0; BAR; SB; STAMP(5);
#pragma unroll
    for (int t = 0; t < 4; t += 2) {
      if (t == 0) mm_(IC<1>{}); else mm();
      colsum((p == 7) == (t == 0)); WAIT_V(0); BAR; SB;
      load(IC<1>{}, IC<1>{}, (unsigned)kt(t + 2) * 128u); WAIT_L0; BAR; SB;
      mm(); colsum((p == 7) == (t == 0)); WAIT_V(0); BAR; SB;
      load(IC<0>{}, IC<1>{}, (unsigned)kt(t + 3) * 128u); if (t == 2) cs_publish(cs0, cs1, wx, wx < 3, tag); WAIT_L0; BAR; SB;
    }
    for (int t = 4; t < 14; t += 2) {
      mm(); WAIT_V(0); BAR; SB;
      load(IC<1>{}, IC<1>{}, (unsigned)kt(t + 2) * 128u); WAIT_L0; BAR; SB;
      mm(); WAIT_V(0); BAR; SB;
      load(IC<0>{}, IC<1>{}, (unsigned)kt(t + 3) * 128u); WAIT_L0; BAR; SB;
    }
    STAMP(6);
    mm(); WAIT_V(0); BAR; SB;
    u32x2 bias[7];
#pragma unroll
    for (int j = 0; j < 7; ++j) bias[j] = *(const LAS u32x2*)(size_t)(lb + L1_BIAS + (j < 6 ? j + odd : (odd ? 0 : 6)) * 32 + g * 8);
    SB;
    { const unsigned dump = lb + L1_DUMP; const int cu = b >> 3, a = (b & 7) >> 1;
      const int l2 = min(lane, 55) + 56 * (wx + 4 * cu);
      if (PFCODE & 2) touch_code(dump, lane, wx);
      if (TOUCHW) TOUCH((const GAS char*)w2 + ((b & 1) * 1792 + a * 448) * 2048 + l2 * 128, dump);
      if (TOUCHW) TOUCH((const GAS char*)wo + (b * 64 + wx * 16 + (lane & 15)) * 128, dump); }
    load(IC<1>{}, IC<0>{}, 0u);
    f32x4 bl[7];
#pragma unroll
    for (int j = 0; j < 7; ++j) { bl[j] = (f32x4){opqf(bflo(bias[j][0]) * LOG2E), opqf(bfhi(bias[j][0]) * LOG2E), opqf(bflo(bias[j][1]) * LOG2E), opqf(bfhi(bias[j][1]) * LOG2E)}; asm volatile("" : "+v"(bl[j])); }
    WAIT_L0; BAR; SB;
    mm(); BAR; MFMA_PAD; STAMP(7);
    f32x4 cs[7]; float rl[4], rml[4];
#pragma unroll
    for (int j = 0; j < 7; ++j) cs[j] = *(const LAS f32x4*)(size_t)(lb + L1_CS + j * 64 + g * 16);
#pragma unroll
    for (int m = 0; m < 4; ++m) { const f32x2 st = *(const LAS f32x2*)(size_t)(lb + L1_ST + (wx * 64 + m * 16 + tok) * 8); rl[m] = opqf(st[0] * LOG2E); rml[m] = opqf(-rl[m] * st[1]); }
    SB;
    Phase2<0> p2; p2.init(lb, lane, wx, P);
    const __amdgpu_buffer_rsrc_t ract = mk_rsrc(ws + WS_ACT, 128 * 112 * 1024);
    auto mish8 = [&](int m0, int j0, int m1, int j1) -> u32x4 {
      const f32x2 a[4] = {acc[m0][j0].xy, acc[m0][j0].zw, acc[m1][j1].xy, acc[m1][j1].zw}, c[4] = {cs[j0].xy, cs[j0].zw, cs[j1].xy, cs[j1].zw}, bb[4] = {bl[j0].xy, bl[j0].zw, bl[j1].xy, bl[j1].zw};
      f32x2 t[4], hs[4], e[4], d[4]; unsigned o[4];
#define ST4(stmt) _Pragma("unroll") for (int i = 0; i < 4; ++i) { stmt; } SB
      ST4(t[i] = c[i] * rml[i < 2 ? m0 : m1] + bb[i]);
      ST4(hs[i] = a[i] * rl[i < 2 ? m0 : m1] + t[i]);
      ST4(e[i] = opq2((f32x2){ex2(hs[i][0]), ex2(hs[i][1])}));
      ST4(t[i] = e[i] + 2.0f);
      ST4(d[i] = opq2(e[i] * t[i] + 2.0f));
      ST4(e[i] = opq2((f32x2){rcp(d[i][0]), rcp(d[i][1])}));
      ST4(t[i] = e[i] * (-2.0f / LOG2E) + 1.0f / LOG2E);
      ST4(d[i] = hs[i] * t[i]);
      ST4(o[i] = pk2(d[i][0], d[i][1]));
      return swap16(o[0], o[1], o[2], o[3]); };
    const unsigned vpair = (unsigned)((((g & 1) << 1) | (g >> 1)) * 256 + tok * 16), mb0 = p * 16 + wx * 4;
    auto pair = [&](auto J, auto M) { constexpr int j = J(), m = M();
      st16_sc1(mish8(m, j, m, j + 1), ract, vpair, ((mb0 + m) * 112 + ((7 * h + odd + j) >> 1)) * 1024); SB;
      constexpr int n = 2 * j + m - BLATE; if constexpr (n >= 0 && n < 6) { p2.dma1<n / 2, 4 + 2 * (n & 1)>(); p2.dma1<n / 2, 5 + 2 * (n & 1)>(); } };
    gu64* fact = flags + FL_ACT / 8 + (((2 * p + (wx >> 1)) * 2 + (h >> 4)) * 32 + (h & 15) * 2 + (wx & 1)) * 16;
    STAMP(13);
    if (XP0) __builtin_amdgcn_s_setprio(2);
    pair(IC<0>{}, IC<0>{}); pair(IC<0>{}, IC<1>{}); pair(IC<0>{}, IC<2>{}); pair(IC<0>{}, IC<3>{});
    if (XP0) __builtin_amdgcn_s_setprio(0);
    STAMP(14);
    pair(IC<2>{}, IC<0>{}); pair(IC<2>{}, IC<1>{});
    STAMP(15);
    WAIT_V(2); set_flag(fact, tag, lane); SB; STAMP(8);
    gu64* fa1 = flags + FL_ACT / 8 + (p2.rt * 2 + p2.kh) * 512; u64 pv;
    const unsigned vsing = (unsigned)((g & 1) * 112 * 1024 + (2 * odd + (g >> 1)) * 256 + tok * 16);
    auto single = [&](int m) {
      st16_sc1(mish8(m, 6, m + 1, 6), ract, vsing, ((mb0 + m) * 112 + ((7 * h + (odd ? 0 : 6)) >> 1)) * 1024); SB; };
    auto geta = [&]() { POLL_PIN(pv); STAMP(10); if (!__all(pv == tag)) wait_flags<32, 16>(fa1, tag, tmo, lane);
      SB; STAMP(11); p2.dma4<0, 0>(); p2.dma4<1, 0>(); p2.dma4<2, 0>(); };
    pair(IC<2>{}, IC<2>{}); pair(IC<2>{}, IC<3>{});
    if (EARLYA == 0) {
      pair(IC<4>{}, IC<0>{}); pair(IC<4>{}, IC<1>{}); pair(IC<4>{}, IC<2>{}); pair(IC<4>{}, IC<3>{});
      POLL_ISSUE(pv, fa1 + (lane & 31) * 16); single(0); single(2);
      STAMP(9); WAIT_V(2); geta(); WAIT_V(12);
    } else {
      pair(IC<4>{}, IC<0>{}); POLL_ISSUE(pv, fa1 + (lane & 31) * 16); pair(IC<4>{}, IC<1>{}); pair(IC<4>{}, IC<2>{});
      WAIT_V(4); geta(); pair(IC<4>{}, IC<3>{}); single(0); single(2); STAMP(9); WAIT_V(0);
    }
    set_flag(fact + 131072 / 8, tag, lane); SB; STAMP(12);
    const bf16x8 none[4][2] = {};
    P2RUN(p2, P, tag, none);
  } else {
    const int srow = p * 256 + h * 8 + wx * 2; u32x4 xa[4];
    { const u32x4 sx = mk_srd(x, S_ * D_ * 2); const unsigned vx = srow * 2048 + lane * 16;
      asm volatile("buffer_load_dwordx4 %0, %4, %5, 0 offen\n\tbuffer_load_dwordx4 %1, %4, %5, 0 offen offset:1024\n\tbuffer_load_dwordx4 %2, %4, %5, 0 offen offset:2048\n\tbuffer_load_dwordx4 %3, %4, %5, 0 offen offset:3072"
                   : "=&v"(xa[0]), "=&v"(xa[1]), "=&v"(xa[2]), "=&v"(xa[3]) : "v"(vx), "s"(sx) : "memory"); SB; }
    const u32x4 sq = mk_srd(wqkv, D_ * 3 * D_ * 2), sh = mk_srd(w1, D_ * F_ * 2), sp = mk_srd(wphi, 32 * 32 * 32 * 2);
    unsigned vq, vh[4];
    { const int r = lane >> 2; vq = (wx * 16 + r) * 6144 + h * 64 + (((lane & 3) ^ (((r >> 3) & 1) << 1)) << 4); }
#pragma unroll
    for (int i = 0; i < 4; ++i) { const int o = (wx + 4 * i) * 1024 + lane * 16, rp = o / 224, c = (o - rp * 224) >> 4, k = rp ^ (((rp >> 3) & 1) << 2); int cs = c + 2 * odd; if (cs >= 14) cs -= 14;
      vh[i] = k * 7168 + h * 224 + cs * 16; }
    const unsigned slq = lb + L1_BQ + wx * 1024, slh = lb + L1_BH + wx * 1024;
    const unsigned rq0 = opq(lb + L1_BQ + (8 * g + q) * 64 + 32 * (g & 1) + 8 * pp), rq1 = opq(rq0 ^ 32);
    f32x4 acc[3][4][2], cs0, cs1;
    bf16x8 fa[2][4], fb[2][6];
    auto dmaQ = [&](auto ST, auto U, unsigned sk) { constexpr int st = ST(), u = U(); DMA(sq, vq, sk, u * 2048, slq, st * 12288 + u * 4096); };
    auto dmaH = [&](auto ST, auto I, unsigned sk) { constexpr int st = ST(), i = I(); if (i < 3 || wx < 2) DMA(sh, vh[i], sk, 0, slh, st * 14336 + i * 4096); };
    auto rdA = [&](auto ST, auto KS, auto M) { constexpr int st = ST(), ks = KS(), m = M(); fa[ks][m] = lds128((ks ? ra1 : ra0) + st * 32768 + m * 2048); SB; };
    auto rdB = [&](auto ST, auto KS, auto J) { constexpr int st = ST(), ks = KS(), j = J(); constexpr int o = st * 12288 + (j >> 1) * 4096 + ks * 2048; const unsigned a = (j & 1) ? rq1 : rq0;
      fb[ks][j] = cat(ldstr(a + o), ldstr(a + o + 256)); SB; };
    auto load = [&](auto ST, auto DQ, auto DH, int t) {
      constexpr int o = ST() ^ 1; constexpr bool dq = DQ(), dh = DH(); const unsigned skq = (unsigned)kt(t + 1) * (64 * 6144), skh = (unsigned)kt(t + 2) * (64 * 7168);
      rdA(ST, IC<0>{}, IC<0>{}); rdB(ST, IC<0>{}, IC<0>{}); rdB(ST, IC<0>{}, IC<1>{}); if (dq) dmaQ(IC<o>{}, IC<0>{}, skq);
      rdA(ST, IC<0>{}, IC<1>{}); rdB(ST, IC<0>{}, IC<2>{}); rdB(ST, IC<0>{}, IC<3>{}); if (dq) dmaQ(IC<o>{}, IC<1>{}, skq);
      rdA(ST, IC<0>{}, IC<2>{}); rdB(ST, IC<0>{}, IC<4>{}); rdB(ST, IC<0>{}, IC<5>{}); if (dq) dmaQ(IC<o>{}, IC<2>{}, skq);
      rdA(ST, IC<0>{}, IC<3>{}); rdB(ST, IC<1>{}, IC<0>{}); if (dh) dmaH(ST, IC<0>{}, skh);
      rdA(ST, IC<1>{}, IC<0>{}); rdB(ST, IC<1>{}, IC<1>{}); rdB(ST, IC<1>{}, IC<2>{}); if (dh) dmaH(ST, IC<1>{}, skh);
      rdA(ST, IC<1>{}, IC<1>{}); rdB(ST, IC<1>{}, IC<3>{}); if (dh) dmaH(ST, IC<2>{}, skh);
      rdA(ST, IC<1>{}, IC<2>{}); rdB(ST, IC<1>{}, IC<4>{});
      rdA(ST, IC<1>{}, IC<3>{}); rdB(ST, IC<1>{}, IC<5>{});
      if (dh) { if (wx < 2) { dmaH(ST, IC<3>{}, skh); WAIT_V(7); } else WAIT_V(6); }
    };
    auto mm_ = [&](auto F) {
#pragma unroll
      for (int m = 0; m < 4; ++m)
#pragma unroll
        for (int j = 0; j < 6; ++j) mk2<F()>(acc[j >> 1][m][j & 1], fb[0][j], fa[0][m], fb[1][j], fa[1][m]);
      SB; };
    auto mm = [&]() { mm_(IC<0>{}); };
#define H4(ST, sk) dmaH(IC<ST>{}, IC<0>{}, sk); dmaH(IC<ST>{}, IC<1>{}, sk); dmaH(IC<ST>{}, IC<2>{}, sk); dmaH(IC<ST>{}, IC<3>{}, sk)
#define Q3(ST, sk) dmaQ(IC<ST>{}, IC<0>{}, sk); dmaQ(IC<ST>{}, IC<1>{}, sk); dmaQ(IC<ST>{}, IC<2>{}, sk)
    H4(0, 0u); Q3(0, 0u);
    cs0 = zacc(); cs1 = zacc();
    auto colsum = [&](bool now) { const bf16x8 a[7] = {fb[0][0], fb[0][1], fb[0][2], fb[0][3], fb[0][4], fb[0][5], ones}, c[7] = {fb[1][0], fb[1][1], fb[1][2], fb[1][3], fb[1][4], fb[1][5], ones};
      colsum7(cs0, cs1, a, c, ones, now ? wx : 4); SB; };
    STAMP(1);
    if (wx < 2) WAIT_V(7); else WAIT_V(6);
    asm volatile("" : "+v"(xa[0]), "+v"(xa[1]), "+v"(xa[2]), "+v"(xa[3]), "+v"(ep));
    const u64 tag = uni64(ep) + 1;
    { float v[2][16], sm[2], sq_[2], mean[2];
#pragma unroll
      for (int r = 0; r < 2; ++r) { sm[r] = 0.f;
#pragma unroll
        for (int i = 0; i < 8; ++i) { const unsigned u = xa[2 * r + (i >> 2)][i & 3]; v[r][2 * i] = bflo(u); v[r][2 * i + 1] = bfhi(u); sm[r] += v[r][2 * i] + v[r][2 * i + 1]; } }
#pragma unroll
      for (int r = 0; r < 2; ++r) { mean[r] = wave_sum(sm[r]) * (1.0f / D_); sq_[r] = 0.f;
#pragma unroll
        for (int i = 0; i < 16; ++i) { const float d = v[r][i] - mean[r]; sq_[r] += d * d; } }
      const float r0 = __builtin_amdgcn_rsqf(wave_sum(sq_[0]) * (1.0f / D_) + 1e-5f), r1 = __builtin_amdgcn_rsqf(wave_sum(sq_[1]) * (1.0f / D_) + 1e-5f);
      const u32x4 gr = {__float_as_uint(lane ? mean[1] : mean[0]), (unsigned)tag, __float_as_uint(lane ? r1 : r0), (unsigned)tag};
      if (lane < 2) st16_sc1(gr, rfl, lane * 16, GR_ST + srow * 16);
      SB; }
    STAMP(2);
    WAIT_V(1);
    STAMP(3); BAR; SB; STAMP(4);
    if (wx < 2) { const unsigned vp = h * 2048 + wx * 1024 + lane * 16, slp = lb + L1_WPHI + wx * 1024; DMA(sp, vp, 0u, 0, slp, 0); }
    H4(1, 64u * 7168u); Q3(1, 64u * 6144u);
    BAR; SB; STAMP(5);
    load(IC<0>{}, IC<0>{}, IC<1>{}, 0); WAIT_L0; BAR; SB;
    mm_(IC<1>{}); colsum(p == 7); WAIT_V(3); BAR; SB;
    load(IC<1>{}, IC<1>{}, IC<1>{}, 1); WAIT_L0; BAR; SB;
    mm(); colsum(p == 7); WAIT_V(3); BAR; SB;
    load(IC<0>{}, IC<1>{}, IC<1>{}, 2); WAIT_L0; BAR; SB;
    mm(); colsum(p != 7); WAIT_V(3); BAR; SB;
    load(IC<1>{}, IC<1>{}, IC<1>{}, 3); WAIT_L0; BAR; SB;
    mm(); colsum(p != 7); WAIT_V(3); BAR; MFMA_PAD;
    cs_publish(cs0, cs1, 7 + wx, wx < 2, tag);
    for (int t = 4; t < 14; t += 2) {
      load(IC<0>{}, IC<1>{}, IC<1>{}, t); WAIT_L0; BAR; SB;
      mm(); WAIT_V(3); BAR; SB;
      load(IC<1>{}, IC<1>{}, IC<1>{}, t + 1); WAIT_L0; BAR; SB;
      mm(); WAIT_V(3); BAR; SB;
    }
    STAMP(6);
    const unsigned vst = GR_ST + (p * 256 + wx * 64 + lane) * 16, vcs = GR_CS + h * (8 * 1664) + min(wx * 64 + lane, 103) * 16;
    const u32x4 sfl = mk_srd(ws + WS_FLAG, 1u << 20);
    u32x4 gs, gc[8]; LD16_ISSUE(gs, sfl, vst, 0u);
#pragma unroll
    for (int i = 0; i < 8; ++i) LD16_ISSUE(gc[i], sfl, vcs, i * 1664u);
    load(IC<0>{}, IC<1>{}, IC<0>{}, 14);
    WAIT_V(12); WAIT_L0; BAR; SB;
    mm(); WAIT_V(0); BAR; SB;
    load(IC<1>{}, IC<0>{}, IC<0>{}, 15);
    { auto stale = [&]() { unsigned d = differ(gs[1], gs[3], (unsigned)tag);
#pragma unroll
        for (int i = 0; i < 8; ++i) d |= differ(gc[i][1], gc[i][3], (unsigned)tag);
        return !__all(d == 0u); };
      asm volatile("" : "+v"(gs), "+v"(gc[0]), "+v"(gc[1]), "+v"(gc[2]), "+v"(gc[3]), "+v"(gc[4]), "+v"(gc[5]), "+v"(gc[6]), "+v"(gc[7]));
      if (__builtin_expect(stale(), 0)) {
#pragma nounroll
        for (unsigned spins = 0; spins < (1u << 16); ++spins) {
          asm volatile("buffer_load_dwordx4 %0, %1, %2, 0 offen sc1" : "+v"(gs) : "v"(vst), "s"(sfl) : "memory");
#pragma unroll
          for (int i = 0; i < 8; ++i) asm volatile("buffer_load_dwordx4 %0, %1, %2, %3 offen sc1" : "+v"(gc[i]) : "v"(vcs), "s"(sfl), "s"(i * 1664u) : "memory");
          asm volatile("s_waitcnt vmcnt(0)" : "+v"(gs), "+v"(gc[0]), "+v"(gc[1]), "+v"(gc[2]), "+v"(gc[3]), "+v"(gc[4]), "+v"(gc[5]), "+v"(gc[6]), "+v"(gc[7]) :: "memory");
          if (!stale()) break; } }
      f32x2 c = {__uint_as_float(gc[0][0]), __uint_as_float(gc[0][2])};
#pragma unroll
      for (int i = 1; i < 8; ++i) c = (f32x2){add1(c[0], __uint_as_float(gc[i][0])), add1(c[1], __uint_as_float(gc[i][2]))};
      *(LAS f32x2*)(size_t)(lb + L1_CS + min(wx * 64 + lane, 103) * 8) = c;
      *(LAS f32x2*)(size_t)(lb + L1_ST + (wx * 64 + lane) * 8) = (f32x2){__uint_as_float(gs[2]), __uint_as_float(gs[0])}; SB; }
    WAIT_L0; BAR; SB;
    mm(); MFMA_PAD; STAMP(7);
    if (YPRIO) __builtin_amdgcn_s_setprio(1);
    {
    const int lane = opq(__lane_id()), tok = lane & 15, g = lane >> 4, q = (lane >> 2) & 3, pp = lane & 3;
    bf16x8 wp[2];
#pragma unroll
    for (int ff = 0; ff < 2; ++ff) { const unsigned a = lb + L1_WPHI + (4 * g + q) * 64 + ff * 32 + 8 * pp; wp[ff] = cat(ldstr(a), ldstr(a + 1024)); }
    f32x4 cs[6]; float rs[4], mean[4], rl[4];
#pragma unroll
    for (int j = 0; j < 6; ++j) cs[j] = *(const LAS f32x4*)(size_t)(lb + L1_CS + (7 + j) * 64 + g * 16);
#pragma unroll
    for (int m = 0; m < 4; ++m) { const f32x2 st = *(const LAS f32x2*)(size_t)(lb + L1_ST + (wx * 64 + m * 16 + tok) * 8); rs[m] = st[0]; mean[m] = opqf(-st[1]); rl[m] = st[0] * LOG2E; }
    const f32x4 zero4 = {0.f, 0.f, 0.f, 0.f};
    auto project = [&](int u, f32x4 (&pr)[4][2]) {
#pragma unroll
      for (int m = 0; m < 4; ++m) { const bf16x8 kb = pk8(acc[u][m][0] + cs[2 * u] * mean[m], acc[u][m][1] + cs[2 * u + 1] * mean[m]); pr[m][0] = mfma(wp[0], kb, zero4); pr[m][1] = mfma(wp[1], kb, zero4); }
      SB; };
    auto extrema = [&](const f32x4 (&pr)[4][2], float (&ma)[4], float (&mb)[4]) {
#pragma unroll
      for (int m = 0; m < 4; ++m) { ma[m] = vmax8(pr[m][0], pr[m][1]); mb[m] = vmax8(-pr[m][0], -pr[m][1]); }
      spread4(gather4(ma[0], mb[0], ma[1], mb[1], vmax), ma[0], mb[0], ma[1], mb[1]);
      spread4(gather4(ma[2], mb[2], ma[3], mb[3], vmax), ma[2], mb[2], ma[3], mb[3]);
#pragma unroll
      for (int m = 0; m < 4; ++m) { ma[m] *= -rl[m]; mb[m] *= -rl[m]; }
      SB; };
    auto expo = [&](int m, const f32x4 (&pr)[2], float ma, float mb, f32x4 (&ep)[2], f32x4 (&en)[2]) {
#pragma unroll
      for (int ff = 0; ff < 2; ++ff) { ep[ff] = pr[ff] * rl[m] + ma; en[ff] = pr[ff] * -rl[m] + mb; }
      SB;
#pragma unroll
      for (int ff = 0; ff < 2; ++ff)
#pragma unroll
        for (int r = 0; r < 4; ++r) { ep[ff][r] = ex2(ep[ff][r]); en[ff][r] = ex2(en[ff][r]); }
      SB; };
    { const unsigned swp = (tok & 1) | (((tok >> 2) & 1) << 1) | (((tok >> 1) & 1) << 2) | (tok & 8);
      const unsigned wph = lb + L1_PHI + (wx * 64 + tok) * 128 + ((g ^ swp) << 3), wv = lb + L1_V + (wx * 64 + tok) * 64 + ((g ^ (tok >> 1)) << 3);
      const unsigned wp0 = opq(wph), wp1 = opq(wph ^ 32), wp2 = opq(wph ^ 64), wp3 = opq(wph ^ 96), wv0 = opq(wv), wv1 = opq(wv ^ 32);
      auto put = [](unsigned a, f32x4 v) { *(LAS u32x2*)(size_t)a = (u32x2){pk2(v[0], v[1]), pk2(v[2], v[3])}; };
      f32x4 pr[4][2], ep[4][2], en[4][2]; float ma[4], mb[4];
      WAIT_L0; project(1, pr);
#pragma unroll
      for (int m = 0; m < 4; ++m) { put(wv0 + m * 1024, (acc[2][m][0] + cs[4] * mean[m]) * rs[m]); put(wv1 + m * 1024, (acc[2][m][1] + cs[5] * mean[m]) * rs[m]); }
      SB; extrema(pr, ma, mb);
#pragma unroll
      for (int m = 0; m < 4; ++m) expo(m, pr[m], ma[m], mb[m], ep[m], en[m]);
#pragma unroll
      for (int m = 0; m < 4; ++m) { const f32x4 a = ep[m][0] + ep[m][1], c = en[m][0] + en[m][1]; const f32x2 a2 = a.xy + a.zw, c2 = c.xy + c.zw; ma[m] = a2[0] + opqf(a2[1]); mb[m] = c2[0] + opqf(c2[1]); }
      auto fadd = [](float a, float c) { return a + c; };
      spread4(rcp(gather4(ma[0], mb[0], ma[1], mb[1], fadd)), ma[0], mb[0], ma[1], mb[1]);
      spread4(rcp(gather4(ma[2], mb[2], ma[3], mb[3], fadd)), ma[2], mb[2], ma[3], mb[3]);
      SB;
#pragma unroll
      for (int m = 0; m < 4; ++m) { ep[m][0] *= ma[m]; ep[m][1] *= ma[m]; en[m][0] *= mb[m]; en[m][1] *= mb[m]; SB;
        put(wp0 + m * 2048, ep[m][0]); put(wp1 + m * 2048, ep[m][1]); put(wp2 + m * 2048, en[m][0]); put(wp3 + m * 2048, en[m][1]); SB; } }
    STAMP(8);
    asm volatile("s_waitcnt lgkmcnt(0)" ::: "memory");
    if (lane == 0) { const unsigned one = 1, a = lb + L1_CNT; asm volatile("ds_add_u32 %0, %1" :: "v"(a), "v"(one) : "memory"); }
    bf16x8 eb[4][2];
    f32x4 pq[4][2]; float qa[4], qb[4];
    project(0, pq); extrema(pq, qa, qb);
    auto qpart = [&](int m0) {
#pragma unroll
      for (int m = m0; m < m0 + 2; ++m) { f32x4 ep[2], en[2]; expo(m, pq[m], qa[m], qb[m], ep, en); eb[m][0] = pk8(ep[0], ep[1]); eb[m][1] = pk8(en[0], en[1]); asm volatile("" : "+v"(eb[m][0]), "+v"(eb[m][1])); SB; } };
    lds_wait(lb + L1_CNT, 4u); SB; STAMP(9);
    { const bf16x8 ones = {0x3F80, 0x3F80, 0x3F80, 0x3F80, 0x3F80, 0x3F80, 0x3F80, 0x3F80};
      unsigned ap[2], av[2][2];
#pragma unroll
      for (int t = 0; t < 2; ++t) { const int tk = 8 * g + 4 * t + q, t15 = tk & 15; const unsigned sw = (t15 & 1) | (((t15 >> 2) & 1) << 1) | (((t15 >> 1) & 1) << 2) | (t15 & 8);
        ap[t] = opq(lb + L1_PHI + tk * 128 + (((4 * wx + pp) ^ sw) << 3));
        av[t][0] = opq(lb + L1_V + tk * 64 + ((pp ^ (t15 >> 1)) << 3)); av[t][1] = opq(av[t][0] ^ 32); }
      bf16x8 a[8], b0[8], b1_[8];
#pragma unroll
      for (int ks = 0; ks < 8; ++ks) { a[ks] = cat(ldstr(ap[0] + ks * 4096), ldstr(ap[1] + ks * 4096));
        b0[ks] = cat(ldstr(av[0][0] + ks * 2048), ldstr(av[1][0] + ks * 2048)); b1_[ks] = cat(ldstr(av[0][1] + ks * 2048), ldstr(av[1][1] + ks * 2048)); }
      SB; qpart(0); STAMP(12);
      f32x4 kv0 = zero4, kv1 = zero4, zz = zero4;
#pragma unroll
      for (int ks = 0; ks < 8; ++ks) { kv0 = mfma(a[ks], b0[ks], kv0); kv1 = mfma(a[ks], b1_[ks], kv1); zz = mfma(a[ks], ones, zz); }
      const __amdgpu_buffer_rsrc_t rkv = mk_rsrc(ws + WS_KVP, 3u << 20);
      st16_sc1(__builtin_bit_cast(u32x4, pk8(kv0, kv1)), rkv, lane * 16, (h * 8 + p) * 4096 + wx * 1024);
      if (tok == 0) st16_sc1(__builtin_bit_cast(u32x4, zz), rkv, g * 16, (unsigned)(WS_ZP - WS_KVP) + (h * 8 + p) * 256 + wx * 64);
      SB; STAMP(13); }
    qpart(2);
    SB; if (YPRIO) __builtin_amdgcn_s_setprio(0); STAMP(10); WAIT_V(0);
    set_flag(flags + (FL_KV + (h * 4 + wx) * 1024) / 8 + p * 16, tag, lane); STAMP(11);
    Phase2<1> p2; p2.init(lb, lane, wx, P);
    P2RUN(p2, P, tag, eb);
    }
  }
}
}

extern "C" void kernel_launch(void* const* d_in, const int* in_sizes, int n_in, void* d_out, int out_size, void* d_ws, size_t ws_size, hipStream_t stream) {
  k_fused<<<256, 512, 0, stream>>>((const u16*)d_in[0], (const u16*)d_in[1], (const u16*)d_in[2], (const u16*)d_in[3], (const u16*)d_in[5], (const u16*)d_in[6], (const u16*)d_in[7], (char*)d_ws, (u16*)d_out);
}
```

```cpp
#define SA 0
#define SBB 0
#define SW 0
#define SBLK 100
#define CLK "s_memtime"
#define MULN 0
#define MULD 4
#include <hip/hip_runtime.h>
#include <hip/hip_bf16.h>
#include <type_traits>

namespace {
#define LAS __attribute__((address_space(3)))
#define GAS __attribute__((address_space(1)))
typedef unsigned short u16;
typedef unsigned u32x4 __attribute__((ext_vector_type(4)));
typedef unsigned u32x2 __attribute__((ext_vector_type(2)));
typedef short bf16x8 __attribute__((ext_vector_type(8)));
typedef short s16x4 __attribute__((ext_vector_type(4)));
typedef float f32x4 __attribute__((ext_vector_type(4)));
typedef float f32x2 __attribute__((ext_vector_type(2)));
typedef __bf16 bf16x2_t __attribute__((ext_vector_type(2)));

constexpr int S_ = 2048, D_ = 1024, F_ = 3584;
constexpr size_t WS_FLAG = 0;
constexpr size_t WS_XN = 1u << 20;
constexpr size_t WS_ACT = WS_XN + (4u << 20);
constexpr size_t WS_O = WS_ACT + (15u << 20);
constexpr size_t WS_KVP = WS_O + (4u << 20);
constexpr size_t WS_ZP = WS_KVP + (2u << 20);
constexpr size_t WS_YX = WS_ZP + (1u << 20);
constexpr size_t WS_STAMP = 64u << 20;
#define DIAG 1
constexpr int NSTAMP = 32;
#define STAMP_DECL unsigned st_c = 0, st_r = 0
#define STAMP(i) do { if ((i) == SA || (i) == SBB) { unsigned long long c_; __builtin_amdgcn_sched_barrier(0); asm volatile(CLK " %0\n\ts_waitcnt lgkmcnt(0)" : "=s"(c_) :: "memory"); \
  if ((i) == SA) st_c = (unsigned)c_; else st_r = (unsigned)c_; asm volatile("" : "+v"(st_c), "+v"(st_r)); __builtin_amdgcn_sched_barrier(0); } } while (0)
#define STAMP_OUT(wv) do { if ((wv) == SW && blockIdx.x == SBLK) { const unsigned d_ = __builtin_amdgcn_readfirstlane(st_r - st_c) * MULN / MULD + 500; const unsigned long long t0_ = __builtin_amdgcn_s_memrealtime(); \
  while (__builtin_amdgcn_s_memrealtime() - t0_ < d_ && d_ < 100000u) __builtin_amdgcn_s_sleep(1); } } while (0)
constexpr unsigned FL_EPOCH = 0;
constexpr unsigned FL_O = 1024;
constexpr unsigned FL_YX = 16384;
constexpr unsigned FL_TMO = 61440;
constexpr unsigned GR_ST = 65536;
constexpr unsigned FL_KV = 131072;
constexpr unsigned FL_ACT = 262144;
constexpr unsigned GR_CS = 524288;
#define YPRIO 0
#define XP0 0
#define EARLYA 0
#define PFCODE 7
#define BLATE 6
#define TOUCH0 0
#define TOUCHW 1
constexpr float LOG2E = 1.4426950408889634f;

#define SB __builtin_amdgcn_sched_barrier(0)
#define BAR __builtin_amdgcn_s_barrier()
#define WAIT_L0 do { __builtin_amdgcn_s_waitcnt(0xC07F); SB; } while (0)
#define WAIT_V(n) asm volatile("s_waitcnt vmcnt(%0)" :: "i"(n) : "memory")
template <int N> using IC = std::integral_constant<int, N>;

__device__ __forceinline__ u32x4 mk_srd(const void* p, unsigned bytes) {
  const unsigned long long a = (unsigned long long)p; u32x4 r;
  r.x = __builtin_amdgcn_readfirstlane((unsigned)a); r.y = __builtin_amdgcn_readfirstlane((unsigned)(a >> 32)) & 0xffffu; r.z = bytes; r.w = 0x00020000u; return r; }
#define DMA(srd, voff, sk, KOFF, sl, LOFF) do { unsigned _t; asm volatile("s_add_u32 m0, %4, %5\n\ts_add_u32 %0, %3, %6\n\tbuffer_load_dwordx4 %1, %2, %0 offen lds" \
    : "=&s"(_t) : "v"(voff), "s"(srd), "s"(sk), "s"(sl), "i"(LOFF), "i"(KOFF) : "memory", "scc"); SB; } while (0)
#define DMA_SC1(srd, voff, sk, KOFF, sl, LOFF) do { unsigned _t; asm volatile("s_add_u32 m0, %4, %5\n\ts_add_u32 %0, %3, %6\n\tbuffer_load_dwordx4 %1, %2, %0 offen sc1 lds" \
    : "=&s"(_t) : "v"(voff), "s"(srd), "s"(sk), "s"(sl), "i"(LOFF), "i"(KOFF) : "memory", "scc"); SB; } while (0)
#define TOUCH(ptr, sl) do { asm volatile("s_mov_b32 m0, %1\n\ts_nop 0\n\tglobal_load_lds_dword %0, off" :: "v"(ptr), "s"(sl) : "memory"); SB; } while (0)
__device__ __forceinline__ bf16x8 lds128(unsigned a) { return *(const LAS bf16x8*)(size_t)a; }
__device__ __forceinline__ s16x4 ldstr(unsigned a) { return __builtin_amdgcn_ds_read_tr16_b64_v4i16((LAS s16x4*)(size_t)a); }
__device__ __forceinline__ bf16x8 cat(s16x4 lo, s16x4 hi) { return __builtin_shufflevector(lo, hi, 0, 1, 2, 3, 4, 5, 6, 7); }
__device__ __forceinline__ unsigned opq(unsigned x) { asm volatile("" : "+v"(x)); return x; }
__device__ __forceinline__ float opqf(float x) { asm("" : "+v"(x)); return x; }
__device__ __forceinline__ f32x2 opq2(f32x2 x) { asm("" : "+v"(x)); return x; }
__device__ __forceinline__ f32x4 mfma(bf16x8 a, bf16x8 b, f32x4 c) { return __builtin_amdgcn_mfma_f32_16x16x32_bf16(a, b, c, 0, 0, 0); }
__device__ __forceinline__ void mfma_acc(f32x4& d, bf16x8 a, bf16x8 b) { asm volatile("v_mfma_f32_16x16x32_bf16 %0, %1, %2, %0" : "+v"(d) : "v"(a), "v"(b)); }
__device__ __forceinline__ void mfma_k2(f32x4& d, bf16x8 a0, bf16x8 b0, bf16x8 a1, bf16x8 b1) {
  asm volatile("v_mfma_f32_16x16x32_bf16 %0, %1, %2, %0\n\tv_mfma_f32_16x16x32_bf16 %0, %3, %4, %0" : "+v"(d) : "v"(a0), "v"(b0), "v"(a1), "v"(b1)); }
__device__ __forceinline__ void mfma_k2_first(f32x4& d, bf16x8 a0, bf16x8 b0, bf16x8 a1, bf16x8 b1) {
  asm volatile("v_mfma_f32_16x16x32_bf16 %0, %1, %2, 0\n\tv_mfma_f32_16x16x32_bf16 %0, %3, %4, %0" : "=&v"(d) : "v"(a0), "v"(b0), "v"(a1), "v"(b1)); }
template <bool FIRST> __device__ __forceinline__ void mk2(f32x4& d, bf16x8 a0, bf16x8 b0, bf16x8 a1, bf16x8 b1) { if (FIRST) mfma_k2_first(d, a0, b0, a1, b1); else mfma_k2(d, a0, b0, a1, b1); }
__device__ __forceinline__ void mfma_k2x2(f32x4& d, f32x4& e, bf16x8 a0, bf16x8 c0, bf16x8 b0, bf16x8 a1, bf16x8 c1, bf16x8 b1) {
  asm volatile("v_mfma_f32_16x16x32_bf16 %0, %2, %4, %0\n\tv_mfma_f32_16x16x32_bf16 %1, %3, %4, %1\n\tv_mfma_f32_16x16x32_bf16 %0, %5, %7, %0\n\tv_mfma_f32_16x16x32_bf16 %1, %6, %7, %1"
               : "+v"(d), "+v"(e) : "v"(a0), "v"(c0), "v"(b0), "v"(a1), "v"(c1), "v"(b1)); }
__device__ __forceinline__ f32x4 zacc() { f32x4 z = {0.f, 0.f, 0.f, 0.f}; asm volatile("" : "+v"(z)); return z; }
#define CS_M(d, a) "v_mfma_f32_16x16x32_bf16 " d ", " a ", %[o], " d "\n\t"
__device__ __forceinline__ void colsum7(f32x4& c0, f32x4& c1, const bf16x8 (&a)[7], const bf16x8 (&b)[7], bf16x8 ones, unsigned sel) {
  asm volatile("s_cmp_lt_u32 %[s], 2\n\ts_cbranch_scc1 .Lcs01_%=\n\ts_cmp_eq_u32 %[s], 2\n\ts_cbranch_scc1 .Lcs2_%=\n\ts_cmp_eq_u32 %[s], 3\n\ts_cbranch_scc0 .Lcs9_%=\n\t"
               CS_M("%0", "%[a3]") CS_M("%0", "%[b3]") "s_branch .Lcs9_%=\n"
               ".Lcs2_%=:\n\t" CS_M("%0", "%[a2]") CS_M("%0", "%[b2]") CS_M("%1", "%[a6]") CS_M("%1", "%[b6]") "s_branch .Lcs9_%=\n"
               ".Lcs01_%=:\n\ts_cmp_eq_u32 %[s], 0\n\ts_cbranch_scc1 .Lcs0_%=\n\t" CS_M("%0", "%[a1]") CS_M("%0", "%[b1]") CS_M("%1", "%[a5]") CS_M("%1", "%[b5]") "s_branch .Lcs9_%=\n"
               ".Lcs0_%=:\n\t" CS_M("%0", "%[a0]") CS_M("%0", "%[b0]") CS_M("%1", "%[a4]") CS_M("%1", "%[b4]") "\n.Lcs9_%=:"
               : "+v"(c0), "+v"(c1) : [a0] "v"(a[0]), [a1] "v"(a[1]), [a2] "v"(a[2]), [a3] "v"(a[3]), [a4] "v"(a[4]), [a5] "v"(a[5]), [a6] "v"(a[6]),
                 [b0] "v"(b[0]), [b1] "v"(b[1]), [b2] "v"(b[2]), [b3] "v"(b[3]), [b4] "v"(b[4]), [b5] "v"(b[5]), [b6] "v"(b[6]), [o] "v"(ones), [s] "s"(sel) : "scc");
}
#define MFMA_PAD do { asm volatile("s_nop 7\n\ts_nop 1" ::: "memory"); SB; } while (0)
__device__ __forceinline__ unsigned pk2(float lo, float hi) { const f32x2 v = {lo, hi}; return __builtin_bit_cast(unsigned, __builtin_convertvector(v, bf16x2_t)); }
__device__ __forceinline__ bf16x8 pk8(f32x4 a, f32x4 b) { const u32x4 u = {pk2(a[0], a[1]), pk2(a[2], a[3]), pk2(b[0], b[1]), pk2(b[2], b[3])}; return __builtin_bit_cast(bf16x8, u); }
__device__ __forceinline__ float bflo(unsigned u) { return __uint_as_float(u << 16); }
__device__ __forceinline__ float bfhi(unsigned u) { return __uint_as_float(u & 0xffff0000u); }
__device__ __forceinline__ float ex2(float x) { return __builtin_amdgcn_exp2f(x); }
__device__ __forceinline__ float rcp(float x) { return __builtin_amdgcn_rcpf(x); }
__device__ __forceinline__ u32x4 swap16(unsigned a0, unsigned a1, unsigned b0, unsigned b1) {
  const auto p = __builtin_amdgcn_permlane16_swap(a0, b0, false, false), q = __builtin_amdgcn_permlane16_swap(a1, b1, false, false);
  return (u32x4){p[0], q[0], p[1], q[1]}; }
__device__ __forceinline__ float vmax(float a, float b) { return __builtin_elementwise_maximum(a, b); }
__device__ __forceinline__ float vmax8(f32x4 a, f32x4 c) { return vmax(vmax(vmax(vmax(a[0], a[1]), a[2]), vmax(vmax(a[3], c[0]), c[1])), vmax(c[2], c[3])); }
__device__ __forceinline__ void sw32(float& a, float& b) { const auto p = __builtin_amdgcn_permlane32_swap(__float_as_uint(a), __float_as_uint(b), false, false); a = __uint_as_float(p[0]); b = __uint_as_float(p[1]); }
__device__ __forceinline__ void sw16(float& a, float& b) { const auto p = __builtin_amdgcn_permlane16_swap(__float_as_uint(a), __float_as_uint(b), false, false); a = __uint_as_float(p[0]); b = __uint_as_float(p[1]); }
template <class Op> __device__ __forceinline__ float gather4(float a0, float a1, float b0, float b1, Op op) { sw32(a0, a1); sw32(b0, b1); float c = op(a0, a1), d = op(b0, b1); sw16(c, d); return op(c, d); }
__device__ __forceinline__ void spread4(float d, float& a0, float& a1, float& b0, float& b1) { a0 = d; b0 = d; sw16(a0, b0); a1 = a0; b1 = b0; sw32(a0, a1); sw32(b0, b1); }
template <class Op> __device__ __forceinline__ float red4(float v, Op op) {
  const unsigned u = __float_as_uint(v); const auto p = __builtin_amdgcn_permlane32_swap(u, u, false, false);
  const float a = op(__uint_as_float(p[0]), __uint_as_float(p[1])); const unsigned ua = __float_as_uint(a);
  const auto q = __builtin_amdgcn_permlane16_swap(ua, ua, false, false); return op(__uint_as_float(q[0]), __uint_as_float(q[1])); }
__device__ __forceinline__ void st16_sc1(u32x4 v, __amdgpu_buffer_rsrc_t r, unsigned voff, unsigned soff) { __builtin_amdgcn_raw_buffer_store_b128(v, r, voff + soff, 0, 16); }
__device__ __forceinline__ void st16(u32x4 v, __amdgpu_buffer_rsrc_t r, unsigned voff, unsigned soff) { __builtin_amdgcn_raw_buffer_store_b128(v, r, voff + soff, 0, 0); }
__device__ __forceinline__ u32x4 ld16_sc1(__amdgpu_buffer_rsrc_t r, unsigned voff, unsigned soff) { return __builtin_amdgcn_raw_buffer_load_b128(r, voff, soff, 16); }
__device__ __forceinline__ __amdgpu_buffer_rsrc_t mk_rsrc(const void* p, unsigned bytes) { return __builtin_amdgcn_make_buffer_rsrc((void*)p, 0, bytes, 0x00020000); }
#define RLX_AGENT __ATOMIC_RELAXED, __HIP_MEMORY_SCOPE_AGENT
typedef unsigned long long u64;
typedef GAS u64 gu64;
__device__ __forceinline__ void set_flag(gu64* w, u64 tag, int lane) { if (lane == 0) __hip_atomic_store(w, tag, RLX_AGENT); }
template <int N, int STRIDE> __device__ __forceinline__ void wait_flags(gu64* w, u64 tag, gu64* tmo, int lane) {
#pragma nounroll
  for (unsigned spins = 0;; ++spins) {
    u64 v = tag; if (N >= 64 || lane < N) v = __hip_atomic_load(w + lane * STRIDE, RLX_AGENT);
    if (__all(v == tag)) break;
    if (spins > (1u << 16)) { if (lane == 0) __hip_atomic_store(tmo, tag, RLX_AGENT); break; }
    __builtin_amdgcn_s_sleep(2);
  }
  __builtin_amdgcn_fence(__ATOMIC_ACQUIRE, "wavefront");
}
__device__ __forceinline__ u64 uni64(u64 v) { return ((u64)(unsigned)__builtin_amdgcn_readfirstlane((unsigned)(v >> 32)) << 32) | (unsigned)__builtin_amdgcn_readfirstlane((unsigned)v); }
__device__ __forceinline__ void lds_wait(unsigned addr, unsigned target) {
  for (;;) { unsigned v; asm volatile("ds_read_b32 %0, %1\n\ts_waitcnt lgkmcnt(0)" : "=v"(v) : "v"(addr) : "memory");
    if ((unsigned)__builtin_amdgcn_readfirstlane(v) >= target) break; }
}
#define POLL_ISSUE(dst, ptr) do { asm volatile("global_load_dwordx2 %0, %1, off sc1" : "=v"(dst) : "v"(ptr) : "memory"); SB; } while (0)
#define LD16_ISSUE(dst, srd, voff, soff) do { asm volatile("buffer_load_dwordx4 %0, %1, %2, %3 offen sc1" : "=v"(dst) : "v"(voff), "s"(srd), "s"(soff) : "memory"); SB; } while (0)
#define POLL_PIN(dst) asm volatile("" : "+v"(dst))
__device__ __forceinline__ float add1(float a, float b) { float r; asm("v_add_f32 %0, %1, %2" : "=v"(r) : "v"(a), "v"(b)); return r; }
__device__ __forceinline__ float mul1(float a, float b) { float r; asm("v_mul_f32 %0, %1, %2" : "=v"(r) : "v"(a), "v"(b)); return r; }
__device__ __forceinline__ float fma1(float a, float b, float c) { float r; asm("v_fma_f32 %0, %1, %2, %3" : "=v"(r) : "v"(a), "v"(b), "v"(c)); return r; }
__device__ __forceinline__ f32x4 add4(f32x4 a, u32x4 b) { return (f32x4){add1(a[0], __uint_as_float(b[0])), add1(a[1], __uint_as_float(b[1])), add1(a[2], __uint_as_float(b[2])), add1(a[3], __uint_as_float(b[3]))}; }
__device__ __forceinline__ unsigned differ(unsigned a, unsigned b, unsigned t) { unsigned r; asm("v_bitop3_b32 %0, %1, %2, %3 bitop3:0x7e" : "=v"(r) : "v"(a), "v"(b), "s"(t)); return r; }
template <int C> __device__ __forceinline__ float dpp(float v) { return __int_as_float(__builtin_amdgcn_update_dpp(0, __float_as_int(v), C, 0xf, 0xf, true)); }
__device__ __forceinline__ float wave_sum(float v) { v += dpp<0x128>(v); v += dpp<0x124>(v); v += dpp<0x122>(v); v += dpp<0x121>(v); return red4(v, [](float a, float c) { return a + c; }); }
__device__ __forceinline__ void lds_sync(unsigned addr, unsigned target, int lane) {
  asm volatile("s_waitcnt lgkmcnt(0)" ::: "memory");
  if (lane == 0) { const unsigned one = 1; asm volatile("ds_add_u32 %0, %1" :: "v"(addr), "v"(one) : "memory"); }
  for (;;) { unsigned v; asm volatile("ds_read_b32 %0, %1\n\ts_waitcnt lgkmcnt(0)" : "=v"(v) : "v"(addr) : "memory");
    if ((unsigned)__builtin_amdgcn_readfirstlane(v) >= target) break; }
}

constexpr unsigned L1_A = 0;
constexpr unsigned L1_BQ = 65536;
constexpr unsigned L1_BH = 90112;
constexpr unsigned L1_WPHI = 118784;
constexpr unsigned L1_CNT = 120832;
constexpr unsigned L1_DUMP = 121088;
constexpr unsigned L1_BIAS = 121344;
constexpr unsigned L1_CS = 128000;
constexpr unsigned L1_ST = 129024;
constexpr unsigned L1_V = 98304;
constexpr unsigned L1_PHI = 131072;
constexpr unsigned L2_EX = 131072;

__device__ __forceinline__ void touch_code(unsigned dump, int lane, int wx) {
  u64 c0; unsigned back, len;
  asm volatile("s_getpc_b64 %0\n.Lpc_%=:\n\ts_mov_b32 %1, .Lpc_%=-.Lcode_begin\n\ts_mov_b32 %2, .Lend_g0-.Lcode_begin\n\ts_max_u32 %2, %2, .Lend_g1-.Lcode_begin" : "=s"(c0), "=s"(back), "=s"(len) :: "scc");
  const unsigned o = min((((blockIdx.x >> 3) * 4 + wx) * 4 + (lane & 3)) * 128u, len) & ~3u; TOUCH((const GAS char*)(c0 - back) + o, dump); }

struct Ptrs { const u16 *x, *wqkv, *wphi, *wo, *w1, *b1, *w2; char* ws; u16* out; };

template <int G> struct Phase2 {
  unsigned lb; int lane, wq, b, kh, rt, ct;
  u32x4 sact, so, sw2, swo;
  unsigned vaa, vao, vb0, vb1, vs0, vs1, sla, sls;
  struct Off { unsigned a0, a1, b; };
  __device__ __forceinline__ Off off_y(int U) const { const int s0 = kh * 56 + (U < 24 ? 7 * (U & 7) + (U >> 3) : 14 * U - 333), s1 = s0 + (U < 24 ? 4 : 7); return {s0 * 1024u, s1 * 1024u, (wq >> 1 ? s1 : s0) * 65536u}; }
  __device__ __forceinline__ Off off_s(int U) const { return {(U - 28) * 2048u, (U - 28) * 2048u + 1024u, (U - 28) * 131072u}; }
  __device__ __forceinline__ void init(unsigned lb_, int lane_, int wq_, const Ptrs& P) {
    lb = lb_; lane = lane_; wq = wq_; b = blockIdx.x; kh = b & 1; rt = ((b & 7) >> 1) * 4 + ((b >> 3) & 3); ct = b >> 5;
    sact = mk_srd(P.ws + WS_ACT, 128 * 112 * 1024); so = mk_srd(P.ws + WS_O, 128 * 32 * 1024); sw2 = mk_srd(P.w2, F_ * D_ * 2); swo = mk_srd(P.wo, D_ * D_ * 2);
    vaa = lane * 16 + (rt * 8 + wq * 2) * (112 * 1024); vao = lane * 16 + (rt * 8 + wq * 2) * (32 * 1024);
    { const int r = lane >> 4, c = lane & 15; vb0 = ((wq & 1) * 16 + r) * 2048 + ct * 256 + (((c >> 1) ^ r) << 5) + ((c & 1) << 4); vb1 = vb0 ^ 128; }
    { const int r = lane >> 3, c = lane & 7; vs0 = (wq * 16 + r) * 2048 + ct * 256 + kh * 128 + (((c >> 1) ^ ((r >> 1) & 1)) << 5) + ((c & 1) << 4); vs1 = vs0 ^ 64; }
    sla = lb + wq * 4096; sls = lb + wq * 2048;
  }
  template <int LO, bool Y, int I> __device__ __forceinline__ void dma(const Off& o) {
    if (I < 4) { const unsigned sk = (I & 1) ? o.a1 : o.a0; if (Y) DMA_SC1(sact, vaa, sk, (I >> 1) * 112 * 1024, sla, LO + I * 1024); else DMA_SC1(so, vao, sk, (I >> 1) * 32 * 1024, sla, LO + I * 1024); }
    else if (Y) { constexpr int j = I - 4; if (j >> 1) DMA(sw2, vb1, o.b, j * 8192, sla, LO + 16384 + j * 1024); else DMA(sw2, vb0, o.b, j * 8192, sla, LO + 16384 + j * 1024); }
    else if (I < 6) { constexpr int j = I - 4; if (j) DMA(swo, vs1, o.b, j * 16384, sls, LO + 16384 + j * 1024); else DMA(swo, vs0, o.b, j * 16384, sls, LO + 16384 + j * 1024); } }
  template <int U, int I> __device__ __forceinline__ void dma1() { dma<(U & 1) * 65536 + ((U >> 1) & 1) * 32768, true, I>(off_y(U)); }
  template <int U, int I0> __device__ __forceinline__ void dma4() { dma1<U, I0>(); dma1<U, I0 + 1>(); dma1<U, I0 + 2>(); dma1<U, I0 + 3>(); }

  __device__ __forceinline__ void run(const Ptrs& P, u64 tag, int p, int h, const bf16x8 (&eb)[4][2], unsigned& st_c, unsigned& st_r) {
    char* ws = P.ws;
    const int tok = lane & 15, g = lane >> 4, q = (lane >> 2) & 3, pp = lane & 3, wi = wq >> 1, jj = wq & 1;
    const unsigned ra = opq(lb + G * 65536 + wi * 8192 + lane * 16);
    unsigned rb[4], rs[2];
#pragma unroll
    for (int n = 0; n < 4; ++n) { const int f = 4 * (kh ^ (n >> 1)) + 2 * jj + (n & 1); rb[n] = opq(lb + G * 65536 + 16384 + (8 * g + q) * 256 + ((f ^ (q | ((g & 1) << 2))) << 5) + 8 * pp); }
#pragma unroll
    for (int n = 0; n < 2; ++n) { const int f = 2 * jj + n; rs[n] = opq(lb + G * 65536 + 16384 + (8 * g + q) * 128 + ((f ^ ((q >> 1) | ((g & 1) << 1))) << 5) + 8 * pp); }
    f32x4 acc[4][4], as[4][2];
    bf16x8 fa[2][4], fb[2][4];
    auto rdA = [&](auto HR, auto I) { constexpr int i = I(); fa[i >> 2][i & 3] = lds128(ra + HR() * 32768 + (i & 3) * 2048 + (i >> 2) * 1024); SB; };
    auto rdB = [&](auto HR, auto Y, auto I) { constexpr int i = I(), ks = i >> 2, n = i & 3; constexpr bool y = Y();
      if (y) { constexpr int o = HR() * 32768 + ks * 8192; fb[ks][n] = cat(ldstr(rb[n] + o), ldstr(rb[n] + o + 1024)); SB; }
      else if (n < 2) { constexpr int o = HR() * 32768 + ks * 4096; fb[ks][n] = cat(ldstr(rs[n & 1] + o), ldstr(rs[n & 1] + o + 512)); SB; } };
    auto loadh = [&](auto HR, auto YR, auto MD, auto HD, int U, auto hook) { constexpr int md = MD(), LO = (G ^ 1) * 65536 + HD() * 32768; constexpr bool d = md != 0, y = md == 1;
      const Off o = y ? off_y(U) : off_s(U);
      rdA(HR, IC<0>{}); rdB(HR, YR, IC<0>{}); if (d) dma<LO, y, 0>(o); hook(IC<0>{}); SB;
      rdA(HR, IC<1>{}); rdB(HR, YR, IC<1>{}); if (d) dma<LO, y, 4>(o); hook(IC<1>{}); SB;
      rdA(HR, IC<2>{}); rdB(HR, YR, IC<2>{}); if (d) dma<LO, y, 1>(o); hook(IC<2>{}); SB;
      rdA(HR, IC<3>{}); rdB(HR, YR, IC<3>{}); if (d) dma<LO, y, 5>(o); hook(IC<3>{}); SB;
      rdA(HR, IC<4>{}); rdB(HR, YR, IC<4>{}); if (d) dma<LO, y, 2>(o); hook(IC<4>{}); SB;
      rdA(HR, IC<5>{}); rdB(HR, YR, IC<5>{}); if (d) dma<LO, y, 6>(o); hook(IC<5>{}); SB;
      rdA(HR, IC<6>{}); rdB(HR, YR, IC<6>{}); if (d) dma<LO, y, 3>(o); hook(IC<6>{}); SB;
      rdA(HR, IC<7>{}); rdB(HR, YR, IC<7>{}); hook(IC<7>{}); SB; if (d) dma<LO, y, 7>(o); };
    auto load = [&](auto HR, auto YR, auto MD, auto HD, int U) { loadh(HR, YR, MD, HD, U, [](auto) {}); };
    auto mmy_ = [&](auto F) {
#pragma unroll
      for (int m = 0; m < 4; ++m)
#pragma unroll
        for (int n = 0; n < 4; ++n) mk2<F()>(acc[m][n], fb[0][n], fa[0][m], fb[1][n], fa[1][m]);
      SB; };
    auto mms_ = [&](auto F) {
#pragma unroll
      for (int m = 0; m < 4; ++m)
#pragma unroll
        for (int n = 0; n < 2; ++n) mk2<F()>(as[m][n], fb[0][n], fa[0][m], fb[1][n], fa[1][m]);
      SB; };
    auto mmy = [&]() { mmy_(IC<0>{}); }; auto mms = [&]() { mms_(IC<0>{}); };
#define ENDL(n) do { WAIT_V(n); WAIT_L0; BAR; SB; } while (0)
#define ENDM do { BAR; SB; } while (0)
    IC<0> c0; IC<1> c1; IC<2> c2; IC<G> hA; IC<G ^ 1> hB;
    gu64* flags = (gu64*)(ws + WS_FLAG); gu64* tmo = flags + FL_TMO / 8;
    const __amdgpu_buffer_rsrc_t ryx = mk_rsrc(ws + WS_YX, 256 * 32768);
    const unsigned exk = lb + L2_EX + wq * 4096 + lane * 16, exs = lb + 24576 + (wq >> 1) * 32768 + (wq & 1) * 4096 + lane * 16;
    f32x4 tk[4];
    auto give = [&](auto K) { constexpr int k = K();
#pragma unroll
      for (int i = 0; i < 4; ++i) *(LAS f32x4*)(size_t)((k ? exs + G * 65536 : exk + G * 16384) + i * 1024) = acc[2 * (G ^ 1) + (i >> 1)][2 * k + (i & 1)];
      SB; };
    auto take_rd = [&](auto K) { constexpr int k = K();
#pragma unroll
      for (int i = 0; i < 4; ++i) tk[i] = *(const LAS f32x4*)(size_t)((k ? exs + (G ^ 1) * 65536 : exk + (G ^ 1) * 16384) + i * 1024);
      SB; };
    auto take = [&](auto K) { constexpr int k = K();
#pragma unroll
      for (int i = 0; i < 4; ++i) { f32x4& a = acc[2 * G + (i >> 1)][2 * k + (i & 1)]; a += tk[i]; if (k) st16_sc1(__builtin_bit_cast(u32x4, a), ryx, lane * 16, b * 32768 + (G * 4 + wq) * 4096 + i * 1024); else asm volatile("" : "+v"(a)); }
      SB; };
    u64 pv;
    if (G == 0) {
      WAIT_V(EARLYA ? 1 : 9); BAR; SB; STAMP(16);
      load(c0, c1, c1, c1, 3); ENDL(13);
      mmy_(c1); WAIT_V(8); ENDM; STAMP(17);
      load(c1, c1, c1, hA, 5); ENDL(8);
      mmy(); ENDM; load(c0, c1, c1, hB, 7); ENDL(8);
      for (int T = 4; T < 20; T += 4) {
        mmy(); ENDM; load(c1, c1, c1, hA, T + 5); ENDL(8);
        mmy(); ENDM; load(c0, c1, c1, hB, T + 7); ENDL(8);
      }
    } else {
      BAR; SB; STAMP(16); BAR; SB;
      load(c0, c1, c1, c0, 4); ENDL(8);
      mmy_(c1); ENDM; STAMP(17);
      gu64* fa2 = flags + (FL_ACT + 131072) / 8 + (rt * 2 + kh) * 512; gu64* fkv = flags + (FL_KV + (h * 4 + wq) * 1024) / 8;
      POLL_ISSUE(pv, lane < 32 ? fa2 + lane * 16 : fkv + (lane & 7) * 16);
      load(c1, c1, c1, hA, 6); ENDL(9);
      mmy(); ENDM;
      WAIT_V(8); POLL_PIN(pv); if (!__all(pv == tag)) { wait_flags<32, 16>(fa2, tag, tmo, lane); wait_flags<8, 16>(fkv, tag, tmo, lane); } SB;
      const u32x4 skv = mk_srd(ws + WS_KVP, 3u << 20);
      u32x4 t[8], tz; LD16_ISSUE(tz, skv, h * 2048 + (lane & 7) * 256 + wq * 64 + g * 16, (unsigned)(WS_ZP - WS_KVP));
      { const unsigned vkv = h * 32768 + wq * 1024 + lane * 16;
#pragma unroll
        for (int i = 0; i < 8; ++i) LD16_ISSUE(t[i], skv, vkv, i * 4096u); }
      load(c0, c1, c1, hB, 8); ENDL(17);
      mmy(); ENDM;
      WAIT_V(8); asm volatile("" : "+v"(t[0]), "+v"(t[1]), "+v"(t[2]), "+v"(t[3]), "+v"(t[4]), "+v"(t[5]), "+v"(t[6]), "+v"(t[7]), "+v"(tz)); SB;
      float sm[8];
      auto addp = [&](int i, int c) { sm[2 * c] = add1(sm[2 * c], bflo(t[i][c])); sm[2 * c + 1] = add1(sm[2 * c + 1], bfhi(t[i][c])); asm volatile("" : "+v"(sm[2 * c]), "+v"(sm[2 * c + 1])); };
      loadh(c1, c1, c1, hA, 10, [&](auto I) { constexpr int i = I();
        if (i == 0) { _Pragma("unroll") for (int c = 0; c < 4; ++c) { sm[2 * c] = bflo(t[0][c]); sm[2 * c + 1] = bfhi(t[0][c]); asm volatile("" : "+v"(sm[2 * c]), "+v"(sm[2 * c + 1])); } }
        else if (i < 7) { addp((i + 1) >> 1, 2 * (~i & 1)); addp((i + 1) >> 1, 2 * (~i & 1) + 1); }
        else { _Pragma("unroll") for (int r = 0; r < 4; ++r) { float z = __uint_as_float(tz[r]); z += dpp<0xB1>(z); z = opqf(z); z += dpp<0x4E>(z); z = opqf(z); z += dpp<0x141>(z); tz[r] = __float_as_uint(opqf(z)); } asm volatile("" : "+v"(tz)); } });
      ENDL(8);
      mmy(); ENDM;
      const unsigned kva = opq(lb + L2_EX + lane * 16);
      loadh(c0, c1, c1, hB, 12, [&](auto I) { constexpr int i = I(); addp(4 + (i >> 1), 2 * (i & 1)); addp(4 + (i >> 1), 2 * (i & 1) + 1); });
      { const unsigned a = kva + (wq >> 1) * 1024 + (wq & 1) * 8;
        *(LAS u32x2*)(size_t)a = (u32x2){pk2(sm[0], sm[1]), pk2(sm[2], sm[3])}; *(LAS u32x2*)(size_t)(a + 2048) = (u32x2){pk2(sm[4], sm[5]), pk2(sm[6], sm[7])};
        u32x2 au = {pk2(__uint_as_float(tz[0]), __uint_as_float(tz[1])), pk2(__uint_as_float(tz[2]), __uint_as_float(tz[3]))};
        if ((lane & 3) == 1) au = (u32x2){0x3F803F80u, 0x3F803F80u}; else if ((lane & 3) != 0) au = (u32x2){0u, 0u};
        *(LAS u32x2*)(size_t)(a + 4096) = au; SB; }
      ENDL(8);
      mmy(); ENDM;
      bf16x8 ka[6]; f32x4 r6[6]; float ca, cc, o8[8];
      const __amdgpu_buffer_rsrc_t ro = mk_rsrc(ws + WS_O, 128 * 32 * 1024);
      const unsigned vo = (unsigned)((((g & 1) << 1) | (g >> 1)) * 256 + tok * 16) + ((p * 16 + wq * 4) * 32 + h) * 1024;
      auto ostep = [&](int m, int i) { const f32x4 z4 = {0.f, 0.f, 0.f, 0.f};
        if (i == 1) { __builtin_amdgcn_s_setprio(1);
#pragma unroll
          for (int k = 5; k >= 0; --k) { r6[k] = mfma(ka[k], eb[m][k & 1], z4); asm volatile("" : "+v"(r6[k])); }
          __builtin_amdgcn_s_setprio(0); }
        if (i == 2) { ca = rcp(r6[4][1]); cc = rcp(r6[5][1]); asm volatile("" : "+v"(ca), "+v"(cc)); }
        if (i == 3) { const float inv = rcp(opqf(r6[4][0] * ca + 1e-6f) + r6[5][0] * cc); ca = opqf(ca * inv); cc = opqf(cc * inv); asm volatile("" : "+v"(ca), "+v"(cc)); }
        if (i == 4 || i == 5) { _Pragma("unroll") for (int r = 0; r < 4; ++r) { const int d = i - 4; o8[4 * d + r] = opqf(r6[2 * d][r] * ca) + r6[2 * d + 1][r] * cc; asm volatile("" : "+v"(o8[4 * d + r])); } }
        if (i == 6) st16_sc1(swap16(pk2(o8[0], o8[1]), pk2(o8[2], o8[3]), pk2(o8[4], o8[5]), pk2(o8[6], o8[7])), ro, vo, m * 32768); };
#pragma unroll
      for (int i = 0; i < 6; ++i) ka[i] = lds128(kva + i * 1024);
      SB;
      loadh(c1, c1, c1, hA, 14, [&](auto I) { ostep(0, I()); }); ENDL(9);
      mmy(); ENDM; loadh(c0, c1, c1, hB, 16, [&](auto I) { ostep(1, I()); }); ENDL(9);
      mmy(); ENDM; loadh(c1, c1, c1, hA, 18, [&](auto I) { ostep(2, I()); }); ENDL(9);
      mmy(); ENDM; loadh(c0, c1, c1, hB, 20, [&](auto I) { ostep(3, I()); }); ENDL(9);
      mmy(); ENDM; load(c1, c1, c1, hA, 22); WAIT_V(8);
      set_flag(flags + FL_O / 8 + (2 * p + (wq >> 1)) * 64 + h * 2 + (wq & 1), tag, lane); WAIT_L0; ENDM;
      mmy(); ENDM; load(c0, c1, c1, hB, 24); ENDL(8);
    }
    STAMP(18);
    gu64* po = flags + FL_O / 8 + rt * 64;
    mmy(); ENDM; if (G == 1) POLL_ISSUE(pv, po + lane); else if (PFCODE & 4) touch_code(lb + L2_EX + 8192, lane, wq); load(c1, c1, c1, hA, 25 + G); ENDL((G == 1 || (PFCODE & 4)) ? 9 : 8);
    mmy(); ENDM;
    if (G == 0) { load(c0, c1, c1, hB, 27); ENDL(8); }
    else { WAIT_V(8); POLL_PIN(pv); if (!__all(pv == tag)) wait_flags<64, 1>(po, tag, tmo, lane); SB; load(c0, c1, c2, hB, 28); ENDL(6); }
    mmy(); ENDM; load(c1, c1, c2, hA, 29 + G); ENDL(6);
    gu64* fy = flags + FL_YX / 8 + b * 8 + G * 4 + wq;
    mmy(); ENDM; STAMP(19); load(c0, c0, c2, hB, 31 + G); ENDL(6); STAMP(25);
    if (G == 0) {
      mms_(c1); give(c1); WAIT_L0; ENDM; load(c1, c0, c2, hA, 33); ENDL(6); STAMP(26);
      mms(); take_rd(c1); take(c1); WAIT_L0; ENDM; load(c0, c0, c2, hB, 35); ENDL(10); STAMP(27);
      mms(); give(c0); WAIT_L0; ENDM; load(c1, c0, c2, hA, 37); WAIT_V(6); set_flag(fy, tag, lane); WAIT_L0; ENDM; STAMP(28);
    } else {
      mms_(c1); take_rd(c1); give(c1); take(c1); WAIT_L0; ENDM; load(c1, c0, c2, hA, 34); ENDL(10); STAMP(26);
      mms(); give(c0); WAIT_L0; ENDM; load(c0, c0, c2, hB, 36); WAIT_V(6); set_flag(fy, tag, lane); WAIT_L0; ENDM; STAMP(27);
      mms(); take_rd(c0); take(c0); WAIT_L0; ENDM; load(c1, c0, c2, hA, 38); ENDL(6); STAMP(28);
    }
    u32x4 py[4], xr[2];
    const unsigned vout = (rt * 128 + wi * 64 + 2 * G * 16 + tok) * 2048 + ct * 256 + kh * 128 + jj * 64 + (((g & 1) << 1) | (g >> 1)) * 16;
    mms(); if (G == 0) { take_rd(c0); take(c0); WAIT_L0; } ENDM; { const u32x4 sx = mk_srd(P.x, S_ * D_ * 2); LD16_ISSUE(xr[0], sx, vout, 0u); LD16_ISSUE(xr[1], sx, vout, 32768u); } load(c0, c0, c2, hB, 39 + G); ENDL(8); STAMP(29);
    gu64* py_ = flags + FL_YX / 8 + (b ^ 1) * 8 + G * 4 + wq;
    mms(); ENDM; if (G == 1) POLL_ISSUE(pv, py_); load(c1, c0, c2, hA, 41 + G); ENDL(6 + G); STAMP(20);
    auto fetch = [&]() { WAIT_V(6); POLL_PIN(pv); if (!__all(pv == tag)) wait_flags<1, 1>(py_, tag, tmo, lane); SB;
      const u32x4 syx = mk_srd(ws + WS_YX, 256 * 32768); const unsigned vy = (b ^ 1) * 32768 + (G * 4 + wq) * 4096 + lane * 16;
#pragma unroll
      for (int i = 0; i < 4; ++i) LD16_ISSUE(py[i], syx, vy, i * 1024u);
      };
    f32x2 gt[8], xv[8];
    auto unpack = [&](auto I) { constexpr int i = I();
      if (i == 0) asm volatile("" : "+v"(xr[0]), "+v"(xr[1]));
      if constexpr (i < 2) { const u32x4 xs = swap16(xr[i][0], xr[i][1], xr[i][2], xr[i][3]);
        _Pragma("unroll") for (int c = 0; c < 4; ++c) { xv[4 * i + c] = (f32x2){bflo(xs[c]), bfhi(xs[c])}; asm volatile("" : "+v"(xv[4 * i + c])); } } };
    auto gate = [&]() { WAIT_V(0); asm volatile("" : "+v"(py[0]), "+v"(py[1]), "+v"(py[2]), "+v"(py[3])); SB; f32x2 y[8], e[8];
#define ST8(stmt) _Pragma("unroll") for (int i = 0; i < 8; ++i) { stmt; } SB
      ST8(const f32x4 a = acc[2 * G + (i >> 2)][(i >> 1) & 1]; const f32x4 c = __builtin_bit_cast(f32x4, py[i >> 1]); y[i] = (i & 1) ? a.zw + c.zw : a.xy + c.xy);
      ST8(e[i] = opq2(y[i] * -LOG2E));
      ST8(e[i] = opq2((f32x2){ex2(e[i][0]), ex2(e[i][1])}));
      ST8(e[i] = opq2(e[i] + 1.0f));
      ST8(e[i] = opq2((f32x2){rcp(e[i][0]), rcp(e[i][1])}));
      ST8(gt[i] = y[i] * e[i]; asm volatile("" : "+v"(gt[i]))); };
    auto pass = [&]() {
#pragma unroll
      for (int i = 0; i < 4; ++i) *(LAS f32x4*)(size_t)(exk + G * 16384 + i * 1024) = as[2 * (G ^ 1) + (i >> 1)][i & 1];
      SB; };
    if (G == 0) {
      mms(); ENDM; POLL_ISSUE(pv, py_); load(c0, c0, c2, hB, 43); ENDL(7); STAMP(30);
      mms(); ENDM; fetch(); loadh(c1, c0, c0, hA, 0, unpack); ENDL(4); STAMP(31);
      mms(); MFMA_PAD; STAMP(21); ENDM;
      pass(); gate();
    } else {
      mms(); ENDM; fetch(); loadh(c0, c0, c0, hB, 0, unpack); ENDL(4); STAMP(30);
      mms(); ENDM; load(c1, c0, c0, hA, 0); gate(); WAIT_L0; ENDM; STAMP(31);
      mms(); MFMA_PAD; STAMP(21);
      pass();
    }
    WAIT_L0; ENDM; STAMP(22);
    const __amdgpu_buffer_rsrc_t rout = mk_rsrc(P.out, S_ * D_ * 2);
#pragma unroll
    for (int i = 0; i < 4; ++i) tk[i] = *(const LAS f32x4*)(size_t)(exk + (G ^ 1) * 16384 + i * 1024);
#pragma unroll
    for (int ml = 0; ml < 2; ++ml) { unsigned ow[4];
#pragma unroll
      for (int n = 0; n < 2; ++n) { const int i = ml * 2 + n; const f32x4 s = as[2 * G + ml][n] + tk[i];
        const f32x2 o0 = s.xy * gt[2 * i] + xv[2 * i], o1 = s.zw * gt[2 * i + 1] + xv[2 * i + 1];
        ow[2 * n] = pk2(o0[0], o0[1]); ow[2 * n + 1] = pk2(o1[0], o1[1]); }
      st16(swap16(ow[0], ow[1], ow[2], ow[3]), rout, vout, ml * 16 * 2048); }
    if (G == 0 && b == 0 && wq == 0) set_flag(flags + FL_EPOCH / 8, tag, lane);
    if (G == 0) asm volatile(".Lend_g0:"); else asm volatile(".Lend_g1:");
    STAMP(23);
    WAIT_V(0); STAMP(24); STAMP_OUT(G * 4 + wq);
  }
};
#define P2RUN(p2, P, tag, eb) p2.run(P, tag, p, h, eb, st_c, st_r)

__global__ __launch_bounds__(512, 2) void k_fused(const u16* x, const u16* wqkv, const u16* wphi, const u16* wo, const u16* w1, const u16* b1, const u16* w2, char* ws, u16* out) {
  __shared__ __attribute__((aligned(16))) char smem[163840];
  const Ptrs P = {x, wqkv, wphi, wo, w1, b1, w2, ws, out};
  const unsigned lb = (unsigned)(size_t)(LAS char*)smem;
  const int tid = threadIdx.x, lane = tid & 63, w = __builtin_amdgcn_readfirstlane(tid >> 6), wx = w & 3;
  const int b = blockIdx.x, h = (b & 7) * 4 + ((b >> 3) & 3), p = b >> 5, odd = h & 1;
  const int tok = lane & 15, g = lane >> 4, q = (lane >> 2) & 3, pp = lane & 3;
  const unsigned ra0 = lb + L1_A + (wx * 64 + tok) * 128 + ((g ^ (tok >> 1)) << 4), ra1 = ra0 ^ 64;
  gu64* flags = (gu64*)(ws + WS_FLAG); gu64* tmo = flags + FL_TMO / 8;
  asm volatile(".Lcode_begin:");
  STAMP_DECL; STAMP(0);
  const int rot = p < 7 ? 2 * p : 0;
  auto kt = [&](int pos) { int t = pos + rot; t -= t >= 16 ? 14 : 0; return pos < 2 ? pos : t; };
  u64 ep; { gu64* pe = flags + FL_EPOCH / 8; POLL_ISSUE(ep, pe); }
  if ((PFCODE & 1) && w < 4) touch_code(lb + L1_DUMP, lane, wx);
  const __amdgpu_buffer_rsrc_t rfl = mk_rsrc(ws + WS_FLAG, 1u << 20);
  bf16x8 ones = {0x3F80, 0x3F80, 0x3F80, 0x3F80, 0x3F80, 0x3F80, 0x3F80, 0x3F80}; asm volatile("" : "+v"(ones));
  auto cs_publish = [&](const f32x4& c0, const f32x4& c1, int j0, bool two, u64 tag) {
    const f32x4 a = (tok & 2) ? c1 : c0; const u32x4 v = {__float_as_uint((tok & 1) ? a[2] : a[0]), (unsigned)tag, __float_as_uint((tok & 1) ? a[3] : a[1]), (unsigned)tag};
    if (tok < (two ? 4 : 2)) st16_sc1(v, rfl, ((j0 + 2 * (tok & 2)) * 16 + 4 * g + 2 * (tok & 1)) * 8, GR_CS + (h * 8 + p) * 1664);
    SB; };

  if (w < 4) {
    const u32x4 sa = mk_srd(x, S_ * D_ * 2);
    unsigned va0, va1;
    { const int r = lane >> 3; va0 = (p * 256 + wx * 64 + r) * 2048 + (((lane & 7) ^ (r >> 1)) << 4); va1 = va0 ^ 64; }
    const unsigned sl = lb + L1_A + wx * 8192;
    const unsigned rb0 = opq(lb + L1_BH + (8 * g + 4 * (g & 1) + q) * 224 + 8 * pp), rb1 = opq(lb + L1_BH + (8 * g + 4 * ((g & 1) ^ 1) + q) * 224 + 8 * pp);
    f32x4 acc[4][7], cs0, cs1;
    bf16x8 fa[2][4], fb[2][7];
    auto dmaA = [&](auto ST, auto J, unsigned sk) { constexpr int st = ST(), j = J(); if (j & 1) DMA(sa, va1, sk, j * 16384, sl, st * 32768 + j * 1024); else DMA(sa, va0, sk, j * 16384, sl, st * 32768 + j * 1024); };
    auto rdA = [&](auto ST, auto KS, auto M) { constexpr int st = ST(), ks = KS(), m = M(); fa[ks][m] = lds128((ks ? ra1 : ra0) + st * 32768 + m * 2048); SB; };
    auto rdB = [&](auto ST, auto KS, auto J) { constexpr int st = ST(), ks = KS(), j = J(); constexpr int o = st * 14336 + ks * 7168 + j * 32; fb[ks][j] = cat(ldstr(rb0 + o), ldstr(rb1 + o)); SB; };
    auto load = [&](auto ST, auto DO, unsigned sk) {
      constexpr int o = ST() ^ 1; constexpr bool d = DO();
      rdA(ST, IC<0>{}, IC<0>{}); rdB(ST, IC<0>{}, IC<0>{}); rdB(ST, IC<0>{}, IC<1>{}); if (d) dmaA(IC<o>{}, IC<0>{}, sk);
      rdA(ST, IC<0>{}, IC<1>{}); rdB(ST, IC<0>{}, IC<2>{}); rdB(ST, IC<0>{}, IC<3>{}); if (d) dmaA(IC<o>{}, IC<1>{}, sk);
      rdA(ST, IC<0>{}, IC<2>{}); rdB(ST, IC<0>{}, IC<4>{}); rdB(ST, IC<0>{}, IC<5>{}); if (d) dmaA(IC<o>{}, IC<2>{}, sk);
      rdA(ST, IC<0>{}, IC<3>{}); rdB(ST, IC<0>{}, IC<6>{}); rdB(ST, IC<1>{}, IC<0>{}); if (d) dmaA(IC<o>{}, IC<3>{}, sk);
      rdA(ST, IC<1>{}, IC<0>{}); rdB(ST, IC<1>{}, IC<1>{}); rdB(ST, IC<1>{}, IC<2>{}); if (d) dmaA(IC<o>{}, IC<4>{}, sk);
      rdA(ST, IC<1>{}, IC<1>{}); rdB(ST, IC<1>{}, IC<3>{}); rdB(ST, IC<1>{}, IC<4>{}); if (d) dmaA(IC<o>{}, IC<5>{}, sk);
      rdA(ST, IC<1>{}, IC<2>{}); rdB(ST, IC<1>{}, IC<5>{}); if (d) dmaA(IC<o>{}, IC<6>{}, sk);
      rdA(ST, IC<1>{}, IC<3>{}); rdB(ST, IC<1>{}, IC<6>{}); if (d) dmaA(IC<o>{}, IC<7>{}, sk);
    };
    auto mm_ = [&](auto F) {
#pragma unroll
      for (int m = 0; m < 4; ++m)
#pragma unroll
        for (int j = 0; j < 7; ++j) mk2<F()>(acc[m][j], fb[0][j], fa[0][m], fb[1][j], fa[1][m]);
      SB; };
    auto mm = [&]() { mm_(IC<0>{}); };
#define A8(ST, sk) dmaA(IC<ST>{}, IC<0>{}, sk); dmaA(IC<ST>{}, IC<1>{}, sk); dmaA(IC<ST>{}, IC<2>{}, sk); dmaA(IC<ST>{}, IC<3>{}, sk); dmaA(IC<ST>{}, IC<4>{}, sk); dmaA(IC<ST>{}, IC<5>{}, sk); dmaA(IC<ST>{}, IC<6>{}, sk); dmaA(IC<ST>{}, IC<7>{}, sk)
    if (wx == 0 && TOUCH0) { const int l = min(lane, 51), ts = l / 26, rem = l - ts * 26, rr = rem / 13, li = rem - rr * 13, k = ts * 64 + (b >> 3) * 2 + rr;
      TOUCH(li < 6 ? (const GAS char*)wqkv + k * 6144 + (li >> 1) * 2048 + (b & 7) * 256 + (li & 1) * 128 : (const GAS char*)w1 + k * 7168 + (b & 7) * 896 + (li - 6) * 128, lb + L1_DUMP); }
    if (wx == 1 && TOUCH0) TOUCH((const GAS char*)x + (p * 256 + ((b >> 3) & 3) * 64 + lane) * 2048 + (b & 1) * 128, lb + L1_DUMP);
    A8(0, 0u);
    if (wx == 0) asm volatile("s_mov_b32 m0, %2\n\ts_nop 0\n\tbuffer_load_dword %0, %1, 0 offen lds" :: "v"(h * 224 + lane * 4), "s"(mk_srd(b1, F_ * 2)), "s"(lb + L1_BIAS) : "memory");
    cs0 = zacc(); cs1 = zacc();
    auto colsum = [&](bool now) { colsum7(cs0, cs1, fb[0], fb[1], ones, now ? wx : 4); SB; };
    if (tid == 0) *(LAS u64*)(size_t)(lb + L1_CNT) = 0ull;
    STAMP(1);
    WAIT_V(0); POLL_PIN(ep); const u64 tag = uni64(ep) + 1;
    A8(1, 128u);
    WAIT_L0; BAR; SB; STAMP(4);
    load(IC<0>{}, IC<0>{}, 0u); WAIT_L0; BAR; SB; STAMP(5);
#pragma unroll
    for (int t = 0; t < 4; t += 2) {
      if (t == 0) mm_(IC<1>{}); else mm();
      colsum((p == 7) == (t == 0)); WAIT_V(0); BAR; SB;
      load(IC<1>{}, IC<1>{}, (unsigned)kt(t + 2) * 128u); WAIT_L0; BAR; SB;
      mm(); colsum((p == 7) == (t == 0)); WAIT_V(0); BAR; SB;
      load(IC<0>{}, IC<1>{}, (unsigned)kt(t + 3) * 128u); if (t == 2) cs_publish(cs0, cs1, wx, wx < 3, tag); WAIT_L0; BAR; SB;
    }
    for (int t = 4; t < 14; t += 2) {
      mm(); WAIT_V(0); BAR; SB;
      load(IC<1>{}, IC<1>{}, (unsigned)kt(t + 2) * 128u); WAIT_L0; BAR; SB;
      mm(); WAIT_V(0); BAR; SB;
      load(IC<0>{}, IC<1>{}, (unsigned)kt(t + 3) * 128u); WAIT_L0; BAR; SB;
    }
    STAMP(6);
    mm(); WAIT_V(0); BAR; SB;
    u32x2 bias[7];
#pragma unroll
    for (int j = 0; j < 7; ++j) bias[j] = *(const LAS u32x2*)(size_t)(lb + L1_BIAS + (j < 6 ? j + odd : (odd ? 0 : 6)) * 32 + g * 8);
    SB;
    { const unsigned dump = lb + L1_DUMP; const int cu = b >> 3, a = (b & 7) >> 1;
      const int l2 = min(lane, 55) + 56 * (wx + 4 * cu);
      if (PFCODE & 2) touch_code(dump, lane, wx);
      if (TOUCHW) TOUCH((const GAS char*)w2 + ((b & 1) * 1792 + a * 448) * 2048 + l2 * 128, dump);
      if (TOUCHW) TOUCH((const GAS char*)wo + (b * 64 + wx * 16 + (lane & 15)) * 128, dump); }
    load(IC<1>{}, IC<0>{}, 0u);
    f32x4 bl[7];
#pragma unroll
    for (int j = 0; j < 7; ++j) { bl[j] = (f32x4){opqf(bflo(bias[j][0]) * LOG2E), opqf(bfhi(bias[j][0]) * LOG2E), opqf(bflo(bias[j][1]) * LOG2E), opqf(bfhi(bias[j][1]) * LOG2E)}; asm volatile("" : "+v"(bl[j])); }
    WAIT_L0; BAR; SB;
    mm(); BAR; MFMA_PAD; STAMP(7);
    f32x4 cs[7]; float rl[4], rml[4];
#pragma unroll
    for (int j = 0; j < 7; ++j) cs[j] = *(const LAS f32x4*)(size_t)(lb + L1_CS + j * 64 + g * 16);
#pragma unroll
    for (int m = 0; m < 4; ++m) { const f32x2 st = *(const LAS f32x2*)(size_t)(lb + L1_ST + (wx * 64 + m * 16 + tok) * 8); rl[m] = opqf(st[0] * LOG2E); rml[m] = opqf(-rl[m] * st[1]); }
    SB;
    Phase2<0> p2; p2.init(lb, lane, wx, P);
    const __amdgpu_buffer_rsrc_t ract = mk_rsrc(ws + WS_ACT, 128 * 112 * 1024);
    auto mish8 = [&](int m0, int j0, int m1, int j1) -> u32x4 {
      const f32x2 a[4] = {acc[m0][j0].xy, acc[m0][j0].zw, acc[m1][j1].xy, acc[m1][j1].zw}, c[4] = {cs[j0].xy, cs[j0].zw, cs[j1].xy, cs[j1].zw}, bb[4] = {bl[j0].xy, bl[j0].zw, bl[j1].xy, bl[j1].zw};
      f32x2 t[4], hs[4], e[4], d[4]; unsigned o[4];
#define ST4(stmt) _Pragma("unroll") for (int i = 0; i < 4; ++i) { stmt; } SB
      ST4(t[i] = c[i] * rml[i < 2 ? m0 : m1] + bb[i]);
      ST4(hs[i] = a[i] * rl[i < 2 ? m0 : m1] + t[i]);
      ST4(e[i] = opq2((f32x2){ex2(hs[i][0]), ex2(hs[i][1])}));
      ST4(t[i] = e[i] + 2.0f);
      ST4(d[i] = opq2(e[i] * t[i] + 2.0f));
      ST4(e[i] = opq2((f32x2){rcp(d[i][0]), rcp(d[i][1])}));
      ST4(t[i] = e[i] * (-2.0f / LOG2E) + 1.0f / LOG2E);
      ST4(d[i] = hs[i] * t[i]);
      ST4(o[i] = pk2(d[i][0], d[i][1]));
      return swap16(o[0], o[1], o[2], o[3]); };
    const unsigned vpair = (unsigned)((((g & 1) << 1) | (g >> 1)) * 256 + tok * 16), mb0 = p * 16 + wx * 4;
    auto pair = [&](auto J, auto M) { constexpr int j = J(), m = M();
      st16_sc1(mish8(m, j, m, j + 1), ract, vpair, ((mb0 + m) * 112 + ((7 * h + odd + j) >> 1)) * 1024); SB;
      constexpr int n = 2 * j + m - BLATE; if constexpr (n >= 0 && n < 6) { p2.dma1<n / 2, 4 + 2 * (n & 1)>(); p2.dma1<n / 2, 5 + 2 * (n & 1)>(); } };
    gu64* fact = flags + FL_ACT / 8 + (((2 * p + (wx >> 1)) * 2 + (h >> 4)) * 32 + (h & 15) * 2 + (wx & 1)) * 16;
    STAMP(13);
    if (XP0) __builtin_amdgcn_s_setprio(2);
    pair(IC<0>{}, IC<0>{}); pair(IC<0>{}, IC<1>{}); pair(IC<0>{}, IC<2>{}); pair(IC<0>{}, IC<3>{});
    if (XP0) __builtin_amdgcn_s_setprio(0);
    STAMP(14);
    pair(IC<2>{}, IC<0>{}); pair(IC<2>{}, IC<1>{});
    STAMP(15);
    WAIT_V(2); set_flag(fact, tag, lane); SB; STAMP(8);
    gu64* fa1 = flags + FL_ACT / 8 + (p2.rt * 2 + p2.kh) * 512; u64 pv;
    const unsigned vsing = (unsigned)((g & 1) * 112 * 1024 + (2 * odd + (g >> 1)) * 256 + tok * 16);
    auto single = [&](int m) {
      st16_sc1(mish8(m, 6, m + 1, 6), ract, vsing, ((mb0 + m) * 112 + ((7 * h + (odd ? 0 : 6)) >> 1)) * 1024); SB; };
    auto geta = [&]() { POLL_PIN(pv); STAMP(10); if (!__all(pv == tag)) wait_flags<32, 16>(fa1, tag, tmo, lane);
      SB; STAMP(11); p2.dma4<0, 0>(); p2.dma4<1, 0>(); p2.dma4<2, 0>(); };
    pair(IC<2>{}, IC<2>{}); pair(IC<2>{}, IC<3>{});
    if (EARLYA == 0) {
      pair(IC<4>{}, IC<0>{}); pair(IC<4>{}, IC<1>{}); pair(IC<4>{}, IC<2>{}); pair(IC<4>{}, IC<3>{});
      POLL_ISSUE(pv, fa1 + (lane & 31) * 16); single(0); single(2);
      STAMP(9); WAIT_V(2); geta(); WAIT_V(12);
    } else {
      pair(IC<4>{}, IC<0>{}); POLL_ISSUE(pv, fa1 + (lane & 31) * 16); pair(IC<4>{}, IC<1>{}); pair(IC<4>{}, IC<2>{});
      WAIT_V(4); geta(); pair(IC<4>{}, IC<3>{}); single(0); single(2); STAMP(9); WAIT_V(0);
    }
    set_flag(fact + 131072 / 8, tag, lane); SB; STAMP(12);
    const bf16x8 none[4][2] = {};
    P2RUN(p2, P, tag, none);
  } else {
    const int srow = p * 256 + h * 8 + wx * 2; u32x4 xa[4];
    { const u32x4 sx = mk_srd(x, S_ * D_ * 2); const unsigned vx = srow * 2048 + lane * 16;
      asm volatile("buffer_load_dwordx4 %0, %4, %5, 0 offen\n\tbuffer_load_dwordx4 %1, %4, %5, 0 offen offset:1024\n\tbuffer_load_dwordx4 %2, %4, %5, 0 offen offset:2048\n\tbuffer_load_dwordx4 %3, %4, %5, 0 offen offset:3072"
                   : "=&v"(xa[0]), "=&v"(xa[1]), "=&v"(xa[2]), "=&v"(xa[3]) : "v"(vx), "s"(sx) : "memory"); SB; }
    const u32x4 sq = mk_srd(wqkv, D_ * 3 * D_ * 2), sh = mk_srd(w1, D_ * F_ * 2), sp = mk_srd(wphi, 32 * 32 * 32 * 2);
    unsigned vq, vh[4];
    { const int r = lane >> 2; vq = (wx * 16 + r) * 6144 + h * 64 + (((lane & 3) ^ (((r >> 3) & 1) << 1)) << 4); }
#pragma unroll
    for (int i = 0; i < 4; ++i) { const int o = (wx + 4 * i) * 1024 + lane * 16, rp = o / 224, c = (o - rp * 224) >> 4, k = rp ^ (((rp >> 3) & 1) << 2); int cs = c + 2 * odd; if (cs >= 14) cs -= 14;
      vh[i] = k * 7168 + h * 224 + cs * 16; }
    const unsigned slq = lb + L1_BQ + wx * 1024, slh = lb + L1_BH + wx * 1024;
    const unsigned rq0 = opq(lb + L1_BQ + (8 * g + q) * 64 + 32 * (g & 1) + 8 * pp), rq1 = opq(rq0 ^ 32);
    f32x4 acc[3][4][2], cs0, cs1;
    bf16x8 fa[2][4], fb[2][6];
    auto dmaQ = [&](auto ST, auto U, unsigned sk) { constexpr int st = ST(), u = U(); DMA(sq, vq, sk, u * 2048, slq, st * 12288 + u * 4096); };
    auto dmaH = [&](auto ST, auto I, unsigned sk) { constexpr int st = ST(), i = I(); if (i < 3 || wx < 2) DMA(sh, vh[i], sk, 0, slh, st * 14336 + i * 4096); };
    auto rdA = [&](auto ST, auto KS, auto M) { constexpr int st = ST(), ks = KS(), m = M(); fa[ks][m] = lds128((ks ? ra1 : ra0) + st * 32768 + m * 2048); SB; };
    auto rdB = [&](auto ST, auto KS, auto J) { constexpr int st = ST(), ks = KS(), j = J(); constexpr int o = st * 12288 + (j >> 1) * 4096 + ks * 2048; const unsigned a = (j & 1) ? rq1 : rq0;
      fb[ks][j] = cat(ldstr(a + o), ldstr(a + o + 256)); SB; };
    auto load = [&](auto ST, auto DQ, auto DH, int t) {
      constexpr int o = ST() ^ 1; constexpr bool dq = DQ(), dh = DH(); const unsigned skq = (unsigned)kt(t + 1) * (64 * 6144), skh = (unsigned)kt(t + 2) * (64 * 7168);
      rdA(ST, IC<0>{}, IC<0>{}); rdB(ST, IC<0>{}, IC<0>{}); rdB(ST, IC<0>{}, IC<1>{}); if (dq) dmaQ(IC<o>{}, IC<0>{}, skq);
      rdA(ST, IC<0>{}, IC<1>{}); rdB(ST, IC<0>{}, IC<2>{}); rdB(ST, IC<0>{}, IC<3>{}); if (dq) dmaQ(IC<o>{}, IC<1>{}, skq);
      rdA(ST, IC<0>{}, IC<2>{}); rdB(ST, IC<0>{}, IC<4>{}); rdB(ST, IC<0>{}, IC<5>{}); if (dq) dmaQ(IC<o>{}, IC<2>{}, skq);
      rdA(ST, IC<0>{}, IC<3>{}); rdB(ST, IC<1>{}, IC<0>{}); if (dh) dmaH(ST, IC<0>{}, skh);
      rdA(ST, IC<1>{}, IC<0>{}); rdB(ST, IC<1>{}, IC<1>{}); rdB(ST, IC<1>{}, IC<2>{}); if (dh) dmaH(ST, IC<1>{}, skh);
      rdA(ST, IC<1>{}, IC<1>{}); rdB(ST, IC<1>{}, IC<3>{}); if (dh) dmaH(ST, IC<2>{}, skh);
      rdA(ST, IC<1>{}, IC<2>{}); rdB(ST, IC<1>{}, IC<4>{});
      rdA(ST, IC<1>{}, IC<3>{}); rdB(ST, IC<1>{}, IC<5>{});
      if (dh) { if (wx < 2) { dmaH(ST, IC<3>{}, skh); WAIT_V(7); } else WAIT_V(6); }
    };
    auto mm_ = [&](auto F) {
#pragma unroll
      for (int m = 0; m < 4; ++m)
#pragma unroll
        for (int j = 0; j < 6; ++j) mk2<F()>(acc[j >> 1][m][j & 1], fb[0][j], fa[0][m], fb[1][j], fa[1][m]);
      SB; };
    auto mm = [&]() { mm_(IC<0>{}); };
#define H4(ST, sk) dmaH(IC<ST>{}, IC<0>{}, sk); dmaH(IC<ST>{}, IC<1>{}, sk); dmaH(IC<ST>{}, IC<2>{}, sk); dmaH(IC<ST>{}, IC<3>{}, sk)
#define Q3(ST, sk) dmaQ(IC<ST>{}, IC<0>{}, sk); dmaQ(IC<ST>{}, IC<1>{}, sk); dmaQ(IC<ST>{}, IC<2>{}, sk)
    H4(0, 0u); Q3(0, 0u);
    cs0 = zacc(); cs1 = zacc();
    auto colsum = [&](bool now) { const bf16x8 a[7] = {fb[0][0], fb[0][1], fb[0][2], fb[0][3], fb[0][4], fb[0][5], ones}, c[7] = {fb[1][0], fb[1][1], fb[1][2], fb[1][3], fb[1][4], fb[1][5], ones};
      colsum7(cs0, cs1, a, c, ones, now ? wx : 4); SB; };
    STAMP(1);
    if (wx < 2) WAIT_V(7); else WAIT_V(6);
    asm volatile("" : "+v"(xa[0]), "+v"(xa[1]), "+v"(xa[2]), "+v"(xa[3]), "+v"(ep));
    const u64 tag = uni64(ep) + 1;
    { float v[2][16], sm[2], sq_[2], mean[2];
#pragma unroll
      for (int r = 0; r < 2; ++r) { sm[r] = 0.f;
#pragma unroll
        for (int i = 0; i < 8; ++i) { const unsigned u = xa[2 * r + (i >> 2)][i & 3]; v[r][2 * i] = bflo(u); v[r][2 * i + 1] = bfhi(u); sm[r] += v[r][2 * i] + v[r][2 * i + 1]; } }
#pragma unroll
      for (int r = 0; r < 2; ++r) { mean[r] = wave_sum(sm[r]) * (1.0f / D_); sq_[r] = 0.f;
#pragma unroll
        for (int i = 0; i < 16; ++i) { const float d = v[r][i] - mean[r]; sq_[r] += d * d; } }
      const float r0 = __builtin_amdgcn_rsqf(wave_sum(sq_[0]) * (1.0f / D_) + 1e-5f), r1 = __builtin_amdgcn_rsqf(wave_sum(sq_[1]) * (1.0f / D_) + 1e-5f);
      const u32x4 gr = {__float_as_uint(lane ? mean[1] : mean[0]), (unsigned)tag, __float_as_uint(lane ? r1 : r0), (unsigned)tag};
      if (lane < 2) st16_sc1(gr, rfl, lane * 16, GR_ST + srow * 16);
      SB; }
    STAMP(2);
    WAIT_V(1);
    STAMP(3); BAR; SB; STAMP(4);
    if (wx < 2) { const unsigned vp = h * 2048 + wx * 1024 + lane * 16, slp = lb + L1_WPHI + wx * 1024; DMA(sp, vp, 0u, 0, slp, 0); }
    H4(1, 64u * 7168u); Q3(1, 64u * 6144u);
    BAR; SB; STAMP(5);
    load(IC<0>{}, IC<0>{}, IC<1>{}, 0); WAIT_L0; BAR; SB;
    mm_(IC<1>{}); colsum(p == 7); WAIT_V(3); BAR; SB;
    load(IC<1>{}, IC<1>{}, IC<1>{}, 1); WAIT_L0; BAR; SB;
    mm(); colsum(p == 7); WAIT_V(3); BAR; SB;
    load(IC<0>{}, IC<1>{}, IC<1>{}, 2); WAIT_L0; BAR; SB;
    mm(); colsum(p != 7); WAIT_V(3); BAR; SB;
    load(IC<1>{}, IC<1>{}, IC<1>{}, 3); WAIT_L0; BAR; SB;
    mm(); colsum(p != 7); WAIT_V(3); BAR; MFMA_PAD;
    cs_publish(cs0, cs1, 7 + wx, wx < 2, tag);
    for (int t = 4; t < 14; t += 2) {
      load(IC<0>{}, IC<1>{}, IC<1>{}, t); WAIT_L0; BAR; SB;
      mm(); WAIT_V(3); BAR; SB;
      load(IC<1>{}, IC<1>{}, IC<1>{}, t + 1); WAIT_L0; BAR; SB;
      mm(); WAIT_V(3); BAR; SB;
    }
    STAMP(6);
    const unsigned vst = GR_ST + (p * 256 + wx * 64 + lane) * 16, vcs = GR_CS + h * (8 * 1664) + min(wx * 64 + lane, 103) * 16;
    const u32x4 sfl = mk_srd(ws + WS_FLAG, 1u << 20);
    u32x4 gs, gc[8]; LD16_ISSUE(gs, sfl, vst, 0u);
#pragma unroll
    for (int i = 0; i < 8; ++i) LD16_ISSUE(gc[i], sfl, vcs, i * 1664u);
    load(IC<0>{}, IC<1>{}, IC<0>{}, 14);
    WAIT_V(12); WAIT_L0; BAR; SB;
    mm(); WAIT_V(0); BAR; SB;
    load(IC<1>{}, IC<0>{}, IC<0>{}, 15);
    { auto stale = [&]() { unsigned d = differ(gs[1], gs[3], (unsigned)tag);
#pragma unroll
        for (int i = 0; i < 8; ++i) d |= differ(gc[i][1], gc[i][3], (unsigned)tag);
        return !__all(d == 0u); };
      asm volatile("" : "+v"(gs), "+v"(gc[0]), "+v"(gc[1]), "+v"(gc[2]), "+v"(gc[3]), "+v"(gc[4]), "+v"(gc[5]), "+v"(gc[6]), "+v"(gc[7]));
      if (__builtin_expect(stale(), 0)) {
#pragma nounroll
        for (unsigned spins = 0; spins < (1u << 16); ++spins) {
          asm volatile("buffer_load_dwordx4 %0, %1, %2, 0 offen sc1" : "+v"(gs) : "v"(vst), "s"(sfl) : "memory");
#pragma unroll
          for (int i = 0; i < 8; ++i) asm volatile("buffer_load_dwordx4 %0, %1, %2, %3 offen sc1" : "+v"(gc[i]) : "v"(vcs), "s"(sfl), "s"(i * 1664u) : "memory");
          asm volatile("s_waitcnt vmcnt(0)" : "+v"(gs), "+v"(gc[0]), "+v"(gc[1]), "+v"(gc[2]), "+v"(gc[3]), "+v"(gc[4]), "+v"(gc[5]), "+v"(gc[6]), "+v"(gc[7]) :: "memory");
          if (!stale()) break; } }
      f32x2 c = {__uint_as_float(gc[0][0]), __uint_as_float(gc[0][2])};
#pragma unroll
      for (int i = 1; i < 8; ++i) c = (f32x2){add1(c[0], __uint_as_float(gc[i][0])), add1(c[1], __uint_as_float(gc[i][2]))};
      *(LAS f32x2*)(size_t)(lb + L1_CS + min(wx * 64 + lane, 103) * 8) = c;
      *(LAS f32x2*)(size_t)(lb + L1_ST + (wx * 64 + lane) * 8) = (f32x2){__uint_as_float(gs[2]), __uint_as_float(gs[0])}; SB; }
    WAIT_L0; BAR; SB;
    mm(); MFMA_PAD; STAMP(7);
    if (YPRIO) __builtin_amdgcn_s_setprio(1);
    {
    const int lane = opq(__lane_id()), tok = lane & 15, g = lane >> 4, q = (lane >> 2) & 3, pp = lane & 3;
    bf16x8 wp[2];
#pragma unroll
    for (int ff = 0; ff < 2; ++ff) { const unsigned a = lb + L1_WPHI + (4 * g + q) * 64 + ff * 32 + 8 * pp; wp[ff] = cat(ldstr(a), ldstr(a + 1024)); }
    f32x4 cs[6]; float rs[4], mean[4], rl[4];
#pragma unroll
    for (int j = 0; j < 6; ++j) cs[j] = *(const LAS f32x4*)(size_t)(lb + L1_CS + (7 + j) * 64 + g * 16);
#pragma unroll
    for (int m = 0; m < 4; ++m) { const f32x2 st = *(const LAS f32x2*)(size_t)(lb + L1_ST + (wx * 64 + m * 16 + tok) * 8); rs[m] = st[0]; mean[m] = opqf(-st[1]); rl[m] = st[0] * LOG2E; }
    const f32x4 zero4 = {0.f, 0.f, 0.f, 0.f};
    auto project = [&](int u, f32x4 (&pr)[4][2]) {
#pragma unroll
      for (int m = 0; m < 4; ++m) { const bf16x8 kb = pk8(acc[u][m][0] + cs[2 * u] * mean[m], acc[u][m][1] + cs[2 * u + 1] * mean[m]); pr[m][0] = mfma(wp[0], kb, zero4); pr[m][1] = mfma(wp[1], kb, zero4); }
      SB; };
    auto extrema = [&](const f32x4 (&pr)[4][2], float (&ma)[4], float (&mb)[4]) {
#pragma unroll
      for (int m = 0; m < 4; ++m) { ma[m] = vmax8(pr[m][0], pr[m][1]); mb[m] = vmax8(-pr[m][0], -pr[m][1]); }
      spread4(gather4(ma[0], mb[0], ma[1], mb[1], vmax), ma[0], mb[0], ma[1], mb[1]);
      spread4(gather4(ma[2], mb[2], ma[3], mb[3], vmax), ma[2], mb[2], ma[3], mb[3]);
#pragma unroll
      for (int m = 0; m < 4; ++m) { ma[m] *= -rl[m]; mb[m] *= -rl[m]; }
      SB; };
    auto expo = [&](int m, const f32x4 (&pr)[2], float ma, float mb, f32x4 (&ep)[2], f32x4 (&en)[2]) {
#pragma unroll
      for (int ff = 0; ff < 2; ++ff) { ep[ff] = pr[ff] * rl[m] + ma; en[ff] = pr[ff] * -rl[m] + mb; }
      SB;
#pragma unroll
      for (int ff = 0; ff < 2; ++ff)
#pragma unroll
        for (int r = 0; r < 4; ++r) { ep[ff][r] = ex2(ep[ff][r]); en[ff][r] = ex2(en[ff][r]); }
      SB; };
    { const unsigned swp = (tok & 1) | (((tok >> 2) & 1) << 1) | (((tok >> 1) & 1) << 2) | (tok & 8);
      const unsigned wph = lb + L1_PHI + (wx * 64 + tok) * 128 + ((g ^ swp) << 3), wv = lb + L1_V + (wx * 64 + tok) * 64 + ((g ^ (tok >> 1)) << 3);
      const unsigned wp0 = opq(wph), wp1 = opq(wph ^ 32), wp2 = opq(wph ^ 64), wp3 = opq(wph ^ 96), wv0 = opq(wv), wv1 = opq(wv ^ 32);
      auto put = [](unsigned a, f32x4 v) { *(LAS u32x2*)(size_t)a = (u32x2){pk2(v[0], v[1]), pk2(v[2], v[3])}; };
      f32x4 pr[4][2], ep[4][2], en[4][2]; float ma[4], mb[4];
      WAIT_L0; project(1, pr);
#pragma unroll
      for (int m = 0; m < 4; ++m) { put(wv0 + m * 1024, (acc[2][m][0] + cs[4] * mean[m]) * rs[m]); put(wv1 + m * 1024, (acc[2][m][1] + cs[5] * mean[m]) * rs[m]); }
      SB; extrema(pr, ma, mb);
#pragma unroll
      for (int m = 0; m < 4; ++m) expo(m, pr[m], ma[m], mb[m], ep[m], en[m]);
#pragma unroll
      for (int m = 0; m < 4; ++m) { const f32x4 a = ep[m][0] + ep[m][1], c = en[m][0] + en[m][1]; const f32x2 a2 = a.xy + a.zw, c2 = c.xy + c.zw; ma[m] = a2[0] + opqf(a2[1]); mb[m] = c2[0] + opqf(c2[1]); }
      auto fadd = [](float a, float c) { return a + c; };
      spread4(rcp(gather4(ma[0], mb[0], ma[1], mb[1], fadd)), ma[0], mb[0], ma[1], mb[1]);
      spread4(rcp(gather4(ma[2], mb[2], ma[3], mb[3], fadd)), ma[2], mb[2], ma[3], mb[3]);
      SB;
#pragma unroll
      for (int m = 0; m < 4; ++m) { ep[m][0] *= ma[m]; ep[m][1] *= ma[m]; en[m][0] *= mb[m]; en[m][1] *= mb[m]; SB;
        put(wp0 + m * 2048, ep[m][0]); put(wp1 + m * 2048, ep[m][1]); put(wp2 + m * 2048, en[m][0]); put(wp3 + m * 2048, en[m][1]); SB; } }
    STAMP(8);
    asm volatile("s_waitcnt lgkmcnt(0)" ::: "memory");
    if (lane == 0) { const unsigned one = 1, a = lb + L1_CNT; asm volatile("ds_add_u32 %0, %1" :: "v"(a), "v"(one) : "memory"); }
    bf16x8 eb[4][2];
    f32x4 pq[4][2]; float qa[4], qb[4];
    project(0, pq); extrema(pq, qa, qb);
    auto qpart = [&](int m0) {
#pragma unroll
      for (int m = m0; m < m0 + 2; ++m) { f32x4 ep[2], en[2]; expo(m, pq[m], qa[m], qb[m], ep, en); eb[m][0] = pk8(ep[0], ep[1]); eb[m][1] = pk8(en[0], en[1]); asm volatile("" : "+v"(eb[m][0]), "+v"(eb[m][1])); SB; } };
    lds_wait(lb + L1_CNT, 4u); SB; STAMP(9);
    { const bf16x8 ones = {0x3F80, 0x3F80, 0x3F80, 0x3F80, 0x3F80, 0x3F80, 0x3F80, 0x3F80};
      unsigned ap[2], av[2][2];
#pragma unroll
      for (int t = 0; t < 2; ++t) { const int tk = 8 * g + 4 * t + q, t15 = tk & 15; const unsigned sw = (t15 & 1) | (((t15 >> 2) & 1) << 1) | (((t15 >> 1) & 1) << 2) | (t15 & 8);
        ap[t] = opq(lb + L1_PHI + tk * 128 + (((4 * wx + pp) ^ sw) << 3));
        av[t][0] = opq(lb + L1_V + tk * 64 + ((pp ^ (t15 >> 1)) << 3)); av[t][1] = opq(av[t][0] ^ 32); }
      bf16x8 a[8], b0[8], b1_[8];
#pragma unroll
      for (int ks = 0; ks < 8; ++ks) { a[ks] = cat(ldstr(ap[0] + ks * 4096), ldstr(ap[1] + ks * 4096));
        b0[ks] = cat(ldstr(av[0][0] + ks * 2048), ldstr(av[1][0] + ks * 2048)); b1_[ks] = cat(ldstr(av[0][1] + ks * 2048), ldstr(av[1][1] + ks * 2048)); }
      SB; qpart(0); STAMP(12);
      f32x4 kv0 = zero4, kv1 = zero4, zz = zero4;
#pragma unroll
      for (int ks = 0; ks < 8; ++ks) { kv0 = mfma(a[ks], b0[ks], kv0); kv1 = mfma(a[ks], b1_[ks], kv1); zz = mfma(a[ks], ones, zz); }
      const __amdgpu_buffer_rsrc_t rkv = mk_rsrc(ws + WS_KVP, 3u << 20);
      st16_sc1(__builtin_bit_cast(u32x4, pk8(kv0, kv1)), rkv, lane * 16, (h * 8 + p) * 4096 + wx * 1024);
      if (tok == 0) st16_sc1(__builtin_bit_cast(u32x4, zz), rkv, g * 16, (unsigned)(WS_ZP - WS_KVP) + (h * 8 + p) * 256 + wx * 64);
      SB; STAMP(13); }
    qpart(2);
    SB; if (YPRIO) __builtin_amdgcn_s_setprio(0); STAMP(10); WAIT_V(0);
    set_flag(flags + (FL_KV + (h * 4 + wx) * 1024) / 8 + p * 16, tag, lane); STAMP(11);
    Phase2<1> p2; p2.init(lb, lane, wx, P);
    P2RUN(p2, P, tag, eb);
    }
  }
}
}

extern "C" void kernel_launch(void* const* d_in, const int* in_sizes, int n_in, void* d_out, int out_size, void* d_ws, size_t ws_size, hipStream_t stream) {
  k_fused<<<256, 512, 0, stream>>>((const u16*)d_in[0], (const u16*)d_in[1], (const u16*)d_in[2], (const u16*)d_in[3], (const u16*)d_in[5], (const u16*)d_in[6], (const u16*)d_in[7], (char*)d_ws, (u16*)d_out);
}
```

```cpp
#define OUTV 1
#include <hip/hip_runtime.h>
#include <hip/hip_bf16.h>
#include <type_traits>

namespace {
#define LAS __attribute__((address_space(3)))
#define GAS __attribute__((address_space(1)))
typedef unsigned short u16;
typedef unsigned u32x4 __attribute__((ext_vector_type(4)));
typedef unsigned u32x2 __attribute__((ext_vector_type(2)));
typedef short bf16x8 __attribute__((ext_vector_type(8)));
typedef short s16x4 __attribute__((ext_vector_type(4)));
typedef float f32x4 __attribute__((ext_vector_type(4)));
typedef float f32x2 __attribute__((ext_vector_type(2)));
typedef __bf16 bf16x2_t __attribute__((ext_vector_type(2)));

constexpr int S_ = 2048, D_ = 1024, F_ = 3584;
constexpr size_t WS_FLAG = 0;
constexpr size_t WS_XN = 1u << 20;
constexpr size_t WS_ACT = WS_XN + (4u << 20);
constexpr size_t WS_O = WS_ACT + (15u << 20);
constexpr size_t WS_KVP = WS_O + (4u << 20);
constexpr size_t WS_ZP = WS_KVP + (2u << 20);
constexpr size_t WS_YX = WS_ZP + (1u << 20);
constexpr size_t WS_STAMP = 64u << 20;
#define STAMP_DECL
#define STAMP(i)
#define STAMP_OUT(wv)
constexpr unsigned FL_EPOCH = 0;
constexpr unsigned FL_O = 1024;
constexpr unsigned FL_YX = 16384;
constexpr unsigned FL_ACT2 = 40960;
constexpr unsigned FL_ACT3 = 49152;
constexpr unsigned FL_TMO = 61440;
constexpr unsigned GR_ST = 65536;
constexpr unsigned FL_KV = 131072;
constexpr unsigned FL_ACT = 262144;
constexpr unsigned GR_CS = 524288;
#define YPRIO 1
#define XP0 10
#define PFCODE 7
#define BLATE 6
#define TOUCHW 2
constexpr float LOG2E = 1.4426950408889634f;

#define SB __builtin_amdgcn_sched_barrier(0)
#define BAR __builtin_amdgcn_s_barrier()
#define WAIT_L0 do { __builtin_amdgcn_s_waitcnt(0xC07F); SB; } while (0)
#define WAIT_V(n) asm volatile("s_waitcnt vmcnt(%0)" :: "i"(n) : "memory")
template <int N> using IC = std::integral_constant<int, N>;

__device__ __forceinline__ u32x4 mk_srd(const void* p, unsigned bytes) {
  const unsigned long long a = (unsigned long long)p; u32x4 r;
  r.x = __builtin_amdgcn_readfirstlane((unsigned)a); r.y = __builtin_amdgcn_readfirstlane((unsigned)(a >> 32)) & 0xffffu; r.z = bytes; r.w = 0x00020000u; return r; }
#define DMA(srd, voff, sk, KOFF, sl, LOFF) do { unsigned _t; asm volatile("s_add_u32 m0, %4, %5\n\ts_add_u32 %0, %3, %6\n\tbuffer_load_dwordx4 %1, %2, %0 offen lds" \
    : "=&s"(_t) : "v"(voff), "s"(srd), "s"(sk), "s"(sl), "i"(LOFF), "i"(KOFF) : "memory", "scc"); SB; } while (0)
#define DMA_SC1(srd, voff, sk, KOFF, sl, LOFF) do { unsigned _t; asm volatile("s_add_u32 m0, %4, %5\n\ts_add_u32 %0, %3, %6\n\tbuffer_load_dwordx4 %1, %2, %0 offen sc1 lds" \
    : "=&s"(_t) : "v"(voff), "s"(srd), "s"(sk), "s"(sl), "i"(LOFF), "i"(KOFF) : "memory", "scc"); SB; } while (0)
#define TOUCH(ptr, sl) do { asm volatile("s_mov_b32 m0, %1\n\ts_nop 0\n\tglobal_load_lds_dword %0, off" :: "v"(ptr), "s"(sl) : "memory"); SB; } while (0)
__device__ __forceinline__ bf16x8 lds128(unsigned a) { return *(const LAS bf16x8*)(size_t)a; }
__device__ __forceinline__ s16x4 ldstr(unsigned a) { return __builtin_amdgcn_ds_read_tr16_b64_v4i16((LAS s16x4*)(size_t)a); }
__device__ __forceinline__ bf16x8 cat(s16x4 lo, s16x4 hi) { return __builtin_shufflevector(lo, hi, 0, 1, 2, 3, 4, 5, 6, 7); }
__device__ __forceinline__ unsigned opq(unsigned x) { asm volatile("" : "+v"(x)); return x; }
__device__ __forceinline__ float opqf(float x) { asm("" : "+v"(x)); return x; }
__device__ __forceinline__ f32x2 opq2(f32x2 x) { asm("" : "+v"(x)); return x; }
__device__ __forceinline__ f32x4 mfma(bf16x8 a, bf16x8 b, f32x4 c) { return __builtin_amdgcn_mfma_f32_16x16x32_bf16(a, b, c, 0, 0, 0); }
__device__ __forceinline__ void mfma_acc(f32x4& d, bf16x8 a, bf16x8 b) { asm volatile("v_mfma_f32_16x16x32_bf16 %0, %1, %2, %0" : "+v"(d) : "v"(a), "v"(b)); }
__device__ __forceinline__ void mfma_k2(f32x4& d, bf16x8 a0, bf16x8 b0, bf16x8 a1, bf16x8 b1) {
  asm volatile("v_mfma_f32_16x16x32_bf16 %0, %1, %2, %0\n\tv_mfma_f32_16x16x32_bf16 %0, %3, %4, %0" : "+v"(d) : "v"(a0), "v"(b0), "v"(a1), "v"(b1)); }
__device__ __forceinline__ void mfma_k2_first(f32x4& d, bf16x8 a0, bf16x8 b0, bf16x8 a1, bf16x8 b1) {
  asm volatile("v_mfma_f32_16x16x32_bf16 %0, %1, %2, 0\n\tv_mfma_f32_16x16x32_bf16 %0, %3, %4, %0" : "=&v"(d) : "v"(a0), "v"(b0), "v"(a1), "v"(b1)); }
template <bool FIRST> __device__ __forceinline__ void mk2(f32x4& d, bf16x8 a0, bf16x8 b0, bf16x8 a1, bf16x8 b1) { if (FIRST) mfma_k2_first(d, a0, b0, a1, b1); else mfma_k2(d, a0, b0, a1, b1); }
template <bool FIRST> __device__ __forceinline__ void mk4(f32x4& d, bf16x8 a0, bf16x8 b0, bf16x8 a1, bf16x8 b1, bf16x8 a2, bf16x8 b2, bf16x8 a3, bf16x8 b3) {
#define MK4_REST "v_mfma_f32_16x16x32_bf16 %0, %3, %4, %0\n\tv_mfma_f32_16x16x32_bf16 %0, %5, %6, %0\n\tv_mfma_f32_16x16x32_bf16 %0, %7, %8, %0"
  if (FIRST) asm volatile("v_mfma_f32_16x16x32_bf16 %0, %1, %2, 0\n\t" MK4_REST : "=&v"(d) : "v"(a0), "v"(b0), "v"(a1), "v"(b1), "v"(a2), "v"(b2), "v"(a3), "v"(b3));
  else asm volatile("v_mfma_f32_16x16x32_bf16 %0, %1, %2, %0\n\t" MK4_REST : "+v"(d) : "v"(a0), "v"(b0), "v"(a1), "v"(b1), "v"(a2), "v"(b2), "v"(a3), "v"(b3)); }
__device__ __forceinline__ void mfma_k2x2(f32x4& d, f32x4& e, bf16x8 a0, bf16x8 c0, bf16x8 b0, bf16x8 a1, bf16x8 c1, bf16x8 b1) {
  asm volatile("v_mfma_f32_16x16x32_bf16 %0, %2, %4, %0\n\tv_mfma_f32_16x16x32_bf16 %1, %3, %4, %1\n\tv_mfma_f32_16x16x32_bf16 %0, %5, %7, %0\n\tv_mfma_f32_16x16x32_bf16 %1, %6, %7, %1"
               : "+v"(d), "+v"(e) : "v"(a0), "v"(c0), "v"(b0), "v"(a1), "v"(c1), "v"(b1)); }
__device__ __forceinline__ f32x4 zacc() { f32x4 z = {0.f, 0.f, 0.f, 0.f}; asm volatile("" : "+v"(z)); return z; }
#define CS_M(d, a) "v_mfma_f32_16x16x32_bf16 " d ", " a ", %[o], " d "\n\t"
__device__ __forceinline__ void colsum7(f32x4& c0, f32x4& c1, const bf16x8 (&a)[7], const bf16x8 (&b)[7], bf16x8 ones, unsigned sel) {
  asm volatile("s_cmp_lt_u32 %[s], 2\n\ts_cbranch_scc1 .Lcs01_%=\n\ts_cmp_eq_u32 %[s], 2\n\ts_cbranch_scc1 .Lcs2_%=\n\ts_cmp_eq_u32 %[s], 3\n\ts_cbranch_scc0 .Lcs9_%=\n\t"
               CS_M("%0", "%[a3]") CS_M("%0", "%[b3]") "s_branch .Lcs9_%=\n"
               ".Lcs2_%=:\n\t" CS_M("%0", "%[a2]") CS_M("%0", "%[b2]") CS_M("%1", "%[a6]") CS_M("%1", "%[b6]") "s_branch .Lcs9_%=\n"
               ".Lcs01_%=:\n\ts_cmp_eq_u32 %[s], 0\n\ts_cbranch_scc1 .Lcs0_%=\n\t" CS_M("%0", "%[a1]") CS_M("%0", "%[b1]") CS_M("%1", "%[a5]") CS_M("%1", "%[b5]") "s_branch .Lcs9_%=\n"
               ".Lcs0_%=:\n\t" CS_M("%0", "%[a0]") CS_M("%0", "%[b0]") CS_M("%1", "%[a4]") CS_M("%1", "%[b4]") "\n.Lcs9_%=:"
               : "+v"(c0), "+v"(c1) : [a0] "v"(a[0]), [a1] "v"(a[1]), [a2] "v"(a[2]), [a3] "v"(a[3]), [a4] "v"(a[4]), [a5] "v"(a[5]), [a6] "v"(a[6]),
                 [b0] "v"(b[0]), [b1] "v"(b[1]), [b2] "v"(b[2]), [b3] "v"(b[3]), [b4] "v"(b[4]), [b5] "v"(b[5]), [b6] "v"(b[6]), [o] "v"(ones), [s] "s"(sel) : "scc");
}
#define MFMA_PAD do { asm volatile("s_nop 7\n\ts_nop 1" ::: "memory"); SB; } while (0)
__device__ __forceinline__ unsigned pk2(float lo, float hi) { const f32x2 v = {lo, hi}; return __builtin_bit_cast(unsigned, __builtin_convertvector(v, bf16x2_t)); }
__device__ __forceinline__ bf16x8 pk8(f32x4 a, f32x4 b) { const u32x4 u = {pk2(a[0], a[1]), pk2(a[2], a[3]), pk2(b[0], b[1]), pk2(b[2], b[3])}; return __builtin_bit_cast(bf16x8, u); }
__device__ __forceinline__ float bflo(unsigned u) { return __uint_as_float(u << 16); }
__device__ __forceinline__ float bfhi(unsigned u) { return __uint_as_float(u & 0xffff0000u); }
__device__ __forceinline__ float ex2(float x) { return __builtin_amdgcn_exp2f(x); }
__device__ __forceinline__ float rcp(float x) { return __builtin_amdgcn_rcpf(x); }
__device__ __forceinline__ u32x4 swap16(unsigned a0, unsigned a1, unsigned b0, unsigned b1) {
  const auto p = __builtin_amdgcn_permlane16_swap(a0, b0, false, false), q = __builtin_amdgcn_permlane16_swap(a1, b1, false, false);
  return (u32x4){p[0], q[0], p[1], q[1]}; }
__device__ __forceinline__ float vmax(float a, float b) { return __builtin_elementwise_maximum(a, b); }
__device__ __forceinline__ float vmax8(f32x4 a, f32x4 c) { return vmax(vmax(vmax(vmax(a[0], a[1]), a[2]), vmax(vmax(a[3], c[0]), c[1])), vmax(c[2], c[3])); }
__device__ __forceinline__ void sw32(float& a, float& b) { const auto p = __builtin_amdgcn_permlane32_swap(__float_as_uint(a), __float_as_uint(b), false, false); a = __uint_as_float(p[0]); b = __uint_as_float(p[1]); }
__device__ __forceinline__ void sw16(float& a, float& b) { const auto p = __builtin_amdgcn_permlane16_swap(__float_as_uint(a), __float_as_uint(b), false, false); a = __uint_as_float(p[0]); b = __uint_as_float(p[1]); }
template <class Op> __device__ __forceinline__ float gather4(float a0, float a1, float b0, float b1, Op op) { sw32(a0, a1); sw32(b0, b1); float c = op(a0, a1), d = op(b0, b1); sw16(c, d); return op(c, d); }
__device__ __forceinline__ void spread4(float d, float& a0, float& a1, float& b0, float& b1) { a0 = d; b0 = d; sw16(a0, b0); a1 = a0; b1 = b0; sw32(a0, a1); sw32(b0, b1); }
template <class Op> __device__ __forceinline__ float red4(float v, Op op) {
  const unsigned u = __float_as_uint(v); const auto p = __builtin_amdgcn_permlane32_swap(u, u, false, false);
  const float a = op(__uint_as_float(p[0]), __uint_as_float(p[1])); const unsigned ua = __float_as_uint(a);
  const auto q = __builtin_amdgcn_permlane16_swap(ua, ua, false, false); return op(__uint_as_float(q[0]), __uint_as_float(q[1])); }
__device__ __forceinline__ void st16_sc1(u32x4 v, __amdgpu_buffer_rsrc_t r, unsigned voff, unsigned soff) { __builtin_amdgcn_raw_buffer_store_b128(v, r, voff + soff, 0, 16); }
__device__ __forceinline__ void st16(u32x4 v, __amdgpu_buffer_rsrc_t r, unsigned voff, unsigned soff) { __builtin_amdgcn_raw_buffer_store_b128(v, r, voff + soff, 0, 0); }
__device__ __forceinline__ u32x4 ld16_sc1(__amdgpu_buffer_rsrc_t r, unsigned voff, unsigned soff) { return __builtin_amdgcn_raw_buffer_load_b128(r, voff, soff, 16); }
__device__ __forceinline__ __amdgpu_buffer_rsrc_t mk_rsrc(const void* p, unsigned bytes) { return __builtin_amdgcn_make_buffer_rsrc((void*)p, 0, bytes, 0x00020000); }
#define RLX_AGENT __ATOMIC_RELAXED, __HIP_MEMORY_SCOPE_AGENT
typedef unsigned long long u64;
typedef GAS u64 gu64;
__device__ __forceinline__ void set_flag(gu64* w, u64 tag, int lane) { if (lane == 0) __hip_atomic_store(w, tag, RLX_AGENT); }
template <int N, int STRIDE> __device__ __forceinline__ void wait_flags(gu64* w, u64 tag, gu64* tmo, int lane) {
#pragma nounroll
  for (unsigned spins = 0;; ++spins) {
    u64 v = tag; if (N >= 64 || lane < N) v = __hip_atomic_load(w + lane * STRIDE, RLX_AGENT);
    if (__all(v == tag)) break;
    if (spins > (1u << 16)) { if (lane == 0) __hip_atomic_store(tmo, tag, RLX_AGENT); break; }
    __builtin_amdgcn_s_sleep(2);
  }
  __builtin_amdgcn_fence(__ATOMIC_ACQUIRE, "wavefront");
}
__device__ __forceinline__ u64 uni64(u64 v) { return ((u64)(unsigned)__builtin_amdgcn_readfirstlane((unsigned)(v >> 32)) << 32) | (unsigned)__builtin_amdgcn_readfirstlane((unsigned)v); }
__device__ __forceinline__ void lds_wait(unsigned addr, unsigned target) {
  for (;;) { unsigned v; asm volatile("ds_read_b32 %0, %1\n\ts_waitcnt lgkmcnt(0)" : "=v"(v) : "v"(addr) : "memory");
    if ((unsigned)__builtin_amdgcn_readfirstlane(v) >= target) break; }
}
#define POLL_ISSUE(dst, ptr) do { asm volatile("global_load_dwordx2 %0, %1, off sc1" : "=v"(dst) : "v"(ptr) : "memory"); SB; } while (0)
#define LD16_ISSUE(dst, srd, voff, soff) do { asm volatile("buffer_load_dwordx4 %0, %1, %2, %3 offen sc1" : "=v"(dst) : "v"(voff), "s"(srd), "s"(soff) : "memory"); SB; } while (0)
#define POLL_PIN(dst) asm volatile("" : "+v"(dst))
__device__ __forceinline__ float add1(float a, float b) { float r; asm("v_add_f32 %0, %1, %2" : "=v"(r) : "v"(a), "v"(b)); return r; }
__device__ __forceinline__ float mul1(float a, float b) { float r; asm("v_mul_f32 %0, %1, %2" : "=v"(r) : "v"(a), "v"(b)); return r; }
__device__ __forceinline__ float fma1(float a, float b, float c) { float r; asm("v_fma_f32 %0, %1, %2, %3" : "=v"(r) : "v"(a), "v"(b), "v"(c)); return r; }
__device__ __forceinline__ f32x4 add4(f32x4 a, u32x4 b) { return (f32x4){add1(a[0], __uint_as_float(b[0])), add1(a[1], __uint_as_float(b[1])), add1(a[2], __uint_as_float(b[2])), add1(a[3], __uint_as_float(b[3]))}; }
__device__ __forceinline__ unsigned differ(unsigned a, unsigned b, unsigned t) { unsigned r; asm("v_bitop3_b32 %0, %1, %2, %3 bitop3:0x7e" : "=v"(r) : "v"(a), "v"(b), "s"(t)); return r; }
template <int C> __device__ __forceinline__ float dpp(float v) { return __int_as_float(__builtin_amdgcn_update_dpp(0, __float_as_int(v), C, 0xf, 0xf, true)); }
__device__ __forceinline__ float wave_sum(float v) { v += dpp<0x128>(v); v += dpp<0x124>(v); v += dpp<0x122>(v); v += dpp<0x121>(v); return red4(v, [](float a, float c) { return a + c; }); }
__device__ __forceinline__ void lds_sync(unsigned addr, unsigned target, int lane) {
  asm volatile("s_waitcnt lgkmcnt(0)" ::: "memory");
  if (lane == 0) { const unsigned one = 1; asm volatile("ds_add_u32 %0, %1" :: "v"(addr), "v"(one) : "memory"); }
  for (;;) { unsigned v; asm volatile("ds_read_b32 %0, %1\n\ts_waitcnt lgkmcnt(0)" : "=v"(v) : "v"(addr) : "memory");
    if ((unsigned)__builtin_amdgcn_readfirstlane(v) >= target) break; }
}

constexpr unsigned L1_A = 0;
constexpr unsigned L1_BQ = 65536;
constexpr unsigned L1_BH = 90112;
constexpr unsigned L1_WPHI = 147456;
constexpr unsigned L1_CNT = 149504;
constexpr unsigned L1_DUMP = 149760;
constexpr unsigned L1_BIAS = 151104;
constexpr unsigned L1_CS = 150272;
constexpr unsigned L1_ST = 151552;
constexpr unsigned L1_V = 49152;
constexpr unsigned L1_PHI = 114688;
constexpr unsigned L2_A = 0;
constexpr unsigned L2_B = 65536;
constexpr unsigned L2_X = L2_B + 16384;
constexpr unsigned L2_KVA = 153600;

__device__ __forceinline__ void touch_code(unsigned dump, int lane, int wx) {
  u64 c0; unsigned back, len;
  asm volatile("s_getpc_b64 %0\n.Lpc_%=:\n\ts_mov_b32 %1, .Lpc_%=-.Lcode_begin\n\ts_mov_b32 %2, .Lend_g0-.Lcode_begin\n\ts_max_u32 %2, %2, .Lend_g1-.Lcode_begin" : "=s"(c0), "=s"(back), "=s"(len) :: "scc");
  const unsigned o = min((((blockIdx.x >> 3) * 4 + wx) * 8 + (lane & 7)) * 128u, len) & ~3u; TOUCH((const GAS char*)(c0 - back) + o, dump); }

struct Ptrs { const u16 *x, *wqkv, *wphi, *wo, *w1, *b1, *w2; char* ws; u16* out; };

struct Query { f32x4 pq[4][2]; float rl[4]; };

template <int G> struct Phase2 {
  unsigned lb; int lane, wq, b, kh, rt, ct;
  u32x4 sact, so, sw2, swo;
  unsigned vaa, vao, vb0, vb1, vs0, vs1, sla, ska, skb4, skb7; gu64* fl3;
  static constexpr int kstep(int U, int i) { return (U < 24 ? 7 * (U & 7) + (U >> 3) : 14 * U - 333) + (i ? (U < 24 ? 4 : 7) : 0); }
  __device__ __forceinline__ void init(unsigned lb_, int lane_, int wq_, const Ptrs& P) {
    lb = lb_; lane = lane_; wq = wq_; b = blockIdx.x; kh = b & 1; rt = ((b & 7) >> 1) * 4 + ((b >> 3) & 3); ct = b >> 5;
    sact = mk_srd(P.ws + WS_ACT, 128 * 112 * 1024); so = mk_srd(P.ws + WS_O, 128 * 32 * 1024); sw2 = mk_srd(P.w2, F_ * D_ * 2); swo = mk_srd(P.wo, D_ * D_ * 2);
    vaa = lane * 16 + (rt * 8 + wq) * (112 * 1024); vao = lane * 16 + (rt * 8 + wq) * (32 * 1024);
    { const int r = lane >> 4, c = lane & 15; vb0 = ((wq & 1) * 16 + r) * 2048 + ct * 256 + (((c >> 1) ^ r) << 5) + ((c & 1) << 4); vb1 = vb0 ^ 128; }
    { const int r = lane >> 3, c = lane & 7; vs0 = (wq * 32 + r) * 2048 + ct * 256 + kh * 128 + (((c >> 1) ^ ((r >> 1) & 1)) << 5) + ((c & 1) << 4); vs1 = vs0 ^ 64; }
    sla = lb + wq * 4096; ska = kh * (56 * 1024); skb4 = (kh * 56 + (wq >> 1) * 4) * 65536; skb7 = (kh * 56 + (wq >> 1) * 7) * 65536;
  }
  template <int GT, int S, int KS> __device__ __forceinline__ void dmaA() { constexpr int LO = L2_A + GT * 32768 + (S & 1) * 16384 + KS * 1024;
    if constexpr (S < 14) DMA_SC1(sact, vaa, ska, (GT * 4 * 112 + kstep(2 * S + (KS >> 1), KS & 1)) * 1024, sla, LO);
    else DMA_SC1(so, vao, 0u, (GT * 4 * 32 + (S - 14) * 4 + KS) * 1024, sla, LO); }
  template <int U, int J> __device__ __forceinline__ void dmaB() { constexpr int LO = L2_B + (U % 5) * 16384 + J * 1024;
    if constexpr (U < 28) { const unsigned sk = U < 24 ? skb4 : skb7; if (J >> 1) DMA(sw2, vb1, sk, kstep(U, 0) * 65536 + J * 8192, sla, LO); else DMA(sw2, vb0, sk, kstep(U, 0) * 65536 + J * 8192, sla, LO); }
    else { if (J & 1) DMA(swo, vs1, 0u, (U - 28) * 262144 + J * 16384, sla, LO); else DMA(swo, vs0, 0u, (U - 28) * 262144 + J * 16384, sla, LO); } }
  template <int GT, int S> __device__ __forceinline__ void dmaA4() { dmaA<GT, S, 0>(); dmaA<GT, S, 1>(); dmaA<GT, S, 2>(); dmaA<GT, S, 3>(); }
  template <int S, int I> __device__ __forceinline__ void dma_slot() {
    if constexpr (G == 0) {
      if constexpr (S <= 12) { if constexpr (I < 4) dmaB<2 * S + 3, I>(); else dmaB<2 * S + 4, I - 4>(); }
      else if constexpr (I < 2) { if constexpr (S <= 20) dmaA<0, S + 1, 2 + I>(); }
      else if constexpr (I < 6 && S <= 19) dmaB<S + 16, I - 2>();
    } else {
      if constexpr (I < 4) { if constexpr (S <= 20) dmaA<1, S + 1, I>(); }
      else if constexpr (S <= 11) dmaA<0, S + 2, I - 4>();
      else if constexpr (I < 6 && S <= 19) dmaA<0, S + 2, I - 4>();
    } }

  __device__ __forceinline__ void run(const Ptrs& P, u64 tag, int p, int h, Query& Q) {
    char* ws = P.ws;
    const int tok = lane & 15, g = lane >> 4, q = (lane >> 2) & 3, pp = lane & 3;
    const unsigned ra = opq(lb + L2_A + G * 32768 + lane * 16);
    unsigned rb[2][2], rs[2];
#pragma unroll
    for (int n = 0; n < 2; ++n) { const int f = 4 * (kh ^ n) + wq; rb[0][n] = opq(lb + L2_B + (8 * g + q) * 256 + ((f ^ (q | ((g & 1) << 2))) << 5) + 8 * pp); rb[1][n] = opq(rb[0][n] + 65536); }
    rs[0] = opq(lb + L2_B + (8 * g + q) * 128 + ((wq ^ ((q >> 1) | ((g & 1) << 1))) << 5) + 8 * pp); rs[1] = opq(rs[0] + 65536);
    f32x4 acc[4][2], as[4];
    bf16x8 fa[4][4], fb[4][2];
    auto load = [&](auto SS, auto hook) { constexpr int S = SS();
      auto step = [&](auto II) { constexpr int i = II(), ks = i >> 1;
        _Pragma("unroll") for (int m = 2 * (i & 1); m < 2 * (i & 1) + 2; ++m) { fa[ks][m] = lds128(ra + (S & 1) * 16384 + m * 4096 + ks * 1024); SB; }
        if constexpr (S < 14) { constexpr int buf = (2 * S + (ks >> 1)) % 5, o = (buf & 3) * 16384 + (ks & 1) * 8192; fb[ks][i & 1] = cat(ldstr(rb[buf >> 2][i & 1] + o), ldstr(rb[buf >> 2][i & 1] + o + 1024)); SB; }
        else if constexpr ((i & 1) == 0) { constexpr int buf = (S + 14) % 5, o = (buf & 3) * 16384 + ks * 4096; fb[ks][0] = cat(ldstr(rs[buf >> 2] + o), ldstr(rs[buf >> 2] + o + 512)); SB; }
        dma_slot<S, i>(); hook(II); SB; };
      step(IC<0>{}); step(IC<1>{}); step(IC<2>{}); step(IC<3>{}); step(IC<4>{}); step(IC<5>{}); step(IC<6>{}); step(IC<7>{}); };
    auto mm = [&](auto SS) { constexpr int S = SS();
#pragma unroll
      for (int m = 0; m < 4; ++m)
#pragma unroll
        for (int n = 0; n < (S < 14 ? 2 : 1); ++n) mk4<S == 0 || S == 14>(S < 14 ? acc[m][n] : as[m], fb[0][n], fa[0][m], fb[1][n], fa[1][m], fb[2][n], fa[2][m], fb[3][n], fa[3][m]);
      SB; };
    auto none = [](auto) {};
#define ENDL(n) do { WAIT_V(n); WAIT_L0; BAR; SB; } while (0)
#define ENDR do { WAIT_L0; BAR; SB; } while (0)
#define ENDM(n) do { WAIT_V(n); BAR; SB; } while (0)
    gu64* flags = (gu64*)(ws + WS_FLAG); gu64* tmo = flags + FL_TMO / 8;
    const unsigned kva = opq(lb + L2_KVA + lane * 16);
    u64 pv;
    const __amdgpu_buffer_rsrc_t ryx = mk_rsrc(ws + WS_YX, 256 * 32768);
    auto send = [&]() {
#pragma unroll
      for (int i = 0; i < 2; ++i) st16_sc1(__builtin_bit_cast(u32x4, pk8(acc[2 * i][1], acc[2 * i + 1][1])), ryx, lane * 16, b * 32768 + (G * 4 + wq) * 2048 + i * 1024);
      SB; };
    gu64* fy = flags + FL_YX / 8 + b * 8 + G * 4 + wq; gu64* py_ = flags + FL_YX / 8 + (b ^ 1) * 8 + G * 4 + wq;
    u32x4 py[2]; u32x2 xr[4]; f32x2 gt[8];
    const unsigned vout = (rt * 128 + G * 64 + (g & 1) * 16 + tok) * 2048 + ct * 256 + kh * 128 + wq * 32 + (g >> 1) * 16;
    auto fetch = [&]() { POLL_PIN(pv); if (!__all(pv == tag)) wait_flags<1, 1>(py_, tag, tmo, lane); SB;
      const u32x4 syx = mk_srd(ws + WS_YX, 256 * 32768); const unsigned vy = (b ^ 1) * 32768 + (G * 4 + wq) * 2048 + lane * 16;
      LD16_ISSUE(py[0], syx, vy, 0u); LD16_ISSUE(py[1], syx, vy, 1024u); };
    auto dmax = [&]() { const u32x4 sx = mk_srd(P.x, S_ * D_ * 2); const int r = lane >> 3;
      const unsigned v0 = (rt * 128 + wq * 32 + r) * 2048 + ct * 256 + kh * 128 + (((lane & 7) ^ (r >> 1)) << 4), v1 = v0 ^ 64;
      DMA(sx, v0, 0u, 0, sla, L2_X); DMA(sx, v1, 0u, 16384, sla, L2_X + 1024); DMA(sx, v0, 0u, 32768, sla, L2_X + 2048); DMA(sx, v1, 0u, 49152, sla, L2_X + 3072); };
    unsigned xad;
    auto readx = [&]() { xad = opq(lb + L2_X + (G * 64 + tok) * 128 + (((2 * wq + (g >> 1)) ^ (tok >> 1)) << 4) + (g & 1) * 8);
#pragma unroll
      for (int m = 0; m < 4; ++m) xr[m] = *(const LAS u32x2*)(size_t)(xad + m * 2048);
      SB; };
    auto gate = [&](int k) { asm volatile("" : "+v"(py[k])); SB; f32x2 y[4], e[4];
#define ST4G(stmt) _Pragma("unroll") for (int i = 0; i < 4; ++i) { stmt; } SB
      ST4G(const unsigned u = py[k][i]; e[i] = opq2((f32x2){bflo(u), bfhi(u)}));
      ST4G(const f32x4 a = acc[2 * k + (i >> 1)][0]; y[i] = ((i & 1) ? a.zw : a.xy) + e[i]);
      ST4G(e[i] = opq2(y[i] * -LOG2E));
      ST4G(e[i] = opq2((f32x2){ex2(e[i][0]), ex2(e[i][1])}));
      ST4G(e[i] = opq2(e[i] + 1.0f));
      ST4G(e[i] = opq2((f32x2){rcp(e[i][0]), rcp(e[i][1])}));
      ST4G(gt[4 * k + i] = y[i] * e[i]; asm volatile("" : "+v"(gt[4 * k + i]))); };
#define S_(n) IC<n>{}
    if constexpr (G == 0) {
      WAIT_V(13); BAR; SB; STAMP(16);
      load(S_(0), none); ENDL(17);
      mm(S_(0)); WAIT_V(4); set_flag(fl3, tag, lane); BAR; SB; STAMP(17); load(S_(1), none); ENDR;
      gu64* fkv = flags + (FL_KV + (h * 4 + wq) * 1024) / 8;
      mm(S_(1)); ENDM(4); POLL_ISSUE(pv, fkv + (lane & 7) * 16); load(S_(2), none); ENDR;
      mm(S_(2)); ENDM(4);
      POLL_PIN(pv); if (!__all(pv == tag)) wait_flags<8, 16>(fkv, tag, tmo, lane); SB;
      const u32x4 skv = mk_srd(ws + WS_KVP, 3u << 20);
      u32x4 t[8], tz; LD16_ISSUE(tz, skv, h * 2048 + (lane & 7) * 256 + wq * 64 + g * 16, (unsigned)(WS_ZP - WS_KVP));
      { const unsigned vkv = h * 32768 + wq * 1024 + lane * 16;
#pragma unroll
        for (int i = 0; i < 8; ++i) LD16_ISSUE(t[i], skv, vkv, i * 4096u); }
      load(S_(3), none); ENDR; STAMP(25);
      f32x2 sm[4];
      auto addp = [&](int i) { f32x2 u[4];
#pragma unroll
        for (int c = 0; c < 4; ++c) u[c] = opq2((f32x2){bflo(t[i][c]), bfhi(t[i][c])});
        SB;
#pragma unroll
        for (int c = 0; c < 4; ++c) { sm[c] = i ? sm[c] + u[c] : u[c]; asm volatile("" : "+v"(sm[c])); }
        SB; };
      mm(S_(3)); WAIT_V(8);
      asm volatile("" : "+v"(t[0]), "+v"(t[1]), "+v"(t[2]), "+v"(t[3]), "+v"(t[4]), "+v"(t[5]), "+v"(t[6]), "+v"(t[7]), "+v"(tz)); SB;
      addp(0); addp(1); addp(2); ENDM(4); load(S_(4), none); ENDR; STAMP(26);
      mm(S_(4)); addp(3); addp(4); addp(5); ENDM(4); load(S_(5), [&](auto I) { constexpr int r = I();
        if constexpr (r < 4) { float z = __uint_as_float(tz[r]); z += dpp<0xB1>(z); z = opqf(z); z += dpp<0x4E>(z); z = opqf(z); z += dpp<0x141>(z); tz[r] = __float_as_uint(opqf(z)); asm volatile("" : "+v"(tz)); } });
      ENDR; STAMP(27);
      mm(S_(5)); addp(6); addp(7);
      { const unsigned a = kva + (wq >> 1) * 1024 + (wq & 1) * 8;
        *(LAS u32x2*)(size_t)a = (u32x2){pk2(sm[0][0], sm[0][1]), pk2(sm[1][0], sm[1][1])}; *(LAS u32x2*)(size_t)(a + 2048) = (u32x2){pk2(sm[2][0], sm[2][1]), pk2(sm[3][0], sm[3][1])};
        u32x2 au = {pk2(__uint_as_float(tz[0]), __uint_as_float(tz[1])), pk2(__uint_as_float(tz[2]), __uint_as_float(tz[3]))};
        if ((lane & 3) == 1) au = (u32x2){0x3F803F80u, 0x3F803F80u}; else if ((lane & 3) != 0) au = (u32x2){0u, 0u};
        *(LAS u32x2*)(size_t)(a + 4096) = au; SB; }
      ENDM(4); load(S_(6), none); ENDR;
      mm(S_(6)); ENDM(4); load(S_(7), none); ENDR;
      mm(S_(7)); ENDM(4); load(S_(8), none); ENDR;
      mm(S_(8)); ENDM(4); load(S_(9), none); ENDR;
      mm(S_(9)); ENDM(4); load(S_(10), none); ENDR; STAMP(18);
      mm(S_(10)); ENDM(4); if (PFCODE & 4) touch_code(lb + L1_DUMP, lane, wq); load(S_(11), none); ENDR;
      mm(S_(11)); ENDM(4); load(S_(12), none); ENDR;
      mm(S_(12)); ENDM(4); load(S_(13), none); ENDR;
      mm(S_(13)); ENDM(4); STAMP(19); load(S_(14), none); send(); ENDR;
      mm(S_(14)); ENDM(6); load(S_(15), none); ENDR; STAMP(28);
      mm(S_(15)); WAIT_V(4); set_flag(fy, tag, lane); BAR; SB; load(S_(16), none); ENDR;
      mm(S_(16)); ENDM(4); load(S_(17), none); ENDR; STAMP(29);
      mm(S_(17)); ENDM(4); POLL_ISSUE(pv, py_); load(S_(18), none); ENDR;
      mm(S_(18)); ENDM(4); fetch(); load(S_(19), none); ENDR; STAMP(20);
      mm(S_(19)); WAIT_V(6); gate(0); ENDM(4); load(S_(20), none); dmax(); ENDR; STAMP(30);
      mm(S_(20)); gate(1); ENDM(4); load(S_(21), none); WAIT_V(0); BAR; SB; readx(); __builtin_amdgcn_s_waitcnt(0xC47F); SB; STAMP(31);
      mm(S_(21));
    } else {
      BAR; SB; STAMP(16); BAR; SB;
      load(S_(0), none); WAIT_V(8); set_flag(flags + (FL_KV + (h * 4 + wq) * 1024) / 8 + p * 16, tag, lane); ENDR;
      const f32x4 z4 = {0.f, 0.f, 0.f, 0.f}; float ma[4], mb[4]; bf16x8 eb[4][2], ka[6];
      mm(S_(0));
#pragma unroll
      for (int m = 0; m < 4; ++m) { ma[m] = vmax8(Q.pq[m][0], Q.pq[m][1]); mb[m] = vmax8(-Q.pq[m][0], -Q.pq[m][1]); asm volatile("" : "+v"(ma[m]), "+v"(mb[m])); }
      SB; ENDM(5); STAMP(17);
      gu64* fa2 = flags + FL_ACT2 / 8 + (rt * 2 + kh) * 32;
      POLL_ISSUE(pv, fa2 + (lane & 31));
      load(S_(1), none); ENDL(9);
      mm(S_(1));
      spread4(gather4(ma[0], mb[0], ma[1], mb[1], vmax), ma[0], mb[0], ma[1], mb[1]);
      spread4(gather4(ma[2], mb[2], ma[3], mb[3], vmax), ma[2], mb[2], ma[3], mb[3]);
#pragma unroll
      for (int m = 0; m < 4; ++m) { ma[m] = opqf(ma[m] * -Q.rl[m]); mb[m] = opqf(mb[m] * -Q.rl[m]); asm volatile("" : "+v"(ma[m]), "+v"(mb[m])); }
      SB; ENDM(4);
      POLL_PIN(pv); if (!__all(pv == tag)) wait_flags<32, 1>(fa2, tag, tmo, lane); SB;
      auto expo = [&](int m) { f32x4 ep[2], en[2];
#pragma unroll
        for (int ff = 0; ff < 2; ++ff) { ep[ff] = Q.pq[m][ff] * Q.rl[m] + ma[m]; en[ff] = Q.pq[m][ff] * -Q.rl[m] + mb[m]; }
        SB;
#pragma unroll
        for (int ff = 0; ff < 2; ++ff)
#pragma unroll
          for (int r = 0; r < 4; ++r) { ep[ff][r] = ex2(ep[ff][r]); en[ff][r] = ex2(en[ff][r]); }
        SB; eb[m][0] = pk8(ep[0], ep[1]); eb[m][1] = pk8(en[0], en[1]); asm volatile("" : "+v"(eb[m][0]), "+v"(eb[m][1])); SB; };
      load(S_(2), none); ENDL(8); STAMP(25);
      mm(S_(2)); expo(0); ENDM(4); load(S_(3), none); ENDL(8);
      mm(S_(3)); expo(1); ENDM(4); load(S_(4), none); ENDL(8); STAMP(26);
      gu64* fa3 = flags + FL_ACT3 / 8 + (rt * 2 + kh) * 32;
      mm(S_(4)); expo(2); ENDM(4); POLL_ISSUE(pv, fa3 + (lane & 31)); load(S_(5), none); ENDL(9); STAMP(27);
      mm(S_(5)); expo(3); ENDM(4);
      POLL_PIN(pv); if (!__all(pv == tag)) wait_flags<32, 1>(fa3, tag, tmo, lane); SB;
      const __amdgpu_buffer_rsrc_t ro = mk_rsrc(ws + WS_O, 128 * 32 * 1024);
      const unsigned vo = (unsigned)((((g & 1) << 1) | (g >> 1)) * 256 + tok * 16) + ((p * 16 + wq * 4) * 32 + h) * 1024;
      auto out_o = [&](int m) { f32x4 r6[6];
#pragma unroll
        for (int k = 5; k >= 0; --k) r6[k] = mfma(ka[k], eb[m][k & 1], z4);
        SB; const float a = rcp(r6[4][1]), c = rcp(r6[5][1]), inv = rcp(opqf(r6[4][0] * a + 1e-6f) + r6[5][0] * c), ca = opqf(a * inv), cc = opqf(c * inv);
        const f32x4 o0 = r6[0] * ca + r6[1] * cc, o1 = r6[2] * ca + r6[3] * cc;
        st16_sc1(swap16(pk2(o0[0], o0[1]), pk2(o0[2], o0[3]), pk2(o1[0], o1[1]), pk2(o1[2], o1[3])), ro, vo, m * 32768); SB; };
      load(S_(6), [&](auto I) { constexpr int i = I(); if constexpr (i >= 2) ka[i - 2] = lds128(kva + (i - 2) * 1024); }); ENDL(8);
      mm(S_(6)); out_o(0); ENDM(5); load(S_(7), none); ENDL(9);
      mm(S_(7)); out_o(1); ENDM(5); load(S_(8), none); ENDL(9);
      mm(S_(8)); out_o(2); ENDM(5); load(S_(9), none); ENDL(9);
      mm(S_(9)); out_o(3); ENDM(5); load(S_(10), none); WAIT_V(8);
      set_flag(flags + FL_O / 8 + (2 * p + (wq >> 1)) * 64 + h * 2 + (wq & 1), tag, lane); ENDR; STAMP(18);
      gu64* po = flags + FL_O / 8 + rt * 64;
      mm(S_(10)); ENDM(5); load(S_(11), none); POLL_ISSUE(pv, po + lane); ENDL(9);
      mm(S_(11)); ENDM(5); WAIT_V(0); POLL_PIN(pv); if (!__all(pv == tag)) wait_flags<64, 1>(po, tag, tmo, lane); SB; load(S_(12), none); ENDL(6);
      mm(S_(12)); ENDM(2); load(S_(13), none); ENDL(6);
      mm(S_(13)); ENDM(2); STAMP(19); load(S_(14), none); send(); ENDL(8);
      mm(S_(14)); ENDM(4); load(S_(15), none); ENDL(8); STAMP(28);
      mm(S_(15)); WAIT_V(2); set_flag(fy, tag, lane); BAR; SB; load(S_(16), none); ENDL(7);
      mm(S_(16)); ENDM(2); load(S_(17), none); ENDL(6); STAMP(29);
      mm(S_(17)); ENDM(2); POLL_ISSUE(pv, py_); load(S_(18), none); ENDL(7);
      mm(S_(18)); ENDM(2); fetch(); load(S_(19), none); ENDL(8); STAMP(20);
      mm(S_(19)); WAIT_V(6); gate(0); ENDM(2); load(S_(20), none); ENDL(4); STAMP(30);
      mm(S_(20)); gate(1); ENDM(0); load(S_(21), none); readx(); __builtin_amdgcn_s_waitcnt(0xC47F); SB; STAMP(31);
      mm(S_(21));
    }
    MFMA_PAD; STAMP(21);
    const __amdgpu_buffer_rsrc_t rout = mk_rsrc(P.out, S_ * D_ * 2);
    if (OUTV == 0) {
#pragma unroll
      for (int i = 0; i < 2; ++i) { unsigned ow[4];
#pragma unroll
        for (int c = 0; c < 4; ++c) { const f32x4 s = as[2 * i + (c >> 1)]; const unsigned u = xr[2 * i + (c >> 1)][c & 1]; const f32x2 o = ((c & 1) ? s.zw : s.xy) * gt[4 * i + c] + opq2((f32x2){bflo(u), bfhi(u)}); ow[c] = pk2(o[0], o[1]); }
        st16(swap16(ow[0], ow[1], ow[2], ow[3]), rout, vout, i * 65536); }
    } else {
#pragma unroll
      for (int m = 0; m < 4; ++m) { const f32x2 o0 = as[m].xy * gt[2 * m] + opq2((f32x2){bflo(xr[m][0]), bfhi(xr[m][0])}), o1 = as[m].zw * gt[2 * m + 1] + opq2((f32x2){bflo(xr[m][1]), bfhi(xr[m][1])});
        *(LAS u32x2*)(size_t)(xad + m * 2048) = (u32x2){pk2(o0[0], o0[1]), pk2(o1[0], o1[1])}; }
      lds_sync(lb + L1_CNT + (G ? 0 : 4), G ? 8u : 4u, lane);
      const unsigned ro = lb + L2_X + (G * 64 + wq * 16) * 128 + lane * 16; const int r = lane >> 3;
      const u32x4 w0 = *(const LAS u32x4*)(size_t)ro, w1 = *(const LAS u32x4*)(size_t)(ro + 1024);
      const unsigned vo = (rt * 128 + G * 64 + wq * 16 + r) * 2048 + ct * 256 + kh * 128 + (((lane & 7) ^ (r >> 1)) << 4);
      if (OUTV == 1) { st16_sc1(w0, rout, vo, 0); st16_sc1(w1, rout, vo ^ 64, 16384); } else { st16(w0, rout, vo, 0); st16(w1, rout, vo ^ 64, 16384); }
    }
    if (G == 0 && b == 0 && wq == 0) set_flag(flags + FL_EPOCH / 8, tag, lane);
    if (G == 0) asm volatile(".Lend_g0:"); else asm volatile(".Lend_g1:");
    STAMP(23);
  }
};
#define P2RUN(p2, P, tag, eb) p2.run(P, tag, p, h, eb)

__global__ __launch_bounds__(512, 2) void k_fused(const u16* x, const u16* wqkv, const u16* wphi, const u16* wo, const u16* w1, const u16* b1, const u16* w2, char* ws, u16* out) {
  __shared__ __attribute__((aligned(16))) char smem[163840];
  const Ptrs P = {x, wqkv, wphi, wo, w1, b1, w2, ws, out};
  const unsigned lb = (unsigned)(size_t)(LAS char*)smem;
  const int tid = threadIdx.x, lane = tid & 63, w = __builtin_amdgcn_readfirstlane(tid >> 6), wx = w & 3;
  const int b = blockIdx.x, h = (b & 7) * 4 + ((b >> 3) & 3), p = b >> 5, odd = h & 1;
  const int tok = lane & 15, g = lane >> 4, q = (lane >> 2) & 3, pp = lane & 3;
  const unsigned ra0 = lb + L1_A + (wx * 64 + tok) * 128 + ((g ^ (tok >> 1)) << 4), ra1 = ra0 ^ 64;
  gu64* flags = (gu64*)(ws + WS_FLAG); gu64* tmo = flags + FL_TMO / 8;
  asm volatile(".Lcode_begin:");
  STAMP_DECL; STAMP(0);
  const int rot = p < 7 ? 2 * p : 0;
  auto kt = [&](int pos) { int t = pos + rot; t -= t >= 16 ? 14 : 0; return pos < 2 ? pos : t; };
  u64 ep; { gu64* pe = flags + FL_EPOCH / 8; POLL_ISSUE(ep, pe); }
  const __amdgpu_buffer_rsrc_t rfl = mk_rsrc(ws + WS_FLAG, 1u << 20);
  bf16x8 ones = {0x3F80, 0x3F80, 0x3F80, 0x3F80, 0x3F80, 0x3F80, 0x3F80, 0x3F80}; asm volatile("" : "+v"(ones));
  auto cs_publish = [&](const f32x4& c0, const f32x4& c1, int j0, bool two, u64 tag) {
    const f32x4 a = (tok & 2) ? c1 : c0; const u32x4 v = {__float_as_uint((tok & 1) ? a[2] : a[0]), (unsigned)tag, __float_as_uint((tok & 1) ? a[3] : a[1]), (unsigned)tag};
    if (tok < (two ? 4 : 2)) st16_sc1(v, rfl, ((j0 + 2 * (tok & 2)) * 16 + 4 * g + 2 * (tok & 1)) * 8, GR_CS + (h * 8 + p) * 1664);
    SB; };

  if (w < 4) {
    const u32x4 sa = mk_srd(x, S_ * D_ * 2);
    unsigned va0, va1;
    { const int r = lane >> 3; va0 = (p * 256 + wx * 64 + r) * 2048 + (((lane & 7) ^ (r >> 1)) << 4); va1 = va0 ^ 64; }
    const unsigned sl = lb + L1_A + wx * 8192;
    const unsigned rb0 = opq(lb + L1_BH + (8 * g + 4 * (g & 1) + q) * 224 + 8 * pp), rb1 = opq(lb + L1_BH + (8 * g + 4 * ((g & 1) ^ 1) + q) * 224 + 8 * pp);
    f32x4 acc[4][7], cs0, cs1;
    bf16x8 fa[2][4], fb[2][7];
    auto dmaA = [&](auto ST, auto J, unsigned sk) { constexpr int st = ST(), j = J(); if (j & 1) DMA(sa, va1, sk, j * 16384, sl, st * 32768 + j * 1024); else DMA(sa, va0, sk, j * 16384, sl, st * 32768 + j * 1024); };
    auto rdA = [&](auto ST, auto KS, auto M) { constexpr int st = ST(), ks = KS(), m = M(); fa[ks][m] = lds128((ks ? ra1 : ra0) + st * 32768 + m * 2048); SB; };
    auto rdB = [&](auto ST, auto KS, auto J) { constexpr int st = ST(), ks = KS(), j = J(); constexpr int o = st * 14336 + ks * 7168 + j * 32; fb[ks][j] = cat(ldstr(rb0 + o), ldstr(rb1 + o)); SB; };
    auto load = [&](auto ST, auto DO, unsigned sk) {
      constexpr int o = ST() ^ 1; constexpr bool d = DO();
      rdA(ST, IC<0>{}, IC<0>{}); rdB(ST, IC<0>{}, IC<0>{}); rdB(ST, IC<0>{}, IC<1>{}); if (d) dmaA(IC<o>{}, IC<0>{}, sk);
      rdA(ST, IC<0>{}, IC<1>{}); rdB(ST, IC<0>{}, IC<2>{}); rdB(ST, IC<0>{}, IC<3>{}); if (d) dmaA(IC<o>{}, IC<1>{}, sk);
      rdA(ST, IC<0>{}, IC<2>{}); rdB(ST, IC<0>{}, IC<4>{}); rdB(ST, IC<0>{}, IC<5>{}); if (d) dmaA(IC<o>{}, IC<2>{}, sk);
      rdA(ST, IC<0>{}, IC<3>{}); rdB(ST, IC<0>{}, IC<6>{}); rdB(ST, IC<1>{}, IC<0>{}); if (d) dmaA(IC<o>{}, IC<3>{}, sk);
      rdA(ST, IC<1>{}, IC<0>{}); rdB(ST, IC<1>{}, IC<1>{}); rdB(ST, IC<1>{}, IC<2>{}); if (d) dmaA(IC<o>{}, IC<4>{}, sk);
      rdA(ST, IC<1>{}, IC<1>{}); rdB(ST, IC<1>{}, IC<3>{}); rdB(ST, IC<1>{}, IC<4>{}); if (d) dmaA(IC<o>{}, IC<5>{}, sk);
      rdA(ST, IC<1>{}, IC<2>{}); rdB(ST, IC<1>{}, IC<5>{}); if (d) dmaA(IC<o>{}, IC<6>{}, sk);
      rdA(ST, IC<1>{}, IC<3>{}); rdB(ST, IC<1>{}, IC<6>{}); if (d) dmaA(IC<o>{}, IC<7>{}, sk);
    };
    auto mm_ = [&](auto F) {
#pragma unroll
      for (int m = 0; m < 4; ++m)
#pragma unroll
        for (int j = 0; j < 7; ++j) mk2<F()>(acc[m][j], fb[0][j], fa[0][m], fb[1][j], fa[1][m]);
      SB; };
    auto mm = [&]() { mm_(IC<0>{}); };
#define A8(ST, sk) dmaA(IC<ST>{}, IC<0>{}, sk); dmaA(IC<ST>{}, IC<1>{}, sk); dmaA(IC<ST>{}, IC<2>{}, sk); dmaA(IC<ST>{}, IC<3>{}, sk); dmaA(IC<ST>{}, IC<4>{}, sk); dmaA(IC<ST>{}, IC<5>{}, sk); dmaA(IC<ST>{}, IC<6>{}, sk); dmaA(IC<ST>{}, IC<7>{}, sk)
#define A4(ST, J, sk) dmaA(IC<ST>{}, IC<J>{}, sk); dmaA(IC<ST>{}, IC<J + 1>{}, sk); dmaA(IC<ST>{}, IC<J + 2>{}, sk); dmaA(IC<ST>{}, IC<J + 3>{}, sk)
    if (odd) { A4(0, 4, 0u); A4(0, 0, 0u); } else { A4(0, 0, 0u); A4(0, 4, 0u); }
    if (PFCODE & 1) touch_code(lb + L1_DUMP, lane, wx);
    unsigned braw; asm volatile("buffer_load_dword %0, %1, %2, 0 offen" : "=v"(braw) : "v"(h * 224 + min(lane, 55) * 4), "s"(mk_srd(b1, F_ * 2)) : "memory"); SB;
    cs0 = zacc(); cs1 = zacc();
    auto colsum = [&](bool now) { colsum7(cs0, cs1, fb[0], fb[1], ones, now ? wx : 4); SB; };
    if (tid == 0) *(LAS u64*)(size_t)(lb + L1_CNT) = 0ull;
    STAMP(1);
    WAIT_V(0); POLL_PIN(ep); const u64 tag = uni64(ep) + 1;
    A8(1, 128u);
    asm volatile("" : "+v"(braw));
    if (wx == 0) { const int d = min(lane, 55), pb = d >> 3, j = odd ? (pb ? pb - 1 : 6) : pb; *(LAS f32x2*)(size_t)(lb + L1_BIAS + j * 64 + (d & 7) * 8) = (f32x2){bflo(braw) * LOG2E, bfhi(braw) * LOG2E}; }
    WAIT_L0; BAR; SB; STAMP(4);
    load(IC<0>{}, IC<0>{}, 0u); WAIT_L0; BAR; SB; STAMP(5);
#pragma unroll
    for (int t = 0; t < 4; t += 2) {
      if (t == 0) mm_(IC<1>{}); else mm();
      colsum((p == 7) == (t == 0)); WAIT_V(0); BAR; SB;
      load(IC<1>{}, IC<1>{}, (unsigned)kt(t + 2) * 128u); WAIT_L0; BAR; SB;
      mm(); colsum((p == 7) == (t == 0)); WAIT_V(0); BAR; SB;
      load(IC<0>{}, IC<1>{}, (unsigned)kt(t + 3) * 128u); if (t == 2) cs_publish(cs0, cs1, wx, wx < 3, tag); WAIT_L0; BAR; SB;
    }
    auto touch_w2 = [&]() { const int l2 = min(lane, 55) + 56 * (wx + 4 * (b >> 3));
      TOUCH((const GAS char*)w2 + ((b & 1) * 1792 + ((b & 7) >> 1) * 448) * 2048 + l2 * 128, lb + L1_DUMP);
      TOUCH((const GAS char*)wo + (b * 64 + wx * 16 + (lane & 15)) * 128, lb + L1_DUMP); };
    const int tw = 4 + 2 * ((b >> 3) % 5);
    for (int t = 4; t < 14; t += 2) {
      mm(); WAIT_V(0); BAR; SB;
      if (TOUCHW == 2 && t == tw) touch_w2();
      load(IC<1>{}, IC<1>{}, (unsigned)kt(t + 2) * 128u);
      if (TOUCHW == 3 && t == tw) touch_w2();
      WAIT_L0; BAR; SB;
      mm(); WAIT_V(0); BAR; SB;
      load(IC<0>{}, IC<1>{}, (unsigned)kt(t + 3) * 128u); WAIT_L0; BAR; SB;
    }
    STAMP(6);
    mm(); WAIT_V(0); BAR; SB;
    load(IC<1>{}, IC<0>{}, 0u);
    if (PFCODE & 2) touch_code(lb + L1_DUMP, lane, wx);
    if (TOUCHW == 1) touch_w2();
    WAIT_L0; BAR; SB;
    mm(); BAR; MFMA_PAD; STAMP(7);
    const unsigned tb = opq(lb + L1_CS + g * 16); f32x4 cs[7], bl[7]; f32x2 rl[4], rml[4];
#pragma unroll
    for (int j = 0; j < 7; ++j) { cs[j] = *(const LAS f32x4*)(size_t)(tb + j * 64); bl[j] = *(const LAS f32x4*)(size_t)(tb + (L1_BIAS - L1_CS) + j * 64); }
#pragma unroll
    for (int m = 0; m < 4; ++m) { const f32x2 st = *(const LAS f32x2*)(size_t)(lb + L1_ST + (wx * 64 + m * 16 + tok) * 8); const float a = opqf(st[0] * LOG2E), c = opqf(-a * st[1]); rl[m] = opq2((f32x2){a, a}); rml[m] = opq2((f32x2){c, c}); }
    SB;
    Phase2<0> p2; p2.init(lb, lane, wx, P);
    const __amdgpu_buffer_rsrc_t ract = mk_rsrc(ws + WS_ACT, 128 * 112 * 1024);
    auto mish8 = [&](int m0, int j0, int m1, int j1) -> u32x4 {
      const f32x2 a[4] = {acc[m0][j0].xy, acc[m0][j0].zw, acc[m1][j1].xy, acc[m1][j1].zw}, c[4] = {cs[j0].xy, cs[j0].zw, cs[j1].xy, cs[j1].zw}, bb[4] = {bl[j0].xy, bl[j0].zw, bl[j1].xy, bl[j1].zw};
      f32x2 t[4], hs[4], e[4], d[4]; unsigned o[4];
#define ST4(stmt) _Pragma("unroll") for (int i = 0; i < 4; ++i) { stmt; } SB
      ST4(t[i] = c[i] * rml[i < 2 ? m0 : m1] + bb[i]);
      ST4(hs[i] = a[i] * rl[i < 2 ? m0 : m1] + t[i]);
      ST4(e[i] = opq2((f32x2){ex2(hs[i][0]), ex2(hs[i][1])}));
      ST4(t[i] = e[i] + 2.0f);
      ST4(d[i] = opq2(e[i] * t[i] + 2.0f));
      ST4(e[i] = opq2((f32x2){rcp(d[i][0]), rcp(d[i][1])}));
      ST4(t[i] = e[i] * (-2.0f / LOG2E) + 1.0f / LOG2E);
      ST4(d[i] = hs[i] * t[i]);
      ST4(o[i] = pk2(d[i][0], d[i][1]));
      return swap16(o[0], o[1], o[2], o[3]); };
    const unsigned vpair = (unsigned)((((g & 1) << 1) | (g >> 1)) * 256 + tok * 16), mb0 = p * 16 + wx * 4;
    auto pair = [&](auto J, auto M) { constexpr int j = J(), m = M();
      st16_sc1(mish8(m, j, m, j + 1), ract, vpair, ((mb0 + m) * 112 + ((7 * h + odd + j) >> 1)) * 1024); SB;
      constexpr int n = 2 * j + m - BLATE; if constexpr (n >= 0 && n < 6) { p2.dmaB<n / 2, 2 * (n & 1)>(); p2.dmaB<n / 2, 1 + 2 * (n & 1)>(); } };
    const int fai = ((2 * p + (wx >> 1)) * 2 + (h >> 4)) * 32 + (h & 15) * 2 + (wx & 1); gu64* fact = flags + FL_ACT / 8 + fai * 16;
    gu64* fa1 = flags + FL_ACT / 8 + (p2.rt * 2 + p2.kh) * 512; u64 pv;
    const unsigned vsing = (unsigned)((g & 1) * 112 * 1024 + (2 * odd + (g >> 1)) * 256 + tok * 16);
    auto single = [&](int m) { st16_sc1(mish8(m, 6, m + 1, 6), ract, vsing, ((mb0 + m) * 112 + ((7 * h + (odd ? 0 : 6)) >> 1)) * 1024); SB; };
    auto prio = [&](int n) { if (XP0 == n) __builtin_amdgcn_s_setprio(0); };
    STAMP(13);
    if (XP0) __builtin_amdgcn_s_setprio(2);
    pair(IC<0>{}, IC<0>{}); pair(IC<0>{}, IC<1>{}); pair(IC<0>{}, IC<2>{}); pair(IC<0>{}, IC<3>{}); prio(4); STAMP(14);
    pair(IC<2>{}, IC<0>{}); prio(5); pair(IC<2>{}, IC<1>{}); prio(6); STAMP(15);
    WAIT_V(2); set_flag(fact, tag, lane); SB; STAMP(8);
    pair(IC<2>{}, IC<2>{}); prio(7); pair(IC<2>{}, IC<3>{}); prio(8);
    pair(IC<4>{}, IC<0>{}); prio(9); POLL_ISSUE(pv, fa1 + (lane & 31) * 16); pair(IC<4>{}, IC<1>{}); prio(10); pair(IC<4>{}, IC<2>{});
    WAIT_V(6); set_flag(flags + FL_ACT2 / 8 + fai, tag, lane); POLL_PIN(pv); STAMP(10);
    if (!__all(pv == tag)) wait_flags<32, 16>(fa1, tag, tmo, lane);
    SB; STAMP(11); p2.dmaA4<0, 0>(); p2.dmaA4<1, 0>(); p2.dmaA4<0, 1>(); STAMP(12);
    pair(IC<4>{}, IC<3>{}); single(0); single(2); STAMP(9);
    p2.fl3 = flags + FL_ACT3 / 8 + fai;
    Query none; P2RUN(p2, P, tag, none);
  } else {
    const int srow = p * 256 + h * 8 + wx * 2; u32x4 xa[4];
    { const u32x4 sx = mk_srd(x, S_ * D_ * 2); const unsigned vx = srow * 2048 + lane * 16;
      asm volatile("buffer_load_dwordx4 %0, %4, %5, 0 offen\n\tbuffer_load_dwordx4 %1, %4, %5, 0 offen offset:1024\n\tbuffer_load_dwordx4 %2, %4, %5, 0 offen offset:2048\n\tbuffer_load_dwordx4 %3, %4, %5, 0 offen offset:3072"
                   : "=&v"(xa[0]), "=&v"(xa[1]), "=&v"(xa[2]), "=&v"(xa[3]) : "v"(vx), "s"(sx) : "memory"); SB; }
    const u32x4 sq = mk_srd(wqkv, D_ * 3 * D_ * 2), sh = mk_srd(w1, D_ * F_ * 2), sp = mk_srd(wphi, 32 * 32 * 32 * 2);
    unsigned vq, vh[4];
    { const int r = lane >> 2; vq = (wx * 16 + r) * 6144 + h * 64 + (((lane & 3) ^ (((r >> 3) & 1) << 1)) << 4); }
#pragma unroll
    for (int i = 0; i < 4; ++i) { const int o = (wx + 4 * i) * 1024 + lane * 16, rp = o / 224, c = (o - rp * 224) >> 4, k = rp ^ (((rp >> 3) & 1) << 2); int cs = c + 2 * odd; if (cs >= 14) cs -= 14;
      vh[i] = k * 7168 + h * 224 + cs * 16; }
    const unsigned slq = lb + L1_BQ + wx * 1024, slh = lb + L1_BH + wx * 1024;
    const unsigned rq0 = opq(lb + L1_BQ + (8 * g + q) * 64 + 32 * (g & 1) + 8 * pp), rq1 = opq(rq0 ^ 32);
    f32x4 acc[3][4][2], cs0, cs1;
    bf16x8 fa[2][4], fb[2][6];
    auto dmaQ = [&](auto ST, auto U, unsigned sk) { constexpr int st = ST(), u = U(); DMA(sq, vq, sk, u * 2048, slq, st * 12288 + u * 4096); };
    auto dmaH = [&](auto ST, auto I, unsigned sk) { constexpr int st = ST(), i = I(); if (i < 3 || wx < 2) DMA(sh, vh[i], sk, 0, slh, st * 14336 + i * 4096); };
    auto rdA = [&](auto ST, auto KS, auto M) { constexpr int st = ST(), ks = KS(), m = M(); fa[ks][m] = lds128((ks ? ra1 : ra0) + st * 32768 + m * 2048); SB; };
    auto rdB = [&](auto ST, auto KS, auto J) { constexpr int st = ST(), ks = KS(), j = J(); constexpr int o = st * 12288 + (j >> 1) * 4096 + ks * 2048; const unsigned a = (j & 1) ? rq1 : rq0;
      fb[ks][j] = cat(ldstr(a + o), ldstr(a + o + 256)); SB; };
    auto load = [&](auto ST, auto DQ, auto DH, int t) {
      constexpr int o = ST() ^ 1; constexpr bool dq = DQ(), dh = DH(); const unsigned skq = (unsigned)kt(t + 1) * (64 * 6144), skh = (unsigned)kt(t + 2) * (64 * 7168);
      rdA(ST, IC<0>{}, IC<0>{}); rdB(ST, IC<0>{}, IC<0>{}); rdB(ST, IC<0>{}, IC<1>{}); if (dq) dmaQ(IC<o>{}, IC<0>{}, skq);
      rdA(ST, IC<0>{}, IC<1>{}); rdB(ST, IC<0>{}, IC<2>{}); rdB(ST, IC<0>{}, IC<3>{}); if (dq) dmaQ(IC<o>{}, IC<1>{}, skq);
      rdA(ST, IC<0>{}, IC<2>{}); rdB(ST, IC<0>{}, IC<4>{}); rdB(ST, IC<0>{}, IC<5>{}); if (dq) dmaQ(IC<o>{}, IC<2>{}, skq);
      rdA(ST, IC<0>{}, IC<3>{}); rdB(ST, IC<1>{}, IC<0>{}); if (dh) dmaH(ST, IC<0>{}, skh);
      rdA(ST, IC<1>{}, IC<0>{}); rdB(ST, IC<1>{}, IC<1>{}); rdB(ST, IC<1>{}, IC<2>{}); if (dh) dmaH(ST, IC<1>{}, skh);
      rdA(ST, IC<1>{}, IC<1>{}); rdB(ST, IC<1>{}, IC<3>{}); if (dh) dmaH(ST, IC<2>{}, skh);
      rdA(ST, IC<1>{}, IC<2>{}); rdB(ST, IC<1>{}, IC<4>{});
      rdA(ST, IC<1>{}, IC<3>{}); rdB(ST, IC<1>{}, IC<5>{});
      if (dh) { if (wx < 2) { dmaH(ST, IC<3>{}, skh); WAIT_V(7); } else WAIT_V(6); }
    };
    auto mm_ = [&](auto F) {
#pragma unroll
      for (int m = 0; m < 4; ++m)
#pragma unroll
        for (int j = 0; j < 6; ++j) mk2<F()>(acc[j >> 1][m][j & 1], fb[0][j], fa[0][m], fb[1][j], fa[1][m]);
      SB; };
    auto mm = [&]() { mm_(IC<0>{}); };
#define H4(ST, sk) dmaH(IC<ST>{}, IC<0>{}, sk); dmaH(IC<ST>{}, IC<1>{}, sk); dmaH(IC<ST>{}, IC<2>{}, sk); dmaH(IC<ST>{}, IC<3>{}, sk)
#define Q3(ST, sk) dmaQ(IC<ST>{}, IC<0>{}, sk); dmaQ(IC<ST>{}, IC<1>{}, sk); dmaQ(IC<ST>{}, IC<2>{}, sk)
    H4(0, 0u);
    cs0 = zacc(); cs1 = zacc();
    auto colsum = [&](bool now) { const bf16x8 a[7] = {fb[0][0], fb[0][1], fb[0][2], fb[0][3], fb[0][4], fb[0][5], ones}, c[7] = {fb[1][0], fb[1][1], fb[1][2], fb[1][3], fb[1][4], fb[1][5], ones};
      colsum7(cs0, cs1, a, c, ones, now ? wx : 4); SB; };
    STAMP(1);
    if (wx < 2) WAIT_V(4); else WAIT_V(3);
    asm volatile("" : "+v"(xa[0]), "+v"(xa[1]), "+v"(xa[2]), "+v"(xa[3]), "+v"(ep));
    const u64 tag = uni64(ep) + 1;
    { f32x2 v[2][8], a[2][4]; float mean[2], var[2];
#pragma unroll
      for (int i = 0; i < 16; ++i) { const unsigned u = xa[i >> 2][i & 3]; v[i >> 3][i & 7] = opq2((f32x2){bflo(u), bfhi(u)}); }
      SB;
      auto fold = [&](float (&o)[2]) {
#pragma unroll
        for (int r = 0; r < 2; ++r) { a[r][0] += a[r][1]; a[r][2] += a[r][3]; }
#pragma unroll
        for (int r = 0; r < 2; ++r) { a[r][0] += a[r][2]; o[r] = a[r][0][0] + opqf(a[r][0][1]); }
        SB; o[0] = wave_sum(o[0]) * (1.0f / D_); o[1] = wave_sum(o[1]) * (1.0f / D_); SB; };
#pragma unroll
      for (int i = 0; i < 8; ++i) a[i >> 2][i & 3] = v[i >> 2][i & 3] + v[i >> 2][4 + (i & 3)];
      fold(mean);
#pragma unroll
      for (int i = 0; i < 16; ++i) v[i >> 3][i & 7] -= opqf(mean[i >> 3]);
      SB;
#pragma unroll
      for (int i = 0; i < 8; ++i) a[i >> 2][i & 3] = v[i >> 2][i & 3] * v[i >> 2][i & 3];
#pragma unroll
      for (int i = 0; i < 8; ++i) a[i >> 2][i & 3] += v[i >> 2][4 + (i & 3)] * v[i >> 2][4 + (i & 3)];
      fold(var);
      const u32x4 gr = {__float_as_uint(lane ? mean[1] : mean[0]), (unsigned)tag, __float_as_uint(__builtin_amdgcn_rsqf((lane ? var[1] : var[0]) + 1e-5f)), (unsigned)tag};
      if (lane < 2) st16_sc1(gr, rfl, lane * 16, GR_ST + srow * 16);
      SB; }
    STAMP(2);
    Q3(0, 0u);
    WAIT_V(4);
    STAMP(3); BAR; SB; STAMP(4);
    if (wx < 2) { const unsigned vp = h * 2048 + wx * 1024 + lane * 16, slp = lb + L1_WPHI + wx * 1024; DMA(sp, vp, 0u, 0, slp, 0); }
    H4(1, 64u * 7168u); Q3(1, 64u * 6144u);
    if (wx < 2) WAIT_V(8); else WAIT_V(6);
    BAR; SB; STAMP(5);
    load(IC<0>{}, IC<0>{}, IC<1>{}, 0); WAIT_L0; BAR; SB;
    mm_(IC<1>{}); colsum(p == 7); WAIT_V(3); BAR; SB;
    load(IC<1>{}, IC<1>{}, IC<1>{}, 1); WAIT_L0; BAR; SB;
    mm(); colsum(p == 7); WAIT_V(3); BAR; SB;
    load(IC<0>{}, IC<1>{}, IC<1>{}, 2); WAIT_L0; BAR; SB;
    mm(); colsum(p != 7); WAIT_V(3); BAR; SB;
    load(IC<1>{}, IC<1>{}, IC<1>{}, 3); WAIT_L0; BAR; SB;
    mm(); colsum(p != 7); WAIT_V(3); BAR; MFMA_PAD;
    cs_publish(cs0, cs1, 7 + wx, wx < 2, tag);
    for (int t = 4; t < 14; t += 2) {
      load(IC<0>{}, IC<1>{}, IC<1>{}, t); WAIT_L0; BAR; SB;
      mm(); WAIT_V(3); BAR; SB;
      load(IC<1>{}, IC<1>{}, IC<1>{}, t + 1); WAIT_L0; BAR; SB;
      mm(); WAIT_V(3); BAR; SB;
    }
    STAMP(6);
    const int ci = min(wx * 64 + lane, 206 + (lane & 1));
    const unsigned vst = GR_ST + (p * 256 + wx * 64 + lane) * 16, vcs = GR_CS + (h * 8 + (ci & 1) * 4) * 1664 + (ci >> 1) * 16;
    const u32x4 sfl = mk_srd(ws + WS_FLAG, 1u << 20);
    u32x4 gs, gc[4]; LD16_ISSUE(gs, sfl, vst, 0u);
#pragma unroll
    for (int i = 0; i < 4; ++i) LD16_ISSUE(gc[i], sfl, vcs, i * 1664u);
    load(IC<0>{}, IC<1>{}, IC<0>{}, 14);
    WAIT_V(8); WAIT_L0; BAR; SB;
    mm(); WAIT_V(0); BAR; SB;
    load(IC<1>{}, IC<0>{}, IC<0>{}, 15);
    { auto stale = [&]() { unsigned d = differ(gs[1], gs[3], (unsigned)tag);
#pragma unroll
        for (int i = 0; i < 4; ++i) d |= differ(gc[i][1], gc[i][3], (unsigned)tag);
        return !__all(d == 0u); };
      asm volatile("" : "+v"(gs), "+v"(gc[0]), "+v"(gc[1]), "+v"(gc[2]), "+v"(gc[3]));
      if (__builtin_expect(stale(), 0)) {
#pragma nounroll
        for (unsigned spins = 0; spins < (1u << 16); ++spins) {
          asm volatile("buffer_load_dwordx4 %0, %1, %2, 0 offen sc1" : "+v"(gs) : "v"(vst), "s"(sfl) : "memory");
#pragma unroll
          for (int i = 0; i < 4; ++i) asm volatile("buffer_load_dwordx4 %0, %1, %2, %3 offen sc1" : "+v"(gc[i]) : "v"(vcs), "s"(sfl), "s"(i * 1664u) : "memory");
          asm volatile("s_waitcnt vmcnt(0)" : "+v"(gs), "+v"(gc[0]), "+v"(gc[1]), "+v"(gc[2]), "+v"(gc[3]) :: "memory");
          if (!stale()) break; } }
      float c0 = opqf(opqf(__uint_as_float(gc[0][0]) + __uint_as_float(gc[1][0])) + opqf(__uint_as_float(gc[2][0]) + __uint_as_float(gc[3][0])));
      float c1 = opqf(opqf(__uint_as_float(gc[0][2]) + __uint_as_float(gc[1][2])) + opqf(__uint_as_float(gc[2][2]) + __uint_as_float(gc[3][2])));
      c0 = opqf(c0 + dpp<0xB1>(c0)); c1 = opqf(c1 + dpp<0xB1>(c1));
      *(LAS f32x2*)(size_t)(lb + L1_CS + (ci >> 1) * 8) = (f32x2){c0, c1};
      *(LAS f32x2*)(size_t)(lb + L1_ST + (wx * 64 + lane) * 8) = (f32x2){__uint_as_float(gs[2]), __uint_as_float(gs[0])}; SB; }
    WAIT_L0; BAR; SB;
    mm(); MFMA_PAD; STAMP(7);
    if (YPRIO) __builtin_amdgcn_s_setprio(1);
    {
    const int lane = opq(__lane_id()), tok = lane & 15, g = lane >> 4, q = (lane >> 2) & 3, pp = lane & 3;
    bf16x8 wp[2];
#pragma unroll
    for (int ff = 0; ff < 2; ++ff) { const unsigned a = lb + L1_WPHI + (4 * g + q) * 64 + ff * 32 + 8 * pp; wp[ff] = cat(ldstr(a), ldstr(a + 1024)); }
    f32x4 cs[6]; float rs[4], mean[4], rl[4];
#pragma unroll
    for (int j = 0; j < 6; ++j) cs[j] = *(const LAS f32x4*)(size_t)(lb + L1_CS + (7 + j) * 64 + g * 16);
#pragma unroll
    for (int m = 0; m < 4; ++m) { const f32x2 st = *(const LAS f32x2*)(size_t)(lb + L1_ST + (wx * 64 + m * 16 + tok) * 8); rs[m] = st[0]; mean[m] = opqf(-st[1]); rl[m] = st[0] * LOG2E; }
    const f32x4 zero4 = {0.f, 0.f, 0.f, 0.f};
    auto project = [&](int u, f32x4 (&pr)[4][2]) {
#pragma unroll
      for (int m = 0; m < 4; ++m) { const bf16x8 kb = pk8(acc[u][m][0] + cs[2 * u] * mean[m], acc[u][m][1] + cs[2 * u + 1] * mean[m]); pr[m][0] = mfma(wp[0], kb, zero4); pr[m][1] = mfma(wp[1], kb, zero4); }
      SB; };
    auto extrema = [&](const f32x4 (&pr)[4][2], float (&ma)[4], float (&mb)[4]) {
#pragma unroll
      for (int m = 0; m < 4; ++m) { ma[m] = vmax8(pr[m][0], pr[m][1]); mb[m] = vmax8(-pr[m][0], -pr[m][1]); }
      spread4(gather4(ma[0], mb[0], ma[1], mb[1], vmax), ma[0], mb[0], ma[1], mb[1]);
      spread4(gather4(ma[2], mb[2], ma[3], mb[3], vmax), ma[2], mb[2], ma[3], mb[3]);
#pragma unroll
      for (int m = 0; m < 4; ++m) { ma[m] *= -rl[m]; mb[m] *= -rl[m]; }
      SB; };
    auto expo = [&](int m, const f32x4 (&pr)[2], float ma, float mb, f32x4 (&ep)[2], f32x4 (&en)[2]) {
#pragma unroll
      for (int ff = 0; ff < 2; ++ff) { ep[ff] = pr[ff] * rl[m] + ma; en[ff] = pr[ff] * -rl[m] + mb; }
      SB;
#pragma unroll
      for (int ff = 0; ff < 2; ++ff)
#pragma unroll
        for (int r = 0; r < 4; ++r) { ep[ff][r] = ex2(ep[ff][r]); en[ff][r] = ex2(en[ff][r]); }
      SB; };
    { const unsigned swp = (tok & 1) | (((tok >> 2) & 1) << 1) | (((tok >> 1) & 1) << 2) | (tok & 8);
      const unsigned wph = lb + L1_PHI + (wx * 64 + tok) * 128 + ((g ^ swp) << 3), wv = lb + L1_V + (wx * 64 + tok) * 64 + ((g ^ (tok >> 1)) << 3);
      const unsigned wp0 = opq(wph), wp1 = opq(wph ^ 32), wp2 = opq(wph ^ 64), wp3 = opq(wph ^ 96), wv0 = opq(wv), wv1 = opq(wv ^ 32);
      auto put = [](unsigned a, f32x4 v) { *(LAS u32x2*)(size_t)a = (u32x2){pk2(v[0], v[1]), pk2(v[2], v[3])}; };
      f32x4 pr[4][2], ep[4][2], en[4][2]; float ma[4], mb[4];
      WAIT_L0; project(1, pr);
#pragma unroll
      for (int m = 0; m < 4; ++m) { put(wv0 + m * 1024, (acc[2][m][0] + cs[4] * mean[m]) * rs[m]); put(wv1 + m * 1024, (acc[2][m][1] + cs[5] * mean[m]) * rs[m]); }
      SB; extrema(pr, ma, mb);
#pragma unroll
      for (int m = 0; m < 4; ++m) expo(m, pr[m], ma[m], mb[m], ep[m], en[m]);
#pragma unroll
      for (int m = 0; m < 4; ++m) { const f32x4 a = ep[m][0] + ep[m][1], c = en[m][0] + en[m][1]; const f32x2 a2 = a.xy + a.zw, c2 = c.xy + c.zw; ma[m] = a2[0] + opqf(a2[1]); mb[m] = c2[0] + opqf(c2[1]); }
      auto fadd = [](float a, float c) { return a + c; };
      spread4(rcp(gather4(ma[0], mb[0], ma[1], mb[1], fadd)), ma[0], mb[0], ma[1], mb[1]);
      spread4(rcp(gather4(ma[2], mb[2], ma[3], mb[3], fadd)), ma[2], mb[2], ma[3], mb[3]);
      SB;
#pragma unroll
      for (int m = 0; m < 4; ++m) { ep[m][0] *= ma[m]; ep[m][1] *= ma[m]; en[m][0] *= mb[m]; en[m][1] *= mb[m]; SB;
        put(wp0 + m * 2048, ep[m][0]); put(wp1 + m * 2048, ep[m][1]); put(wp2 + m * 2048, en[m][0]); put(wp3 + m * 2048, en[m][1]); SB; } }
    STAMP(8);
    asm volatile("s_waitcnt lgkmcnt(0)" ::: "memory");
    if (lane == 0) { const unsigned one = 1, a = lb + L1_CNT; asm volatile("ds_add_u32 %0, %1" :: "v"(a), "v"(one) : "memory"); }
    Query Q; project(0, Q.pq);
#pragma unroll
    for (int m = 0; m < 4; ++m) { Q.rl[m] = rl[m]; asm volatile("" : "+v"(Q.pq[m][0]), "+v"(Q.pq[m][1]), "+v"(Q.rl[m])); }
    SB;
    lds_wait(lb + L1_CNT, 4u); SB; STAMP(9);
    { const bf16x8 ones = {0x3F80, 0x3F80, 0x3F80, 0x3F80, 0x3F80, 0x3F80, 0x3F80, 0x3F80};
      unsigned ap[2], av[2][2];
#pragma unroll
      for (int t = 0; t < 2; ++t) { const int tk = 8 * g + 4 * t + q, t15 = tk & 15; const unsigned sw = (t15 & 1) | (((t15 >> 2) & 1) << 1) | (((t15 >> 1) & 1) << 2) | (t15 & 8);
        ap[t] = opq(lb + L1_PHI + tk * 128 + (((4 * wx + pp) ^ sw) << 3));
        av[t][0] = opq(lb + L1_V + tk * 64 + ((pp ^ (t15 >> 1)) << 3)); av[t][1] = opq(av[t][0] ^ 32); }
      bf16x8 a[8], b0[8], b1_[8];
#pragma unroll
      for (int ks = 0; ks < 8; ++ks) { a[ks] = cat(ldstr(ap[0] + ks * 4096), ldstr(ap[1] + ks * 4096));
        b0[ks] = cat(ldstr(av[0][0] + ks * 2048), ldstr(av[1][0] + ks * 2048)); b1_[ks] = cat(ldstr(av[0][1] + ks * 2048), ldstr(av[1][1] + ks * 2048)); }
      SB; STAMP(12);
      f32x4 kv0 = zero4, kv1 = zero4, zz = zero4;
#pragma unroll
      for (int ks = 0; ks < 8; ++ks) { kv0 = mfma(a[ks], b0[ks], kv0); kv1 = mfma(a[ks], b1_[ks], kv1); zz = mfma(a[ks], ones, zz); }
      const __amdgpu_buffer_rsrc_t rkv = mk_rsrc(ws + WS_KVP, 3u << 20);
      st16_sc1(__builtin_bit_cast(u32x4, pk8(kv0, kv1)), rkv, lane * 16, (h * 8 + p) * 4096 + wx * 1024);
      if (tok == 0) st16_sc1(__builtin_bit_cast(u32x4, zz), rkv, g * 16, (unsigned)(WS_ZP - WS_KVP) + (h * 8 + p) * 256 + wx * 64);
      SB; STAMP(13); }
    SB; if (YPRIO) __builtin_amdgcn_s_setprio(0); WAIT_L0; STAMP(11);
    Phase2<1> p2; p2.init(lb, lane, wx, P);
    P2RUN(p2, P, tag, Q);
    }
  }
}
}

extern "C" void kernel_launch(void* const* d_in, const int* in_sizes, int n_in, void* d_out, int out_size, void* d_ws, size_t ws_size, hipStream_t stream) {
  k_fused<<<256, 512, 0, stream>>>((const u16*)d_in[0], (const u16*)d_in[1], (const u16*)d_in[2], (const u16*)d_in[3], (const u16*)d_in[5], (const u16*)d_in[6], (const u16*)d_in[7], (char*)d_ws, (u16*)d_out);
}
```

```cpp
#define SA 0
#define SBB 0
#define SW 0
#define SBLK 100
#define CLK "s_memtime"
#define MULN 0
#define MULD 4
#include <hip/hip_runtime.h>
#include <hip/hip_bf16.h>
#include <type_traits>

namespace {
#define LAS __attribute__((address_space(3)))
#define GAS __attribute__((address_space(1)))
typedef unsigned short u16;
typedef unsigned u32x4 __attribute__((ext_vector_type(4)));
typedef unsigned u32x2 __attribute__((ext_vector_type(2)));
typedef short bf16x8 __attribute__((ext_vector_type(8)));
typedef short s16x4 __attribute__((ext_vector_type(4)));
typedef float f32x4 __attribute__((ext_vector_type(4)));
typedef float f32x2 __attribute__((ext_vector_type(2)));
typedef __bf16 bf16x2_t __attribute__((ext_vector_type(2)));

constexpr int S_ = 2048, D_ = 1024, F_ = 3584;
constexpr size_t WS_FLAG = 0;
constexpr size_t WS_XN = 1u << 20;
constexpr size_t WS_ACT = WS_XN + (4u << 20);
constexpr size_t WS_O = WS_ACT + (15u << 20);
constexpr size_t WS_KVP = WS_O + (4u << 20);
constexpr size_t WS_ZP = WS_KVP + (2u << 20);
constexpr size_t WS_YX = WS_ZP + (1u << 20);
constexpr size_t WS_STAMP = 64u << 20;
#define DIAG 1
constexpr int NSTAMP = 32;
#define STAMP_DECL unsigned st_c = 0, st_r = 0
#define STAMP(i) do { if ((i) == SA || (i) == SBB) { unsigned long long c_; __builtin_amdgcn_sched_barrier(0); asm volatile(CLK " %0\n\ts_waitcnt lgkmcnt(0)" : "=s"(c_) :: "memory"); \
  if ((i) == SA) st_c = (unsigned)c_; else st_r = (unsigned)c_; asm volatile("" : "+v"(st_c), "+v"(st_r)); __builtin_amdgcn_sched_barrier(0); } } while (0)
#define STAMP_OUT(wv) do { if ((wv) == SW && blockIdx.x == SBLK) { const unsigned d_ = __builtin_amdgcn_readfirstlane(st_r - st_c) * MULN / MULD + 500; const unsigned long long t0_ = __builtin_amdgcn_s_memrealtime(); \
  while (__builtin_amdgcn_s_memrealtime() - t0_ < d_ && d_ < 100000u) __builtin_amdgcn_s_sleep(1); } } while (0)
constexpr unsigned FL_EPOCH = 0;
constexpr unsigned FL_O = 1024;
constexpr unsigned FL_YX = 16384;
constexpr unsigned FL_ACT2 = 40960;
constexpr unsigned FL_TMO = 61440;
constexpr unsigned GR_ST = 65536;
constexpr unsigned FL_KV = 131072;
constexpr unsigned FL_ACT = 262144;
constexpr unsigned GR_CS = 524288;
#define YPRIO 0
#define XP0 0
#define EARLYA 0
#define PFCODE 7
#define SENDV 0
#define BLATE 6
#define TOUCH0 0
#define TOUCHW 1
constexpr float LOG2E = 1.4426950408889634f;

#define SB __builtin_amdgcn_sched_barrier(0)
#define BAR __builtin_amdgcn_s_barrier()
#define WAIT_L0 do { __builtin_amdgcn_s_waitcnt(0xC07F); SB; } while (0)
#define WAIT_V(n) asm volatile("s_waitcnt vmcnt(%0)" :: "i"(n) : "memory")
template <int N> using IC = std::integral_constant<int, N>;

__device__ __forceinline__ u32x4 mk_srd(const void* p, unsigned bytes) {
  const unsigned long long a = (unsigned long long)p; u32x4 r;
  r.x = __builtin_amdgcn_readfirstlane((unsigned)a); r.y = __builtin_amdgcn_readfirstlane((unsigned)(a >> 32)) & 0xffffu; r.z = bytes; r.w = 0x00020000u; return r; }
#define DMA(srd, voff, sk, KOFF, sl, LOFF) do { unsigned _t; asm volatile("s_add_u32 m0, %4, %5\n\ts_add_u32 %0, %3, %6\n\tbuffer_load_dwordx4 %1, %2, %0 offen lds" \
    : "=&s"(_t) : "v"(voff), "s"(srd), "s"(sk), "s"(sl), "i"(LOFF), "i"(KOFF) : "memory", "scc"); SB; } while (0)
#define DMA_SC1(srd, voff, sk, KOFF, sl, LOFF) do { unsigned _t; asm volatile("s_add_u32 m0, %4, %5\n\ts_add_u32 %0, %3, %6\n\tbuffer_load_dwordx4 %1, %2, %0 offen sc1 lds" \
    : "=&s"(_t) : "v"(voff), "s"(srd), "s"(sk), "s"(sl), "i"(LOFF), "i"(KOFF) : "memory", "scc"); SB; } while (0)
#define TOUCH(ptr, sl) do { asm volatile("s_mov_b32 m0, %1\n\ts_nop 0\n\tglobal_load_lds_dword %0, off" :: "v"(ptr), "s"(sl) : "memory"); SB; } while (0)
__device__ __forceinline__ bf16x8 lds128(unsigned a) { return *(const LAS bf16x8*)(size_t)a; }
__device__ __forceinline__ s16x4 ldstr(unsigned a) { return __builtin_amdgcn_ds_read_tr16_b64_v4i16((LAS s16x4*)(size_t)a); }
__device__ __forceinline__ bf16x8 cat(s16x4 lo, s16x4 hi) { return __builtin_shufflevector(lo, hi, 0, 1, 2, 3, 4, 5, 6, 7); }
__device__ __forceinline__ unsigned opq(unsigned x) { asm volatile("" : "+v"(x)); return x; }
__device__ __forceinline__ float opqf(float x) { asm("" : "+v"(x)); return x; }
__device__ __forceinline__ f32x2 opq2(f32x2 x) { asm("" : "+v"(x)); return x; }
__device__ __forceinline__ f32x4 mfma(bf16x8 a, bf16x8 b, f32x4 c) { return __builtin_amdgcn_mfma_f32_16x16x32_bf16(a, b, c, 0, 0, 0); }
__device__ __forceinline__ void mfma_acc(f32x4& d, bf16x8 a, bf16x8 b) { asm volatile("v_mfma_f32_16x16x32_bf16 %0, %1, %2, %0" : "+v"(d) : "v"(a), "v"(b)); }
__device__ __forceinline__ void mfma_k2(f32x4& d, bf16x8 a0, bf16x8 b0, bf16x8 a1, bf16x8 b1) {
  asm volatile("v_mfma_f32_16x16x32_bf16 %0, %1, %2, %0\n\tv_mfma_f32_16x16x32_bf16 %0, %3, %4, %0" : "+v"(d) : "v"(a0), "v"(b0), "v"(a1), "v"(b1)); }
__device__ __forceinline__ void mfma_k2_first(f32x4& d, bf16x8 a0, bf16x8 b0, bf16x8 a1, bf16x8 b1) {
  asm volatile("v_mfma_f32_16x16x32_bf16 %0, %1, %2, 0\n\tv_mfma_f32_16x16x32_bf16 %0, %3, %4, %0" : "=&v"(d) : "v"(a0), "v"(b0), "v"(a1), "v"(b1)); }
template <bool FIRST> __device__ __forceinline__ void mk2(f32x4& d, bf16x8 a0, bf16x8 b0, bf16x8 a1, bf16x8 b1) { if (FIRST) mfma_k2_first(d, a0, b0, a1, b1); else mfma_k2(d, a0, b0, a1, b1); }
template <bool FIRST> __device__ __forceinline__ void mk4(f32x4& d, bf16x8 a0, bf16x8 b0, bf16x8 a1, bf16x8 b1, bf16x8 a2, bf16x8 b2, bf16x8 a3, bf16x8 b3) {
#define MK4_REST "v_mfma_f32_16x16x32_bf16 %0, %3, %4, %0\n\tv_mfma_f32_16x16x32_bf16 %0, %5, %6, %0\n\tv_mfma_f32_16x16x32_bf16 %0, %7, %8, %0"
  if (FIRST) asm volatile("v_mfma_f32_16x16x32_bf16 %0, %1, %2, 0\n\t" MK4_REST : "=&v"(d) : "v"(a0), "v"(b0), "v"(a1), "v"(b1), "v"(a2), "v"(b2), "v"(a3), "v"(b3));
  else asm volatile("v_mfma_f32_16x16x32_bf16 %0, %1, %2, %0\n\t" MK4_REST : "+v"(d) : "v"(a0), "v"(b0), "v"(a1), "v"(b1), "v"(a2), "v"(b2), "v"(a3), "v"(b3)); }
__device__ __forceinline__ void mfma_k2x2(f32x4& d, f32x4& e, bf16x8 a0, bf16x8 c0, bf16x8 b0, bf16x8 a1, bf16x8 c1, bf16x8 b1) {
  asm volatile("v_mfma_f32_16x16x32_bf16 %0, %2, %4, %0\n\tv_mfma_f32_16x16x32_bf16 %1, %3, %4, %1\n\tv_mfma_f32_16x16x32_bf16 %0, %5, %7, %0\n\tv_mfma_f32_16x16x32_bf16 %1, %6, %7, %1"
               : "+v"(d), "+v"(e) : "v"(a0), "v"(c0), "v"(b0), "v"(a1), "v"(c1), "v"(b1)); }
__device__ __forceinline__ f32x4 zacc() { f32x4 z = {0.f, 0.f, 0.f, 0.f}; asm volatile("" : "+v"(z)); return z; }
#define CS_M(d, a) "v_mfma_f32_16x16x32_bf16 " d ", " a ", %[o], " d "\n\t"
__device__ __forceinline__ void colsum7(f32x4& c0, f32x4& c1, const bf16x8 (&a)[7], const bf16x8 (&b)[7], bf16x8 ones, unsigned sel) {
  asm volatile("s_cmp_lt_u32 %[s], 2\n\ts_cbranch_scc1 .Lcs01_%=\n\ts_cmp_eq_u32 %[s], 2\n\ts_cbranch_scc1 .Lcs2_%=\n\ts_cmp_eq_u32 %[s], 3\n\ts_cbranch_scc0 .Lcs9_%=\n\t"
               CS_M("%0", "%[a3]") CS_M("%0", "%[b3]") "s_branch .Lcs9_%=\n"
               ".Lcs2_%=:\n\t" CS_M("%0", "%[a2]") CS_M("%0", "%[b2]") CS_M("%1", "%[a6]") CS_M("%1", "%[b6]") "s_branch .Lcs9_%=\n"
               ".Lcs01_%=:\n\ts_cmp_eq_u32 %[s], 0\n\ts_cbranch_scc1 .Lcs0_%=\n\t" CS_M("%0", "%[a1]") CS_M("%0", "%[b1]") CS_M("%1", "%[a5]") CS_M("%1", "%[b5]") "s_branch .Lcs9_%=\n"
               ".Lcs0_%=:\n\t" CS_M("%0", "%[a0]") CS_M("%0", "%[b0]") CS_M("%1", "%[a4]") CS_M("%1", "%[b4]") "\n.Lcs9_%=:"
               : "+v"(c0), "+v"(c1) : [a0] "v"(a[0]), [a1] "v"(a[1]), [a2] "v"(a[2]), [a3] "v"(a[3]), [a4] "v"(a[4]), [a5] "v"(a[5]), [a6] "v"(a[6]),
                 [b0] "v"(b[0]), [b1] "v"(b[1]), [b2] "v"(b[2]), [b3] "v"(b[3]), [b4] "v"(b[4]), [b5] "v"(b[5]), [b6] "v"(b[6]), [o] "v"(ones), [s] "s"(sel) : "scc");
}
#define MFMA_PAD do { asm volatile("s_nop 7\n\ts_nop 1" ::: "memory"); SB; } while (0)
__device__ __forceinline__ unsigned pk2(float lo, float hi) { const f32x2 v = {lo, hi}; return __builtin_bit_cast(unsigned, __builtin_convertvector(v, bf16x2_t)); }
__device__ __forceinline__ bf16x8 pk8(f32x4 a, f32x4 b) { const u32x4 u = {pk2(a[0], a[1]), pk2(a[2], a[3]), pk2(b[0], b[1]), pk2(b[2], b[3])}; return __builtin_bit_cast(bf16x8, u); }
__device__ __forceinline__ float bflo(unsigned u) { return __uint_as_float(u << 16); }
__device__ __forceinline__ float bfhi(unsigned u) { return __uint_as_float(u & 0xffff0000u); }
__device__ __forceinline__ float ex2(float x) { return __builtin_amdgcn_exp2f(x); }
__device__ __forceinline__ float rcp(float x) { return __builtin_amdgcn_rcpf(x); }
__device__ __forceinline__ u32x4 swap16(unsigned a0, unsigned a1, unsigned b0, unsigned b1) {
  const auto p = __builtin_amdgcn_permlane16_swap(a0, b0, false, false), q = __builtin_amdgcn_permlane16_swap(a1, b1, false, false);
  return (u32x4){p[0], q[0], p[1], q[1]}; }
__device__ __forceinline__ float vmax(float a, float b) { return __builtin_elementwise_maximum(a, b); }
__device__ __forceinline__ float vmax8(f32x4 a, f32x4 c) { return vmax(vmax(vmax(vmax(a[0], a[1]), a[2]), vmax(vmax(a[3], c[0]), c[1])), vmax(c[2], c[3])); }
__device__ __forceinline__ void sw32(float& a, float& b) { const auto p = __builtin_amdgcn_permlane32_swap(__float_as_uint(a), __float_as_uint(b), false, false); a = __uint_as_float(p[0]); b = __uint_as_float(p[1]); }
__device__ __forceinline__ void sw16(float& a, float& b) { const auto p = __builtin_amdgcn_permlane16_swap(__float_as_uint(a), __float_as_uint(b), false, false); a = __uint_as_float(p[0]); b = __uint_as_float(p[1]); }
template <class Op> __device__ __forceinline__ float gather4(float a0, float a1, float b0, float b1, Op op) { sw32(a0, a1); sw32(b0, b1); float c = op(a0, a1), d = op(b0, b1); sw16(c, d); return op(c, d); }
__device__ __forceinline__ void spread4(float d, float& a0, float& a1, float& b0, float& b1) { a0 = d; b0 = d; sw16(a0, b0); a1 = a0; b1 = b0; sw32(a0, a1); sw32(b0, b1); }
template <class Op> __device__ __forceinline__ float red4(float v, Op op) {
  const unsigned u = __float_as_uint(v); const auto p = __builtin_amdgcn_permlane32_swap(u, u, false, false);
  const float a = op(__uint_as_float(p[0]), __uint_as_float(p[1])); const unsigned ua = __float_as_uint(a);
  const auto q = __builtin_amdgcn_permlane16_swap(ua, ua, false, false); return op(__uint_as_float(q[0]), __uint_as_float(q[1])); }
__device__ __forceinline__ void st16_sc1(u32x4 v, __amdgpu_buffer_rsrc_t r, unsigned voff, unsigned soff) { __builtin_amdgcn_raw_buffer_store_b128(v, r, voff + soff, 0, 16); }
__device__ __forceinline__ void st16(u32x4 v, __amdgpu_buffer_rsrc_t r, unsigned voff, unsigned soff) { __builtin_amdgcn_raw_buffer_store_b128(v, r, voff + soff, 0, 0); }
__device__ __forceinline__ u32x4 ld16_sc1(__amdgpu_buffer_rsrc_t r, unsigned voff, unsigned soff) { return __builtin_amdgcn_raw_buffer_load_b128(r, voff, soff, 16); }
__device__ __forceinline__ __amdgpu_buffer_rsrc_t mk_rsrc(const void* p, unsigned bytes) { return __builtin_amdgcn_make_buffer_rsrc((void*)p, 0, bytes, 0x00020000); }
#define RLX_AGENT __ATOMIC_RELAXED, __HIP_MEMORY_SCOPE_AGENT
typedef unsigned long long u64;
typedef GAS u64 gu64;
__device__ __forceinline__ void set_flag(gu64* w, u64 tag, int lane) { if (lane == 0) __hip_atomic_store(w, tag, RLX_AGENT); }
template <int N, int STRIDE> __device__ __forceinline__ void wait_flags(gu64* w, u64 tag, gu64* tmo, int lane) {
#pragma nounroll
  for (unsigned spins = 0;; ++spins) {
    u64 v = tag; if (N >= 64 || lane < N) v = __hip_atomic_load(w + lane * STRIDE, RLX_AGENT);
    if (__all(v == tag)) break;
    if (spins > (1u << 16)) { if (lane == 0) __hip_atomic_store(tmo, tag, RLX_AGENT); break; }
    __builtin_amdgcn_s_sleep(2);
  }
  __builtin_amdgcn_fence(__ATOMIC_ACQUIRE, "wavefront");
}
__device__ __forceinline__ u64 uni64(u64 v) { return ((u64)(unsigned)__builtin_amdgcn_readfirstlane((unsigned)(v >> 32)) << 32) | (unsigned)__builtin_amdgcn_readfirstlane((unsigned)v); }
__device__ __forceinline__ void lds_wait(unsigned addr, unsigned target) {
  for (;;) { unsigned v; asm volatile("ds_read_b32 %0, %1\n\ts_waitcnt lgkmcnt(0)" : "=v"(v) : "v"(addr) : "memory");
    if ((unsigned)__builtin_amdgcn_readfirstlane(v) >= target) break; }
}
#define POLL_ISSUE(dst, ptr) do { asm volatile("global_load_dwordx2 %0, %1, off sc1" : "=v"(dst) : "v"(ptr) : "memory"); SB; } while (0)
#define LD16_ISSUE(dst, srd, voff, soff) do { asm volatile("buffer_load_dwordx4 %0, %1, %2, %3 offen sc1" : "=v"(dst) : "v"(voff), "s"(srd), "s"(soff) : "memory"); SB; } while (0)
#define POLL_PIN(dst) asm volatile("" : "+v"(dst))
__device__ __forceinline__ float add1(float a, float b) { float r; asm("v_add_f32 %0, %1, %2" : "=v"(r) : "v"(a), "v"(b)); return r; }
__device__ __forceinline__ float mul1(float a, float b) { float r; asm("v_mul_f32 %0, %1, %2" : "=v"(r) : "v"(a), "v"(b)); return r; }
__device__ __forceinline__ float fma1(float a, float b, float c) { float r; asm("v_fma_f32 %0, %1, %2, %3" : "=v"(r) : "v"(a), "v"(b), "v"(c)); return r; }
__device__ __forceinline__ f32x4 add4(f32x4 a, u32x4 b) { return (f32x4){add1(a[0], __uint_as_float(b[0])), add1(a[1], __uint_as_float(b[1])), add1(a[2], __uint_as_float(b[2])), add1(a[3], __uint_as_float(b[3]))}; }
__device__ __forceinline__ unsigned differ(unsigned a, unsigned b, unsigned t) { unsigned r; asm("v_bitop3_b32 %0, %1, %2, %3 bitop3:0x7e" : "=v"(r) : "v"(a), "v"(b), "s"(t)); return r; }
template <int C> __device__ __forceinline__ float dpp(float v) { return __int_as_float(__builtin_amdgcn_update_dpp(0, __float_as_int(v), C, 0xf, 0xf, true)); }
__device__ __forceinline__ float wave_sum(float v) { v += dpp<0x128>(v); v += dpp<0x124>(v); v += dpp<0x122>(v); v += dpp<0x121>(v); return red4(v, [](float a, float c) { return a + c; }); }
__device__ __forceinline__ void lds_sync(unsigned addr, unsigned target, int lane) {
  asm volatile("s_waitcnt lgkmcnt(0)" ::: "memory");
  if (lane == 0) { const unsigned one = 1; asm volatile("ds_add_u32 %0, %1" :: "v"(addr), "v"(one) : "memory"); }
  for (;;) { unsigned v; asm volatile("ds_read_b32 %0, %1\n\ts_waitcnt lgkmcnt(0)" : "=v"(v) : "v"(addr) : "memory");
    if ((unsigned)__builtin_amdgcn_readfirstlane(v) >= target) break; }
}

constexpr unsigned L1_A = 0;
constexpr unsigned L1_BQ = 65536;
constexpr unsigned L1_BH = 90112;
constexpr unsigned L1_WPHI = 147456;
constexpr unsigned L1_CNT = 149504;
constexpr unsigned L1_DUMP = 149760;
constexpr unsigned L1_BIAS = 150016;
constexpr unsigned L1_CS = 150272;
constexpr unsigned L1_ST = 151552;
constexpr unsigned L1_V = 49152;
constexpr unsigned L1_PHI = 114688;
constexpr unsigned L2_A = 0;
constexpr unsigned L2_B = 65536;
constexpr unsigned L2_X = L2_B + 16384;
constexpr unsigned L2_KVA = 153600;

__device__ __forceinline__ void touch_code(unsigned dump, int lane, int wx) {
  u64 c0; unsigned back, len;
  asm volatile("s_getpc_b64 %0\n.Lpc_%=:\n\ts_mov_b32 %1, .Lpc_%=-.Lcode_begin\n\ts_mov_b32 %2, .Lend_g0-.Lcode_begin\n\ts_max_u32 %2, %2, .Lend_g1-.Lcode_begin" : "=s"(c0), "=s"(back), "=s"(len) :: "scc");
  const unsigned o = min((((blockIdx.x >> 3) * 4 + wx) * 8 + (lane & 7)) * 128u, len) & ~3u; TOUCH((const GAS char*)(c0 - back) + o, dump); }

struct Ptrs { const u16 *x, *wqkv, *wphi, *wo, *w1, *b1, *w2; char* ws; u16* out; };

struct Query { f32x4 pq[4][2]; float rl[4]; };

template <int G> struct Phase2 {
  unsigned lb; int lane, wq, b, kh, rt, ct;
  u32x4 sact, so, sw2, swo;
  unsigned vaa, vao, vb0, vb1, vs0, vs1, sla, ska, skb4, skb7;
  static constexpr int kstep(int U, int i) { return (U < 24 ? 7 * (U & 7) + (U >> 3) : 14 * U - 333) + (i ? (U < 24 ? 4 : 7) : 0); }
  __device__ __forceinline__ void init(unsigned lb_, int lane_, int wq_, const Ptrs& P) {
    lb = lb_; lane = lane_; wq = wq_; b = blockIdx.x; kh = b & 1; rt = ((b & 7) >> 1) * 4 + ((b >> 3) & 3); ct = b >> 5;
    sact = mk_srd(P.ws + WS_ACT, 128 * 112 * 1024); so = mk_srd(P.ws + WS_O, 128 * 32 * 1024); sw2 = mk_srd(P.w2, F_ * D_ * 2); swo = mk_srd(P.wo, D_ * D_ * 2);
    vaa = lane * 16 + (rt * 8 + wq) * (112 * 1024); vao = lane * 16 + (rt * 8 + wq) * (32 * 1024);
    { const int r = lane >> 4, c = lane & 15; vb0 = ((wq & 1) * 16 + r) * 2048 + ct * 256 + (((c >> 1) ^ r) << 5) + ((c & 1) << 4); vb1 = vb0 ^ 128; }
    { const int r = lane >> 3, c = lane & 7; vs0 = (wq * 32 + r) * 2048 + ct * 256 + kh * 128 + (((c >> 1) ^ ((r >> 1) & 1)) << 5) + ((c & 1) << 4); vs1 = vs0 ^ 64; }
    sla = lb + wq * 4096; ska = kh * (56 * 1024); skb4 = (kh * 56 + (wq >> 1) * 4) * 65536; skb7 = (kh * 56 + (wq >> 1) * 7) * 65536;
  }
  template <int GT, int S, int KS> __device__ __forceinline__ void dmaA() { constexpr int LO = L2_A + GT * 32768 + (S & 1) * 16384 + KS * 1024;
    if constexpr (S < 14) DMA_SC1(sact, vaa, ska, (GT * 4 * 112 + kstep(2 * S + (KS >> 1), KS & 1)) * 1024, sla, LO);
    else DMA_SC1(so, vao, 0u, (GT * 4 * 32 + (S - 14) * 4 + KS) * 1024, sla, LO); }
  template <int U, int J> __device__ __forceinline__ void dmaB() { constexpr int LO = L2_B + (U % 5) * 16384 + J * 1024;
    if constexpr (U < 28) { const unsigned sk = U < 24 ? skb4 : skb7; if (J >> 1) DMA(sw2, vb1, sk, kstep(U, 0) * 65536 + J * 8192, sla, LO); else DMA(sw2, vb0, sk, kstep(U, 0) * 65536 + J * 8192, sla, LO); }
    else { if (J & 1) DMA(swo, vs1, 0u, (U - 28) * 262144 + J * 16384, sla, LO); else DMA(swo, vs0, 0u, (U - 28) * 262144 + J * 16384, sla, LO); } }
  template <int GT, int S> __device__ __forceinline__ void dmaA4() { dmaA<GT, S, 0>(); dmaA<GT, S, 1>(); dmaA<GT, S, 2>(); dmaA<GT, S, 3>(); }
  template <int S, int I> __device__ __forceinline__ void dma_slot() {
    if constexpr (G == 0) {
      if constexpr (S <= 12) { if constexpr (I < 4) dmaB<2 * S + 3, I>(); else dmaB<2 * S + 4, I - 4>(); }
      else if constexpr (I < 2) { if constexpr (S <= 20) dmaA<0, S + 1, 2 + I>(); }
      else if constexpr (I < 6 && S <= 19) dmaB<S + 16, I - 2>();
    } else {
      if constexpr (I < 4) { if constexpr (S <= 20) dmaA<1, S + 1, I>(); }
      else if constexpr (S <= 11) dmaA<0, S + 2, I - 4>();
      else if constexpr (I < 6 && S <= 19) dmaA<0, S + 2, I - 4>();
    } }

  __device__ __forceinline__ void run(const Ptrs& P, u64 tag, int p, int h, Query& Q, unsigned& st_c, unsigned& st_r) {
    char* ws = P.ws;
    const int tok = lane & 15, g = lane >> 4, q = (lane >> 2) & 3, pp = lane & 3;
    const unsigned ra = opq(lb + L2_A + G * 32768 + lane * 16);
    unsigned rb[2][2], rs[2];
#pragma unroll
    for (int n = 0; n < 2; ++n) { const int f = 4 * (kh ^ n) + wq; rb[0][n] = opq(lb + L2_B + (8 * g + q) * 256 + ((f ^ (q | ((g & 1) << 2))) << 5) + 8 * pp); rb[1][n] = opq(rb[0][n] + 65536); }
    rs[0] = opq(lb + L2_B + (8 * g + q) * 128 + ((wq ^ ((q >> 1) | ((g & 1) << 1))) << 5) + 8 * pp); rs[1] = opq(rs[0] + 65536);
    f32x4 acc[4][2], as[4];
    bf16x8 fa[4][4], fb[4][2];
    auto load = [&](auto SS, auto hook) { constexpr int S = SS();
      auto step = [&](auto II) { constexpr int i = II(), ks = i >> 1;
        _Pragma("unroll") for (int m = 2 * (i & 1); m < 2 * (i & 1) + 2; ++m) { fa[ks][m] = lds128(ra + (S & 1) * 16384 + m * 4096 + ks * 1024); SB; }
        if constexpr (S < 14) { constexpr int buf = (2 * S + (ks >> 1)) % 5, o = (buf & 3) * 16384 + (ks & 1) * 8192; fb[ks][i & 1] = cat(ldstr(rb[buf >> 2][i & 1] + o), ldstr(rb[buf >> 2][i & 1] + o + 1024)); SB; }
        else if constexpr ((i & 1) == 0) { constexpr int buf = (S + 14) % 5, o = (buf & 3) * 16384 + ks * 4096; fb[ks][0] = cat(ldstr(rs[buf >> 2] + o), ldstr(rs[buf >> 2] + o + 512)); SB; }
        dma_slot<S, i>(); hook(II); SB; };
      step(IC<0>{}); step(IC<1>{}); step(IC<2>{}); step(IC<3>{}); step(IC<4>{}); step(IC<5>{}); step(IC<6>{}); step(IC<7>{}); };
    auto mm = [&](auto SS) { constexpr int S = SS();
#pragma unroll
      for (int m = 0; m < 4; ++m)
#pragma unroll
        for (int n = 0; n < (S < 14 ? 2 : 1); ++n) mk4<S == 0 || S == 14>(S < 14 ? acc[m][n] : as[m], fb[0][n], fa[0][m], fb[1][n], fa[1][m], fb[2][n], fa[2][m], fb[3][n], fa[3][m]);
      SB; };
    auto none = [](auto) {};
#define ENDL(n) do { WAIT_V(n); WAIT_L0; BAR; SB; } while (0)
#define ENDR do { WAIT_L0; BAR; SB; } while (0)
#define ENDM(n) do { WAIT_V(n); BAR; SB; } while (0)
    gu64* flags = (gu64*)(ws + WS_FLAG); gu64* tmo = flags + FL_TMO / 8;
    const unsigned kva = opq(lb + L2_KVA + lane * 16);
    u64 pv;
    const __amdgpu_buffer_rsrc_t ryx = mk_rsrc(ws + WS_YX, 256 * 32768);
    auto send = [&]() {
#pragma unroll
      for (int i = 0; i < 2; ++i) st16_sc1(__builtin_bit_cast(u32x4, pk8(acc[2 * i][1], acc[2 * i + 1][1])), ryx, lane * 16, b * 32768 + (G * 4 + wq) * 2048 + i * 1024);
      SB; };
    gu64* fy = flags + FL_YX / 8 + b * 8 + G * 4 + wq; gu64* py_ = flags + FL_YX / 8 + (b ^ 1) * 8 + G * 4 + wq;
    u32x4 py[2]; u32x2 xr[4]; f32x2 gt[8];
    const unsigned vout = (rt * 128 + G * 64 + (g & 1) * 16 + tok) * 2048 + ct * 256 + kh * 128 + wq * 32 + (g >> 1) * 16;
    auto fetch = [&]() { POLL_PIN(pv); if (!__all(pv == tag)) wait_flags<1, 1>(py_, tag, tmo, lane); SB;
      const u32x4 syx = mk_srd(ws + WS_YX, 256 * 32768); const unsigned vy = (b ^ 1) * 32768 + (G * 4 + wq) * 2048 + lane * 16;
      LD16_ISSUE(py[0], syx, vy, 0u); LD16_ISSUE(py[1], syx, vy, 1024u); };
    auto dmax = [&]() { const u32x4 sx = mk_srd(P.x, S_ * D_ * 2); const int r = lane >> 3;
      const unsigned v0 = (rt * 128 + wq * 32 + r) * 2048 + ct * 256 + kh * 128 + (((lane & 7) ^ (r >> 1)) << 4), v1 = v0 ^ 64;
      DMA(sx, v0, 0u, 0, sla, L2_X); DMA(sx, v1, 0u, 16384, sla, L2_X + 1024); DMA(sx, v0, 0u, 32768, sla, L2_X + 2048); DMA(sx, v1, 0u, 49152, sla, L2_X + 3072); };
    auto readx = [&]() { const unsigned a = lb + L2_X + (G * 64 + tok) * 128 + (((2 * wq + (g >> 1)) ^ (tok >> 1)) << 4) + (g & 1) * 8;
#pragma unroll
      for (int m = 0; m < 4; ++m) xr[m] = *(const LAS u32x2*)(size_t)(a + m * 2048);
      SB; };
    auto gate = [&](int k) { asm volatile("" : "+v"(py[k])); SB; f32x2 y[4], e[4];
#define ST4G(stmt) _Pragma("unroll") for (int i = 0; i < 4; ++i) { stmt; } SB
      ST4G(const unsigned u = py[k][i]; e[i] = opq2((f32x2){bflo(u), bfhi(u)}));
      ST4G(const f32x4 a = acc[2 * k + (i >> 1)][0]; y[i] = ((i & 1) ? a.zw : a.xy) + e[i]);
      ST4G(e[i] = opq2(y[i] * -LOG2E));
      ST4G(e[i] = opq2((f32x2){ex2(e[i][0]), ex2(e[i][1])}));
      ST4G(e[i] = opq2(e[i] + 1.0f));
      ST4G(e[i] = opq2((f32x2){rcp(e[i][0]), rcp(e[i][1])}));
      ST4G(gt[4 * k + i] = y[i] * e[i]; asm volatile("" : "+v"(gt[4 * k + i]))); };
#define S_(n) IC<n>{}
    if constexpr (G == 0) {
      WAIT_V(9); BAR; SB; STAMP(16);
      load(S_(0), none); ENDL(13);
      mm(S_(0)); ENDM(4); STAMP(17); load(S_(1), none); ENDR;
      gu64* fkv = flags + (FL_KV + (h * 4 + wq) * 1024) / 8;
      mm(S_(1)); ENDM(4); POLL_ISSUE(pv, fkv + (lane & 7) * 16); load(S_(2), none); ENDR;
      mm(S_(2)); ENDM(4);
      POLL_PIN(pv); if (!__all(pv == tag)) wait_flags<8, 16>(fkv, tag, tmo, lane); SB;
      const u32x4 skv = mk_srd(ws + WS_KVP, 3u << 20);
      u32x4 t[8], tz; LD16_ISSUE(tz, skv, h * 2048 + (lane & 7) * 256 + wq * 64 + g * 16, (unsigned)(WS_ZP - WS_KVP));
      { const unsigned vkv = h * 32768 + wq * 1024 + lane * 16;
#pragma unroll
        for (int i = 0; i < 8; ++i) LD16_ISSUE(t[i], skv, vkv, i * 4096u); }
      load(S_(3), none); ENDR; STAMP(25);
      f32x2 sm[4];
      auto addp = [&](int i) { f32x2 u[4];
#pragma unroll
        for (int c = 0; c < 4; ++c) u[c] = opq2((f32x2){bflo(t[i][c]), bfhi(t[i][c])});
        SB;
#pragma unroll
        for (int c = 0; c < 4; ++c) { sm[c] = i ? sm[c] + u[c] : u[c]; asm volatile("" : "+v"(sm[c])); }
        SB; };
      mm(S_(3)); WAIT_V(8);
      asm volatile("" : "+v"(t[0]), "+v"(t[1]), "+v"(t[2]), "+v"(t[3]), "+v"(t[4]), "+v"(t[5]), "+v"(t[6]), "+v"(t[7]), "+v"(tz)); SB;
      addp(0); addp(1); addp(2); ENDM(4); load(S_(4), none); ENDR; STAMP(26);
      mm(S_(4)); addp(3); addp(4); addp(5); ENDM(4); load(S_(5), [&](auto I) { constexpr int r = I();
        if constexpr (r < 4) { float z = __uint_as_float(tz[r]); z += dpp<0xB1>(z); z = opqf(z); z += dpp<0x4E>(z); z = opqf(z); z += dpp<0x141>(z); tz[r] = __float_as_uint(opqf(z)); asm volatile("" : "+v"(tz)); } });
      ENDR; STAMP(27);
      mm(S_(5)); addp(6); addp(7);
      { const unsigned a = kva + (wq >> 1) * 1024 + (wq & 1) * 8;
        *(LAS u32x2*)(size_t)a = (u32x2){pk2(sm[0][0], sm[0][1]), pk2(sm[1][0], sm[1][1])}; *(LAS u32x2*)(size_t)(a + 2048) = (u32x2){pk2(sm[2][0], sm[2][1]), pk2(sm[3][0], sm[3][1])};
        u32x2 au = {pk2(__uint_as_float(tz[0]), __uint_as_float(tz[1])), pk2(__uint_as_float(tz[2]), __uint_as_float(tz[3]))};
        if ((lane & 3) == 1) au = (u32x2){0x3F803F80u, 0x3F803F80u}; else if ((lane & 3) != 0) au = (u32x2){0u, 0u};
        *(LAS u32x2*)(size_t)(a + 4096) = au; SB; }
      ENDM(4); load(S_(6), none); ENDR;
      mm(S_(6)); ENDM(4); load(S_(7), none); ENDR;
      mm(S_(7)); ENDM(4); load(S_(8), none); ENDR;
      mm(S_(8)); ENDM(4); load(S_(9), none); ENDR;
      mm(S_(9)); ENDM(4); load(S_(10), none); ENDR; STAMP(18);
      mm(S_(10)); ENDM(4); if (PFCODE & 4) touch_code(lb + L1_DUMP, lane, wq); load(S_(11), none); ENDR;
      mm(S_(11)); ENDM(4); load(S_(12), none); ENDR;
      mm(S_(12)); ENDM(4); load(S_(13), none); ENDR;
      mm(S_(13)); ENDM(4); STAMP(19); load(S_(14), none); send(); ENDR;
      mm(S_(14)); ENDM(6); load(S_(15), none); ENDR; STAMP(28);
      mm(S_(15)); WAIT_V(4); set_flag(fy, tag, lane); BAR; SB; load(S_(16), none); ENDR;
      mm(S_(16)); ENDM(4); load(S_(17), none); ENDR; STAMP(29);
      mm(S_(17)); ENDM(4); POLL_ISSUE(pv, py_); load(S_(18), none); ENDR;
      mm(S_(18)); ENDM(4); fetch(); load(S_(19), none); ENDR; STAMP(20);
      mm(S_(19)); WAIT_V(6); gate(0); ENDM(4); load(S_(20), none); dmax(); ENDR; STAMP(30);
      mm(S_(20)); gate(1); ENDM(4); load(S_(21), none); WAIT_V(0); BAR; SB; readx(); __builtin_amdgcn_s_waitcnt(0xC47F); SB; STAMP(31);
      mm(S_(21));
    } else {
      BAR; SB; STAMP(16); BAR; SB;
      load(S_(0), none); WAIT_V(8); set_flag(flags + (FL_KV + (h * 4 + wq) * 1024) / 8 + p * 16, tag, lane); ENDR;
      const f32x4 z4 = {0.f, 0.f, 0.f, 0.f}; float ma[4], mb[4]; bf16x8 eb[4][2], ka[6];
      mm(S_(0));
#pragma unroll
      for (int m = 0; m < 4; ++m) { ma[m] = vmax8(Q.pq[m][0], Q.pq[m][1]); mb[m] = vmax8(-Q.pq[m][0], -Q.pq[m][1]); asm volatile("" : "+v"(ma[m]), "+v"(mb[m])); }
      SB; ENDM(5); STAMP(17);
      gu64* fa2 = flags + FL_ACT2 / 8 + (rt * 2 + kh) * 32;
      POLL_ISSUE(pv, fa2 + (lane & 31));
      load(S_(1), none); ENDL(9);
      mm(S_(1));
      spread4(gather4(ma[0], mb[0], ma[1], mb[1], vmax), ma[0], mb[0], ma[1], mb[1]);
      spread4(gather4(ma[2], mb[2], ma[3], mb[3], vmax), ma[2], mb[2], ma[3], mb[3]);
#pragma unroll
      for (int m = 0; m < 4; ++m) { ma[m] = opqf(ma[m] * -Q.rl[m]); mb[m] = opqf(mb[m] * -Q.rl[m]); asm volatile("" : "+v"(ma[m]), "+v"(mb[m])); }
      SB; ENDM(4);
      POLL_PIN(pv); if (!__all(pv == tag)) wait_flags<32, 1>(fa2, tag, tmo, lane); SB;
      auto expo = [&](int m) { f32x4 ep[2], en[2];
#pragma unroll
        for (int ff = 0; ff < 2; ++ff) { ep[ff] = Q.pq[m][ff] * Q.rl[m] + ma[m]; en[ff] = Q.pq[m][ff] * -Q.rl[m] + mb[m]; }
        SB;
#pragma unroll
        for (int ff = 0; ff < 2; ++ff)
#pragma unroll
          for (int r = 0; r < 4; ++r) { ep[ff][r] = ex2(ep[ff][r]); en[ff][r] = ex2(en[ff][r]); }
        SB; eb[m][0] = pk8(ep[0], ep[1]); eb[m][1] = pk8(en[0], en[1]); asm volatile("" : "+v"(eb[m][0]), "+v"(eb[m][1])); SB; };
      load(S_(2), none); ENDL(8); STAMP(25);
      mm(S_(2)); expo(0); ENDM(4); load(S_(3), none); ENDL(8);
      mm(S_(3)); expo(1); ENDM(4); load(S_(4), none); ENDL(8); STAMP(26);
      mm(S_(4)); expo(2); ENDM(4); load(S_(5), none); ENDL(8); STAMP(27);
      mm(S_(5)); expo(3); ENDM(4);
      const __amdgpu_buffer_rsrc_t ro = mk_rsrc(ws + WS_O, 128 * 32 * 1024);
      const unsigned vo = (unsigned)((((g & 1) << 1) | (g >> 1)) * 256 + tok * 16) + ((p * 16 + wq * 4) * 32 + h) * 1024;
      auto out_o = [&](int m) { f32x4 r6[6];
#pragma unroll
        for (int k = 5; k >= 0; --k) r6[k] = mfma(ka[k], eb[m][k & 1], z4);
        SB; const float a = rcp(r6[4][1]), c = rcp(r6[5][1]), inv = rcp(opqf(r6[4][0] * a + 1e-6f) + r6[5][0] * c), ca = opqf(a * inv), cc = opqf(c * inv);
        const f32x4 o0 = r6[0] * ca + r6[1] * cc, o1 = r6[2] * ca + r6[3] * cc;
        st16_sc1(swap16(pk2(o0[0], o0[1]), pk2(o0[2], o0[3]), pk2(o1[0], o1[1]), pk2(o1[2], o1[3])), ro, vo, m * 32768); SB; };
      load(S_(6), [&](auto I) { constexpr int i = I(); if constexpr (i >= 2) ka[i - 2] = lds128(kva + (i - 2) * 1024); }); ENDL(8);
      mm(S_(6)); out_o(0); ENDM(5); load(S_(7), none); ENDL(9);
      mm(S_(7)); out_o(1); ENDM(5); load(S_(8), none); ENDL(9);
      mm(S_(8)); out_o(2); ENDM(5); load(S_(9), none); ENDL(9);
      mm(S_(9)); out_o(3); ENDM(5); load(S_(10), none); WAIT_V(8);
      set_flag(flags + FL_O / 8 + (2 * p + (wq >> 1)) * 64 + h * 2 + (wq & 1), tag, lane); ENDR; STAMP(18);
      gu64* po = flags + FL_O / 8 + rt * 64;
      mm(S_(10)); ENDM(5); load(S_(11), none); POLL_ISSUE(pv, po + lane); ENDL(9);
      mm(S_(11)); ENDM(5); WAIT_V(0); POLL_PIN(pv); if (!__all(pv == tag)) wait_flags<64, 1>(po, tag, tmo, lane); SB; load(S_(12), none); ENDL(6);
      mm(S_(12)); ENDM(2); load(S_(13), none); ENDL(6);
      mm(S_(13)); ENDM(2); STAMP(19); load(S_(14), none); send(); ENDL(8);
      mm(S_(14)); ENDM(4); load(S_(15), none); ENDL(8); STAMP(28);
      mm(S_(15)); WAIT_V(2); set_flag(fy, tag, lane); BAR; SB; load(S_(16), none); ENDL(7);
      mm(S_(16)); ENDM(2); load(S_(17), none); ENDL(6); STAMP(29);
      mm(S_(17)); ENDM(2); POLL_ISSUE(pv, py_); load(S_(18), none); ENDL(7);
      mm(S_(18)); ENDM(2); fetch(); load(S_(19), none); ENDL(8); STAMP(20);
      mm(S_(19)); WAIT_V(6); gate(0); ENDM(2); load(S_(20), none); ENDL(4); STAMP(30);
      mm(S_(20)); gate(1); ENDM(0); load(S_(21), none); readx(); __builtin_amdgcn_s_waitcnt(0xC47F); SB; STAMP(31);
      mm(S_(21));
    }
    MFMA_PAD; STAMP(21);
    const __amdgpu_buffer_rsrc_t rout = mk_rsrc(P.out, S_ * D_ * 2);
#pragma unroll
    for (int i = 0; i < 2; ++i) { unsigned ow[4];
#pragma unroll
      for (int c = 0; c < 4; ++c) { const f32x4 s = as[2 * i + (c >> 1)]; const unsigned u = xr[2 * i + (c >> 1)][c & 1]; const f32x2 o = ((c & 1) ? s.zw : s.xy) * gt[4 * i + c] + opq2((f32x2){bflo(u), bfhi(u)}); ow[c] = pk2(o[0], o[1]); }
      st16(swap16(ow[0], ow[1], ow[2], ow[3]), rout, vout, i * 65536); }
    if (G == 0 && b == 0 && wq == 0) set_flag(flags + FL_EPOCH / 8, tag, lane);
    if (G == 0) asm volatile(".Lend_g0:"); else asm volatile(".Lend_g1:");
    STAMP(23);
    WAIT_V(0); STAMP(24); STAMP_OUT(G * 4 + wq);
  }
};
#define P2RUN(p2, P, tag, eb) p2.run(P, tag, p, h, eb, st_c, st_r)

__global__ __launch_bounds__(512, 2) void k_fused(const u16* x, const u16* wqkv, const u16* wphi, const u16* wo, const u16* w1, const u16* b1, const u16* w2, char* ws, u16* out) {
  __shared__ __attribute__((aligned(16))) char smem[163840];
  const Ptrs P = {x, wqkv, wphi, wo, w1, b1, w2, ws, out};
  const unsigned lb = (unsigned)(size_t)(LAS char*)smem;
  const int tid = threadIdx.x, lane = tid & 63, w = __builtin_amdgcn_readfirstlane(tid >> 6), wx = w & 3;
  const int b = blockIdx.x, h = (b & 7) * 4 + ((b >> 3) & 3), p = b >> 5, odd = h & 1;
  const int tok = lane & 15, g = lane >> 4, q = (lane >> 2) & 3, pp = lane & 3;
  const unsigned ra0 = lb + L1_A + (wx * 64 + tok) * 128 + ((g ^ (tok >> 1)) << 4), ra1 = ra0 ^ 64;
  gu64* flags = (gu64*)(ws + WS_FLAG); gu64* tmo = flags + FL_TMO / 8;
  asm volatile(".Lcode_begin:");
  STAMP_DECL; STAMP(0);
  const int rot = p < 7 ? 2 * p : 0;
  auto kt = [&](int pos) { int t = pos + rot; t -= t >= 16 ? 14 : 0; return pos < 2 ? pos : t; };
  u64 ep; { gu64* pe = flags + FL_EPOCH / 8; POLL_ISSUE(ep, pe); }
  const __amdgpu_buffer_rsrc_t rfl = mk_rsrc(ws + WS_FLAG, 1u << 20);
  bf16x8 ones = {0x3F80, 0x3F80, 0x3F80, 0x3F80, 0x3F80, 0x3F80, 0x3F80, 0x3F80}; asm volatile("" : "+v"(ones));
  auto cs_publish = [&](const f32x4& c0, const f32x4& c1, int j0, bool two, u64 tag) {
    const f32x4 a = (tok & 2) ? c1 : c0; const u32x4 v = {__float_as_uint((tok & 1) ? a[2] : a[0]), (unsigned)tag, __float_as_uint((tok & 1) ? a[3] : a[1]), (unsigned)tag};
    if (tok < (two ? 4 : 2)) st16_sc1(v, rfl, ((j0 + 2 * (tok & 2)) * 16 + 4 * g + 2 * (tok & 1)) * 8, GR_CS + (h * 8 + p) * 1664);
    SB; };

  if (w < 4) {
    const u32x4 sa = mk_srd(x, S_ * D_ * 2);
    unsigned va0, va1;
    { const int r = lane >> 3; va0 = (p * 256 + wx * 64 + r) * 2048 + (((lane & 7) ^ (r >> 1)) << 4); va1 = va0 ^ 64; }
    const unsigned sl = lb + L1_A + wx * 8192;
    const unsigned rb0 = opq(lb + L1_BH + (8 * g + 4 * (g & 1) + q) * 224 + 8 * pp), rb1 = opq(lb + L1_BH + (8 * g + 4 * ((g & 1) ^ 1) + q) * 224 + 8 * pp);
    f32x4 acc[4][7], cs0, cs1;
    bf16x8 fa[2][4], fb[2][7];
    auto dmaA = [&](auto ST, auto J, unsigned sk) { constexpr int st = ST(), j = J(); if (j & 1) DMA(sa, va1, sk, j * 16384, sl, st * 32768 + j * 1024); else DMA(sa, va0, sk, j * 16384, sl, st * 32768 + j * 1024); };
    auto rdA = [&](auto ST, auto KS, auto M) { constexpr int st = ST(), ks = KS(), m = M(); fa[ks][m] = lds128((ks ? ra1 : ra0) + st * 32768 + m * 2048); SB; };
    auto rdB = [&](auto ST, auto KS, auto J) { constexpr int st = ST(), ks = KS(), j = J(); constexpr int o = st * 14336 + ks * 7168 + j * 32; fb[ks][j] = cat(ldstr(rb0 + o), ldstr(rb1 + o)); SB; };
    auto load = [&](auto ST, auto DO, unsigned sk) {
      constexpr int o = ST() ^ 1; constexpr bool d = DO();
      rdA(ST, IC<0>{}, IC<0>{}); rdB(ST, IC<0>{}, IC<0>{}); rdB(ST, IC<0>{}, IC<1>{}); if (d) dmaA(IC<o>{}, IC<0>{}, sk);
      rdA(ST, IC<0>{}, IC<1>{}); rdB(ST, IC<0>{}, IC<2>{}); rdB(ST, IC<0>{}, IC<3>{}); if (d) dmaA(IC<o>{}, IC<1>{}, sk);
      rdA(ST, IC<0>{}, IC<2>{}); rdB(ST, IC<0>{}, IC<4>{}); rdB(ST, IC<0>{}, IC<5>{}); if (d) dmaA(IC<o>{}, IC<2>{}, sk);
      rdA(ST, IC<0>{}, IC<3>{}); rdB(ST, IC<0>{}, IC<6>{}); rdB(ST, IC<1>{}, IC<0>{}); if (d) dmaA(IC<o>{}, IC<3>{}, sk);
      rdA(ST, IC<1>{}, IC<0>{}); rdB(ST, IC<1>{}, IC<1>{}); rdB(ST, IC<1>{}, IC<2>{}); if (d) dmaA(IC<o>{}, IC<4>{}, sk);
      rdA(ST, IC<1>{}, IC<1>{}); rdB(ST, IC<1>{}, IC<3>{}); rdB(ST, IC<1>{}, IC<4>{}); if (d) dmaA(IC<o>{}, IC<5>{}, sk);
      rdA(ST, IC<1>{}, IC<2>{}); rdB(ST, IC<1>{}, IC<5>{}); if (d) dmaA(IC<o>{}, IC<6>{}, sk);
      rdA(ST, IC<1>{}, IC<3>{}); rdB(ST, IC<1>{}, IC<6>{}); if (d) dmaA(IC<o>{}, IC<7>{}, sk);
    };
    auto mm_ = [&](auto F) {
#pragma unroll
      for (int m = 0; m < 4; ++m)
#pragma unroll
        for (int j = 0; j < 7; ++j) mk2<F()>(acc[m][j], fb[0][j], fa[0][m], fb[1][j], fa[1][m]);
      SB; };
    auto mm = [&]() { mm_(IC<0>{}); };
#define A8(ST, sk) dmaA(IC<ST>{}, IC<0>{}, sk); dmaA(IC<ST>{}, IC<1>{}, sk); dmaA(IC<ST>{}, IC<2>{}, sk); dmaA(IC<ST>{}, IC<3>{}, sk); dmaA(IC<ST>{}, IC<4>{}, sk); dmaA(IC<ST>{}, IC<5>{}, sk); dmaA(IC<ST>{}, IC<6>{}, sk); dmaA(IC<ST>{}, IC<7>{}, sk)
    if (wx == 0 && TOUCH0) { const int l = min(lane, 51), ts = l / 26, rem = l - ts * 26, rr = rem / 13, li = rem - rr * 13, k = ts * 64 + (b >> 3) * 2 + rr;
      TOUCH(li < 6 ? (const GAS char*)wqkv + k * 6144 + (li >> 1) * 2048 + (b & 7) * 256 + (li & 1) * 128 : (const GAS char*)w1 + k * 7168 + (b & 7) * 896 + (li - 6) * 128, lb + L1_DUMP); }
    if (wx == 1 && TOUCH0) TOUCH((const GAS char*)x + (p * 256 + ((b >> 3) & 3) * 64 + lane) * 2048 + (b & 1) * 128, lb + L1_DUMP);
    A8(0, 0u);
    if (PFCODE & 1) touch_code(lb + L1_DUMP, lane, wx);
    if (wx == 0) asm volatile("s_mov_b32 m0, %2\n\ts_nop 0\n\tbuffer_load_dword %0, %1, 0 offen lds" :: "v"(h * 224 + lane * 4), "s"(mk_srd(b1, F_ * 2)), "s"(lb + L1_BIAS) : "memory");
    cs0 = zacc(); cs1 = zacc();
    auto colsum = [&](bool now) { colsum7(cs0, cs1, fb[0], fb[1], ones, now ? wx : 4); SB; };
    if (tid == 0) *(LAS u64*)(size_t)(lb + L1_CNT) = 0ull;
    STAMP(1);
    WAIT_V(0); POLL_PIN(ep); const u64 tag = uni64(ep) + 1;
    A8(1, 128u);
    WAIT_L0; BAR; SB; STAMP(4);
    load(IC<0>{}, IC<0>{}, 0u); WAIT_L0; BAR; SB; STAMP(5);
#pragma unroll
    for (int t = 0; t < 4; t += 2) {
      if (t == 0) mm_(IC<1>{}); else mm();
      colsum((p == 7) == (t == 0)); WAIT_V(0); BAR; SB;
      load(IC<1>{}, IC<1>{}, (unsigned)kt(t + 2) * 128u); WAIT_L0; BAR; SB;
      mm(); colsum((p == 7) == (t == 0)); WAIT_V(0); BAR; SB;
      load(IC<0>{}, IC<1>{}, (unsigned)kt(t + 3) * 128u); if (t == 2) cs_publish(cs0, cs1, wx, wx < 3, tag); WAIT_L0; BAR; SB;
    }
    for (int t = 4; t < 14; t += 2) {
      mm(); WAIT_V(0); BAR; SB;
      load(IC<1>{}, IC<1>{}, (unsigned)kt(t + 2) * 128u); WAIT_L0; BAR; SB;
      mm(); WAIT_V(0); BAR; SB;
      load(IC<0>{}, IC<1>{}, (unsigned)kt(t + 3) * 128u); WAIT_L0; BAR; SB;
    }
    STAMP(6);
    mm(); WAIT_V(0); BAR; SB;
    u32x2 bias[7];
#pragma unroll
    for (int j = 0; j < 7; ++j) bias[j] = *(const LAS u32x2*)(size_t)(lb + L1_BIAS + (j < 6 ? j + odd : (odd ? 0 : 6)) * 32 + g * 8);
    SB;
    { const unsigned dump = lb + L1_DUMP; const int cu = b >> 3, a = (b & 7) >> 1;
      const int l2 = min(lane, 55) + 56 * (wx + 4 * cu);
      if (PFCODE & 2) touch_code(dump, lane, wx);
      if (TOUCHW) TOUCH((const GAS char*)w2 + ((b & 1) * 1792 + a * 448) * 2048 + l2 * 128, dump);
      if (TOUCHW) TOUCH((const GAS char*)wo + (b * 64 + wx * 16 + (lane & 15)) * 128, dump); }
    load(IC<1>{}, IC<0>{}, 0u);
    f32x4 bl[7];
#pragma unroll
    for (int j = 0; j < 7; ++j) { bl[j] = (f32x4){opqf(bflo(bias[j][0]) * LOG2E), opqf(bfhi(bias[j][0]) * LOG2E), opqf(bflo(bias[j][1]) * LOG2E), opqf(bfhi(bias[j][1]) * LOG2E)}; asm volatile("" : "+v"(bl[j])); }
    WAIT_L0; BAR; SB;
    mm(); BAR; MFMA_PAD; STAMP(7);
    f32x4 cs[7]; float rl[4], rml[4];
#pragma unroll
    for (int j = 0; j < 7; ++j) cs[j] = *(const LAS f32x4*)(size_t)(lb + L1_CS + j * 64 + g * 16);
#pragma unroll
    for (int m = 0; m < 4; ++m) { const f32x2 st = *(const LAS f32x2*)(size_t)(lb + L1_ST + (wx * 64 + m * 16 + tok) * 8); rl[m] = opqf(st[0] * LOG2E); rml[m] = opqf(-rl[m] * st[1]); }
    SB;
    Phase2<0> p2; p2.init(lb, lane, wx, P);
    const __amdgpu_buffer_rsrc_t ract = mk_rsrc(ws + WS_ACT, 128 * 112 * 1024);
    auto mish8 = [&](int m0, int j0, int m1, int j1) -> u32x4 {
      const f32x2 a[4] = {acc[m0][j0].xy, acc[m0][j0].zw, acc[m1][j1].xy, acc[m1][j1].zw}, c[4] = {cs[j0].xy, cs[j0].zw, cs[j1].xy, cs[j1].zw}, bb[4] = {bl[j0].xy, bl[j0].zw, bl[j1].xy, bl[j1].zw};
      f32x2 t[4], hs[4], e[4], d[4]; unsigned o[4];
#define ST4(stmt) _Pragma("unroll") for (int i = 0; i < 4; ++i) { stmt; } SB
      ST4(t[i] = c[i] * rml[i < 2 ? m0 : m1] + bb[i]);
      ST4(hs[i] = a[i] * rl[i < 2 ? m0 : m1] + t[i]);
      ST4(e[i] = opq2((f32x2){ex2(hs[i][0]), ex2(hs[i][1])}));
      ST4(t[i] = e[i] + 2.0f);
      ST4(d[i] = opq2(e[i] * t[i] + 2.0f));
      ST4(e[i] = opq2((f32x2){rcp(d[i][0]), rcp(d[i][1])}));
      ST4(t[i] = e[i] * (-2.0f / LOG2E) + 1.0f / LOG2E);
      ST4(d[i] = hs[i] * t[i]);
      ST4(o[i] = pk2(d[i][0], d[i][1]));
      return swap16(o[0], o[1], o[2], o[3]); };
    const unsigned vpair = (unsigned)((((g & 1) << 1) | (g >> 1)) * 256 + tok * 16), mb0 = p * 16 + wx * 4;
    auto pair = [&](auto J, auto M) { constexpr int j = J(), m = M();
      st16_sc1(mish8(m, j, m, j + 1), ract, vpair, ((mb0 + m) * 112 + ((7 * h + odd + j) >> 1)) * 1024); SB;
      constexpr int n = 2 * j + m - BLATE; if constexpr (n >= 0 && n < 6) { p2.dmaB<n / 2, 2 * (n & 1)>(); p2.dmaB<n / 2, 1 + 2 * (n & 1)>(); } };
    const int fai = ((2 * p + (wx >> 1)) * 2 + (h >> 4)) * 32 + (h & 15) * 2 + (wx & 1); gu64* fact = flags + FL_ACT / 8 + fai * 16;
    STAMP(13);
    if (XP0) __builtin_amdgcn_s_setprio(2);
    pair(IC<0>{}, IC<0>{}); pair(IC<0>{}, IC<1>{}); pair(IC<0>{}, IC<2>{}); pair(IC<0>{}, IC<3>{});
    if (XP0 == 4) __builtin_amdgcn_s_setprio(0);
    STAMP(14);
    pair(IC<2>{}, IC<0>{}); pair(IC<2>{}, IC<1>{});
    STAMP(15);
    WAIT_V(2); set_flag(fact, tag, lane); SB; STAMP(8);
    gu64* fa1 = flags + FL_ACT / 8 + (p2.rt * 2 + p2.kh) * 512; u64 pv;
    const unsigned vsing = (unsigned)((g & 1) * 112 * 1024 + (2 * odd + (g >> 1)) * 256 + tok * 16);
    auto single = [&](int m) {
      st16_sc1(mish8(m, 6, m + 1, 6), ract, vsing, ((mb0 + m) * 112 + ((7 * h + (odd ? 0 : 6)) >> 1)) * 1024); SB; };
    auto geta = [&]() { POLL_PIN(pv); STAMP(10); if (!__all(pv == tag)) wait_flags<32, 16>(fa1, tag, tmo, lane);
      SB; STAMP(11); p2.dmaA4<0, 0>(); p2.dmaA4<1, 0>(); p2.dmaA4<0, 1>(); };
    if (XP0 == 6) __builtin_amdgcn_s_setprio(0);
    pair(IC<2>{}, IC<2>{}); pair(IC<2>{}, IC<3>{});
    if (XP0 == 8) __builtin_amdgcn_s_setprio(0);
    if (EARLYA == 0) {
      pair(IC<4>{}, IC<0>{}); pair(IC<4>{}, IC<1>{}); pair(IC<4>{}, IC<2>{}); pair(IC<4>{}, IC<3>{});
      POLL_ISSUE(pv, fa1 + (lane & 31) * 16); single(0); single(2);
      STAMP(9); WAIT_V(2); geta(); WAIT_V(12);
    } else {
      pair(IC<4>{}, IC<0>{}); POLL_ISSUE(pv, fa1 + (lane & 31) * 16); pair(IC<4>{}, IC<1>{}); pair(IC<4>{}, IC<2>{});
      WAIT_V(6); geta(); pair(IC<4>{}, IC<3>{}); single(0); single(2); STAMP(9); WAIT_V(0);
    }
    set_flag(flags + FL_ACT2 / 8 + fai, tag, lane); SB; STAMP(12);
    Query none; P2RUN(p2, P, tag, none);
  } else {
    const int srow = p * 256 + h * 8 + wx * 2; u32x4 xa[4];
    { const u32x4 sx = mk_srd(x, S_ * D_ * 2); const unsigned vx = srow * 2048 + lane * 16;
      asm volatile("buffer_load_dwordx4 %0, %4, %5, 0 offen\n\tbuffer_load_dwordx4 %1, %4, %5, 0 offen offset:1024\n\tbuffer_load_dwordx4 %2, %4, %5, 0 offen offset:2048\n\tbuffer_load_dwordx4 %3, %4, %5, 0 offen offset:3072"
                   : "=&v"(xa[0]), "=&v"(xa[1]), "=&v"(xa[2]), "=&v"(xa[3]) : "v"(vx), "s"(sx) : "memory"); SB; }
    const u32x4 sq = mk_srd(wqkv, D_ * 3 * D_ * 2), sh = mk_srd(w1, D_ * F_ * 2), sp = mk_srd(wphi, 32 * 32 * 32 * 2);
    unsigned vq, vh[4];
    { const int r = lane >> 2; vq = (wx * 16 + r) * 6144 + h * 64 + (((lane & 3) ^ (((r >> 3) & 1) << 1)) << 4); }
#pragma unroll
    for (int i = 0; i < 4; ++i) { const int o = (wx + 4 * i) * 1024 + lane * 16, rp = o / 224, c = (o - rp * 224) >> 4, k = rp ^ (((rp >> 3) & 1) << 2); int cs = c + 2 * odd; if (cs >= 14) cs -= 14;
      vh[i] = k * 7168 + h * 224 + cs * 16; }
    const unsigned slq = lb + L1_BQ + wx * 1024, slh = lb + L1_BH + wx * 1024;
    const unsigned rq0 = opq(lb + L1_BQ + (8 * g + q) * 64 + 32 * (g & 1) + 8 * pp), rq1 = opq(rq0 ^ 32);
    f32x4 acc[3][4][2], cs0, cs1;
    bf16x8 fa[2][4], fb[2][6];
    auto dmaQ = [&](auto ST, auto U, unsigned sk) { constexpr int st = ST(), u = U(); DMA(sq, vq, sk, u * 2048, slq, st * 12288 + u * 4096); };
    auto dmaH = [&](auto ST, auto I, unsigned sk) { constexpr int st = ST(), i = I(); if (i < 3 || wx < 2) DMA(sh, vh[i], sk, 0, slh, st * 14336 + i * 4096); };
    auto rdA = [&](auto ST, auto KS, auto M) { constexpr int st = ST(), ks = KS(), m = M(); fa[ks][m] = lds128((ks ? ra1 : ra0) + st * 32768 + m * 2048); SB; };
    auto rdB = [&](auto ST, auto KS, auto J) { constexpr int st = ST(), ks = KS(), j = J(); constexpr int o = st * 12288 + (j >> 1) * 4096 + ks * 2048; const unsigned a = (j & 1) ? rq1 : rq0;
      fb[ks][j] = cat(ldstr(a + o), ldstr(a + o + 256)); SB; };
    auto load = [&](auto ST, auto DQ, auto DH, int t) {
      constexpr int o = ST() ^ 1; constexpr bool dq = DQ(), dh = DH(); const unsigned skq = (unsigned)kt(t + 1) * (64 * 6144), skh = (unsigned)kt(t + 2) * (64 * 7168);
      rdA(ST, IC<0>{}, IC<0>{}); rdB(ST, IC<0>{}, IC<0>{}); rdB(ST, IC<0>{}, IC<1>{}); if (dq) dmaQ(IC<o>{}, IC<0>{}, skq);
      rdA(ST, IC<0>{}, IC<1>{}); rdB(ST, IC<0>{}, IC<2>{}); rdB(ST, IC<0>{}, IC<3>{}); if (dq) dmaQ(IC<o>{}, IC<1>{}, skq);
      rdA(ST, IC<0>{}, IC<2>{}); rdB(ST, IC<0>{}, IC<4>{}); rdB(ST, IC<0>{}, IC<5>{}); if (dq) dmaQ(IC<o>{}, IC<2>{}, skq);
      rdA(ST, IC<0>{}, IC<3>{}); rdB(ST, IC<1>{}, IC<0>{}); if (dh) dmaH(ST, IC<0>{}, skh);
      rdA(ST, IC<1>{}, IC<0>{}); rdB(ST, IC<1>{}, IC<1>{}); rdB(ST, IC<1>{}, IC<2>{}); if (dh) dmaH(ST, IC<1>{}, skh);
      rdA(ST, IC<1>{}, IC<1>{}); rdB(ST, IC<1>{}, IC<3>{}); if (dh) dmaH(ST, IC<2>{}, skh);
      rdA(ST, IC<1>{}, IC<2>{}); rdB(ST, IC<1>{}, IC<4>{});
      rdA(ST, IC<1>{}, IC<3>{}); rdB(ST, IC<1>{}, IC<5>{});
      if (dh) { if (wx < 2) { dmaH(ST, IC<3>{}, skh); WAIT_V(7); } else WAIT_V(6); }
    };
    auto mm_ = [&](auto F) {
#pragma unroll
      for (int m = 0; m < 4; ++m)
#pragma unroll
        for (int j = 0; j < 6; ++j) mk2<F()>(acc[j >> 1][m][j & 1], fb[0][j], fa[0][m], fb[1][j], fa[1][m]);
      SB; };
    auto mm = [&]() { mm_(IC<0>{}); };
#define H4(ST, sk) dmaH(IC<ST>{}, IC<0>{}, sk); dmaH(IC<ST>{}, IC<1>{}, sk); dmaH(IC<ST>{}, IC<2>{}, sk); dmaH(IC<ST>{}, IC<3>{}, sk)
#define Q3(ST, sk) dmaQ(IC<ST>{}, IC<0>{}, sk); dmaQ(IC<ST>{}, IC<1>{}, sk); dmaQ(IC<ST>{}, IC<2>{}, sk)
    H4(0, 0u); Q3(0, 0u);
    cs0 = zacc(); cs1 = zacc();
    auto colsum = [&](bool now) { const bf16x8 a[7] = {fb[0][0], fb[0][1], fb[0][2], fb[0][3], fb[0][4], fb[0][5], ones}, c[7] = {fb[1][0], fb[1][1], fb[1][2], fb[1][3], fb[1][4], fb[1][5], ones};
      colsum7(cs0, cs1, a, c, ones, now ? wx : 4); SB; };
    STAMP(1);
    if (wx < 2) WAIT_V(7); else WAIT_V(6);
    asm volatile("" : "+v"(xa[0]), "+v"(xa[1]), "+v"(xa[2]), "+v"(xa[3]), "+v"(ep));
    const u64 tag = uni64(ep) + 1;
    { float v[2][16], sm[2], sq_[2], mean[2];
#pragma unroll
      for (int r = 0; r < 2; ++r) { sm[r] = 0.f;
#pragma unroll
        for (int i = 0; i < 8; ++i) { const unsigned u = xa[2 * r + (i >> 2)][i & 3]; v[r][2 * i] = bflo(u); v[r][2 * i + 1] = bfhi(u); sm[r] += v[r][2 * i] + v[r][2 * i + 1]; } }
#pragma unroll
      for (int r = 0; r < 2; ++r) { mean[r] = wave_sum(sm[r]) * (1.0f / D_); sq_[r] = 0.f;
#pragma unroll
        for (int i = 0; i < 16; ++i) { const float d = v[r][i] - mean[r]; sq_[r] += d * d; } }
      const float r0 = __builtin_amdgcn_rsqf(wave_sum(sq_[0]) * (1.0f / D_) + 1e-5f), r1 = __builtin_amdgcn_rsqf(wave_sum(sq_[1]) * (1.0f / D_) + 1e-5f);
      const u32x4 gr = {__float_as_uint(lane ? mean[1] : mean[0]), (unsigned)tag, __float_as_uint(lane ? r1 : r0), (unsigned)tag};
      if (lane < 2) st16_sc1(gr, rfl, lane * 16, GR_ST + srow * 16);
      SB; }
    STAMP(2);
    WAIT_V(1);
    STAMP(3); BAR; SB; STAMP(4);
    if (wx < 2) { const unsigned vp = h * 2048 + wx * 1024 + lane * 16, slp = lb + L1_WPHI + wx * 1024; DMA(sp, vp, 0u, 0, slp, 0); }
    H4(1, 64u * 7168u); Q3(1, 64u * 6144u);
    BAR; SB; STAMP(5);
    load(IC<0>{}, IC<0>{}, IC<1>{}, 0); WAIT_L0; BAR; SB;
    mm_(IC<1>{}); colsum(p == 7); WAIT_V(3); BAR; SB;
    load(IC<1>{}, IC<1>{}, IC<1>{}, 1); WAIT_L0; BAR; SB;
    mm(); colsum(p == 7); WAIT_V(3); BAR; SB;
    load(IC<0>{}, IC<1>{}, IC<1>{}, 2); WAIT_L0; BAR; SB;
    mm(); colsum(p != 7); WAIT_V(3); BAR; SB;
    load(IC<1>{}, IC<1>{}, IC<1>{}, 3); WAIT_L0; BAR; SB;
    mm(); colsum(p != 7); WAIT_V(3); BAR; MFMA_PAD;
    cs_publish(cs0, cs1, 7 + wx, wx < 2, tag);
    for (int t = 4; t < 14; t += 2) {
      load(IC<0>{}, IC<1>{}, IC<1>{}, t); WAIT_L0; BAR; SB;
      mm(); WAIT_V(3); BAR; SB;
      load(IC<1>{}, IC<1>{}, IC<1>{}, t + 1); WAIT_L0; BAR; SB;
      mm(); WAIT_V(3); BAR; SB;
    }
    STAMP(6);
    const unsigned vst = GR_ST + (p * 256 + wx * 64 + lane) * 16, vcs = GR_CS + h * (8 * 1664) + min(wx * 64 + lane, 103) * 16;
    const u32x4 sfl = mk_srd(ws + WS_FLAG, 1u << 20);
    u32x4 gs, gc[8]; LD16_ISSUE(gs, sfl, vst, 0u);
#pragma unroll
    for (int i = 0; i < 8; ++i) LD16_ISSUE(gc[i], sfl, vcs, i * 1664u);
    load(IC<0>{}, IC<1>{}, IC<0>{}, 14);
    WAIT_V(12); WAIT_L0; BAR; SB;
    mm(); WAIT_V(0); BAR; SB;
    load(IC<1>{}, IC<0>{}, IC<0>{}, 15);
    { auto stale = [&]() { unsigned d = differ(gs[1], gs[3], (unsigned)tag);
#pragma unroll
        for (int i = 0; i < 8; ++i) d |= differ(gc[i][1], gc[i][3], (unsigned)tag);
        return !__all(d == 0u); };
      asm volatile("" : "+v"(gs), "+v"(gc[0]), "+v"(gc[1]), "+v"(gc[2]), "+v"(gc[3]), "+v"(gc[4]), "+v"(gc[5]), "+v"(gc[6]), "+v"(gc[7]));
      if (__builtin_expect(stale(), 0)) {
#pragma nounroll
        for (unsigned spins = 0; spins < (1u << 16); ++spins) {
          asm volatile("buffer_load_dwordx4 %0, %1, %2, 0 offen sc1" : "+v"(gs) : "v"(vst), "s"(sfl) : "memory");
#pragma unroll
          for (int i = 0; i < 8; ++i) asm volatile("buffer_load_dwordx4 %0, %1, %2, %3 offen sc1" : "+v"(gc[i]) : "v"(vcs), "s"(sfl), "s"(i * 1664u) : "memory");
          asm volatile("s_waitcnt vmcnt(0)" : "+v"(gs), "+v"(gc[0]), "+v"(gc[1]), "+v"(gc[2]), "+v"(gc[3]), "+v"(gc[4]), "+v"(gc[5]), "+v"(gc[6]), "+v"(gc[7]) :: "memory");
          if (!stale()) break; } }
      f32x2 c = {__uint_as_float(gc[0][0]), __uint_as_float(gc[0][2])};
#pragma unroll
      for (int i = 1; i < 8; ++i) c = (f32x2){add1(c[0], __uint_as_float(gc[i][0])), add1(c[1], __uint_as_float(gc[i][2]))};
      *(LAS f32x2*)(size_t)(lb + L1_CS + min(wx * 64 + lane, 103) * 8) = c;
      *(LAS f32x2*)(size_t)(lb + L1_ST + (wx * 64 + lane) * 8) = (f32x2){__uint_as_float(gs[2]), __uint_as_float(gs[0])}; SB; }
    WAIT_L0; BAR; SB;
    mm(); MFMA_PAD; STAMP(7);
    if (YPRIO) __builtin_amdgcn_s_setprio(1);
    {
    const int lane = opq(__lane_id()), tok = lane & 15, g = lane >> 4, q = (lane >> 2) & 3, pp = lane & 3;
    bf16x8 wp[2];
#pragma unroll
    for (int ff = 0; ff < 2; ++ff) { const unsigned a = lb + L1_WPHI + (4 * g + q) * 64 + ff * 32 + 8 * pp; wp[ff] = cat(ldstr(a), ldstr(a + 1024)); }
    f32x4 cs[6]; float rs[4], mean[4], rl[4];
#pragma unroll
    for (int j = 0; j < 6; ++j) cs[j] = *(const LAS f32x4*)(size_t)(lb + L1_CS + (7 + j) * 64 + g * 16);
#pragma unroll
    for (int m = 0; m < 4; ++m) { const f32x2 st = *(const LAS f32x2*)(size_t)(lb + L1_ST + (wx * 64 + m * 16 + tok) * 8); rs[m] = st[0]; mean[m] = opqf(-st[1]); rl[m] = st[0] * LOG2E; }
    const f32x4 zero4 = {0.f, 0.f, 0.f, 0.f};
    auto project = [&](int u, f32x4 (&pr)[4][2]) {
#pragma unroll
      for (int m = 0; m < 4; ++m) { const bf16x8 kb = pk8(acc[u][m][0] + cs[2 * u] * mean[m], acc[u][m][1] + cs[2 * u + 1] * mean[m]); pr[m][0] = mfma(wp[0], kb, zero4); pr[m][1] = mfma(wp[1], kb, zero4); }
      SB; };
    auto extrema = [&](const f32x4 (&pr)[4][2], float (&ma)[4], float (&mb)[4]) {
#pragma unroll
      for (int m = 0; m < 4; ++m) { ma[m] = vmax8(pr[m][0], pr[m][1]); mb[m] = vmax8(-pr[m][0], -pr[m][1]); }
      spread4(gather4(ma[0], mb[0], ma[1], mb[1], vmax), ma[0], mb[0], ma[1], mb[1]);
      spread4(gather4(ma[2], mb[2], ma[3], mb[3], vmax), ma[2], mb[2], ma[3], mb[3]);
#pragma unroll
      for (int m = 0; m < 4; ++m) { ma[m] *= -rl[m]; mb[m] *= -rl[m]; }
      SB; };
    auto expo = [&](int m, const f32x4 (&pr)[2], float ma, float mb, f32x4 (&ep)[2], f32x4 (&en)[2]) {
#pragma unroll
      for (int ff = 0; ff < 2; ++ff) { ep[ff] = pr[ff] * rl[m] + ma; en[ff] = pr[ff] * -rl[m] + mb; }
      SB;
#pragma unroll
      for (int ff = 0; ff < 2; ++ff)
#pragma unroll
        for (int r = 0; r < 4; ++r) { ep[ff][r] = ex2(ep[ff][r]); en[ff][r] = ex2(en[ff][r]); }
      SB; };
    { const unsigned swp = (tok & 1) | (((tok >> 2) & 1) << 1) | (((tok >> 1) & 1) << 2) | (tok & 8);
      const unsigned wph = lb + L1_PHI + (wx * 64 + tok) * 128 + ((g ^ swp) << 3), wv = lb + L1_V + (wx * 64 + tok) * 64 + ((g ^ (tok >> 1)) << 3);
      const unsigned wp0 = opq(wph), wp1 = opq(wph ^ 32), wp2 = opq(wph ^ 64), wp3 = opq(wph ^ 96), wv0 = opq(wv), wv1 = opq(wv ^ 32);
      auto put = [](unsigned a, f32x4 v) { *(LAS u32x2*)(size_t)a = (u32x2){pk2(v[0], v[1]), pk2(v[2], v[3])}; };
      f32x4 pr[4][2], ep[4][2], en[4][2]; float ma[4], mb[4];
      WAIT_L0; project(1, pr);
#pragma unroll
      for (int m = 0; m < 4; ++m) { put(wv0 + m * 1024, (acc[2][m][0] + cs[4] * mean[m]) * rs[m]); put(wv1 + m * 1024, (acc[2][m][1] + cs[5] * mean[m]) * rs[m]); }
      SB; extrema(pr, ma, mb);
#pragma unroll
      for (int m = 0; m < 4; ++m) expo(m, pr[m], ma[m], mb[m], ep[m], en[m]);
#pragma unroll
      for (int m = 0; m < 4; ++m) { const f32x4 a = ep[m][0] + ep[m][1], c = en[m][0] + en[m][1]; const f32x2 a2 = a.xy + a.zw, c2 = c.xy + c.zw; ma[m] = a2[0] + opqf(a2[1]); mb[m] = c2[0] + opqf(c2[1]); }
      auto fadd = [](float a, float c) { return a + c; };
      spread4(rcp(gather4(ma[0], mb[0], ma[1], mb[1], fadd)), ma[0], mb[0], ma[1], mb[1]);
      spread4(rcp(gather4(ma[2], mb[2], ma[3], mb[3], fadd)), ma[2], mb[2], ma[3], mb[3]);
      SB;
#pragma unroll
      for (int m = 0; m < 4; ++m) { ep[m][0] *= ma[m]; ep[m][1] *= ma[m]; en[m][0] *= mb[m]; en[m][1] *= mb[m]; SB;
        put(wp0 + m * 2048, ep[m][0]); put(wp1 + m * 2048, ep[m][1]); put(wp2 + m * 2048, en[m][0]); put(wp3 + m * 2048, en[m][1]); SB; } }
    STAMP(8);
    asm volatile("s_waitcnt lgkmcnt(0)" ::: "memory");
    if (lane == 0) { const unsigned one = 1, a = lb + L1_CNT; asm volatile("ds_add_u32 %0, %1" :: "v"(a), "v"(one) : "memory"); }
    Query Q; project(0, Q.pq);
#pragma unroll
    for (int m = 0; m < 4; ++m) { Q.rl[m] = rl[m]; asm volatile("" : "+v"(Q.pq[m][0]), "+v"(Q.pq[m][1]), "+v"(Q.rl[m])); }
    SB;
    lds_wait(lb + L1_CNT, 4u); SB; STAMP(9);
    { const bf16x8 ones = {0x3F80, 0x3F80, 0x3F80, 0x3F80, 0x3F80, 0x3F80, 0x3F80, 0x3F80};
      unsigned ap[2], av[2][2];
#pragma unroll
      for (int t = 0; t < 2; ++t) { const int tk = 8 * g + 4 * t + q, t15 = tk & 15; const unsigned sw = (t15 & 1) | (((t15 >> 2) & 1) << 1) | (((t15 >> 1) & 1) << 2) | (t15 & 8);
        ap[t] = opq(lb + L1_PHI + tk * 128 + (((4 * wx + pp) ^ sw) << 3));
        av[t][0] = opq(lb + L1_V + tk * 64 + ((pp ^ (t15 >> 1)) << 3)); av[t][1] = opq(av[t][0] ^ 32); }
      bf16x8 a[8], b0[8], b1_[8];
#pragma unroll
      for (int ks = 0; ks < 8; ++ks) { a[ks] = cat(ldstr(ap[0] + ks * 4096), ldstr(ap[1] + ks * 4096));
        b0[ks] = cat(ldstr(av[0][0] + ks * 2048), ldstr(av[1][0] + ks * 2048)); b1_[ks] = cat(ldstr(av[0][1] + ks * 2048), ldstr(av[1][1] + ks * 2048)); }
      SB; STAMP(12);
      f32x4 kv0 = zero4, kv1 = zero4, zz = zero4;
#pragma unroll
      for (int ks = 0; ks < 8; ++ks) { kv0 = mfma(a[ks], b0[ks], kv0); kv1 = mfma(a[ks], b1_[ks], kv1); zz = mfma(a[ks], ones, zz); }
      const __amdgpu_buffer_rsrc_t rkv = mk_rsrc(ws + WS_KVP, 3u << 20);
      st16_sc1(__builtin_bit_cast(u32x4, pk8(kv0, kv1)), rkv, lane * 16, (h * 8 + p) * 4096 + wx * 1024);
      if (tok == 0) st16_sc1(__builtin_bit_cast(u32x4, zz), rkv, g * 16, (unsigned)(WS_ZP - WS_KVP) + (h * 8 + p) * 256 + wx * 64);
      SB; STAMP(13); }
    SB; if (YPRIO) __builtin_amdgcn_s_setprio(0); WAIT_L0; STAMP(11);
    Phase2<1> p2; p2.init(lb, lane, wx, P);
    P2RUN(p2, P, tag, Q);
    }
  }
}
}

extern "C" void kernel_launch(void* const* d_in, const int* in_sizes, int n_in, void* d_out, int out_size, void* d_ws, size_t ws_size, hipStream_t stream) {
  k_fused<<<256, 512, 0, stream>>>((const u16*)d_in[0], (const u16*)d_in[1], (const u16*)d_in[2], (const u16*)d_in[3], (const u16*)d_in[5], (const u16*)d_in[6], (const u16*)d_in[7], (char*)d_ws, (u16*)d_out);
}
```

```cpp
#include <hip/hip_runtime.h>
#include <hip/hip_bf16.h>
#include <type_traits>

namespace {
#define LAS __attribute__((address_space(3)))
#define GAS __attribute__((address_space(1)))
typedef unsigned short u16;
typedef unsigned u32x4 __attribute__((ext_vector_type(4)));
typedef unsigned u32x2 __attribute__((ext_vector_type(2)));
typedef short bf16x8 __attribute__((ext_vector_type(8)));
typedef short s16x4 __attribute__((ext_vector_type(4)));
typedef float f32x4 __attribute__((ext_vector_type(4)));
typedef float f32x2 __attribute__((ext_vector_type(2)));
typedef __bf16 bf16x2_t __attribute__((ext_vector_type(2)));

constexpr int S_ = 2048, D_ = 1024, F_ = 3584;
constexpr size_t WS_FLAG = 0;
constexpr size_t WS_XN = 1u << 20;
constexpr size_t WS_ACT = WS_XN + (4u << 20);
constexpr size_t WS_O = WS_ACT + (15u << 20);
constexpr size_t WS_KVP = WS_O + (4u << 20);
constexpr size_t WS_ZP = WS_KVP + (2u << 20);
constexpr size_t WS_YX = WS_ZP + (1u << 20);
constexpr size_t WS_STAMP = 64u << 20;
#define STAMP_DECL
#define STAMP(i)
#define STAMP_OUT(wv)
constexpr unsigned FL_EPOCH = 0;
constexpr unsigned FL_O = 1024;
constexpr unsigned FL_YX = 16384;
constexpr unsigned FL_ACT2 = 40960;
constexpr unsigned FL_TMO = 61440;
constexpr unsigned GR_ST = 65536;
constexpr unsigned FL_KV = 131072;
constexpr unsigned FL_ACT = 262144;
constexpr unsigned GR_CS = 524288;
#define YPRIO 0
#define XP0 0
#define EARLYA 0
#define PFCODE 7
#define SENDV 0
#define BLATE 6
#define TOUCH0 0
#define TOUCHW 1
constexpr float LOG2E = 1.4426950408889634f;

#define SB __builtin_amdgcn_sched_barrier(0)
#define BAR __builtin_amdgcn_s_barrier()
#define WAIT_L0 do { __builtin_amdgcn_s_waitcnt(0xC07F); SB; } while (0)
#define WAIT_V(n) asm volatile("s_waitcnt vmcnt(%0)" :: "i"(n) : "memory")
template <int N> using IC = std::integral_constant<int, N>;

__device__ __forceinline__ u32x4 mk_srd(const void* p, unsigned bytes) {
  const unsigned long long a = (unsigned long long)p; u32x4 r;
  r.x = __builtin_amdgcn_readfirstlane((unsigned)a); r.y = __builtin_amdgcn_readfirstlane((unsigned)(a >> 32)) & 0xffffu; r.z = bytes; r.w = 0x00020000u; return r; }
#define DMA(srd, voff, sk, KOFF, sl, LOFF) do { unsigned _t; asm volatile("s_add_u32 m0, %4, %5\n\ts_add_u32 %0, %3, %6\n\tbuffer_load_dwordx4 %1, %2, %0 offen lds" \
    : "=&s"(_t) : "v"(voff), "s"(srd), "s"(sk), "s"(sl), "i"(LOFF), "i"(KOFF) : "memory", "scc"); SB; } while (0)
#define DMA_SC1(srd, voff, sk, KOFF, sl, LOFF) do { unsigned _t; asm volatile("s_add_u32 m0, %4, %5\n\ts_add_u32 %0, %3, %6\n\tbuffer_load_dwordx4 %1, %2, %0 offen sc1 lds" \
    : "=&s"(_t) : "v"(voff), "s"(srd), "s"(sk), "s"(sl), "i"(LOFF), "i"(KOFF) : "memory", "scc"); SB; } while (0)
#define TOUCH(ptr, sl) do { asm volatile("s_mov_b32 m0, %1\n\ts_nop 0\n\tglobal_load_lds_dword %0, off" :: "v"(ptr), "s"(sl) : "memory"); SB; } while (0)
__device__ __forceinline__ bf16x8 lds128(unsigned a) { return *(const LAS bf16x8*)(size_t)a; }
__device__ __forceinline__ s16x4 ldstr(unsigned a) { return __builtin_amdgcn_ds_read_tr16_b64_v4i16((LAS s16x4*)(size_t)a); }
__device__ __forceinline__ bf16x8 cat(s16x4 lo, s16x4 hi) { return __builtin_shufflevector(lo, hi, 0, 1, 2, 3, 4, 5, 6, 7); }
__device__ __forceinline__ unsigned opq(unsigned x) { asm volatile("" : "+v"(x)); return x; }
__device__ __forceinline__ float opqf(float x) { asm("" : "+v"(x)); return x; }
__device__ __forceinline__ f32x2 opq2(f32x2 x) { asm("" : "+v"(x)); return x; }
__device__ __forceinline__ f32x4 mfma(bf16x8 a, bf16x8 b, f32x4 c) { return __builtin_amdgcn_mfma_f32_16x16x32_bf16(a, b, c, 0, 0, 0); }
__device__ __forceinline__ void mfma_acc(f32x4& d, bf16x8 a, bf16x8 b) { asm volatile("v_mfma_f32_16x16x32_bf16 %0, %1, %2, %0" : "+v"(d) : "v"(a), "v"(b)); }
__device__ __forceinline__ void mfma_k2(f32x4& d, bf16x8 a0, bf16x8 b0, bf16x8 a1, bf16x8 b1) {
  asm volatile("v_mfma_f32_16x16x32_bf16 %0, %1, %2, %0\n\tv_mfma_f32_16x16x32_bf16 %0, %3, %4, %0" : "+v"(d) : "v"(a0), "v"(b0), "v"(a1), "v"(b1)); }
__device__ __forceinline__ void mfma_k2_first(f32x4& d, bf16x8 a0, bf16x8 b0, bf16x8 a1, bf16x8 b1) {
  asm volatile("v_mfma_f32_16x16x32_bf16 %0, %1, %2, 0\n\tv_mfma_f32_16x16x32_bf16 %0, %3, %4, %0" : "=&v"(d) : "v"(a0), "v"(b0), "v"(a1), "v"(b1)); }
template <bool FIRST> __device__ __forceinline__ void mk2(f32x4& d, bf16x8 a0, bf16x8 b0, bf16x8 a1, bf16x8 b1) { if (FIRST) mfma_k2_first(d, a0, b0, a1, b1); else mfma_k2(d, a0, b0, a1, b1); }
template <bool FIRST> __device__ __forceinline__ void mk4(f32x4& d, bf16x8 a0, bf16x8 b0, bf16x8 a1, bf16x8 b1, bf16x8 a2, bf16x8 b2, bf16x8 a3, bf16x8 b3) {
#define MK4_REST "v_mfma_f32_16x16x32_bf16 %0, %3, %4, %0\n\tv_mfma_f32_16x16x32_bf16 %0, %5, %6, %0\n\tv_mfma_f32_16x16x32_bf16 %0, %7, %8, %0"
  if (FIRST) asm volatile("v_mfma_f32_16x16x32_bf16 %0, %1, %2, 0\n\t" MK4_REST : "=&v"(d) : "v"(a0), "v"(b0), "v"(a1), "v"(b1), "v"(a2), "v"(b2), "v"(a3), "v"(b3));
  else asm volatile("v_mfma_f32_16x16x32_bf16 %0, %1, %2, %0\n\t" MK4_REST : "+v"(d) : "v"(a0), "v"(b0), "v"(a1), "v"(b1), "v"(a2), "v"(b2), "v"(a3), "v"(b3)); }
__device__ __forceinline__ void mfma_k2x2(f32x4& d, f32x4& e, bf16x8 a0, bf16x8 c0, bf16x8 b0, bf16x8 a1, bf16x8 c1, bf16x8 b1) {
  asm volatile("v_mfma_f32_16x16x32_bf16 %0, %2, %4, %0\n\tv_mfma_f32_16x16x32_bf16 %1, %3, %4, %1\n\tv_mfma_f32_16x16x32_bf16 %0, %5, %7, %0\n\tv_mfma_f32_16x16x32_bf16 %1, %6, %7, %1"
               : "+v"(d), "+v"(e) : "v"(a0), "v"(c0), "v"(b0), "v"(a1), "v"(c1), "v"(b1)); }
__device__ __forceinline__ f32x4 zacc() { f32x4 z = {0.f, 0.f, 0.f, 0.f}; asm volatile("" : "+v"(z)); return z; }
#define CS_M(d, a) "v_mfma_f32_16x16x32_bf16 " d ", " a ", %[o], " d "\n\t"
__device__ __forceinline__ void colsum7(f32x4& c0, f32x4& c1, const bf16x8 (&a)[7], const bf16x8 (&b)[7], bf16x8 ones, unsigned sel) {
  asm volatile("s_cmp_lt_u32 %[s], 2\n\ts_cbranch_scc1 .Lcs01_%=\n\ts_cmp_eq_u32 %[s], 2\n\ts_cbranch_scc1 .Lcs2_%=\n\ts_cmp_eq_u32 %[s], 3\n\ts_cbranch_scc0 .Lcs9_%=\n\t"
               CS_M("%0", "%[a3]") CS_M("%0", "%[b3]") "s_branch .Lcs9_%=\n"
               ".Lcs2_%=:\n\t" CS_M("%0", "%[a2]") CS_M("%0", "%[b2]") CS_M("%1", "%[a6]") CS_M("%1", "%[b6]") "s_branch .Lcs9_%=\n"
               ".Lcs01_%=:\n\ts_cmp_eq_u32 %[s], 0\n\ts_cbranch_scc1 .Lcs0_%=\n\t" CS_M("%0", "%[a1]") CS_M("%0", "%[b1]") CS_M("%1", "%[a5]") CS_M("%1", "%[b5]") "s_branch .Lcs9_%=\n"
               ".Lcs0_%=:\n\t" CS_M("%0", "%[a0]") CS_M("%0", "%[b0]") CS_M("%1", "%[a4]") CS_M("%1", "%[b4]") "\n.Lcs9_%=:"
               : "+v"(c0), "+v"(c1) : [a0] "v"(a[0]), [a1] "v"(a[1]), [a2] "v"(a[2]), [a3] "v"(a[3]), [a4] "v"(a[4]), [a5] "v"(a[5]), [a6] "v"(a[6]),
                 [b0] "v"(b[0]), [b1] "v"(b[1]), [b2] "v"(b[2]), [b3] "v"(b[3]), [b4] "v"(b[4]), [b5] "v"(b[5]), [b6] "v"(b[6]), [o] "v"(ones), [s] "s"(sel) : "scc");
}
#define MFMA_PAD do { asm volatile("s_nop 7\n\ts_nop 1" ::: "memory"); SB; } while (0)
__device__ __forceinline__ unsigned pk2(float lo, float hi) { const f32x2 v = {lo, hi}; return __builtin_bit_cast(unsigned, __builtin_convertvector(v, bf16x2_t)); }
__device__ __forceinline__ bf16x8 pk8(f32x4 a, f32x4 b) { const u32x4 u = {pk2(a[0], a[1]), pk2(a[2], a[3]), pk2(b[0], b[1]), pk2(b[2], b[3])}; return __builtin_bit_cast(bf16x8, u); }
__device__ __forceinline__ float bflo(unsigned u) { return __uint_as_float(u << 16); }
__device__ __forceinline__ float bfhi(unsigned u) { return __uint_as_float(u & 0xffff0000u); }
__device__ __forceinline__ float ex2(float x) { return __builtin_amdgcn_exp2f(x); }
__device__ __forceinline__ float rcp(float x) { return __builtin_amdgcn_rcpf(x); }
__device__ __forceinline__ u32x4 swap16(unsigned a0, unsigned a1, unsigned b0, unsigned b1) {
  const auto p = __builtin_amdgcn_permlane16_swap(a0, b0, false, false), q = __builtin_amdgcn_permlane16_swap(a1, b1, false, false);
  return (u32x4){p[0], q[0], p[1], q[1]}; }
__device__ __forceinline__ float vmax(float a, float b) { return __builtin_elementwise_maximum(a, b); }
__device__ __forceinline__ float vmax8(f32x4 a, f32x4 c) { return vmax(vmax(vmax(vmax(a[0], a[1]), a[2]), vmax(vmax(a[3], c[0]), c[1])), vmax(c[2], c[3])); }
__device__ __forceinline__ void sw32(float& a, float& b) { const auto p = __builtin_amdgcn_permlane32_swap(__float_as_uint(a), __float_as_uint(b), false, false); a = __uint_as_float(p[0]); b = __uint_as_float(p[1]); }
__device__ __forceinline__ void sw16(float& a, float& b) { const auto p = __builtin_amdgcn_permlane16_swap(__float_as_uint(a), __float_as_uint(b), false, false); a = __uint_as_float(p[0]); b = __uint_as_float(p[1]); }
template <class Op> __device__ __forceinline__ float gather4(float a0, float a1, float b0, float b1, Op op) { sw32(a0, a1); sw32(b0, b1); float c = op(a0, a1), d = op(b0, b1); sw16(c, d); return op(c, d); }
__device__ __forceinline__ void spread4(float d, float& a0, float& a1, float& b0, float& b1) { a0 = d; b0 = d; sw16(a0, b0); a1 = a0; b1 = b0; sw32(a0, a1); sw32(b0, b1); }
template <class Op> __device__ __forceinline__ float red4(float v, Op op) {
  const unsigned u = __float_as_uint(v); const auto p = __builtin_amdgcn_permlane32_swap(u, u, false, false);
  const float a = op(__uint_as_float(p[0]), __uint_as_float(p[1])); const unsigned ua = __float_as_uint(a);
  const auto q = __builtin_amdgcn_permlane16_swap(ua, ua, false, false); return op(__uint_as_float(q[0]), __uint_as_float(q[1])); }
__device__ __forceinline__ void st16_sc1(u32x4 v, __amdgpu_buffer_rsrc_t r, unsigned voff, unsigned soff) { __builtin_amdgcn_raw_buffer_store_b128(v, r, voff + soff, 0, 16); }
__device__ __forceinline__ void st16(u32x4 v, __amdgpu_buffer_rsrc_t r, unsigned voff, unsigned soff) { __builtin_amdgcn_raw_buffer_store_b128(v, r, voff + soff, 0, 0); }
__device__ __forceinline__ u32x4 ld16_sc1(__amdgpu_buffer_rsrc_t r, unsigned voff, unsigned soff) { return __builtin_amdgcn_raw_buffer_load_b128(r, voff, soff, 16); }
__device__ __forceinline__ __amdgpu_buffer_rsrc_t mk_rsrc(const void* p, unsigned bytes) { return __builtin_amdgcn_make_buffer_rsrc((void*)p, 0, bytes, 0x00020000); }
#define RLX_AGENT __ATOMIC_RELAXED, __HIP_MEMORY_SCOPE_AGENT
typedef unsigned long long u64;
typedef GAS u64 gu64;
__device__ __forceinline__ void set_flag(gu64* w, u64 tag, int lane) { if (lane == 0) __hip_atomic_store(w, tag, RLX_AGENT); }
template <int N, int STRIDE> __device__ __forceinline__ void wait_flags(gu64* w, u64 tag, gu64* tmo, int lane) {
#pragma nounroll
  for (unsigned spins = 0;; ++spins) {
    u64 v = tag; if (N >= 64 || lane < N) v = __hip_atomic_load(w + lane * STRIDE, RLX_AGENT);
    if (__all(v == tag)) break;
    if (spins > (1u << 16)) { if (lane == 0) __hip_atomic_store(tmo, tag, RLX_AGENT); break; }
    __builtin_amdgcn_s_sleep(2);
  }
  __builtin_amdgcn_fence(__ATOMIC_ACQUIRE, "wavefront");
}
__device__ __forceinline__ u64 uni64(u64 v) { return ((u64)(unsigned)__builtin_amdgcn_readfirstlane((unsigned)(v >> 32)) << 32) | (unsigned)__builtin_amdgcn_readfirstlane((unsigned)v); }
__device__ __forceinline__ void lds_wait(unsigned addr, unsigned target) {
  for (;;) { unsigned v; asm volatile("ds_read_b32 %0, %1\n\ts_waitcnt lgkmcnt(0)" : "=v"(v) : "v"(addr) : "memory");
    if ((unsigned)__builtin_amdgcn_readfirstlane(v) >= target) break; }
}
#define POLL_ISSUE(dst, ptr) do { asm volatile("global_load_dwordx2 %0, %1, off sc1" : "=v"(dst) : "v"(ptr) : "memory"); SB; } while (0)
#define LD16_ISSUE(dst, srd, voff, soff) do { asm volatile("buffer_load_dwordx4 %0, %1, %2, %3 offen sc1" : "=v"(dst) : "v"(voff), "s"(srd), "s"(soff) : "memory"); SB; } while (0)
#define POLL_PIN(dst) asm volatile("" : "+v"(dst))
__device__ __forceinline__ float add1(float a, float b) { float r; asm("v_add_f32 %0, %1, %2" : "=v"(r) : "v"(a), "v"(b)); return r; }
__device__ __forceinline__ float mul1(float a, float b) { float r; asm("v_mul_f32 %0, %1, %2" : "=v"(r) : "v"(a), "v"(b)); return r; }
__device__ __forceinline__ float fma1(float a, float b, float c) { float r; asm("v_fma_f32 %0, %1, %2, %3" : "=v"(r) : "v"(a), "v"(b), "v"(c)); return r; }
__device__ __forceinline__ f32x4 add4(f32x4 a, u32x4 b) { return (f32x4){add1(a[0], __uint_as_float(b[0])), add1(a[1], __uint_as_float(b[1])), add1(a[2], __uint_as_float(b[2])), add1(a[3], __uint_as_float(b[3]))}; }
__device__ __forceinline__ unsigned differ(unsigned a, unsigned b, unsigned t) { unsigned r; asm("v_bitop3_b32 %0, %1, %2, %3 bitop3:0x7e" : "=v"(r) : "v"(a), "v"(b), "s"(t)); return r; }
template <int C> __device__ __forceinline__ float dpp(float v) { return __int_as_float(__builtin_amdgcn_update_dpp(0, __float_as_int(v), C, 0xf, 0xf, true)); }
__device__ __forceinline__ float wave_sum(float v) { v += dpp<0x128>(v); v += dpp<0x124>(v); v += dpp<0x122>(v); v += dpp<0x121>(v); return red4(v, [](float a, float c) { return a + c; }); }
__device__ __forceinline__ void lds_sync(unsigned addr, unsigned target, int lane) {
  asm volatile("s_waitcnt lgkmcnt(0)" ::: "memory");
  if (lane == 0) { const unsigned one = 1; asm volatile("ds_add_u32 %0, %1" :: "v"(addr), "v"(one) : "memory"); }
  for (;;) { unsigned v; asm volatile("ds_read_b32 %0, %1\n\ts_waitcnt lgkmcnt(0)" : "=v"(v) : "v"(addr) : "memory");
    if ((unsigned)__builtin_amdgcn_readfirstlane(v) >= target) break; }
}

constexpr unsigned L1_A = 0;
constexpr unsigned L1_BQ = 65536;
constexpr unsigned L1_BH = 90112;
constexpr unsigned L1_WPHI = 147456;
constexpr unsigned L1_CNT = 149504;
constexpr unsigned L1_DUMP = 149760;
constexpr unsigned L1_BIAS = 150016;
constexpr unsigned L1_CS = 150272;
constexpr unsigned L1_ST = 151552;
constexpr unsigned L1_V = 49152;
constexpr unsigned L1_PHI = 114688;
constexpr unsigned L2_A = 0;
constexpr unsigned L2_B = 65536;
constexpr unsigned L2_X = L2_B + 16384;
constexpr unsigned L2_KVA = 153600;

__device__ __forceinline__ void touch_code(unsigned dump, int lane, int wx) {
  u64 c0; unsigned back, len;
  asm volatile("s_getpc_b64 %0\n.Lpc_%=:\n\ts_mov_b32 %1, .Lpc_%=-.Lcode_begin\n\ts_mov_b32 %2, .Lend_g0-.Lcode_begin\n\ts_max_u32 %2, %2, .Lend_g1-.Lcode_begin" : "=s"(c0), "=s"(back), "=s"(len) :: "scc");
  const unsigned o = min((((blockIdx.x >> 3) * 4 + wx) * 8 + (lane & 7)) * 128u, len) & ~3u; TOUCH((const GAS char*)(c0 - back) + o, dump); }

struct Ptrs { const u16 *x, *wqkv, *wphi, *wo, *w1, *b1, *w2; char* ws; u16* out; };

struct Query { f32x4 pq[4][2]; float rl[4]; };

template <int G> struct Phase2 {
  unsigned lb; int lane, wq, b, kh, rt, ct;
  u32x4 sact, so, sw2, swo;
  unsigned vaa, vao, vb0, vb1, vs0, vs1, sla, ska, skb4, skb7;
  static constexpr int kstep(int U, int i) { return (U < 24 ? 7 * (U & 7) + (U >> 3) : 14 * U - 333) + (i ? (U < 24 ? 4 : 7) : 0); }
  __device__ __forceinline__ void init(unsigned lb_, int lane_, int wq_, const Ptrs& P) {
    lb = lb_; lane = lane_; wq = wq_; b = blockIdx.x; kh = b & 1; rt = ((b & 7) >> 1) * 4 + ((b >> 3) & 3); ct = b >> 5;
    sact = mk_srd(P.ws + WS_ACT, 128 * 112 * 1024); so = mk_srd(P.ws + WS_O, 128 * 32 * 1024); sw2 = mk_srd(P.w2, F_ * D_ * 2); swo = mk_srd(P.wo, D_ * D_ * 2);
    vaa = lane * 16 + (rt * 8 + wq) * (112 * 1024); vao = lane * 16 + (rt * 8 + wq) * (32 * 1024);
    { const int r = lane >> 4, c = lane & 15; vb0 = ((wq & 1) * 16 + r) * 2048 + ct * 256 + (((c >> 1) ^ r) << 5) + ((c & 1) << 4); vb1 = vb0 ^ 128; }
    { const int r = lane >> 3, c = lane & 7; vs0 = (wq * 32 + r) * 2048 + ct * 256 + kh * 128 + (((c >> 1) ^ ((r >> 1) & 1)) << 5) + ((c & 1) << 4); vs1 = vs0 ^ 64; }
    sla = lb + wq * 4096; ska = kh * (56 * 1024); skb4 = (kh * 56 + (wq >> 1) * 4) * 65536; skb7 = (kh * 56 + (wq >> 1) * 7) * 65536;
  }
  template <int GT, int S, int KS> __device__ __forceinline__ void dmaA() { constexpr int LO = L2_A + GT * 32768 + (S & 1) * 16384 + KS * 1024;
    if constexpr (S < 14) DMA_SC1(sact, vaa, ska, (GT * 4 * 112 + kstep(2 * S + (KS >> 1), KS & 1)) * 1024, sla, LO);
    else DMA_SC1(so, vao, 0u, (GT * 4 * 32 + (S - 14) * 4 + KS) * 1024, sla, LO); }
  template <int U, int J> __device__ __forceinline__ void dmaB() { constexpr int LO = L2_B + (U % 5) * 16384 + J * 1024;
    if constexpr (U < 28) { const unsigned sk = U < 24 ? skb4 : skb7; if (J >> 1) DMA(sw2, vb1, sk, kstep(U, 0) * 65536 + J * 8192, sla, LO); else DMA(sw2, vb0, sk, kstep(U, 0) * 65536 + J * 8192, sla, LO); }
    else { if (J & 1) DMA(swo, vs1, 0u, (U - 28) * 262144 + J * 16384, sla, LO); else DMA(swo, vs0, 0u, (U - 28) * 262144 + J * 16384, sla, LO); } }
  template <int GT, int S> __device__ __forceinline__ void dmaA4() { dmaA<GT, S, 0>(); dmaA<GT, S, 1>(); dmaA<GT, S, 2>(); dmaA<GT, S, 3>(); }
  template <int S, int I> __device__ __forceinline__ void dma_slot() {
    if constexpr (G == 0) {
      if constexpr (S <= 12) { if constexpr (I < 4) dmaB<2 * S + 3, I>(); else dmaB<2 * S + 4, I - 4>(); }
      else if constexpr (I < 2) { if constexpr (S <= 20) dmaA<0, S + 1, 2 + I>(); }
      else if constexpr (I < 6 && S <= 19) dmaB<S + 16, I - 2>();
    } else {
      if constexpr (I < 4) { if constexpr (S <= 20) dmaA<1, S + 1, I>(); }
      else if constexpr (S <= 11) dmaA<0, S + 2, I - 4>();
      else if constexpr (I < 6 && S <= 19) dmaA<0, S + 2, I - 4>();
    } }

  __device__ __forceinline__ void run(const Ptrs& P, u64 tag, int p, int h, Query& Q) {
    char* ws = P.ws;
    const int tok = lane & 15, g = lane >> 4, q = (lane >> 2) & 3, pp = lane & 3;
    const unsigned ra = opq(lb + L2_A + G * 32768 + lane * 16);
    unsigned rb[2][2], rs[2];
#pragma unroll
    for (int n = 0; n < 2; ++n) { const int f = 4 * (kh ^ n) + wq; rb[0][n] = opq(lb + L2_B + (8 * g + q) * 256 + ((f ^ (q | ((g & 1) << 2))) << 5) + 8 * pp); rb[1][n] = opq(rb[0][n] + 65536); }
    rs[0] = opq(lb + L2_B + (8 * g + q) * 128 + ((wq ^ ((q >> 1) | ((g & 1) << 1))) << 5) + 8 * pp); rs[1] = opq(rs[0] + 65536);
    f32x4 acc[4][2], as[4];
    bf16x8 fa[4][4], fb[4][2];
    auto load = [&](auto SS, auto hook) { constexpr int S = SS();
      auto step = [&](auto II) { constexpr int i = II(), ks = i >> 1;
        _Pragma("unroll") for (int m = 2 * (i & 1); m < 2 * (i & 1) + 2; ++m) { fa[ks][m] = lds128(ra + (S & 1) * 16384 + m * 4096 + ks * 1024); SB; }
        if constexpr (S < 14) { constexpr int buf = (2 * S + (ks >> 1)) % 5, o = (buf & 3) * 16384 + (ks & 1) * 8192; fb[ks][i & 1] = cat(ldstr(rb[buf >> 2][i & 1] + o), ldstr(rb[buf >> 2][i & 1] + o + 1024)); SB; }
        else if constexpr ((i & 1) == 0) { constexpr int buf = (S + 14) % 5, o = (buf & 3) * 16384 + ks * 4096; fb[ks][0] = cat(ldstr(rs[buf >> 2] + o), ldstr(rs[buf >> 2] + o + 512)); SB; }
        dma_slot<S, i>(); hook(II); SB; };
      step(IC<0>{}); step(IC<1>{}); step(IC<2>{}); step(IC<3>{}); step(IC<4>{}); step(IC<5>{}); step(IC<6>{}); step(IC<7>{}); };
    auto mm = [&](auto SS) { constexpr int S = SS();
#pragma unroll
      for (int m = 0; m < 4; ++m)
#pragma unroll
        for (int n = 0; n < (S < 14 ? 2 : 1); ++n) mk4<S == 0 || S == 14>(S < 14 ? acc[m][n] : as[m], fb[0][n], fa[0][m], fb[1][n], fa[1][m], fb[2][n], fa[2][m], fb[3][n], fa[3][m]);
      SB; };
    auto none = [](auto) {};
#define ENDL(n) do { WAIT_V(n); WAIT_L0; BAR; SB; } while (0)
#define ENDR do { WAIT_L0; BAR; SB; } while (0)
#define ENDM(n) do { WAIT_V(n); BAR; SB; } while (0)
    gu64* flags = (gu64*)(ws + WS_FLAG); gu64* tmo = flags + FL_TMO / 8;
    const unsigned kva = opq(lb + L2_KVA + lane * 16);
    u64 pv;
    const __amdgpu_buffer_rsrc_t ryx = mk_rsrc(ws + WS_YX, 256 * 32768);
    auto send = [&]() {
#pragma unroll
      for (int i = 0; i < 2; ++i) st16_sc1(__builtin_bit_cast(u32x4, pk8(acc[2 * i][1], acc[2 * i + 1][1])), ryx, lane * 16, b * 32768 + (G * 4 + wq) * 2048 + i * 1024);
      SB; };
    gu64* fy = flags + FL_YX / 8 + b * 8 + G * 4 + wq; gu64* py_ = flags + FL_YX / 8 + (b ^ 1) * 8 + G * 4 + wq;
    u32x4 py[2]; u32x2 xr[4]; f32x2 gt[8];
    const unsigned vout = (rt * 128 + G * 64 + (g & 1) * 16 + tok) * 2048 + ct * 256 + kh * 128 + wq * 32 + (g >> 1) * 16;
    auto fetch = [&]() { POLL_PIN(pv); if (!__all(pv == tag)) wait_flags<1, 1>(py_, tag, tmo, lane); SB;
      const u32x4 syx = mk_srd(ws + WS_YX, 256 * 32768); const unsigned vy = (b ^ 1) * 32768 + (G * 4 + wq) * 2048 + lane * 16;
      LD16_ISSUE(py[0], syx, vy, 0u); LD16_ISSUE(py[1], syx, vy, 1024u); };
    auto dmax = [&]() { const u32x4 sx = mk_srd(P.x, S_ * D_ * 2); const int r = lane >> 3;
      const unsigned v0 = (rt * 128 + wq * 32 + r) * 2048 + ct * 256 + kh * 128 + (((lane & 7) ^ (r >> 1)) << 4), v1 = v0 ^ 64;
      DMA(sx, v0, 0u, 0, sla, L2_X); DMA(sx, v1, 0u, 16384, sla, L2_X + 1024); DMA(sx, v0, 0u, 32768, sla, L2_X + 2048); DMA(sx, v1, 0u, 49152, sla, L2_X + 3072); };
    auto readx = [&]() { const unsigned a = lb + L2_X + (G * 64 + tok) * 128 + (((2 * wq + (g >> 1)) ^ (tok >> 1)) << 4) + (g & 1) * 8;
#pragma unroll
      for (int m = 0; m < 4; ++m) xr[m] = *(const LAS u32x2*)(size_t)(a + m * 2048);
      SB; };
    auto gate = [&](int k) { asm volatile("" : "+v"(py[k])); SB; f32x2 y[4], e[4];
#define ST4G(stmt) _Pragma("unroll") for (int i = 0; i < 4; ++i) { stmt; } SB
      ST4G(const unsigned u = py[k][i]; e[i] = opq2((f32x2){bflo(u), bfhi(u)}));
      ST4G(const f32x4 a = acc[2 * k + (i >> 1)][0]; y[i] = ((i & 1) ? a.zw : a.xy) + e[i]);
      ST4G(e[i] = opq2(y[i] * -LOG2E));
      ST4G(e[i] = opq2((f32x2){ex2(e[i][0]), ex2(e[i][1])}));
      ST4G(e[i] = opq2(e[i] + 1.0f));
      ST4G(e[i] = opq2((f32x2){rcp(e[i][0]), rcp(e[i][1])}));
      ST4G(gt[4 * k + i] = y[i] * e[i]; asm volatile("" : "+v"(gt[4 * k + i]))); };
#define S_(n) IC<n>{}
    if constexpr (G == 0) {
      WAIT_V(9); BAR; SB; STAMP(16);
      load(S_(0), none); ENDL(13);
      mm(S_(0)); ENDM(4); STAMP(17); load(S_(1), none); ENDR;
      gu64* fkv = flags + (FL_KV + (h * 4 + wq) * 1024) / 8;
      mm(S_(1)); ENDM(4); POLL_ISSUE(pv, fkv + (lane & 7) * 16); load(S_(2), none); ENDR;
      mm(S_(2)); ENDM(4);
      POLL_PIN(pv); if (!__all(pv == tag)) wait_flags<8, 16>(fkv, tag, tmo, lane); SB;
      const u32x4 skv = mk_srd(ws + WS_KVP, 3u << 20);
      u32x4 t[8], tz; LD16_ISSUE(tz, skv, h * 2048 + (lane & 7) * 256 + wq * 64 + g * 16, (unsigned)(WS_ZP - WS_KVP));
      { const unsigned vkv = h * 32768 + wq * 1024 + lane * 16;
#pragma unroll
        for (int i = 0; i < 8; ++i) LD16_ISSUE(t[i], skv, vkv, i * 4096u); }
      load(S_(3), none); ENDR; STAMP(25);
      f32x2 sm[4];
      auto addp = [&](int i) { f32x2 u[4];
#pragma unroll
        for (int c = 0; c < 4; ++c) u[c] = opq2((f32x2){bflo(t[i][c]), bfhi(t[i][c])});
        SB;
#pragma unroll
        for (int c = 0; c < 4; ++c) { sm[c] = i ? sm[c] + u[c] : u[c]; asm volatile("" : "+v"(sm[c])); }
        SB; };
      mm(S_(3)); WAIT_V(8);
      asm volatile("" : "+v"(t[0]), "+v"(t[1]), "+v"(t[2]), "+v"(t[3]), "+v"(t[4]), "+v"(t[5]), "+v"(t[6]), "+v"(t[7]), "+v"(tz)); SB;
      addp(0); addp(1); addp(2); ENDM(4); load(S_(4), none); ENDR; STAMP(26);
      mm(S_(4)); addp(3); addp(4); addp(5); ENDM(4); load(S_(5), [&](auto I) { constexpr int r = I();
        if constexpr (r < 4) { float z = __uint_as_float(tz[r]); z += dpp<0xB1>(z); z = opqf(z); z += dpp<0x4E>(z); z = opqf(z); z += dpp<0x141>(z); tz[r] = __float_as_uint(opqf(z)); asm volatile("" : "+v"(tz)); } });
      ENDR; STAMP(27);
      mm(S_(5)); addp(6); addp(7);
      { const unsigned a = kva + (wq >> 1) * 1024 + (wq & 1) * 8;
        *(LAS u32x2*)(size_t)a = (u32x2){pk2(sm[0][0], sm[0][1]), pk2(sm[1][0], sm[1][1])}; *(LAS u32x2*)(size_t)(a + 2048) = (u32x2){pk2(sm[2][0], sm[2][1]), pk2(sm[3][0], sm[3][1])};
        u32x2 au = {pk2(__uint_as_float(tz[0]), __uint_as_float(tz[1])), pk2(__uint_as_float(tz[2]), __uint_as_float(tz[3]))};
        if ((lane & 3) == 1) au = (u32x2){0x3F803F80u, 0x3F803F80u}; else if ((lane & 3) != 0) au = (u32x2){0u, 0u};
        *(LAS u32x2*)(size_t)(a + 4096) = au; SB; }
      ENDM(4); load(S_(6), none); ENDR;
      mm(S_(6)); ENDM(4); load(S_(7), none); ENDR;
      mm(S_(7)); ENDM(4); load(S_(8), none); ENDR;
      mm(S_(8)); ENDM(4); load(S_(9), none); ENDR;
      mm(S_(9)); ENDM(4); load(S_(10), none); ENDR; STAMP(18);
      mm(S_(10)); ENDM(4); if (PFCODE & 4) touch_code(lb + L1_DUMP, lane, wq); load(S_(11), none); ENDR;
      mm(S_(11)); ENDM(4); load(S_(12), none); ENDR;
      mm(S_(12)); ENDM(4); load(S_(13), none); ENDR;
      mm(S_(13)); ENDM(4); STAMP(19); load(S_(14), none); send(); ENDR;
      mm(S_(14)); ENDM(6); load(S_(15), none); ENDR; STAMP(28);
      mm(S_(15)); WAIT_V(4); set_flag(fy, tag, lane); BAR; SB; load(S_(16), none); ENDR;
      mm(S_(16)); ENDM(4); load(S_(17), none); ENDR; STAMP(29);
      mm(S_(17)); ENDM(4); POLL_ISSUE(pv, py_); load(S_(18), none); ENDR;
      mm(S_(18)); ENDM(4); fetch(); load(S_(19), none); ENDR; STAMP(20);
      mm(S_(19)); WAIT_V(6); gate(0); ENDM(4); load(S_(20), none); dmax(); ENDR; STAMP(30);
      mm(S_(20)); gate(1); ENDM(4); load(S_(21), none); WAIT_V(0); BAR; SB; readx(); __builtin_amdgcn_s_waitcnt(0xC47F); SB; STAMP(31);
      mm(S_(21));
    } else {
      BAR; SB; STAMP(16); BAR; SB;
      load(S_(0), none); WAIT_V(8); set_flag(flags + (FL_KV + (h * 4 + wq) * 1024) / 8 + p * 16, tag, lane); ENDR;
      const f32x4 z4 = {0.f, 0.f, 0.f, 0.f}; float ma[4], mb[4]; bf16x8 eb[4][2], ka[6];
      mm(S_(0));
#pragma unroll
      for (int m = 0; m < 4; ++m) { ma[m] = vmax8(Q.pq[m][0], Q.pq[m][1]); mb[m] = vmax8(-Q.pq[m][0], -Q.pq[m][1]); asm volatile("" : "+v"(ma[m]), "+v"(mb[m])); }
      SB; ENDM(5); STAMP(17);
      gu64* fa2 = flags + FL_ACT2 / 8 + (rt * 2 + kh) * 32;
      POLL_ISSUE(pv, fa2 + (lane & 31));
      load(S_(1), none); ENDL(9);
      mm(S_(1));
      spread4(gather4(ma[0], mb[0], ma[1], mb[1], vmax), ma[0], mb[0], ma[1], mb[1]);
      spread4(gather4(ma[2], mb[2], ma[3], mb[3], vmax), ma[2], mb[2], ma[3], mb[3]);
#pragma unroll
      for (int m = 0; m < 4; ++m) { ma[m] = opqf(ma[m] * -Q.rl[m]); mb[m] = opqf(mb[m] * -Q.rl[m]); asm volatile("" : "+v"(ma[m]), "+v"(mb[m])); }
      SB; ENDM(4);
      POLL_PIN(pv); if (!__all(pv == tag)) wait_flags<32, 1>(fa2, tag, tmo, lane); SB;
      auto expo = [&](int m) { f32x4 ep[2], en[2];
#pragma unroll
        for (int ff = 0; ff < 2; ++ff) { ep[ff] = Q.pq[m][ff] * Q.rl[m] + ma[m]; en[ff] = Q.pq[m][ff] * -Q.rl[m] + mb[m]; }
        SB;
#pragma unroll
        for (int ff = 0; ff < 2; ++ff)
#pragma unroll
          for (int r = 0; r < 4; ++r) { ep[ff][r] = ex2(ep[ff][r]); en[ff][r] = ex2(en[ff][r]); }
        SB; eb[m][0] = pk8(ep[0], ep[1]); eb[m][1] = pk8(en[0], en[1]); asm volatile("" : "+v"(eb[m][0]), "+v"(eb[m][1])); SB; };
      load(S_(2), none); ENDL(8); STAMP(25);
      mm(S_(2)); expo(0); ENDM(4); load(S_(3), none); ENDL(8);
      mm(S_(3)); expo(1); ENDM(4); load(S_(4), none); ENDL(8); STAMP(26);
      mm(S_(4)); expo(2); ENDM(4); load(S_(5), none); ENDL(8); STAMP(27);
      mm(S_(5)); expo(3); ENDM(4);
      const __amdgpu_buffer_rsrc_t ro = mk_rsrc(ws + WS_O, 128 * 32 * 1024);
      const unsigned vo = (unsigned)((((g & 1) << 1) | (g >> 1)) * 256 + tok * 16) + ((p * 16 + wq * 4) * 32 + h) * 1024;
      auto out_o = [&](int m) { f32x4 r6[6];
#pragma unroll
        for (int k = 5; k >= 0; --k) r6[k] = mfma(ka[k], eb[m][k & 1], z4);
        SB; const float a = rcp(r6[4][1]), c = rcp(r6[5][1]), inv = rcp(opqf(r6[4][0] * a + 1e-6f) + r6[5][0] * c), ca = opqf(a * inv), cc = opqf(c * inv);
        const f32x4 o0 = r6[0] * ca + r6[1] * cc, o1 = r6[2] * ca + r6[3] * cc;
        st16_sc1(swap16(pk2(o0[0], o0[1]), pk2(o0[2], o0[3]), pk2(o1[0], o1[1]), pk2(o1[2], o1[3])), ro, vo, m * 32768); SB; };
      load(S_(6), [&](auto I) { constexpr int i = I(); if constexpr (i >= 2) ka[i - 2] = lds128(kva + (i - 2) * 1024); }); ENDL(8);
      mm(S_(6)); out_o(0); ENDM(5); load(S_(7), none); ENDL(9);
      mm(S_(7)); out_o(1); ENDM(5); load(S_(8), none); ENDL(9);
      mm(S_(8)); out_o(2); ENDM(5); load(S_(9), none); ENDL(9);
      mm(S_(9)); out_o(3); ENDM(5); load(S_(10), none); WAIT_V(8);
      set_flag(flags + FL_O / 8 + (2 * p + (wq >> 1)) * 64 + h * 2 + (wq & 1), tag, lane); ENDR; STAMP(18);
      gu64* po = flags + FL_O / 8 + rt * 64;
      mm(S_(10)); ENDM(5); load(S_(11), none); POLL_ISSUE(pv, po + lane); ENDL(9);
      mm(S_(11)); ENDM(5); WAIT_V(0); POLL_PIN(pv); if (!__all(pv == tag)) wait_flags<64, 1>(po, tag, tmo, lane); SB; load(S_(12), none); ENDL(6);
      mm(S_(12)); ENDM(2); load(S_(13), none); ENDL(6);
      mm(S_(13)); ENDM(2); STAMP(19); load(S_(14), none); send(); ENDL(8);
      mm(S_(14)); ENDM(4); load(S_(15), none); ENDL(8); STAMP(28);
      mm(S_(15)); WAIT_V(2); set_flag(fy, tag, lane); BAR; SB; load(S_(16), none); ENDL(7);
      mm(S_(16)); ENDM(2); load(S_(17), none); ENDL(6); STAMP(29);
      mm(S_(17)); ENDM(2); POLL_ISSUE(pv, py_); load(S_(18), none); ENDL(7);
      mm(S_(18)); ENDM(2); fetch(); load(S_(19), none); ENDL(8); STAMP(20);
      mm(S_(19)); WAIT_V(6); gate(0); ENDM(2); load(S_(20), none); ENDL(4); STAMP(30);
      mm(S_(20)); gate(1); ENDM(0); load(S_(21), none); readx(); __builtin_amdgcn_s_waitcnt(0xC47F); SB; STAMP(31);
      mm(S_(21));
    }
    MFMA_PAD; STAMP(21);
    const __amdgpu_buffer_rsrc_t rout = mk_rsrc(P.out, S_ * D_ * 2);
#pragma unroll
    for (int i = 0; i < 2; ++i) { unsigned ow[4];
#pragma unroll
      for (int c = 0; c < 4; ++c) { const f32x4 s = as[2 * i + (c >> 1)]; const unsigned u = xr[2 * i + (c >> 1)][c & 1]; const f32x2 o = ((c & 1) ? s.zw : s.xy) * gt[4 * i + c] + opq2((f32x2){bflo(u), bfhi(u)}); ow[c] = pk2(o[0], o[1]); }
      st16(swap16(ow[0], ow[1], ow[2], ow[3]), rout, vout, i * 65536); }
    if (G == 0 && b == 0 && wq == 0) set_flag(flags + FL_EPOCH / 8, tag, lane);
    if (G == 0) asm volatile(".Lend_g0:"); else asm volatile(".Lend_g1:");
    STAMP(23);
  }
};
#define P2RUN(p2, P, tag, eb) p2.run(P, tag, p, h, eb)

__global__ __launch_bounds__(512, 2) void k_fused(const u16* x, const u16* wqkv, const u16* wphi, const u16* wo, const u16* w1, const u16* b1, const u16* w2, char* ws, u16* out) {
  __shared__ __attribute__((aligned(16))) char smem[163840];
  const Ptrs P = {x, wqkv, wphi, wo, w1, b1, w2, ws, out};
  const unsigned lb = (unsigned)(size_t)(LAS char*)smem;
  const int tid = threadIdx.x, lane = tid & 63, w = __builtin_amdgcn_readfirstlane(tid >> 6), wx = w & 3;
  const int b = blockIdx.x, h = (b & 7) * 4 + ((b >> 3) & 3), p = b >> 5, odd = h & 1;
  const int tok = lane & 15, g = lane >> 4, q = (lane >> 2) & 3, pp = lane & 3;
  const unsigned ra0 = lb + L1_A + (wx * 64 + tok) * 128 + ((g ^ (tok >> 1)) << 4), ra1 = ra0 ^ 64;
  gu64* flags = (gu64*)(ws + WS_FLAG); gu64* tmo = flags + FL_TMO / 8;
  asm volatile(".Lcode_begin:");
  STAMP_DECL; STAMP(0);
  const int rot = p < 7 ? 2 * p : 0;
  auto kt = [&](int pos) { int t = pos + rot; t -= t >= 16 ? 14 : 0; return pos < 2 ? pos : t; };
  u64 ep; { gu64* pe = flags + FL_EPOCH / 8; POLL_ISSUE(ep, pe); }
  const __amdgpu_buffer_rsrc_t rfl = mk_rsrc(ws + WS_FLAG, 1u << 20);
  bf16x8 ones = {0x3F80, 0x3F80, 0x3F80, 0x3F80, 0x3F80, 0x3F80, 0x3F80, 0x3F80}; asm volatile("" : "+v"(ones));
  auto cs_publish = [&](const f32x4& c0, const f32x4& c1, int j0, bool two, u64 tag) {
    const f32x4 a = (tok & 2) ? c1 : c0; const u32x4 v = {__float_as_uint((tok & 1) ? a[2] : a[0]), (unsigned)tag, __float_as_uint((tok & 1) ? a[3] : a[1]), (unsigned)tag};
    if (tok < (two ? 4 : 2)) st16_sc1(v, rfl, ((j0 + 2 * (tok & 2)) * 16 + 4 * g + 2 * (tok & 1)) * 8, GR_CS + (h * 8 + p) * 1664);
    SB; };

  if (w < 4) {
    const u32x4 sa = mk_srd(x, S_ * D_ * 2);
    unsigned va0, va1;
    { const int r = lane >> 3; va0 = (p * 256 + wx * 64 + r) * 2048 + (((lane & 7) ^ (r >> 1)) << 4); va1 = va0 ^ 64; }
    const unsigned sl = lb + L1_A + wx * 8192;
    const unsigned rb0 = opq(lb + L1_BH + (8 * g + 4 * (g & 1) + q) * 224 + 8 * pp), rb1 = opq(lb + L1_BH + (8 * g + 4 * ((g & 1) ^ 1) + q) * 224 + 8 * pp);
    f32x4 acc[4][7], cs0, cs1;
    bf16x8 fa[2][4], fb[2][7];
    auto dmaA = [&](auto ST, auto J, unsigned sk) { constexpr int st = ST(), j = J(); if (j & 1) DMA(sa, va1, sk, j * 16384, sl, st * 32768 + j * 1024); else DMA(sa, va0, sk, j * 16384, sl, st * 32768 + j * 1024); };
    auto rdA = [&](auto ST, auto KS, auto M) { constexpr int st = ST(), ks = KS(), m = M(); fa[ks][m] = lds128((ks ? ra1 : ra0) + st * 32768 + m * 2048); SB; };
    auto rdB = [&](auto ST, auto KS, auto J) { constexpr int st = ST(), ks = KS(), j = J(); constexpr int o = st * 14336 + ks * 7168 + j * 32; fb[ks][j] = cat(ldstr(rb0 + o), ldstr(rb1 + o)); SB; };
    auto load = [&](auto ST, auto DO, unsigned sk) {
      constexpr int o = ST() ^ 1; constexpr bool d = DO();
      rdA(ST, IC<0>{}, IC<0>{}); rdB(ST, IC<0>{}, IC<0>{}); rdB(ST, IC<0>{}, IC<1>{}); if (d) dmaA(IC<o>{}, IC<0>{}, sk);
      rdA(ST, IC<0>{}, IC<1>{}); rdB(ST, IC<0>{}, IC<2>{}); rdB(ST, IC<0>{}, IC<3>{}); if (d) dmaA(IC<o>{}, IC<1>{}, sk);
      rdA(ST, IC<0>{}, IC<2>{}); rdB(ST, IC<0>{}, IC<4>{}); rdB(ST, IC<0>{}, IC<5>{}); if (d) dmaA(IC<o>{}, IC<2>{}, sk);
      rdA(ST, IC<0>{}, IC<3>{}); rdB(ST, IC<0>{}, IC<6>{}); rdB(ST, IC<1>{}, IC<0>{}); if (d) dmaA(IC<o>{}, IC<3>{}, sk);
      rdA(ST, IC<1>{}, IC<0>{}); rdB(ST, IC<1>{}, IC<1>{}); rdB(ST, IC<1>{}, IC<2>{}); if (d) dmaA(IC<o>{}, IC<4>{}, sk);
      rdA(ST, IC<1>{}, IC<1>{}); rdB(ST, IC<1>{}, IC<3>{}); rdB(ST, IC<1>{}, IC<4>{}); if (d) dmaA(IC<o>{}, IC<5>{}, sk);
      rdA(ST, IC<1>{}, IC<2>{}); rdB(ST, IC<1>{}, IC<5>{}); if (d) dmaA(IC<o>{}, IC<6>{}, sk);
      rdA(ST, IC<1>{}, IC<3>{}); rdB(ST, IC<1>{}, IC<6>{}); if (d) dmaA(IC<o>{}, IC<7>{}, sk);
    };
    auto mm_ = [&](auto F) {
#pragma unroll
      for (int m = 0; m < 4; ++m)
#pragma unroll
        for (int j = 0; j < 7; ++j) mk2<F()>(acc[m][j], fb[0][j], fa[0][m], fb[1][j], fa[1][m]);
      SB; };
    auto mm = [&]() { mm_(IC<0>{}); };
#define A8(ST, sk) dmaA(IC<ST>{}, IC<0>{}, sk); dmaA(IC<ST>{}, IC<1>{}, sk); dmaA(IC<ST>{}, IC<2>{}, sk); dmaA(IC<ST>{}, IC<3>{}, sk); dmaA(IC<ST>{}, IC<4>{}, sk); dmaA(IC<ST>{}, IC<5>{}, sk); dmaA(IC<ST>{}, IC<6>{}, sk); dmaA(IC<ST>{}, IC<7>{}, sk)
    if (wx == 0 && TOUCH0) { const int l = min(lane, 51), ts = l / 26, rem = l - ts * 26, rr = rem / 13, li = rem - rr * 13, k = ts * 64 + (b >> 3) * 2 + rr;
      TOUCH(li < 6 ? (const GAS char*)wqkv + k * 6144 + (li >> 1) * 2048 + (b & 7) * 256 + (li & 1) * 128 : (const GAS char*)w1 + k * 7168 + (b & 7) * 896 + (li - 6) * 128, lb + L1_DUMP); }
    if (wx == 1 && TOUCH0) TOUCH((const GAS char*)x + (p * 256 + ((b >> 3) & 3) * 64 + lane) * 2048 + (b & 1) * 128, lb + L1_DUMP);
    A8(0, 0u);
    if (PFCODE & 1) touch_code(lb + L1_DUMP, lane, wx);
    if (wx == 0) asm volatile("s_mov_b32 m0, %2\n\ts_nop 0\n\tbuffer_load_dword %0, %1, 0 offen lds" :: "v"(h * 224 + lane * 4), "s"(mk_srd(b1, F_ * 2)), "s"(lb + L1_BIAS) : "memory");
    cs0 = zacc(); cs1 = zacc();
    auto colsum = [&](bool now) { colsum7(cs0, cs1, fb[0], fb[1], ones, now ? wx : 4); SB; };
    if (tid == 0) *(LAS u64*)(size_t)(lb + L1_CNT) = 0ull;
    STAMP(1);
    WAIT_V(0); POLL_PIN(ep); const u64 tag = uni64(ep) + 1;
    A8(1, 128u);
    WAIT_L0; BAR; SB; STAMP(4);
    load(IC<0>{}, IC<0>{}, 0u); WAIT_L0; BAR; SB; STAMP(5);
#pragma unroll
    for (int t = 0; t < 4; t += 2) {
      if (t == 0) mm_(IC<1>{}); else mm();
      colsum((p == 7) == (t == 0)); WAIT_V(0); BAR; SB;
      load(IC<1>{}, IC<1>{}, (unsigned)kt(t + 2) * 128u); WAIT_L0; BAR; SB;
      mm(); colsum((p == 7) == (t == 0)); WAIT_V(0); BAR; SB;
      load(IC<0>{}, IC<1>{}, (unsigned)kt(t + 3) * 128u); if (t == 2) cs_publish(cs0, cs1, wx, wx < 3, tag); WAIT_L0; BAR; SB;
    }
    for (int t = 4; t < 14; t += 2) {
      mm(); WAIT_V(0); BAR; SB;
      load(IC<1>{}, IC<1>{}, (unsigned)kt(t + 2) * 128u); WAIT_L0; BAR; SB;
      mm(); WAIT_V(0); BAR; SB;
      load(IC<0>{}, IC<1>{}, (unsigned)kt(t + 3) * 128u); WAIT_L0; BAR; SB;
    }
    STAMP(6);
    mm(); WAIT_V(0); BAR; SB;
    u32x2 bias[7];
#pragma unroll
    for (int j = 0; j < 7; ++j) bias[j] = *(const LAS u32x2*)(size_t)(lb + L1_BIAS + (j < 6 ? j + odd : (odd ? 0 : 6)) * 32 + g * 8);
    SB;
    { const unsigned dump = lb + L1_DUMP; const int cu = b >> 3, a = (b & 7) >> 1;
      const int l2 = min(lane, 55) + 56 * (wx + 4 * cu);
      if (PFCODE & 2) touch_code(dump, lane, wx);
      if (TOUCHW) TOUCH((const GAS char*)w2 + ((b & 1) * 1792 + a * 448) * 2048 + l2 * 128, dump);
      if (TOUCHW) TOUCH((const GAS char*)wo + (b * 64 + wx * 16 + (lane & 15)) * 128, dump); }
    load(IC<1>{}, IC<0>{}, 0u);
    f32x4 bl[7];
#pragma unroll
    for (int j = 0; j < 7; ++j) { bl[j] = (f32x4){opqf(bflo(bias[j][0]) * LOG2E), opqf(bfhi(bias[j][0]) * LOG2E), opqf(bflo(bias[j][1]) * LOG2E), opqf(bfhi(bias[j][1]) * LOG2E)}; asm volatile("" : "+v"(bl[j])); }
    WAIT_L0; BAR; SB;
    mm(); BAR; MFMA_PAD; STAMP(7);
    f32x4 cs[7]; float rl[4], rml[4];
#pragma unroll
    for (int j = 0; j < 7; ++j) cs[j] = *(const LAS f32x4*)(size_t)(lb + L1_CS + j * 64 + g * 16);
#pragma unroll
    for (int m = 0; m < 4; ++m) { const f32x2 st = *(const LAS f32x2*)(size_t)(lb + L1_ST + (wx * 64 + m * 16 + tok) * 8); rl[m] = opqf(st[0] * LOG2E); rml[m] = opqf(-rl[m] * st[1]); }
    SB;
    Phase2<0> p2; p2.init(lb, lane, wx, P);
    const __amdgpu_buffer_rsrc_t ract = mk_rsrc(ws + WS_ACT, 128 * 112 * 1024);
    auto mish8 = [&](int m0, int j0, int m1, int j1) -> u32x4 {
      const f32x2 a[4] = {acc[m0][j0].xy, acc[m0][j0].zw, acc[m1][j1].xy, acc[m1][j1].zw}, c[4] = {cs[j0].xy, cs[j0].zw, cs[j1].xy, cs[j1].zw}, bb[4] = {bl[j0].xy, bl[j0].zw, bl[j1].xy, bl[j1].zw};
      f32x2 t[4], hs[4], e[4], d[4]; unsigned o[4];
#define ST4(stmt) _Pragma("unroll") for (int i = 0; i < 4; ++i) { stmt; } SB
      ST4(t[i] = c[i] * rml[i < 2 ? m0 : m1] + bb[i]);
      ST4(hs[i] = a[i] * rl[i < 2 ? m0 : m1] + t[i]);
      ST4(e[i] = opq2((f32x2){ex2(hs[i][0]), ex2(hs[i][1])}));
      ST4(t[i] = e[i] + 2.0f);
      ST4(d[i] = opq2(e[i] * t[i] + 2.0f));
      ST4(e[i] = opq2((f32x2){rcp(d[i][0]), rcp(d[i][1])}));
      ST4(t[i] = e[i] * (-2.0f / LOG2E) + 1.0f / LOG2E);
      ST4(d[i] = hs[i] * t[i]);
      ST4(o[i] = pk2(d[i][0], d[i][1]));
      return swap16(o[0], o[1], o[2], o[3]); };
    const unsigned vpair = (unsigned)((((g & 1) << 1) | (g >> 1)) * 256 + tok * 16), mb0 = p * 16 + wx * 4;
    auto pair = [&](auto J, auto M) { constexpr int j = J(), m = M();
      st16_sc1(mish8(m, j, m, j + 1), ract, vpair, ((mb0 + m) * 112 + ((7 * h + odd + j) >> 1)) * 1024); SB;
      constexpr int n = 2 * j + m - BLATE; if constexpr (n >= 0 && n < 6) { p2.dmaB<n / 2, 2 * (n & 1)>(); p2.dmaB<n / 2, 1 + 2 * (n & 1)>(); } };
    const int fai = ((2 * p + (wx >> 1)) * 2 + (h >> 4)) * 32 + (h & 15) * 2 + (wx & 1); gu64* fact = flags + FL_ACT / 8 + fai * 16;
    STAMP(13);
    if (XP0) __builtin_amdgcn_s_setprio(2);
    pair(IC<0>{}, IC<0>{}); pair(IC<0>{}, IC<1>{}); pair(IC<0>{}, IC<2>{}); pair(IC<0>{}, IC<3>{});
    if (XP0 == 4) __builtin_amdgcn_s_setprio(0);
    STAMP(14);
    pair(IC<2>{}, IC<0>{}); pair(IC<2>{}, IC<1>{});
    STAMP(15);
    WAIT_V(2); set_flag(fact, tag, lane); SB; STAMP(8);
    gu64* fa1 = flags + FL_ACT / 8 + (p2.rt * 2 + p2.kh) * 512; u64 pv;
    const unsigned vsing = (unsigned)((g & 1) * 112 * 1024 + (2 * odd + (g >> 1)) * 256 + tok * 16);
    auto single = [&](int m) {
      st16_sc1(mish8(m, 6, m + 1, 6), ract, vsing, ((mb0 + m) * 112 + ((7 * h + (odd ? 0 : 6)) >> 1)) * 1024); SB; };
    auto geta = [&]() { POLL_PIN(pv); STAMP(10); if (!__all(pv == tag)) wait_flags<32, 16>(fa1, tag, tmo, lane);
      SB; STAMP(11); p2.dmaA4<0, 0>(); p2.dmaA4<1, 0>(); p2.dmaA4<0, 1>(); };
    if (XP0 == 6) __builtin_amdgcn_s_setprio(0);
    pair(IC<2>{}, IC<2>{}); pair(IC<2>{}, IC<3>{});
    if (XP0 == 8) __builtin_amdgcn_s_setprio(0);
    if (EARLYA == 0) {
      pair(IC<4>{}, IC<0>{}); pair(IC<4>{}, IC<1>{}); pair(IC<4>{}, IC<2>{}); pair(IC<4>{}, IC<3>{});
      POLL_ISSUE(pv, fa1 + (lane & 31) * 16); single(0); single(2);
      STAMP(9); WAIT_V(2); geta(); WAIT_V(12);
    } else {
      pair(IC<4>{}, IC<0>{}); POLL_ISSUE(pv, fa1 + (lane & 31) * 16); pair(IC<4>{}, IC<1>{}); pair(IC<4>{}, IC<2>{});
      WAIT_V(6); geta(); pair(IC<4>{}, IC<3>{}); single(0); single(2); STAMP(9); WAIT_V(0);
    }
    set_flag(flags + FL_ACT2 / 8 + fai, tag, lane); SB; STAMP(12);
    Query none; P2RUN(p2, P, tag, none);
  } else {
    const int srow = p * 256 + h * 8 + wx * 2; u32x4 xa[4];
    { const u32x4 sx = mk_srd(x, S_ * D_ * 2); const unsigned vx = srow * 2048 + lane * 16;
      asm volatile("buffer_load_dwordx4 %0, %4, %5, 0 offen\n\tbuffer_load_dwordx4 %1, %4, %5, 0 offen offset:1024\n\tbuffer_load_dwordx4 %2, %4, %5, 0 offen offset:2048\n\tbuffer_load_dwordx4 %3, %4, %5, 0 offen offset:3072"
                   : "=&v"(xa[0]), "=&v"(xa[1]), "=&v"(xa[2]), "=&v"(xa[3]) : "v"(vx), "s"(sx) : "memory"); SB; }
    const u32x4 sq = mk_srd(wqkv, D_ * 3 * D_ * 2), sh = mk_srd(w1, D_ * F_ * 2), sp = mk_srd(wphi, 32 * 32 * 32 * 2);
    unsigned vq, vh[4];
    { const int r = lane >> 2; vq = (wx * 16 + r) * 6144 + h * 64 + (((lane & 3) ^ (((r >> 3) & 1) << 1)) << 4); }
#pragma unroll
    for (int i = 0; i < 4; ++i) { const int o = (wx + 4 * i) * 1024 + lane * 16, rp = o / 224, c = (o - rp * 224) >> 4, k = rp ^ (((rp >> 3) & 1) << 2); int cs = c + 2 * odd; if (cs >= 14) cs -= 14;
      vh[i] = k * 7168 + h * 224 + cs * 16; }
    const unsigned slq = lb + L1_BQ + wx * 1024, slh = lb + L1_BH + wx * 1024;
    const unsigned rq0 = opq(lb + L1_BQ + (8 * g + q) * 64 + 32 * (g & 1) + 8 * pp), rq1 = opq(rq0 ^ 32);
    f32x4 acc[3][4][2], cs0, cs1;
    bf16x8 fa[2][4], fb[2][6];
    auto dmaQ = [&](auto ST, auto U, unsigned sk) { constexpr int st = ST(), u = U(); DMA(sq, vq, sk, u * 2048, slq, st * 12288 + u * 4096); };
    auto dmaH = [&](auto ST, auto I, unsigned sk) { constexpr int st = ST(), i = I(); if (i < 3 || wx < 2) DMA(sh, vh[i], sk, 0, slh, st * 14336 + i * 4096); };
    auto rdA = [&](auto ST, auto KS, auto M) { constexpr int st = ST(), ks = KS(), m = M(); fa[ks][m] = lds128((ks ? ra1 : ra0) + st * 32768 + m * 2048); SB; };
    auto rdB = [&](auto ST, auto KS, auto J) { constexpr int st = ST(), ks = KS(), j = J(); constexpr int o = st * 12288 + (j >> 1) * 4096 + ks * 2048; const unsigned a = (j & 1) ? rq1 : rq0;
      fb[ks][j] = cat(ldstr(a + o), ldstr(a + o + 256)); SB; };
    auto load = [&](auto ST, auto DQ, auto DH, int t) {
      constexpr int o = ST() ^ 1; constexpr bool dq = DQ(), dh = DH(); const unsigned skq = (unsigned)kt(t + 1) * (64 * 6144), skh = (unsigned)kt(t + 2) * (64 * 7168);
      rdA(ST, IC<0>{}, IC<0>{}); rdB(ST, IC<0>{}, IC<0>{}); rdB(ST, IC<0>{}, IC<1>{}); if (dq) dmaQ(IC<o>{}, IC<0>{}, skq);
      rdA(ST, IC<0>{}, IC<1>{}); rdB(ST, IC<0>{}, IC<2>{}); rdB(ST, IC<0>{}, IC<3>{}); if (dq) dmaQ(IC<o>{}, IC<1>{}, skq);
      rdA(ST, IC<0>{}, IC<2>{}); rdB(ST, IC<0>{}, IC<4>{}); rdB(ST, IC<0>{}, IC<5>{}); if (dq) dmaQ(IC<o>{}, IC<2>{}, skq);
      rdA(ST, IC<0>{}, IC<3>{}); rdB(ST, IC<1>{}, IC<0>{}); if (dh) dmaH(ST, IC<0>{}, skh);
      rdA(ST, IC<1>{}, IC<0>{}); rdB(ST, IC<1>{}, IC<1>{}); rdB(ST, IC<1>{}, IC<2>{}); if (dh) dmaH(ST, IC<1>{}, skh);
      rdA(ST, IC<1>{}, IC<1>{}); rdB(ST, IC<1>{}, IC<3>{}); if (dh) dmaH(ST, IC<2>{}, skh);
      rdA(ST, IC<1>{}, IC<2>{}); rdB(ST, IC<1>{}, IC<4>{});
      rdA(ST, IC<1>{}, IC<3>{}); rdB(ST, IC<1>{}, IC<5>{});
      if (dh) { if (wx < 2) { dmaH(ST, IC<3>{}, skh); WAIT_V(7); } else WAIT_V(6); }
    };
    auto mm_ = [&](auto F) {
#pragma unroll
      for (int m = 0; m < 4; ++m)
#pragma unroll
        for (int j = 0; j < 6; ++j) mk2<F()>(acc[j >> 1][m][j & 1], fb[0][j], fa[0][m], fb[1][j], fa[1][m]);
      SB; };
    auto mm = [&]() { mm_(IC<0>{}); };
#define H4(ST, sk) dmaH(IC<ST>{}, IC<0>{}, sk); dmaH(IC<ST>{}, IC<1>{}, sk); dmaH(IC<ST>{}, IC<2>{}, sk); dmaH(IC<ST>{}, IC<3>{}, sk)
#define Q3(ST, sk) dmaQ(IC<ST>{}, IC<0>{}, sk); dmaQ(IC<ST>{}, IC<1>{}, sk); dmaQ(IC<ST>{}, IC<2>{}, sk)
    H4(0, 0u); Q3(0, 0u);
    cs0 = zacc(); cs1 = zacc();
    auto colsum = [&](bool now) { const bf16x8 a[7] = {fb[0][0], fb[0][1], fb[0][2], fb[0][3], fb[0][4], fb[0][5], ones}, c[7] = {fb[1][0], fb[1][1], fb[1][2], fb[1][3], fb[1][4], fb[1][5], ones};
      colsum7(cs0, cs1, a, c, ones, now ? wx : 4); SB; };
    STAMP(1);
    if (wx < 2) WAIT_V(7); else WAIT_V(6);
    asm volatile("" : "+v"(xa[0]), "+v"(xa[1]), "+v"(xa[2]), "+v"(xa[3]), "+v"(ep));
    const u64 tag = uni64(ep) + 1;
    { float v[2][16], sm[2], sq_[2], mean[2];
#pragma unroll
      for (int r = 0; r < 2; ++r) { sm[r] = 0.f;
#pragma unroll
        for (int i = 0; i < 8; ++i) { const unsigned u = xa[2 * r + (i >> 2)][i & 3]; v[r][2 * i] = bflo(u); v[r][2 * i + 1] = bfhi(u); sm[r] += v[r][2 * i] + v[r][2 * i + 1]; } }
#pragma unroll
      for (int r = 0; r < 2; ++r) { mean[r] = wave_sum(sm[r]) * (1.0f / D_); sq_[r] = 0.f;
#pragma unroll
        for (int i = 0; i < 16; ++i) { const float d = v[r][i] - mean[r]; sq_[r] += d * d; } }
      const float r0 = __builtin_amdgcn_rsqf(wave_sum(sq_[0]) * (1.0f / D_) + 1e-5f), r1 = __builtin_amdgcn_rsqf(wave_sum(sq_[1]) * (1.0f / D_) + 1e-5f);
      const u32x4 gr = {__float_as_uint(lane ? mean[1] : mean[0]), (unsigned)tag, __float_as_uint(lane ? r1 : r0), (unsigned)tag};
      if (lane < 2) st16_sc1(gr, rfl, lane * 16, GR_ST + srow * 16);
      SB; }
    STAMP(2);
    WAIT_V(1);
    STAMP(3); BAR; SB; STAMP(4);
    if (wx < 2) { const unsigned vp = h * 2048 + wx * 1024 + lane * 16, slp = lb + L1_WPHI + wx * 1024; DMA(sp, vp, 0u, 0, slp, 0); }
    H4(1, 64u * 7168u); Q3(1, 64u * 6144u);
    BAR; SB; STAMP(5);
    load(IC<0>{}, IC<0>{}, IC<1>{}, 0); WAIT_L0; BAR; SB;
    mm_(IC<1>{}); colsum(p == 7); WAIT_V(3); BAR; SB;
    load(IC<1>{}, IC<1>{}, IC<1>{}, 1); WAIT_L0; BAR; SB;
    mm(); colsum(p == 7); WAIT_V(3); BAR; SB;
    load(IC<0>{}, IC<1>{}, IC<1>{}, 2); WAIT_L0; BAR; SB;
    mm(); colsum(p != 7); WAIT_V(3); BAR; SB;
    load(IC<1>{}, IC<1>{}, IC<1>{}, 3); WAIT_L0; BAR; SB;
    mm(); colsum(p != 7); WAIT_V(3); BAR; MFMA_PAD;
    cs_publish(cs0, cs1, 7 + wx, wx < 2, tag);
    for (int t = 4; t < 14; t += 2) {
      load(IC<0>{}, IC<1>{}, IC<1>{}, t); WAIT_L0; BAR; SB;
      mm(); WAIT_V(3); BAR; SB;
      load(IC<1>{}, IC<1>{}, IC<1>{}, t + 1); WAIT_L0; BAR; SB;
      mm(); WAIT_V(3); BAR; SB;
    }
    STAMP(6);
    const unsigned vst = GR_ST + (p * 256 + wx * 64 + lane) * 16, vcs = GR_CS + h * (8 * 1664) + min(wx * 64 + lane, 103) * 16;
    const u32x4 sfl = mk_srd(ws + WS_FLAG, 1u << 20);
    u32x4 gs, gc[8]; LD16_ISSUE(gs, sfl, vst, 0u);
#pragma unroll
    for (int i = 0; i < 8; ++i) LD16_ISSUE(gc[i], sfl, vcs, i * 1664u);
    load(IC<0>{}, IC<1>{}, IC<0>{}, 14);
    WAIT_V(12); WAIT_L0; BAR; SB;
    mm(); WAIT_V(0); BAR; SB;
    load(IC<1>{}, IC<0>{}, IC<0>{}, 15);
    { auto stale = [&]() { unsigned d = differ(gs[1], gs[3], (unsigned)tag);
#pragma unroll
        for (int i = 0; i < 8; ++i) d |= differ(gc[i][1], gc[i][3], (unsigned)tag);
        return !__all(d == 0u); };
      asm volatile("" : "+v"(gs), "+v"(gc[0]), "+v"(gc[1]), "+v"(gc[2]), "+v"(gc[3]), "+v"(gc[4]), "+v"(gc[5]), "+v"(gc[6]), "+v"(gc[7]));
      if (__builtin_expect(stale(), 0)) {
#pragma nounroll
        for (unsigned spins = 0; spins < (1u << 16); ++spins) {
          asm volatile("buffer_load_dwordx4 %0, %1, %2, 0 offen sc1" : "+v"(gs) : "v"(vst), "s"(sfl) : "memory");
#pragma unroll
          for (int i = 0; i < 8; ++i) asm volatile("buffer_load_dwordx4 %0, %1, %2, %3 offen sc1" : "+v"(gc[i]) : "v"(vcs), "s"(sfl), "s"(i * 1664u) : "memory");
          asm volatile("s_waitcnt vmcnt(0)" : "+v"(gs), "+v"(gc[0]), "+v"(gc[1]), "+v"(gc[2]), "+v"(gc[3]), "+v"(gc[4]), "+v"(gc[5]), "+v"(gc[6]), "+v"(gc[7]) :: "memory");
          if (!stale()) break; } }
      f32x2 c = {__uint_as_float(gc[0][0]), __uint_as_float(gc[0][2])};
#pragma unroll
      for (int i = 1; i < 8; ++i) c = (f32x2){add1(c[0], __uint_as_float(gc[i][0])), add1(c[1], __uint_as_float(gc[i][2]))};
      *(LAS f32x2*)(size_t)(lb + L1_CS + min(wx * 64 + lane, 103) * 8) = c;
      *(LAS f32x2*)(size_t)(lb + L1_ST + (wx * 64 + lane) * 8) = (f32x2){__uint_as_float(gs[2]), __uint_as_float(gs[0])}; SB; }
    WAIT_L0; BAR; SB;
    mm(); MFMA_PAD; STAMP(7);
    if (YPRIO) __builtin_amdgcn_s_setprio(1);
    {
    const int lane = opq(__lane_id()), tok = lane & 15, g = lane >> 4, q = (lane >> 2) & 3, pp = lane & 3;
    bf16x8 wp[2];
#pragma unroll
    for (int ff = 0; ff < 2; ++ff) { const unsigned a = lb + L1_WPHI + (4 * g + q) * 64 + ff * 32 + 8 * pp; wp[ff] = cat(ldstr(a), ldstr(a + 1024)); }
    f32x4 cs[6]; float rs[4], mean[4], rl[4];
#pragma unroll
    for (int j = 0; j < 6; ++j) cs[j] = *(const LAS f32x4*)(size_t)(lb + L1_CS + (7 + j) * 64 + g * 16);
#pragma unroll
    for (int m = 0; m < 4; ++m) { const f32x2 st = *(const LAS f32x2*)(size_t)(lb + L1_ST + (wx * 64 + m * 16 + tok) * 8); rs[m] = st[0]; mean[m] = opqf(-st[1]); rl[m] = st[0] * LOG2E; }
    const f32x4 zero4 = {0.f, 0.f, 0.f, 0.f};
    auto project = [&](int u, f32x4 (&pr)[4][2]) {
#pragma unroll
      for (int m = 0; m < 4; ++m) { const bf16x8 kb = pk8(acc[u][m][0] + cs[2 * u] * mean[m], acc[u][m][1] + cs[2 * u + 1] * mean[m]); pr[m][0] = mfma(wp[0], kb, zero4); pr[m][1] = mfma(wp[1], kb, zero4); }
      SB; };
    auto extrema = [&](const f32x4 (&pr)[4][2], float (&ma)[4], float (&mb)[4]) {
#pragma unroll
      for (int m = 0; m < 4; ++m) { ma[m] = vmax8(pr[m][0], pr[m][1]); mb[m] = vmax8(-pr[m][0], -pr[m][1]); }
      spread4(gather4(ma[0], mb[0], ma[1], mb[1], vmax), ma[0], mb[0], ma[1], mb[1]);
      spread4(gather4(ma[2], mb[2], ma[3], mb[3], vmax), ma[2], mb[2], ma[3], mb[3]);
#pragma unroll
      for (int m = 0; m < 4; ++m) { ma[m] *= -rl[m]; mb[m] *= -rl[m]; }
      SB; };
    auto expo = [&](int m, const f32x4 (&pr)[2], float ma, float mb, f32x4 (&ep)[2], f32x4 (&en)[2]) {
#pragma unroll
      for (int ff = 0; ff < 2; ++ff) { ep[ff] = pr[ff] * rl[m] + ma; en[ff] = pr[ff] * -rl[m] + mb; }
      SB;
#pragma unroll
      for (int ff = 0; ff < 2; ++ff)
#pragma unroll
        for (int r = 0; r < 4; ++r) { ep[ff][r] = ex2(ep[ff][r]); en[ff][r] = ex2(en[ff][r]); }
      SB; };
    { const unsigned swp = (tok & 1) | (((tok >> 2) & 1) << 1) | (((tok >> 1) & 1) << 2) | (tok & 8);
      const unsigned wph = lb + L1_PHI + (wx * 64 + tok) * 128 + ((g ^ swp) << 3), wv = lb + L1_V + (wx * 64 + tok) * 64 + ((g ^ (tok >> 1)) << 3);
      const unsigned wp0 = opq(wph), wp1 = opq(wph ^ 32), wp2 = opq(wph ^ 64), wp3 = opq(wph ^ 96), wv0 = opq(wv), wv1 = opq(wv ^ 32);
      auto put = [](unsigned a, f32x4 v) { *(LAS u32x2*)(size_t)a = (u32x2){pk2(v[0], v[1]), pk2(v[2], v[3])}; };
      f32x4 pr[4][2], ep[4][2], en[4][2]; float ma[4], mb[4];
      WAIT_L0; project(1, pr);
#pragma unroll
      for (int m = 0; m < 4; ++m) { put(wv0 + m * 1024, (acc[2][m][0] + cs[4] * mean[m]) * rs[m]); put(wv1 + m * 1024, (acc[2][m][1] + cs[5] * mean[m]) * rs[m]); }
      SB; extrema(pr, ma, mb);
#pragma unroll
      for (int m = 0; m < 4; ++m) expo(m, pr[m], ma[m], mb[m], ep[m], en[m]);
#pragma unroll
      for (int m = 0; m < 4; ++m) { const f32x4 a = ep[m][0] + ep[m][1], c = en[m][0] + en[m][1]; const f32x2 a2 = a.xy + a.zw, c2 = c.xy + c.zw; ma[m] = a2[0] + opqf(a2[1]); mb[m] = c2[0] + opqf(c2[1]); }
      auto fadd = [](float a, float c) { return a + c; };
      spread4(rcp(gather4(ma[0], mb[0], ma[1], mb[1], fadd)), ma[0], mb[0], ma[1], mb[1]);
      spread4(rcp(gather4(ma[2], mb[2], ma[3], mb[3], fadd)), ma[2], mb[2], ma[3], mb[3]);
      SB;
#pragma unroll
      for (int m = 0; m < 4; ++m) { ep[m][0] *= ma[m]; ep[m][1] *= ma[m]; en[m][0] *= mb[m]; en[m][1] *= mb[m]; SB;
        put(wp0 + m * 2048, ep[m][0]); put(wp1 + m * 2048, ep[m][1]); put(wp2 + m * 2048, en[m][0]); put(wp3 + m * 2048, en[m][1]); SB; } }
    STAMP(8);
    asm volatile("s_waitcnt lgkmcnt(0)" ::: "memory");
    if (lane == 0) { const unsigned one = 1, a = lb + L1_CNT; asm volatile("ds_add_u32 %0, %1" :: "v"(a), "v"(one) : "memory"); }
    Query Q; project(0, Q.pq);
#pragma unroll
    for (int m = 0; m < 4; ++m) { Q.rl[m] = rl[m]; asm volatile("" : "+v"(Q.pq[m][0]), "+v"(Q.pq[m][1]), "+v"(Q.rl[m])); }
    SB;
    lds_wait(lb + L1_CNT, 4u); SB; STAMP(9);
    { const bf16x8 ones = {0x3F80, 0x3F80, 0x3F80, 0x3F80, 0x3F80, 0x3F80, 0x3F80, 0x3F80};
      unsigned ap[2], av[2][2];
#pragma unroll
      for (int t = 0; t < 2; ++t) { const int tk = 8 * g + 4 * t + q, t15 = tk & 15; const unsigned sw = (t15 & 1) | (((t15 >> 2) & 1) << 1) | (((t15 >> 1) & 1) << 2) | (t15 & 8);
        ap[t] = opq(lb + L1_PHI + tk * 128 + (((4 * wx + pp) ^ sw) << 3));
        av[t][0] = opq(lb + L1_V + tk * 64 + ((pp ^ (t15 >> 1)) << 3)); av[t][1] = opq(av[t][0] ^ 32); }
      bf16x8 a[8], b0[8], b1_[8];
#pragma unroll
      for (int ks = 0; ks < 8; ++ks) { a[ks] = cat(ldstr(ap[0] + ks * 4096), ldstr(ap[1] + ks * 4096));
        b0[ks] = cat(ldstr(av[0][0] + ks * 2048), ldstr(av[1][0] + ks * 2048)); b1_[ks] = cat(ldstr(av[0][1] + ks * 2048), ldstr(av[1][1] + ks * 2048)); }
      SB; STAMP(12);
      f32x4 kv0 = zero4, kv1 = zero4, zz = zero4;
#pragma unroll
      for (int ks = 0; ks < 8; ++ks) { kv0 = mfma(a[ks], b0[ks], kv0); kv1 = mfma(a[ks], b1_[ks], kv1); zz = mfma(a[ks], ones, zz); }
      const __amdgpu_buffer_rsrc_t rkv = mk_rsrc(ws + WS_KVP, 3u << 20);
      st16_sc1(__builtin_bit_cast(u32x4, pk8(kv0, kv1)), rkv, lane * 16, (h * 8 + p) * 4096 + wx * 1024);
      if (tok == 0) st16_sc1(__builtin_bit_cast(u32x4, zz), rkv, g * 16, (unsigned)(WS_ZP - WS_KVP) + (h * 8 + p) * 256 + wx * 64);
      SB; STAMP(13); }
    SB; if (YPRIO) __builtin_amdgcn_s_setprio(0); WAIT_L0; STAMP(11);
    Phase2<1> p2; p2.init(lb, lane, wx, P);
    P2RUN(p2, P, tag, Q);
    }
  }
}
}

extern "C" void kernel_launch(void* const* d_in, const int* in_sizes, int n_in, void* d_out, int out_size, void* d_ws, size_t ws_size, hipStream_t stream) {
  k_fused<<<256, 512, 0, stream>>>((const u16*)d_in[0], (const u16*)d_in[1], (const u16*)d_in[2], (const u16*)d_in[3], (const u16*)d_in[5], (const u16*)d_in[6], (const u16*)d_in[7], (char*)d_ws, (u16*)d_out);
}
```
